```python
import torch

def _softmax(x, axis=-1):
    return torch.softmax(x, dim=axis)
def _gelu(x):
    return torch.nn.functional.gelu(x, approximate="tanh")
def _silu(x):
    return torch.nn.functional.silu(x)
def _sigmoid(x):
    return torch.sigmoid(x)
def _layernorm(x, axis=-1, eps=1e-5):
    m = x.mean(dim=axis, keepdim=True)
    v = x.var(dim=axis, keepdim=True, unbiased=False)
    return (x - m) / torch.sqrt(v + eps)
def _scatter_add(tgt, idx, src, axis=0):
    return tgt.clone().index_add_(axis, idx, src)
def _segment_sum(src, seg, n):
    out = torch.zeros((n,) + src.shape[1:], dtype=src.dtype)
    return out.index_add_(0, seg, src)
def _pad(x, pw):
    flat = tuple(v for pair in reversed(pw) for v in pair)
    return torch.nn.functional.pad(x, flat)

kernel_name = 'concat_heads__ssm_conv_s5__grouped_kv_mqa__prompt'

def setup_inputs():

    _w = (lambda a: (lambda t: t.to(torch.bfloat16) if t.is_floating_point() else t)(torch.as_tensor(a)))
    def _A_setup():
        B, S, D = 1, 2048, 448
        P = D
        Lambda_re = -0.5 * (lambda _x: torch.exp(torch.as_tensor(_x)))((lambda *s: torch.randn(*s, dtype=torch.bfloat16) * 0.4)(P))
        Lambda_im = (lambda *s: torch.randn(*s, dtype=torch.bfloat16) * 0.8)(P) * 1.0
        B_re = (lambda *s: torch.randn(*s, dtype=torch.bfloat16) * 0.4)(P, D) * (D ** -0.5)
        B_im = (lambda *s: torch.randn(*s, dtype=torch.bfloat16) * 0.4)(P, D) * (D ** -0.5)
        C_re = (lambda *s: torch.randn(*s, dtype=torch.bfloat16) * 0.4)(D, P) * (P ** -0.5)
        C_im = (lambda *s: torch.randn(*s, dtype=torch.bfloat16) * 0.4)(D, P) * (P ** -0.5)
        log_dt = (lambda *s: torch.randn(*s, dtype=torch.bfloat16) * 0.4)(P) * 0.3 - 2.0
        Dskip = (lambda *s: torch.randn(*s, dtype=torch.bfloat16) * 0.1)(D)
        return {
            "x": _w((lambda *s: torch.randn(*s, dtype=torch.bfloat16) * 0.05)(B, S, D)),
            "Lambda_re": _w(Lambda_re),
            "Lambda_im": _w(Lambda_im),
            "B_re": _w(B_re),
            "B_im": _w(B_im),
            "C_re": _w(C_re),
            "C_im": _w(C_im),
            "log_dt": _w(log_dt),
            "Dskip": _w(Dskip),
        }
    _A_in = _A_setup()

    def _B_setup():
        B, S, D, H = 1, 2048, 448, 16
        Dh = D // H
        return {
            "x": _w((lambda *s: torch.randn(*s, dtype=torch.bfloat16) * 0.05)(B, S, D)),
            "wq": _w((lambda *s: torch.randn(*s, dtype=torch.bfloat16) * 1.6)(D, H * Dh)),
            "wk": _w((lambda *s: torch.randn(*s, dtype=torch.bfloat16) * 1.6)(D, Dh)),
            "wv": _w((lambda *s: torch.randn(*s, dtype=torch.bfloat16) * 0.05)(D, Dh)),
            "wo": _w((lambda *s: torch.randn(*s, dtype=torch.bfloat16) * 0.05)(H * Dh, D)),
        }
    _B_in = _B_setup()

    return {
        "A_x": _A_in["x"],
        "A_Lambda_re": _A_in["Lambda_re"],
        "A_Lambda_im": _A_in["Lambda_im"],
        "A_B_re": _A_in["B_re"],
        "A_B_im": _A_in["B_im"],
        "A_C_re": _A_in["C_re"],
        "A_C_im": _A_in["C_im"],
        "A_log_dt": _A_in["log_dt"],
        "A_Dskip": _A_in["Dskip"],
        "B_x": _B_in["x"],
        "B_wq": _B_in["wq"],
        "B_wk": _B_in["wk"],
        "B_wv": _B_in["wv"],
        "B_wo": _B_in["wo"],
    }

def reference(A_x, A_Lambda_re, A_Lambda_im, A_B_re, A_B_im, A_C_re, A_C_im, A_log_dt, A_Dskip, B_x, B_wq, B_wk, B_wv, B_wo):
    def _A_ref(x, Lambda_re, Lambda_im, B_re, B_im, C_re, C_im, log_dt, Dskip):
        Bsz, L, H = x.shape
        Lam = Lambda_re + 1j * Lambda_im
        Bmat = B_re + 1j * B_im
        Cmat = C_re + 1j * C_im
        dt = torch.exp(log_dt)
        Lam_bar = torch.exp(dt * Lam)
        B_bar = ((Lam_bar - 1.0) / Lam)[:, None] * Bmat
        Bu = torch.einsum("bld,pd->blp", x + 0j, B_bar)
        h = torch.zeros_like(Bu[:, 0, :])
        hs = []
        for t in range(L):
            h = Lam_bar[None, :] * h + Bu[:, t, :]
            hs.append(h)
        hseq = torch.stack(hs, 1)
        y = 2.0 * torch.einsum("blp,dp->bld", hseq, Cmat).real + x * Dskip[None, None, :]
        return y

    def _B_ref(x, wq, wk, wv, wo):
        B, S, D = x.shape
        H = 16
        Dh = D // H
        q = (lambda x, *d: x.permute(*d))((x @ wq).reshape(B, S, H, Dh), 0, 2, 1, 3)
        k = x @ wk
        v = x @ wv
        scores = torch.einsum("bhqd,bkd->bhqk", q, k) * (Dh ** -0.5)
        p = _softmax(scores, axis=-1)
        o = torch.einsum("bhqk,bkd->bhqd", p, v)
        return (lambda x, *d: x.permute(*d))(o, 0, 2, 1, 3).reshape(B, S, H * Dh) @ wo

    a = _A_ref(A_x, A_Lambda_re, A_Lambda_im, A_B_re, A_B_im, A_C_re, A_C_im, A_log_dt, A_Dskip)
    b = _B_ref(B_x, B_wq, B_wk, B_wv, B_wo)
    return torch.concatenate([a, b], axis=-1)
```

```cpp
#include <hip/hip_runtime.h>
#include <hip/hip_bf16.h>
#include <stdint.h>

typedef unsigned short u16;
__device__ __forceinline__ float bf2f(u16 v){ return __uint_as_float(((unsigned)v)<<16); }
__device__ __forceinline__ u16 f2bf(float f){ __hip_bfloat16 b = __float2bfloat16(f); return *(u16*)&b; }
__device__ __forceinline__ float rbf(float f){ return bf2f(f2bf(f)); }

constexpr int S=2048, D=448, P=448, H=16, DH=28;

__global__ void k_params(const u16* lre, const u16* lim, const u16* ldt, float* prm){
  int p = blockIdx.x*blockDim.x+threadIdx.x; if(p>=P) return;
  float a=bf2f(lre[p]), b=bf2f(lim[p]); float dt=rbf(expf(bf2f(ldt[p])));
  float er=expf(dt*a); float lr=er*cosf(dt*b), li=er*sinf(dt*b);
  float nr=lr-1.f, ni=li; float den=a*a+b*b;
  float cr=(nr*a+ni*b)/den, ci=(ni*a-nr*b)/den;
  prm[p*4+0]=lr; prm[p*4+1]=li; prm[p*4+2]=cr; prm[p*4+3]=ci;
}
__global__ void k_bu(const u16* x, const u16* bre, const u16* bim, const float* prm, float* bu){
  int p = threadIdx.x; int t=blockIdx.x; if(p>=P) return;
  float sr=0, si=0;
  for(int d=0; d<D; d++){ float xv=bf2f(x[t*D+d]); sr+=xv*bf2f(bre[p*D+d]); si+=xv*bf2f(bim[p*D+d]); }
  float cr=prm[p*4+2], ci=prm[p*4+3];
  bu[(t*P+p)*2+0]=cr*sr-ci*si; bu[(t*P+p)*2+1]=cr*si+ci*sr;
}
__global__ void k_scan(const float* prm, float* bu){
  int p = blockIdx.x*blockDim.x+threadIdx.x; if(p>=P) return;
  float lr=prm[p*4+0], li=prm[p*4+1]; float hr=0, hi=0;
  for(int t=0;t<S;t++){ float ur=bu[(t*P+p)*2], ui=bu[(t*P+p)*2+1];
    float nr=lr*hr-li*hi+ur, ni=lr*hi+li*hr+ui; hr=nr; hi=ni; bu[(t*P+p)*2]=hr; bu[(t*P+p)*2+1]=hi; }
}
__global__ void k_y(const u16* x, const u16* cre, const u16* cim, const u16* dsk, const float* h, float* out){
  int d=threadIdx.x; int t=blockIdx.x; if(d>=D) return;
  float s=0;
  for(int p=0;p<P;p++){ s+=h[(t*P+p)*2]*bf2f(cre[d*P+p]) - h[(t*P+p)*2+1]*bf2f(cim[d*P+p]); }
  out[t*896+d]=2.f*s+rbf(bf2f(x[t*D+d])*bf2f(dsk[d]));
}
__global__ void k_qkv(const u16* x, const u16* wq, const u16* wk, const u16* wv, float* q, float* k, float* v){
  int n=threadIdx.x; int t=blockIdx.x; if(n>=504) return;
  float s=0;
  if(n<448){ for(int d=0;d<D;d++) s+=bf2f(x[t*D+d])*bf2f(wq[d*448+n]); q[t*448+n]=rbf(s);}
  else if(n<476){ int m=n-448; for(int d=0;d<D;d++) s+=bf2f(x[t*D+d])*bf2f(wk[d*28+m]); k[t*28+m]=rbf(s);}
  else { int m=n-476; for(int d=0;d<D;d++) s+=bf2f(x[t*D+d])*bf2f(wv[d*28+m]); v[t*28+m]=rbf(s);}
}
__global__ void k_attn(const float* q, const float* k, const float* v, float* o){
  int row = blockIdx.x*blockDim.x+threadIdx.x;
  int t=row/16, h=row%16; float qq[28]; for(int d=0;d<28;d++) qq[d]=q[t*448+h*28+d];
  float m=-1e30f, l=0; float acc[28]; for(int d=0;d<28;d++) acc[d]=0;
  const float sc=0.18898223650461361f;
  for(int j=0;j<S;j++){ float s=0; for(int d=0;d<28;d++) s+=qq[d]*k[j*28+d]; s*=sc;
    float mn=fmaxf(m,s); float a=__expf(m-mn), p=__expf(s-mn); l=l*a+p; for(int d=0;d<28;d++) acc[d]=acc[d]*a+p*v[j*28+d]; m=mn; }
  for(int d=0;d<28;d++) o[t*448+h*28+d]=rbf(acc[d]/l);
}
__global__ void k_out(const float* o, const u16* wo, float* out){
  int n=threadIdx.x; int t=blockIdx.x; if(n>=448) return;
  float s=0; for(int d=0;d<448;d++) s+=o[t*448+d]*bf2f(wo[d*448+n]);
  out[t*896+448+n]=rbf(s);
}

extern "C" void kernel_launch(void* const* d_in, const int* in_sizes, int n_in,
                              void* d_out, int out_size, void* d_ws, size_t ws_size,
                              hipStream_t stream) {
  const u16* Ax=(const u16*)d_in[0]; const u16* lre=(const u16*)d_in[1]; const u16* lim=(const u16*)d_in[2];
  const u16* bre=(const u16*)d_in[3]; const u16* bim=(const u16*)d_in[4]; const u16* cre=(const u16*)d_in[5]; const u16* cim=(const u16*)d_in[6];
  const u16* ldt=(const u16*)d_in[7]; const u16* dsk=(const u16*)d_in[8];
  const u16* Bx=(const u16*)d_in[9]; const u16* wq=(const u16*)d_in[10]; const u16* wk=(const u16*)d_in[11]; const u16* wv=(const u16*)d_in[12]; const u16* wo=(const u16*)d_in[13];
  float* out=(float*)d_out; float* ws=(float*)d_ws;
  float* prm=ws; float* bu=ws+4096; float* q=bu+S*P*2; float* k=q+S*448; float* v=k+S*28; float* o=v+S*28;
  k_params<<<2,256,0,stream>>>(lre,lim,ldt,prm);
  k_bu<<<S,448,0,stream>>>(Ax,bre,bim,prm,bu);
  k_scan<<<7,64,0,stream>>>(prm,bu);
  k_y<<<S,448,0,stream>>>(Ax,cre,cim,dsk,bu,out);
  k_qkv<<<S,512,0,stream>>>(Bx,wq,wk,wv,q,k,v);
  k_attn<<<S*16/64,64,0,stream>>>(q,k,v,o);
  k_out<<<S,448,0,stream>>>(o,wo,out);
}
```

```cpp
#include <hip/hip_runtime.h>
#include <stdint.h>

#define DI __device__ __forceinline__
#define GAS __attribute__((address_space(1)))
#define LAS __attribute__((address_space(3)))
#define CAS __attribute__((address_space(4)))
typedef short bf16x8 __attribute__((ext_vector_type(8)));
typedef short s16x4 __attribute__((ext_vector_type(4)));
typedef float f32x16 __attribute__((ext_vector_type(16)));
typedef float f32x4 __attribute__((ext_vector_type(4)));
typedef float f32x2 __attribute__((ext_vector_type(2)));
typedef unsigned u32x4 __attribute__((ext_vector_type(4)));
typedef unsigned u32x2 __attribute__((ext_vector_type(2)));
typedef __bf16 bf16x2_t __attribute__((ext_vector_type(2)));
typedef unsigned long long u64;
typedef unsigned short u16;
typedef LAS char* lptr;
#define RLX_AGENT __ATOMIC_RELAXED, __HIP_MEMORY_SCOPE_AGENT
#define RLX_WG __ATOMIC_RELAXED, __HIP_MEMORY_SCOPE_WORKGROUP
#define MFMA(a, b, c) __builtin_amdgcn_mfma_f32_32x32x16_bf16((a), (b), (c), 0, 0, 0)
#define SBAR() __builtin_amdgcn_sched_barrier(0)
#define PIN(x) asm volatile("" : "+v"(x))
#define MEMBAR() asm volatile("" ::: "memory")
#define SC1 16
#define APRIO 1
#define GPRIO 1
#define CPACE 6

constexpr unsigned WS_EPOCH = 0, WS_STARTED = 1024, WS_FKV = 4096, WS_FE = 8192, WS_FH = 16384, WS_E = 32768,
                   WS_KV = 0x40000, WS_O = 0x80000, WS_H = 0x280000, WS_END = 0x600000, WS_DBG = 0x600000;
constexpr unsigned SL = 39936, S_XB = 0, S_WQ = 8192, S_XA = 15360, S_BR = 23552, S_BI = 31744;
constexpr unsigned SK = 15360, K_W = 8192, K_WQ = 107520;
constexpr unsigned L_QS = 99328, L_R1 = 107520, L_HT = 136192, L_WO = 0, L_OC = 57856, L_G2 = 107520, L_MISC = 159744, L_EX = L_MISC + 128, L_TOTAL = 163840;
constexpr unsigned SPIN_CAP = 1u << 18;

struct Params {
  const u16 *xa, *lre, *lim, *bre, *bim, *cre, *cim, *ldt, *dsk, *xb, *wq, *wk, *wv, *wo;
  float* out; char* ws;
};

DI float bf2f(unsigned v) { return __uint_as_float(v << 16); }
DI unsigned pk(float a, float b) { f32x2 v = {a, b}; return __builtin_bit_cast(unsigned, __builtin_convertvector(v, bf16x2_t)); }
DI float rbf(float a) { return bf2f(pk(a, 0.f) & 0xffffu); }
DI bf16x8 pack8(const f32x16& x, int s) {
  u32x4 p = {pk(x[8 * s], x[8 * s + 1]), pk(x[8 * s + 2], x[8 * s + 3]), pk(x[8 * s + 4], x[8 * s + 5]), pk(x[8 * s + 6], x[8 * s + 7])};
  return __builtin_bit_cast(bf16x8, p);
}
DI void glds(const void* base, unsigned off, unsigned lds) {
  unsigned sv;
  asm volatile("s_mov_b32 %0, m0\n\ts_mov_b32 m0, %3\n\ts_nop 0\n\tglobal_load_lds_dwordx4 %1, %2\n\ts_mov_b32 m0, %0"
               : "=&s"(sv) : "v"(off), "s"(base), "s"(lds) : "memory");
}
DI bf16x8 ldsf(lptr p) { return *(LAS bf16x8*)p; }
DI s16x4 ldstr(lptr p) { return __builtin_amdgcn_ds_read_tr16_b64_v4i16((LAS s16x4*)p); }
DI bf16x8 ldstr2(lptr p, int second) { s16x4 lo = ldstr(p), hi = ldstr(p + second); return __builtin_shufflevector(lo, hi, 0, 1, 2, 3, 4, 5, 6, 7); }
DI void lds_signal(lptr c, int lane) {
  asm volatile("s_waitcnt lgkmcnt(0)" ::: "memory");
  if (lane == 0) __hip_atomic_fetch_add((LAS unsigned*)c, 1u, RLX_WG);
}
DI void glds64(const void* addr, unsigned lds) {
  unsigned sv;
  asm volatile("s_mov_b32 %0, m0\n\ts_mov_b32 m0, %2\n\ts_nop 0\n\tglobal_load_lds_dwordx4 %1, off\n\ts_mov_b32 m0, %0"
               : "=&s"(sv) : "v"(addr), "s"(lds) : "memory");
}
DI void lds_wait(lptr c, unsigned target) {
#pragma unroll 1
  for (unsigned s = 0; s < (SPIN_CAP << 4); ++s) {
    if ((unsigned)__builtin_amdgcn_readfirstlane(__hip_atomic_load((LAS unsigned*)c, RLX_WG)) >= target) break;
  }
  MEMBAR();
}
DI void lds_barrier(lptr c, unsigned target, int lane) { lds_signal(c, lane); lds_wait(c, target); }
DI void poll(const char* ws, unsigned off, u64 tag) {
#pragma unroll 1
  for (unsigned s = 0; s < SPIN_CAP; ++s) {
    u64 v = __hip_atomic_load((GAS u64*)(ws + off), RLX_AGENT);
    if (__all(v == tag)) break;
    __builtin_amdgcn_s_sleep(1);
  }
  MEMBAR();
}
DI void publish(char* ws, unsigned off, u64 tag, int lane) {
  asm volatile("s_waitcnt vmcnt(0)" ::: "memory");
  if (lane == 0) __hip_atomic_store((GAS u64*)(ws + off), tag, RLX_AGENT);
}
DI float swap_lo(float x) {
  auto r = __builtin_amdgcn_permlane32_swap(__float_as_uint(x), __float_as_uint(x), false, false); return __uint_as_float(r[0]); }
DI float swap_hi(float x) {
  auto r = __builtin_amdgcn_permlane32_swap(__float_as_uint(x), __float_as_uint(x), false, false); return __uint_as_float(r[1]); }
struct cf { float r, i; };
DI cf cmul(cf a, cf b) { return {a.r * b.r - a.i * b.i, a.r * b.i + a.i * b.r}; }
DI cf cfma(cf a, cf b, cf c) { return {fmaf(a.r, b.r, fmaf(-a.i, b.i, c.r)), fmaf(a.r, b.i, fmaf(a.i, b.r, c.i))}; }

#define STAMP(k) do {} while (0)

struct KV { bf16x8 k0, k1, v0, v1; };

DI void rs_write(lptr base, int w, int lane, const f32x16& t1, const f32x16& t2, const f32x16& t3) {
#define RSW(T, I) _Pragma("unroll") for (int q = 0; q < 4; ++q) *(LAS f32x4*)(base + ((w ^ I) * 3 + I - 1) * 4096 + q * 1024 + lane * 16) = (f32x4){T[4 * q], T[4 * q + 1], T[4 * q + 2], T[4 * q + 3]}
  RSW(t1, 1); RSW(t2, 2); RSW(t3, 3);
}
DI f32x16 rs_read(lptr base, int w, int lane, f32x16 a) {
#pragma unroll
  for (int s = 0; s < 3; ++s)
#pragma unroll
    for (int q = 0; q < 4; ++q) {
      f32x4 v = *(LAS f32x4*)(base + (w * 3 + s) * 4096 + q * 1024 + lane * 16);
      a[4 * q] += v[0]; a[4 * q + 1] += v[1]; a[4 * q + 2] += v[2]; a[4 * q + 3] += v[3];
    }
  return a;
}

#define VMW_(n) asm volatile("s_waitcnt vmcnt(" #n ")" ::: "memory")
#define VMW(n) VMW_(n)
#define BAR() asm volatile("s_waitcnt lgkmcnt(0)\n\ts_barrier" ::: "memory")
#define BLD(R, V, S) __builtin_amdgcn_raw_buffer_load_b128(R, V, S, SC1)
#define BF(x) __builtin_bit_cast(bf16x8, x)
#define EX(x) __builtin_amdgcn_exp2f(x)

__global__ void __launch_bounds__(512, 2) fused_kernel(Params p) {
  __shared__ __attribute__((aligned(128))) char smem[L_TOTAL];
  const lptr lds = (lptr)smem;
  const unsigned lds0 = (unsigned)(uintptr_t)smem;
  const int tid = threadIdx.x, lane = tid & 63, wave = __builtin_amdgcn_readfirstlane(tid >> 6);
  const int r32 = lane & 31, hi = lane >> 5;
  const int b = blockIdx.x, loc = b >> 3, c = (b & 7) * 4 + (loc >> 3), j = loc & 7;
  char* const ws = p.ws;
  u64 tag;
  const __amdgpu_buffer_rsrc_t rs_ws = __builtin_amdgcn_make_buffer_rsrc(ws, 0, WS_END, 0x00020000);
  const f32x16 zero16 = {};
  const int g4 = lane >> 4, tq = (lane & 15) >> 2, tp = lane & 3, lrow = lane >> 3;
  const unsigned xsw = (r32 >> 1) & 7, lo16 = lane * 16;
  const lptr cBB = lds + L_MISC, cAB = cBB + 4, cCR = cBB + 8, cWO = cBB + 12, cOC = cBB + 16;
  const bool isB = wave < 4;
  const int wa = wave & 3;
  STAMP(0);
  const unsigned vrow = (8 * wave + lrow) * 896 + (((lane & 7) ^ ((4 * wave + (lrow >> 1)) & 7)) << 4);
  const char* const xb = (const char*)p.xb + (size_t)c * 57344;
  const char* const wq = (const char*)p.wq + j * 112;
  const unsigned o_own = WS_O + (((2 * c + (wa >> 1)) * 16 + 2 * j + (wa & 1)) * 2) * 1024 + lo16;
  if (isB) { const u32x4 ones = {~0u, ~0u, ~0u, ~0u};
    __builtin_amdgcn_raw_buffer_store_b128(ones, rs_ws, o_own, 0, SC1); __builtin_amdgcn_raw_buffer_store_b128(ones, rs_ws, o_own + 1024, 0, SC1); }
  if (tid < 32) ((LAS unsigned*)(lds + L_MISC))[tid] = 0;
  const unsigned xq_off = (32 * (wa >> 1) + r32) * 128;
  const unsigned wq_off = (8 * (g4 >> 1) + tq) * 112 + (28 * (wa & 1) + 16 * (g4 & 1) + 4 * tp) * 2;
  f32x16 q = zero16;
#define QCHUNK(X, W) do { bf16x8 xf_[4], wf_[4]; \
    _Pragma("unroll") for (int ks = 0; ks < 4; ++ks) { xf_[ks] = ldsf(lds + (X) + xq_off + (((2 * ks + hi) ^ xsw) << 4)); wf_[ks] = ldstr2(lds + (W) + wq_off + ks * 1792, 448); } \
    SBAR(); _Pragma("unroll") for (int ks = 0; ks < 4; ++ks) q = MFMA(wf_[ks], xf_[ks], q); PIN(q); } while (0)

  const int th = wa >> 1, chh = wa & 1;
  cf lam, coef, l16, l64; f32x16 ur = zero16, ui = zero16; unsigned xd[8];

  if (j == 7) {
    const int iw = wave * 64 + lane;
    const char* wsrc = iw < 224 ? (const char*)p.wk + iw * 16 : (const char*)p.wv + (iw - 224) * 16;
#pragma unroll
    for (int kc = 0; kc < 7; ++kc) {
      glds(xb + kc * 128, vrow, lds0 + kc * SK + wave * 1024);
      if (wave < 7) glds64(wsrc + kc * 3584, lds0 + kc * SK + K_W + wave * 1024);
    }
    if (isB) {
#pragma unroll
      for (int m = 0; m < 13; ++m) { const int n = wave + 4 * m;
        if (n < 49) { const int i = n * 64 + lane, row = (i * 9363) >> 16; glds(wq, row * 896 + (i - row * 7) * 16, lds0 + K_WQ + n * 1024); } }
    }
    asm volatile("s_load_dwordx2 %0, %1, 0x0" : "=s"(tag) : "s"(ws) : "memory");
    STAMP(8);
    const int isv = wa >> 1;
    const unsigned xk_off = (32 * (wa & 1) + r32) * 128, wk_off = K_W + isv * 3584 + (8 * (g4 >> 1) + tq) * 56 + (16 * (g4 & 1) + 4 * tp) * 2;
    f32x16 a = zero16;
#define KVSTEP(kc, NB, NA, N7) do { if (isB) { if (wave == 0) VMW(NB); else asm volatile("s_waitcnt vmcnt(" #NB "-1)" ::: "memory"); } else if (wave < 7) VMW(NA); else VMW(N7); BAR(); \
      if (!isB) { bf16x8 xf_[4], wf_[4]; \
        _Pragma("unroll") for (int ks = 0; ks < 4; ++ks) { xf_[ks] = ldsf(lds + kc * SK + xk_off + (((2 * ks + hi) ^ xsw) << 4)); wf_[ks] = ldstr2(lds + kc * SK + wk_off + ks * 896, 224); } \
        SBAR(); if (isv) { _Pragma("unroll") for (int ks = 0; ks < 4; ++ks) a = MFMA(xf_[ks], wf_[ks], a); } else { _Pragma("unroll") for (int ks = 0; ks < 4; ++ks) a = MFMA(wf_[ks], xf_[ks], a); } PIN(a); } } while (0)
    KVSTEP(0, 25, 12, 6); asm volatile("" : "+s"(tag)); tag += 1; KVSTEP(1, 23, 10, 5); KVSTEP(2, 21, 8, 4); KVSTEP(3, 19, 6, 3); KVSTEP(4, 17, 4, 2); KVSTEP(5, 15, 2, 1); KVSTEP(6, 13, 0, 0);
    STAMP(1);
    if (!isB) {
      if (isv) {
#pragma unroll
        for (int r = 0; r < 16; ++r) a[r] = r32 < 28 ? a[r] : (r32 == 28 ? 1.f : 0.f);
      } else if (hi) { a[12] = 1.f; a[13] = 1.f; a[14] = 1.f; a[15] = 0.f; }
      const unsigned ko = WS_KV + (2 * c + (wa & 1)) * 4096 + isv * 2048 + lo16;
      __builtin_amdgcn_raw_buffer_store_b128(__builtin_bit_cast(u32x4, pack8(a, 0)), rs_ws, ko, 0, SC1);
      __builtin_amdgcn_raw_buffer_store_b128(__builtin_bit_cast(u32x4, pack8(a, 1)), rs_ws, ko + 1024, 0, SC1);
      publish(ws, WS_FKV + (c * 4 + wa) * 8, tag, lane);
      STAMP(2);
      lds_signal(cCR, lane);
      if (c == 0 && wa == 0) {
        for (int k = 0; k < 4; ++k) poll(ws, WS_STARTED + (k * 64 + lane) * 8, tag);
        if (lane == 0) __hip_atomic_store((GAS u64*)(ws + WS_EPOCH), tag, RLX_AGENT);
      }
      return;
    }
    VMW(0);
    lds_barrier(cOC, 4, lane);
#pragma unroll
    for (int kc = 0; kc < 7; ++kc) QCHUNK(kc * SK, K_WQ + kc * 7168);
    lds_wait(cCR, 4);
    lds_barrier(cOC, 8, lane);
  } else {
    unsigned ua = 0, ub = 0, uc = 0, ud = 0;
    if (!isB) { const unsigned co = (64 * j + 32 * chh + r32) * 2;
      asm volatile("global_load_ushort %0, %4, %5\n\tglobal_load_ushort %1, %4, %6\n\tglobal_load_ushort %2, %4, %7\n\tglobal_load_ushort %3, %4, %8"
                   : "=&v"(ua), "=&v"(ub), "=&v"(uc), "=&v"(ud) : "v"(co), "s"(p.lre), "s"(p.lim), "s"(p.ldt), "s"(p.dsk) : "memory"); }
    const int iw = wave * 64 + lane, wrow = (iw * 9363) >> 16;
    const unsigned vwq = wrow * 896 + (iw - wrow * 7) * 16;
    const char* const xa = (const char*)p.xa + (size_t)c * 57344;
    const char* const br = (const char*)p.bre + (size_t)j * 57344;
    const char* const bi = (const char*)p.bim + (size_t)j * 57344;
#define LDCH(kc) do { const unsigned s_ = lds0 + ((kc) & 3) * SL + wave * 1024; \
      glds(xb + (kc) * 128, vrow, s_ + S_XB); if (wave < 7) glds(wq + (kc) * 57344, vwq, s_ + S_WQ); \
      glds(xa + (kc) * 128, vrow, s_ + S_XA); glds(br + (kc) * 128, vrow, s_ + S_BR); glds(bi + (kc) * 128, vrow, s_ + S_BI); } while (0)
    LDCH(0); LDCH(1); LDCH(2); LDCH(3);
    asm volatile("s_load_dwordx2 %0, %1, 0x0" : "=s"(tag) : "s"(ws) : "memory");
    STAMP(8);
    float pd = 0.f;
    if (!isB) {
      asm volatile("s_waitcnt vmcnt(16)" : "+v"(ua), "+v"(ub), "+v"(uc), "+v"(ud) :: "memory");
      const float pa = bf2f(ua), pb = bf2f(ub), pc = bf2f(uc); pd = bf2f(ud);
      const float dt = rbf(expf(pc));
      const float er = expf(dt * pa); float sn, cs; sincosf(dt * pb, &sn, &cs);
      lam = {er * cs, er * sn};
      const float nr = lam.r - 1.f, ni = lam.i, den = pa * pa + pb * pb;
      coef = {2.f * (nr * pa + ni * pb) / den, 2.f * (ni * pa - nr * pb) / den};
      l16 = cmul(lam, lam); l16 = cmul(l16, l16); l16 = cmul(l16, l16); l16 = cmul(l16, l16);
      l64 = cmul(l16, l16); l64 = cmul(l64, l64);
    }
    const int trow = 32 * th + 16 * ((r32 >> 2) & 1) + (r32 & 3) + 4 * (r32 >> 3);
    const unsigned xa_off = S_XA + trow * 128, xa_sw = (trow >> 1) & 7, b_off = S_BR + (32 * chh + r32) * 128;
#define STEP(kc, N, N7) do { if (wave < 7) VMW(N); else VMW(N7); BAR(); \
      if ((kc) >= 1 && (kc) <= 3) LDCH((kc) + 3); \
      const unsigned s_ = ((kc) & 3) * SL; \
      if (isB) QCHUNK(s_ + S_XB, s_ + S_WQ); \
      else { bf16x8 xf_[4], rf_[4], if_[4]; \
        _Pragma("unroll") for (int ks = 0; ks < 4; ++ks) { xf_[ks] = ldsf(lds + s_ + xa_off + (((2 * ks + hi) ^ xa_sw) << 4)); \
          rf_[ks] = ldsf(lds + s_ + b_off + (((2 * ks + hi) ^ xsw) << 4)); if_[ks] = ldsf(lds + s_ + b_off + 8192 + (((2 * ks + hi) ^ xsw) << 4)); } \
        SBAR(); _Pragma("unroll") for (int ks = 0; ks < 4; ++ks) { ur = MFMA(xf_[ks], rf_[ks], ur); ui = MFMA(xf_[ks], if_[ks], ui); } PIN(ur); PIN(ui); \
        if (j == (kc)) {                                       \
          _Pragma("unroll") for (int r = 0; r < 8; ++r) { const int r0 = 32 * th + 4 * hi + ((2 * r) & 3) + 8 * (r >> 1), cq = (32 * chh + r32) >> 3; \
            const unsigned x0 = *(LAS u16*)(lds + s_ + S_XA + r0 * 128 + ((cq ^ ((r0 >> 1) & 7)) << 4) + (r32 & 7) * 2); \
            const unsigned x1 = *(LAS u16*)(lds + s_ + S_XA + (r0 + 1) * 128 + ((cq ^ ((r0 >> 1) & 7)) << 4) + (r32 & 7) * 2); \
            xd[r] = pk(bf2f(x0) * pd, bf2f(x1) * pd); } } } } while (0)
    STEP(0, 15, 12); asm volatile("" : "+s"(tag)); tag += 1; STEP(1, 10, 8); STEP(2, 10, 8); STEP(3, 10, 8); STEP(4, 10, 8); STEP(5, 5, 4); STEP(6, 0, 0);
    STAMP(1);
    BAR();
  }

  if (isB) {
    if (tid == 0) __hip_atomic_store((GAS u64*)(ws + WS_STARTED) + b, tag, RLX_AGENT);
    const float qs = 0.18898223650461361f * 1.4426950408889634f;
#pragma unroll
    for (int r = 0; r < 16; ++r) q[r] *= qs;
    if (hi) { q[12] = 0.f; q[13] = 0.f; q[14] = 0.f; q[15] = 0.f; }
    const bf16x8 qo0 = pack8(q, 0), qo1 = pack8(q, 1);
    *(LAS bf16x8*)(lds + L_QS + wave * 2048 + lo16) = qo0;
    *(LAS bf16x8*)(lds + L_QS + wave * 2048 + 1024 + lo16) = qo1;
    lds_signal(cBB, lane);
    STAMP(2);
    poll(ws, WS_FKV + (wave * 32 + r32) * 8, tag);
    STAMP(3);
    const __amdgpu_buffer_rsrc_t rs_kv = __builtin_amdgcn_make_buffer_rsrc(ws + WS_KV + wave * 65536, 0, 65536, 0x00020000);
#define LDKV(B, T) do { B.k0 = BF(BLD(rs_kv, lo16, (T) * 4096)); B.k1 = BF(BLD(rs_kv, lo16 + 1024, (T) * 4096)); \
                        B.v0 = BF(BLD(rs_kv, lo16 + 2048, (T) * 4096)); B.v1 = BF(BLD(rs_kv, lo16 + 3072, (T) * 4096)); } while (0)
    KV A0, A1, A2, A3;
    LDKV(A0, 0); LDKV(A1, 1); LDKV(A2, 2);
    A3.k0 = A3.k1 = A3.v0 = A3.v1 = (bf16x8){};
    lds_wait(cBB, 4);
    bf16x8 Q0[4], Q1[4];
    Q0[0] = qo0; Q1[0] = qo1;
#pragma unroll
    for (int i = 1; i < 4; ++i) { Q0[i] = ldsf(lds + L_QS + (wave ^ i) * 2048 + lo16); Q1[i] = ldsf(lds + L_QS + (wave ^ i) * 2048 + 1024 + lo16); }
    f32x16 O[4] = {zero16, zero16, zero16, zero16};
    f32x16 Sa, Sb = zero16; u32x4 Pa, Pb = {};
    Sa = MFMA(A0.k0, Q0[0], zero16); Sa = MFMA(A0.k1, Q1[0], Sa);
    STAMP(9);
#define UNIT(S, Sn, KN, QN, VP, OP, VC, OC) do { \
      Pb[2] = pk(Sn[12], Sn[13]); Pb[3] = pk(Sn[14], Sn[15]); \
      Sn = MFMA(KN.k0, Q0[QN], zero16); S[0] = EX(S[0]); S[1] = EX(S[1]); S[2] = EX(S[2]); S[3] = EX(S[3]); SBAR(); \
      Sn = MFMA(KN.k1, Q1[QN], Sn); Pa[0] = pk(S[0], S[1]); Pa[1] = pk(S[2], S[3]); S[4] = EX(S[4]); S[5] = EX(S[5]); S[6] = EX(S[6]); S[7] = EX(S[7]); SBAR(); \
      OP = MFMA(VP.v1, BF(Pb), OP); Pa[2] = pk(S[4], S[5]); Pa[3] = pk(S[6], S[7]); S[8] = EX(S[8]); S[9] = EX(S[9]); S[10] = EX(S[10]); S[11] = EX(S[11]); SBAR(); \
      OC = MFMA(VC.v0, BF(Pa), OC); Pb[0] = pk(S[8], S[9]); Pb[1] = pk(S[10], S[11]); S[12] = EX(S[12]); S[13] = EX(S[13]); S[14] = EX(S[14]); S[15] = EX(S[15]); SBAR(); \
    } while (0)
#define TILE(CUR, NXT, PRV, T) do { \
      UNIT(Sa, Sb, CUR, 1, PRV, O[3], CUR, O[0]); \
      LDKV(PRV, (T) + 3); SBAR(); \
      UNIT(Sb, Sa, CUR, 2, CUR, O[0], CUR, O[1]); \
      UNIT(Sa, Sb, CUR, 3, CUR, O[1], CUR, O[2]); \
      UNIT(Sb, Sa, NXT, 0, CUR, O[2], CUR, O[3]); } while (0)
#pragma unroll 1
    for (int t = 0; t < 16; t += 4) {
      TILE(A0, A1, A3, t); TILE(A1, A2, A0, t + 1); TILE(A2, A3, A1, t + 2); TILE(A3, A0, A2, t + 3);
    }
    Pb[2] = pk(Sb[12], Sb[13]); Pb[3] = pk(Sb[14], Sb[15]);
    O[3] = MFMA(A3.v1, BF(Pb), O[3]);
    PIN(O[3]);
    STAMP(4);
    lds_wait(cCR, 4);
    rs_write(lds + L_OC, wave, lane, O[1], O[2], O[3]);
    lds_barrier(cBB, 8, lane);
    f32x16 o = rs_read(lds + L_OC, wave, lane, O[0]);
    {
      float l = swap_hi(o[12]);
      float am = 0.f;
#pragma unroll
      for (int r = 0; r < 16; ++r) am = fmaxf(am, fabsf(o[r]));
      const bool ok = (l >= 0x1p-64f) && (l <= 0x1p64f) && (am <= 0x1p120f);
      if (!__all(ok)) {
        const __amdgpu_buffer_rsrc_t rk = __builtin_amdgcn_make_buffer_rsrc(ws + WS_KV, 0, 262144, 0x00020000);
#pragma unroll 1
        for (int w2 = 0; w2 < 4; ++w2) poll(ws, WS_FKV + (w2 * 32 + r32) * 8, tag);
        float m = -INFINITY;
#pragma unroll 1
        for (int t = 0; t < 64; ++t) {
          f32x16 s = MFMA(BF(BLD(rk, lo16, t * 4096)), qo0, zero16); s = MFMA(BF(BLD(rk, lo16 + 1024, t * 4096)), qo1, s);
#pragma unroll
          for (int r = 0; r < 16; ++r) m = fmaxf(m, s[r]);
        }
        m = fmaxf(swap_lo(m), swap_hi(m));
        o = zero16;
#pragma unroll 1
        for (int t = 0; t < 64; ++t) {
          f32x16 s = MFMA(BF(BLD(rk, lo16, t * 4096)), qo0, zero16); s = MFMA(BF(BLD(rk, lo16 + 1024, t * 4096)), qo1, s);
#pragma unroll
          for (int r = 0; r < 16; ++r) s[r] = EX(s[r] - m);
          o = MFMA(BF(BLD(rk, lo16 + 2048, t * 4096)), pack8(s, 0), o); o = MFMA(BF(BLD(rk, lo16 + 3072, t * 4096)), pack8(s, 1), o);
        }
        l = swap_hi(o[12]);
      }
      const float inv = 1.f / l;
#pragma unroll
      for (int r = 0; r < 16; ++r) o[r] *= inv;
      if (hi) { o[12] = 0.f; o[13] = 0.f; o[14] = 0.f; o[15] = 0.f; }
      u32x4 f0 = __builtin_bit_cast(u32x4, pack8(o, 0)), f1 = __builtin_bit_cast(u32x4, pack8(o, 1));
#pragma unroll
      for (int k = 0; k < 4; ++k) { f0[k] = min(f0[k], 0xfffffffeu); f1[k] = min(f1[k], 0xfffffffeu); }
      __builtin_amdgcn_raw_buffer_store_b128(f0, rs_ws, o_own, 0, SC1); __builtin_amdgcn_raw_buffer_store_b128(f1, rs_ws, o_own + 1024, 0, SC1);
    }
    STAMP(5);
    if (j == 7) return;
    {
      const int m = wave >> 1, kh = wave & 1;
      poll(ws, WS_STARTED + ((b & 7) + 8 * ((loc & ~7) + (lane & 7))) * 8, tag);
      u32x4 of[16];
      const unsigned ob = WS_O + (((2 * c + m) * 16 + 8 * kh) * 2) * 1024;
#pragma unroll 1
      for (unsigned s = 0; s < SPIN_CAP; ++s) {
        unsigned mx = 0;
#pragma unroll
        for (int k = 0; k < 16; ++k) of[k] = BLD(rs_ws, lo16 + (k & 3) * 1024, ob + (k >> 2) * 4096);
        SBAR();
#pragma unroll
        for (int k = 0; k < 16; ++k) mx = max(max(mx, max(of[k][0], of[k][1])), max(of[k][2], of[k][3]));
        if (__all(mx != ~0u)) break;
        __builtin_amdgcn_s_sleep(2);
      }
      STAMP(6);
      lds_wait(cWO, 4);
      f32x16 acc0 = zero16, acc1 = zero16;
      const int rowb = 224 * kh + 4 * (g4 >> 1) + tq;
      const unsigned wb = L_WO + rowb * 128 + ((32 * (g4 & 1) + 8 * tp) ^ ((tq & 2) << 5));
#pragma unroll
      for (int kk = 0; kk < 4; ++kk) {
        bf16x8 w0[4], w1[4];
#pragma unroll
        for (int k4 = 0; k4 < 4; ++k4) { const int k = 4 * kk + k4; const unsigned a_ = wb + (28 * (k >> 1) + 16 * (k & 1)) * 128;
          w0[k4] = ldstr2(lds + (a_ ^ (kh << 6)), 1024); w1[k4] = ldstr2(lds + (a_ ^ (kh << 6) ^ 64), 1024); }
        SBAR();
#pragma unroll
        for (int k4 = 0; k4 < 4; ++k4) { acc0 = MFMA(BF(of[4 * kk + k4]), w0[k4], acc0); acc1 = MFMA(BF(of[4 * kk + k4]), w1[k4], acc1); }
      }
      PIN(acc0); PIN(acc1);
      STAMP(11);
      lds_signal(cOC, lane); lds_wait(cOC, 4);
#pragma unroll
      for (int k = 0; k < 4; ++k) *(LAS f32x4*)(lds + L_OC + (wave ^ 1) * 4096 + k * 1024 + lo16) = (f32x4){acc1[4 * k], acc1[4 * k + 1], acc1[4 * k + 2], acc1[4 * k + 3]};
      lds_barrier(cBB, 12, lane);
#pragma unroll
      for (int k = 0; k < 4; ++k) { const f32x4 v = *(LAS f32x4*)(lds + L_OC + wave * 4096 + k * 1024 + lo16);
        acc0[4 * k] += v[0]; acc0[4 * k + 1] += v[1]; acc0[4 * k + 2] += v[2]; acc0[4 * k + 3] += v[3]; }
      float* op = p.out + (size_t)(64 * c + 32 * m + 4 * hi) * 896 + 448 + 64 * j + 32 * kh + r32;
#pragma unroll
      for (int r = 0; r < 16; ++r) op[((r & 3) + 8 * (r >> 2)) * 896] = rbf(acc0[r]);
    }
    STAMP(7);
  } else {
    __builtin_amdgcn_s_setprio(APRIO);
    cf e = {ur[0], ui[0]};
#pragma unroll
    for (int r = 1; r < 16; ++r) e = cfma(lam, e, (cf){ur[r], ui[r]});
    const int seg = 2 * th + hi;
    *(LAS f32x2*)(lds + L_EX + (seg * 64 + 32 * chh + r32) * 8) = (f32x2){e.r, e.i};
    lds_barrier(cAB, 4, lane);
    cf g0 = {0.f, 0.f}, lseg = {1.f, 0.f};
    {
      cf E = {0.f, 0.f};
#pragma unroll
      for (int s = 0; s < 4; ++s) {
        const f32x2 v = *(LAS f32x2*)(lds + L_EX + (s * 64 + 32 * chh + r32) * 8);
        if (s == seg) g0 = E;
        if (s < seg) lseg = cmul(lseg, l16);
        E = cfma(l16, E, (cf){v[0], v[1]});
      }
      if (th == 0 && c < 31) {
        if (hi == 0) __hip_atomic_store((GAS u64*)(ws + WS_E + ((2 * j + chh) * 32 + c) * 256 + r32 * 8), __builtin_bit_cast(u64, (f32x2){E.r, E.i}), RLX_AGENT);
        publish(ws, WS_FE + ((2 * j + chh) * 32 + c) * 8, tag, lane);
      }
    }
    STAMP(2);
    cf G = g0;
    if (c > 0) {
      cf L = cmul(l64, l64); L = cmul(L, L); L = cmul(L, L); L = cmul(L, L);
      poll(ws, WS_FE + ((2 * j + chh) * 32 + min(r32, c - 1)) * 8, tag);
      STAMP(3);
      const __amdgpu_buffer_rsrc_t re = __builtin_amdgcn_make_buffer_rsrc(ws + WS_E + (2 * j + chh) * 8192, 0, c * 256, 0x00020000);
      u32x2 ev[16];
      const int e0 = (c - 16 - 16 * hi) * 256 + r32 * 8;
#pragma unroll
      for (int k = 0; k < 16; ++k) ev[k] = __builtin_amdgcn_raw_buffer_load_b64(re, e0 + k * 256 >= 0 ? (unsigned)(e0 + k * 256) : 0x40000000u, 0, SC1);
      SBAR();
      cf H = {0.f, 0.f};
#pragma unroll
      for (int k = 0; k < 16; ++k) H = cfma(l64, H, (cf){__uint_as_float(ev[k][0]), __uint_as_float(ev[k][1])});
      H = cfma(L, (cf){swap_hi(H.r), swap_hi(H.i)}, (cf){swap_lo(H.r), swap_lo(H.i)});
      G = cfma(lseg, H, g0);
    }
    STAMP(4);
    {
      const lptr ht = lds + L_HT + wa * 4096;
#pragma unroll
      for (int r = 0; r < 16; ++r) { G = cfma(lam, G, (cf){ur[r], ui[r]}); ur[r] = coef.r * G.r - coef.i * G.i; ui[r] = -(coef.r * G.i + coef.i * G.r); }
      *(LAS bf16x8*)(ht + r32 * 64 + hi * 32) = pack8(ur, 0); *(LAS bf16x8*)(ht + r32 * 64 + hi * 32 + 16) = pack8(ur, 1);
      *(LAS bf16x8*)(ht + 2048 + r32 * 64 + hi * 32) = pack8(ui, 0); *(LAS bf16x8*)(ht + 2048 + r32 * 64 + hi * 32 + 16) = pack8(ui, 1);
      asm volatile("s_waitcnt lgkmcnt(0)" ::: "memory");
#pragma unroll
      for (int k = 0; k < 4; ++k) {
        const bf16x8 f = ldstr2(ht + (k >> 1) * 2048 + (16 * (k & 1) + 8 * (g4 >> 1) + tq) * 64 + (g4 & 1) * 32 + tp * 8, 256);
        __builtin_amdgcn_raw_buffer_store_b128(__builtin_bit_cast(u32x4, f), rs_ws, lo16, WS_H + ((2 * c + th) * 56 + (k >> 1) * 28 + 4 * j + 2 * chh + (k & 1)) * 1024, SC1);
      }
      publish(ws, WS_FH + (c * 32 + j * 4 + wa) * 8, tag, lane);
    }
    STAMP(5);
    const unsigned cst = wa < 3 ? wa * 28672u : L_R1;
    {
      const char* src = (const char*)(wa < 2 ? p.cre : p.cim) + (size_t)j * 57344;
      const char* sf = src + chh * 512; const char* sh = src + 384 + chh * 64;
      const unsigned ve = lrow * 896 + (((lane & 7) ^ (lrow >> 1)) << 4), vh = (lane >> 2) * 896 + (((lane & 3) ^ ((lane >> 4) & 3)) << 4);
#pragma unroll
      for (int n = 0; n < 24; ++n) { glds(sf + (n & 7) * 7168 + (n >> 3) * 128, ve ^ ((n & 1) << 6), lds0 + cst + n * 1024); if (n >= CPACE) VMW(CPACE); }
#pragma unroll
      for (int n = 0; n < 4; ++n) glds(sh + n * 14336, vh, lds0 + cst + 24576 + n * 1024);
    }
    VMW(0);
    bf16x8 cfr[14][2];
#pragma unroll
    for (int f = 0; f < 14; ++f)
#pragma unroll
      for (int n = 0; n < 2; ++n) { const int row = 32 * (n ^ chh) + r32;
        cfr[f][n] = f < 12 ? ldsf(lds + cst + (f >> 2) * 8192 + row * 128 + (((2 * (f & 3) + hi) ^ xsw) << 4))
                           : ldsf(lds + cst + 24576 + row * 64 + (((2 * (f & 1) + hi) ^ ((r32 >> 2) & 3)) << 4)); }
    lds_signal(cCR, lane);
    STAMP(10);
    f32x16 acc[4] = {zero16, zero16, zero16, zero16};
#pragma unroll
    for (int r = 0; r < 8; ++r) { acc[0][2 * r] = __uint_as_float(xd[r] << 16); acc[0][2 * r + 1] = __uint_as_float(xd[r] & 0xffff0000u); }
    PIN(acc[0]);
    poll(ws, WS_FH + (c * 32 + min(r32, 27)) * 8, tag);
    STAMP(6);
    {
      const unsigned hb = WS_H + (2 * c * 56 + 14 * wa) * 1024, h0 = hb + th * 57344, h1 = hb + (th ^ 1) * 57344;
      const unsigned fa = chh * 2048, fb = chh ? 0u - 12288u : 0u;
      u32x4 hf[14][2];
#define LDH(f) do { hf[f][0] = BLD(rs_ws, lo16, h0 + (f) * 1024 + ((f) < 12 ? fa : fb)); hf[f][1] = BLD(rs_ws, lo16, h1 + (f) * 1024 + ((f) < 12 ? fa : fb)); } while (0)
#define MMH(f) do { acc[0] = MFMA(BF(hf[f][0]), cfr[f][0], acc[0]); acc[1] = MFMA(BF(hf[f][0]), cfr[f][1], acc[1]); \
                    acc[2] = MFMA(BF(hf[f][1]), cfr[f][0], acc[2]); acc[3] = MFMA(BF(hf[f][1]), cfr[f][1], acc[3]); \
                    PIN(acc[0]); PIN(acc[1]); PIN(acc[2]); PIN(acc[3]); } while (0)
#pragma unroll
      for (int f = 0; f < 5; ++f) LDH(f);
      SBAR();
      __builtin_amdgcn_s_setprio(GPRIO);
#pragma unroll
      for (int f = 0; f < 9; ++f) { MMH(f); SBAR(); LDH(f + 5); SBAR(); }
      lds_wait(cCR, 4);
      {
        const char* src = (const char*)p.wo + j * 128 + wa * 7168;
        const unsigned vw = lrow * 896 + (((lane & 7) ^ ((lrow & 2) << 1)) << 4);
#pragma unroll
        for (int m = 0; m < 14; ++m) glds(src + m * 28672, vw, lds0 + L_WO + wa * 1024 + m * 4096);
        if (wa == 0 && lane < 32) *(LAS u32x4*)(lds + L_WO + 57344 + lo16) = (u32x4){0, 0, 0, 0};
      }
      asm volatile("s_waitcnt vmcnt(14)" ::: "memory");
      SBAR();
#pragma unroll
      for (int f = 9; f < 14; ++f) MMH(f);
    }
    STAMP(11);
    VMW(0);
    lds_signal(cWO, lane);
    rs_write(lds + L_G2, wa, lane, acc[1], acc[2], acc[3]);
    lds_barrier(cAB, 8, lane);
    f32x16 y = rs_read(lds + L_G2, wa, lane, acc[0]);
    float* op = p.out + (size_t)(64 * c + 32 * th + 4 * hi) * 896 + 64 * j + 32 * chh + r32;
#pragma unroll
    for (int r = 0; r < 16; ++r) op[((r & 3) + 8 * (r >> 2)) * 896] = y[r];
    STAMP(7);
  }
}

extern "C" void kernel_launch(void* const* d_in, const int* in_sizes, int n_in, void* d_out, int out_size, void* d_ws, size_t ws_size,
                              hipStream_t stream) {
  Params p;
  p.xa = (const u16*)d_in[0]; p.lre = (const u16*)d_in[1]; p.lim = (const u16*)d_in[2]; p.bre = (const u16*)d_in[3]; p.bim = (const u16*)d_in[4];
  p.cre = (const u16*)d_in[5]; p.cim = (const u16*)d_in[6]; p.ldt = (const u16*)d_in[7]; p.dsk = (const u16*)d_in[8];
  p.xb = (const u16*)d_in[9]; p.wq = (const u16*)d_in[10]; p.wk = (const u16*)d_in[11]; p.wv = (const u16*)d_in[12]; p.wo = (const u16*)d_in[13];
  p.out = (float*)d_out; p.ws = (char*)d_ws;
  fused_kernel<<<256, 512, 0, stream>>>(p);
}
```

```cpp
#include <hip/hip_runtime.h>
#include <stdint.h>

#define DI __device__ __forceinline__
#define GAS __attribute__((address_space(1)))
#define LAS __attribute__((address_space(3)))
#define CAS __attribute__((address_space(4)))
typedef short bf16x8 __attribute__((ext_vector_type(8)));
typedef short s16x4 __attribute__((ext_vector_type(4)));
typedef float f32x16 __attribute__((ext_vector_type(16)));
typedef float f32x4 __attribute__((ext_vector_type(4)));
typedef float f32x2 __attribute__((ext_vector_type(2)));
typedef unsigned u32x4 __attribute__((ext_vector_type(4)));
typedef unsigned u32x2 __attribute__((ext_vector_type(2)));
typedef __bf16 bf16x2_t __attribute__((ext_vector_type(2)));
typedef unsigned long long u64;
typedef unsigned short u16;
typedef LAS char* lptr;
#define RLX_AGENT __ATOMIC_RELAXED, __HIP_MEMORY_SCOPE_AGENT
#define MFMA(a, b, c) __builtin_amdgcn_mfma_f32_32x32x16_bf16((a), (b), (c), 0, 0, 0)
#define SBAR() __builtin_amdgcn_sched_barrier(0)
#define PIN(x) asm volatile("" : "+v"(x))
#define MEMBAR() asm volatile("" ::: "memory")
#define SC1 16
#define APRIO 1
#define GPRIO 1
#define PSLEEP 2
#define APACE 63
#define OAUX 16
#define CPACE 6

constexpr unsigned WS_EPOCH = 0, WS_FKV = 4096, WS_FH = 8192, WS_E = 0x10000, WS_STARTED = 0x48000,
                   WS_KV = 0x50000, WS_O = 0x100000, WS_H = 0x300000, WS_END = 0x680000, WS_DBG = 0x700000;
constexpr unsigned SK = 15360, K_W = 8192, K_WQ = 107520;
constexpr unsigned CH = 24576;
constexpr unsigned L_QS = 99328, L_R1 = 107520, L_HT = 136192, L_WO = 0, L_OC = 57856, L_G2 = 107520, L_MISC = 159744, L_EX = L_MISC + 128, L_TOTAL = 163840;
constexpr unsigned cBB = L_MISC, cAB = L_MISC + 4, cCR = L_MISC + 8, cWO = L_MISC + 12, cOC = L_MISC + 16, cPB = L_MISC + 20, L_TAG = L_MISC + 64;
constexpr unsigned SPIN_CAP = 1u << 18;

struct Params {
  const u16 *xb, *wq, *wk, *wv; char* ws; const u16 *lre, *lim, *ldt, *dsk, *xa, *bre, *bim, *cre, *cim, *wo;
  float* out;
};

DI float u2f(unsigned v) { return __builtin_bit_cast(float, v); }
DI unsigned f2u(float v) { return __builtin_bit_cast(unsigned, v); }
DI unsigned umin(unsigned a, unsigned b) { return a < b ? a : b; }
DI unsigned umax(unsigned a, unsigned b) { return a > b ? a : b; }
DI bool wall(bool x) { return __builtin_amdgcn_ballot_w64(!x) == 0; }
DI float bf2f(unsigned v) { return u2f(v << 16); }
DI unsigned pk(float a, float b) { f32x2 v = {a, b}; return __builtin_bit_cast(unsigned, __builtin_convertvector(v, bf16x2_t)); }
DI float rbf(float a) { return u2f(pk(0.f, a)); }
DI bf16x8 pack8(const f32x16& x, int s) {
  u32x4 p = {pk(x[8 * s], x[8 * s + 1]), pk(x[8 * s + 2], x[8 * s + 3]), pk(x[8 * s + 4], x[8 * s + 5]), pk(x[8 * s + 6], x[8 * s + 7])};
  return __builtin_bit_cast(bf16x8, p);
}
DI u32x4 mkrs(const void* base, unsigned bytes) { const u64 a = (u64)base; return (u32x4){(unsigned)a, (unsigned)(a >> 32) & 0xffffu, bytes, 0x20000u}; }
#define DMA(rs, voff, soff, ldsb, IMM) asm volatile("s_add_u32 m0, %3, %4\n\ts_nop 0\n\tbuffer_load_dwordx4 %0, %1, %2 offen lds" \
                                                    :: "v"(voff), "s"(rs), "s"(soff), "s"(ldsb), "i"(IMM) : "memory", "scc")
DI bf16x8 ldsf(lptr p) { return *(LAS bf16x8*)p; }
DI s16x4 ldstr(lptr p) { return __builtin_amdgcn_ds_read_tr16_b64_v4i16((LAS s16x4*)p); }
DI bf16x8 ldstr2(lptr p, int second) { s16x4 lo = ldstr(p), hi = ldstr(p + second); return __builtin_shufflevector(lo, hi, 0, 1, 2, 3, 4, 5, 6, 7); }
DI void lds_signal(unsigned c) {
  asm volatile("s_waitcnt lgkmcnt(0)\n\ts_mov_b64 exec, 1\n\tds_add_u32 %0, %1\n\ts_mov_b64 exec, -1" :: "v"(c), "v"(1u) : "memory");
}
template <int SLEEP> DI void lds_wait_(unsigned c, unsigned target) {
  unsigned v, s, n;
  asm volatile("s_mov_b32 %2, 0x400000\n1:\n\tds_read_b32 %0, %3\n\ts_waitcnt lgkmcnt(0)\n\tv_readfirstlane_b32 %1, %0\n\ts_cmp_ge_u32 %1, %4\n\ts_cbranch_scc1 2f\n\t"
               "s_sleep %5\n\ts_sub_u32 %2, %2, 1\n\ts_cbranch_scc0 1b\n2:" : "=&v"(v), "=&s"(s), "=&s"(n) : "v"(c), "i"(target), "i"(SLEEP) : "memory", "scc");
}
#define lds_wait(c, t) lds_wait_<0>(c, t)
#define lds_barrier(c, t) do { lds_signal(c); lds_wait(c, t); } while (0)
DI void poll(const char* ws, unsigned off, u64 tag) {
#pragma unroll 1
  for (unsigned s = 0; s < SPIN_CAP; ++s) {
    u64 v = __hip_atomic_load((GAS u64*)(ws + off), RLX_AGENT);
    if (wall(v == tag)) break;
    __builtin_amdgcn_s_sleep(PSLEEP);
  }
  MEMBAR();
}
DI void publish4(char* ws, unsigned off, u64 tag, int lane) {
  unsigned old;
  asm volatile("s_waitcnt vmcnt(0)\n\ts_mov_b64 exec, 1\n\tds_add_rtn_u32 %0, %1, %2\n\ts_waitcnt lgkmcnt(0)\n\ts_mov_b64 exec, -1" : "=&v"(old) : "v"(cPB), "v"(1u) : "memory");
  if (__builtin_amdgcn_readfirstlane(old) == 3 && lane == 0) __hip_atomic_store((GAS u64*)(ws + off), tag, RLX_AGENT);
}
DI float swap_lo(float x) {
  auto r = __builtin_amdgcn_permlane32_swap(f2u(x), f2u(x), false, false); return u2f(r[0]); }
DI float swap_hi(float x) {
  auto r = __builtin_amdgcn_permlane32_swap(f2u(x), f2u(x), false, false); return u2f(r[1]); }
struct cf { float r, i; };
DI cf cmul(cf a, cf b) { return {a.r * b.r - a.i * b.i, a.r * b.i + a.i * b.r}; }
DI cf cfma(cf a, cf b, cf c) { return {__builtin_fmaf(a.r, b.r, __builtin_fmaf(-a.i, b.i, c.r)), __builtin_fmaf(a.r, b.i, __builtin_fmaf(a.i, b.r, c.i))}; }

#define STAMP(k) do {} while (0)
#define STAMP_OUT() do {} while (0)

struct KV { bf16x8 k0, k1, v0, v1; };

DI void rs_write(lptr base, int w, int lane, const f32x16& t1, const f32x16& t2, const f32x16& t3) {
#define RSW(T, I) _Pragma("unroll") for (int q = 0; q < 4; ++q) *(LAS f32x4*)(base + ((w ^ I) * 3 + I - 1) * 4096 + q * 1024 + lane * 16) = (f32x4){T[4 * q], T[4 * q + 1], T[4 * q + 2], T[4 * q + 3]}
  RSW(t1, 1); RSW(t2, 2); RSW(t3, 3);
}
DI f32x16 rs_read(lptr base, int w, int lane, f32x16 a) {
  f32x4 v[12];
#pragma unroll
  for (int s = 0; s < 12; ++s) v[s] = *(LAS f32x4*)(base + w * 12288 + s * 1024 + lane * 16);
  SBAR();
#pragma unroll
  for (int s = 0; s < 12; ++s) { const int q = s & 3; a[4 * q] += v[s][0]; a[4 * q + 1] += v[s][1]; a[4 * q + 2] += v[s][2]; a[4 * q + 3] += v[s][3]; }
  return a;
}

#define VMW_(n) asm volatile("s_waitcnt vmcnt(" #n ")" ::: "memory")
#define VMW(n) VMW_(n)
#define BAR() asm volatile("s_waitcnt lgkmcnt(0)\n\ts_barrier" ::: "memory")
#define BLD(R, V, S) __builtin_amdgcn_raw_buffer_load_b128(R, V, S, SC1)
#define BST(D, R, V, S) __builtin_amdgcn_raw_buffer_store_b128(D, R, (V) + (S), 0, SC1)
#define BF(x) __builtin_bit_cast(bf16x8, x)
#define U4(x) __builtin_bit_cast(u32x4, x)
#define EX(x) __builtin_amdgcn_exp2f(x)
#define KARGS() ({ const CAS Params* kp_ = (const CAS Params*)__builtin_amdgcn_kernarg_segment_ptr(); asm volatile("" : "+s"(kp_)); kp_; })
#define NEXTK() do { k += step; k = k >= 7 ? k - 7 : k; } while (0)

#if defined(__HIP_DEVICE_COMPILE__)
#define NO_PK __attribute__((target("no-packed-fp32-ops")))
#else
#define NO_PK
#endif
__global__ void __launch_bounds__(512, 2) NO_PK fused_kernel(Params p) {
  __shared__ __attribute__((aligned(128))) char smem[L_TOTAL];
  const lptr lds = (lptr)smem;
  const int tid = threadIdx.x, lane = tid & 63, wave = __builtin_amdgcn_readfirstlane(tid >> 6);
  const int b = blockIdx.x, loc = b >> 3, cl = loc >> 3, c = (b & 7) * 4 + cl, j = loc & 7;
  const int lrow = lane >> 3;
  const unsigned ve = lrow * 896 + (((lane & 7) ^ (lrow >> 1)) << 4);
  const int step = cl + 1, k0 = j == 7 ? 0 : j;
  const bool isB = wave < 4;
  const int wa = wave & 3;
  char* const ws = p.ws;
  STAMP(0);
  unsigned ua = 0, ub = 0, uc = 0, ud = 0;
  {
    const u32x4 rx = mkrs((const char*)p.xb + (size_t)c * 57344 + wave * 7168, 7168);
    const unsigned vx = ve ^ ((wave & 1) << 6), lb = wave * 1024;
    int k = k0;
    if (j == 7) {
      const u32x4 rw = mkrs(isB ? p.wk : p.wv, 25088);
      const unsigned vw = wa * 1024 + lane * 16, lw = K_W + (isB ? 0 : 3584) + wa * 1024; const bool half = wa < 3 || lane < 32;
#pragma unroll
      for (int i = 0; i < 7; ++i) { NEXTK(); DMA(rx, vx, k * 128, lb, i * SK); if (half) DMA(rw, vw, k * 3584, lw, i * SK); }
    } else {
      const int iw = wave * 64 + lane, wrow = (iw * 9363) >> 16;
      const unsigned vwq = wrow * 896 + (iw - wrow * 7) * 16;
      const u32x4 rw = mkrs((const char*)p.wq + j * 112, 401408 - j * 112);
      if (wave < 7) {
#pragma unroll
        for (int i = 0; i < 7; ++i) { NEXTK(); DMA(rx, vx, k * 128, lb, i * SK); DMA(rw, vwq, k * 57344, lb, i * SK + K_W); }
      } else {
#pragma unroll
        for (int i = 0; i < 7; ++i) { NEXTK(); DMA(rx, vx, k * 128, lb, i * SK); }
      }
    }
  }
  STAMP(8);
  if (tid < 16) ((LAS unsigned*)(lds + L_MISC))[tid] = 0;
  const int r32 = lane & 31, hi = lane >> 5;
  const int g4 = lane >> 4, tq = (lane & 15) >> 2, tp = lane & 3;
  const unsigned xsw = (r32 >> 1) & 7, lo16 = lane * 16;
  const int th = wa >> 1, chh = wa & 1;
  u64 tag;
  const __amdgpu_buffer_rsrc_t rs_ws = __builtin_amdgcn_make_buffer_rsrc(ws, 0, WS_END, 0x00020000);
  const f32x16 zero16 = {};
  const unsigned o_own = WS_O + (((2 * c + th) * 16 + 2 * j + chh) * 2) * 1024;
  const unsigned xq_off = (32 * th + r32) * 128;
  const unsigned wq_off = (8 * (g4 >> 1) + tq) * 112 + (28 * chh + 16 * (g4 & 1) + 4 * tp) * 2;
  f32x16 q = zero16;
#define MARK() do { const u32x4 ones = {~0u, ~0u, ~0u, ~0u};       \
    BST(ones, rs_ws, lo16, o_own); BST(ones, rs_ws, lo16 + 1024, o_own); } while (0)
#define LDEPOCH() asm volatile("s_load_dwordx2 %0, %1, 0x0\n\ts_waitcnt lgkmcnt(0)" : "=s"(tag) : "s"(ws) : "memory"); tag = (tag | 1) + 2
#define QRD(B, X, W) _Pragma("unroll") for (int ks = 0; ks < 4; ++ks) { B##x[ks] = ldsf(lds + (X) + xq_off + (((2 * ks + hi) ^ xsw) << 4)); B##w[ks] = ldstr2(lds + (W) + wq_off + ks * 1792, 448); } SBAR()
#define QMM(B) _Pragma("unroll") for (int ks = 0; ks < 4; ++ks) q = MFMA(B##w[ks], B##x[ks], q); PIN(q); SBAR()
  bf16x8 fax[4], faw[4], fbx[4], fbw[4];

  if (j == 7) {
    LDEPOCH();
    VMW(6); BAR();
    if (!isB) {
      const unsigned xk_off = (32 * chh + r32) * 128, wk_off = K_W + th * 3584 + (8 * (g4 >> 1) + tq) * 56 + (16 * (g4 & 1) + 4 * tp) * 2;
      f32x16 a = zero16;
#define KRD(B, i) _Pragma("unroll") for (int ks = 0; ks < 4; ++ks) { B##x[ks] = ldsf(lds + (i) * SK + xk_off + (((2 * ks + hi) ^ xsw) << 4)); B##w[ks] = ldstr2(lds + (i) * SK + wk_off + ks * 896, 224); } SBAR()
#define KMM(B) if (th) { _Pragma("unroll") for (int ks = 0; ks < 4; ++ks) a = MFMA(B##x[ks], B##w[ks], a); } else { _Pragma("unroll") for (int ks = 0; ks < 4; ++ks) a = MFMA(B##w[ks], B##x[ks], a); } PIN(a); SBAR()
      KRD(fa, 0); KRD(fb, 1); KMM(fa); KRD(fa, 2); KMM(fb); KRD(fb, 3); KMM(fa); KMM(fb);
      VMW(0); BAR();
      KRD(fa, 4); KRD(fb, 5); KMM(fa); KRD(fa, 6); KMM(fb); KMM(fa);
      STAMP(1);
      if (th) {
#pragma unroll
        for (int r = 0; r < 16; ++r) a[r] = r32 < 28 ? a[r] : (r32 == 28 ? 1.f : 0.f);
      } else if (hi) { a[12] = 1.f; a[13] = 1.f; a[14] = 1.f; a[15] = 0.f; }
      const unsigned ko = WS_KV + (2 * c + chh) * 4096 + th * 2048;
      BST(U4(pack8(a, 0)), rs_ws, lo16, ko); BST(U4(pack8(a, 1)), rs_ws, lo16 + 1024, ko);
      lds_signal(cCR);
      publish4(ws, WS_FKV + c * 128, tag, lane);
      STAMP(2);
      if (c == 0 && wa == 0) {
        __builtin_amdgcn_s_sleep(127);
#pragma unroll 1
        for (int k = 0; k < 4; ++k) poll(ws, WS_STARTED + (k * 64 + lane) * 128, tag);
        if (lane == 0) __hip_atomic_store((GAS u64*)(ws + WS_EPOCH), tag, RLX_AGENT);
      }
      STAMP_OUT();
      return;
    }
    VMW(0); BAR();
    const int i0 = wa * 128 + lane, r0 = (i0 * 9363) >> 16, r1 = ((i0 + 64) * 9363) >> 16;
    const unsigned vq0 = r0 * 896 + (i0 - r0 * 7) * 16, vq1 = r1 * 896 + (i0 + 64 - r1 * 7) * 16, lb = wave * 2048;
    const u32x4 rw = mkrs((const char*)p.wq + 784, 401408 - 784);
    int k = k0;
#pragma unroll
    for (int i = 0; i < 7; ++i) { NEXTK(); DMA(rw, vq0, k * 57344, lb, K_WQ + i * 7168); if (wave < 3) DMA(rw, vq1, k * 57344, lb, K_WQ + i * 7168 + 1024); }
    MARK();
    VMW(0);
    lds_barrier(cOC, 4);
    QRD(fa, 0, K_WQ); QRD(fb, SK, K_WQ + 7168); QMM(fa); QRD(fa, 2 * SK, K_WQ + 2 * 7168); QMM(fb); QRD(fb, 3 * SK, K_WQ + 3 * 7168); QMM(fa);
    QRD(fa, 4 * SK, K_WQ + 4 * 7168); QMM(fb); QRD(fb, 5 * SK, K_WQ + 5 * 7168); QMM(fa); QRD(fa, 6 * SK, K_WQ + 6 * 7168); QMM(fb); QMM(fa);
    lds_wait(cCR, 4);
    STAMP(1);
  } else {
    if (isB) { MARK(); LDEPOCH(); }
    if (wave < 7) VMW(6); else VMW(3);
    BAR();
    if (isB) { QRD(fa, 0, K_W); QRD(fb, SK, SK + K_W); QMM(fa); QRD(fa, 2 * SK, 2 * SK + K_W); QMM(fb); QRD(fb, 3 * SK, 3 * SK + K_W); QMM(fa); QMM(fb); }
    else { const CAS Params* kp = KARGS(); const unsigned co = (64 * j + 32 * chh + r32) * 2;
      asm volatile("s_nop 4\n\tglobal_load_ushort %0, %4, %5\n\tglobal_load_ushort %1, %4, %6\n\tglobal_load_ushort %2, %4, %7\n\tglobal_load_ushort %3, %4, %8"
                   : "=&v"(ua), "=&v"(ub), "=&v"(uc), "=&v"(ud) : "v"(co), "s"(kp->lre), "s"(kp->lim), "s"(kp->ldt), "s"(kp->dsk) : "memory"); }
    asm volatile("s_waitcnt vmcnt(0) lgkmcnt(0)\n\ts_barrier" : "+v"(ua), "+v"(ub), "+v"(uc), "+v"(ud) :: "memory");
    if (isB) { QRD(fa, 4 * SK, 4 * SK + K_W); QRD(fb, 5 * SK, 5 * SK + K_W); QMM(fa); QRD(fa, 6 * SK, 6 * SK + K_W); QMM(fb); QMM(fa); STAMP(1); }
  }

  if (isB) {
    if (tid == 0) *(LAS u64*)(lds + L_TAG) = tag;
    lds_barrier(cBB, 4);
    if (tid == 0) __hip_atomic_store((GAS u64*)(ws + WS_STARTED + b * 128), tag, RLX_AGENT);
    const float qs = 0.18898223650461361f * 1.4426950408889634f;
#pragma unroll
    for (int r = 0; r < 16; ++r) q[r] *= qs;
    if (hi) { q[12] = 0.f; q[13] = 0.f; q[14] = 0.f; q[15] = 0.f; }
    const bf16x8 qo0 = pack8(q, 0), qo1 = pack8(q, 1);
    *(LAS bf16x8*)(lds + L_QS + wave * 2048 + lo16) = qo0;
    *(LAS bf16x8*)(lds + L_QS + wave * 2048 + 1024 + lo16) = qo1;
    lds_barrier(cBB, 8);
    bf16x8 Q0[4], Q1[4];
    Q0[0] = qo0; Q1[0] = qo1;
#pragma unroll
    for (int i = 1; i < 4; ++i) { Q0[i] = ldsf(lds + L_QS + (wave ^ i) * 2048 + lo16); Q1[i] = ldsf(lds + L_QS + (wave ^ i) * 2048 + 1024 + lo16); }
    STAMP(2);
    poll(ws, WS_FKV + (wave * 8 + (lane & 7)) * 128, tag);
    STAMP(3);
    const __amdgpu_buffer_rsrc_t rs_kv = __builtin_amdgcn_make_buffer_rsrc(ws + WS_KV + wave * 65536, 0, 65536, 0x00020000);
#define LDKV(B, T) do { const unsigned so_ = (T) < 16 ? (((T) + loc) & 15) * 4096 : 65536; \
                        B.k0 = BF(BLD(rs_kv, lo16, so_)); B.k1 = BF(BLD(rs_kv, lo16 + 1024, so_)); \
                        B.v0 = BF(BLD(rs_kv, lo16 + 2048, so_)); B.v1 = BF(BLD(rs_kv, lo16 + 3072, so_)); } while (0)
    KV A0, A1, A2, A3;
    LDKV(A0, 0); LDKV(A1, 1); LDKV(A2, 2);
    SBAR();
    A3.k0 = A3.k1 = A3.v0 = A3.v1 = (bf16x8){};
    f32x16 O[4] = {zero16, zero16, zero16, zero16};
    f32x16 Sa, Sb = zero16; u32x4 Pa, Pb = {};
    Sa = MFMA(A0.k0, Q0[0], zero16); Sa = MFMA(A0.k1, Q1[0], Sa);
    STAMP(9);
#define UNIT(S, Sn, KN, QN, VP, OP, VC, OC) do { \
      Pb[2] = pk(Sn[12], Sn[13]); Pb[3] = pk(Sn[14], Sn[15]); \
      Sn = MFMA(KN.k0, Q0[QN], zero16); S[0] = EX(S[0]); S[1] = EX(S[1]); S[2] = EX(S[2]); S[3] = EX(S[3]); SBAR(); \
      Sn = MFMA(KN.k1, Q1[QN], Sn); Pa[0] = pk(S[0], S[1]); Pa[1] = pk(S[2], S[3]); S[4] = EX(S[4]); S[5] = EX(S[5]); S[6] = EX(S[6]); S[7] = EX(S[7]); SBAR(); \
      OP = MFMA(VP.v1, BF(Pb), OP); Pa[2] = pk(S[4], S[5]); Pa[3] = pk(S[6], S[7]); S[8] = EX(S[8]); S[9] = EX(S[9]); S[10] = EX(S[10]); S[11] = EX(S[11]); SBAR(); \
      OC = MFMA(VC.v0, BF(Pa), OC); Pb[0] = pk(S[8], S[9]); Pb[1] = pk(S[10], S[11]); S[12] = EX(S[12]); S[13] = EX(S[13]); S[14] = EX(S[14]); S[15] = EX(S[15]); SBAR(); \
    } while (0)
#define TILE(CUR, NXT, PRV, T) do { \
      UNIT(Sa, Sb, CUR, 1, PRV, O[3], CUR, O[0]); \
      LDKV(PRV, (T) + 3); SBAR(); \
      UNIT(Sb, Sa, CUR, 2, CUR, O[0], CUR, O[1]); \
      UNIT(Sa, Sb, CUR, 3, CUR, O[1], CUR, O[2]); \
      UNIT(Sb, Sa, NXT, 0, CUR, O[2], CUR, O[3]); } while (0)
#pragma unroll 1
    for (int t = 0; t < 16; t += 4) {
      TILE(A0, A1, A3, t); TILE(A1, A2, A0, t + 1); TILE(A2, A3, A1, t + 2); TILE(A3, A0, A2, t + 3);
    }
    Pb[2] = pk(Sb[12], Sb[13]); Pb[3] = pk(Sb[14], Sb[15]);
    O[3] = MFMA(A3.v1, BF(Pb), O[3]);
    PIN(O[3]);
    STAMP(4);
    lds_wait(cCR, 4);
    rs_write(lds + L_OC, wave, lane, O[1], O[2], O[3]);
    lds_barrier(cBB, 12);
    STAMP(10);
    f32x16 o = rs_read(lds + L_OC, wave, lane, O[0]);
    {
      float l = swap_hi(o[12]);
      float am = 0.f;
#pragma unroll
      for (int r = 0; r < 16; ++r) am = __builtin_fmaxf(am, __builtin_fabsf(o[r]));
      const bool ok = (l >= 0x1p-64f) && (l <= 0x1p64f) && (am <= 0x1p120f);
      if (!wall(ok)) {
        const __amdgpu_buffer_rsrc_t rk = __builtin_amdgcn_make_buffer_rsrc(ws + WS_KV, 0, 262144, 0x00020000);
        poll(ws, WS_FKV + r32 * 128, tag);
        float m = -__builtin_inff();
#pragma unroll 1
        for (int t = 0; t < 64; ++t) {
          f32x16 s = MFMA(BF(BLD(rk, lo16, t * 4096)), qo0, zero16); s = MFMA(BF(BLD(rk, lo16 + 1024, t * 4096)), qo1, s);
#pragma unroll
          for (int r = 0; r < 16; ++r) m = __builtin_fmaxf(m, s[r]);
        }
        m = __builtin_fmaxf(swap_lo(m), swap_hi(m));
        o = zero16;
#pragma unroll 1
        for (int t = 0; t < 64; ++t) {
          f32x16 s = MFMA(BF(BLD(rk, lo16, t * 4096)), qo0, zero16); s = MFMA(BF(BLD(rk, lo16 + 1024, t * 4096)), qo1, s);
#pragma unroll
          for (int r = 0; r < 16; ++r) s[r] = EX(s[r] - m);
          o = MFMA(BF(BLD(rk, lo16 + 2048, t * 4096)), pack8(s, 0), o); o = MFMA(BF(BLD(rk, lo16 + 3072, t * 4096)), pack8(s, 1), o);
        }
        l = swap_hi(o[12]);
      }
      const float inv = 1.f / l;
#pragma unroll
      for (int r = 0; r < 16; ++r) o[r] *= inv;
      if (hi) { o[12] = 0.f; o[13] = 0.f; o[14] = 0.f; o[15] = 0.f; }
      u32x4 f0 = U4(pack8(o, 0)), f1 = U4(pack8(o, 1));
#pragma unroll
      for (int k = 0; k < 4; ++k) { f0[k] = umin(f0[k], 0xfffffffeu); f1[k] = umin(f1[k], 0xfffffffeu); }
      BST(f0, rs_ws, lo16, o_own); BST(f1, rs_ws, lo16 + 1024, o_own);
    }
    lds_signal(cOC);
    STAMP(5);
    if (j == 7) { STAMP_OUT(); return; }
    {
      const CAS Params* kp = KARGS();
      const __amdgpu_buffer_rsrc_t rs_out = __builtin_amdgcn_make_buffer_rsrc(kp->out, 0, 2048 * 896 * 4, 0x00020000);
      lds_wait(cWO, 4);
      u32x4 of[16];
      const unsigned ob = WS_O + (((2 * c + th) * 16 + 8 * chh) * 2) * 1024;
#pragma unroll 1
      for (unsigned s = 0; s < SPIN_CAP; ++s) {
        unsigned mx = 0;
#pragma unroll
        for (int k = 0; k < 16; ++k) of[k] = BLD(rs_ws, lo16 + (k & 3) * 1024, ob + (k >> 2) * 4096);
        SBAR();
#pragma unroll
        for (int k = 0; k < 16; ++k) mx = umax(umax(mx, umax(of[k][0], of[k][1])), umax(of[k][2], of[k][3]));
        if (wall(mx != ~0u)) break;
        __builtin_amdgcn_s_sleep(2);
      }
      STAMP(6);
      f32x16 acc0 = zero16, acc1 = zero16;
      const int rowb = 224 * chh + 4 * (g4 >> 1) + tq;
      const unsigned wb = (L_WO + rowb * 128 + ((32 * (g4 & 1) + 8 * tp) ^ ((tq & 2) << 5))) ^ (chh << 6);
      bf16x8 w0[2][4], w1[2][4];
#define WRD(kk) _Pragma("unroll") for (int k4 = 0; k4 < 4; ++k4) { const int k = 4 * (kk) + k4; const unsigned a_ = (28 * (k >> 1) + 16 * (k & 1)) * 128; \
          w0[(kk) & 1][k4] = ldstr2(lds + wb + a_, 1024); w1[(kk) & 1][k4] = ldstr2(lds + (wb ^ 64) + a_, 1024); } SBAR()
#define WMM(kk) _Pragma("unroll") for (int k4 = 0; k4 < 4; ++k4) { acc1 = MFMA(BF(of[4 * (kk) + k4]), w1[(kk) & 1][k4], acc1); acc0 = MFMA(BF(of[4 * (kk) + k4]), w0[(kk) & 1][k4], acc0); } PIN(acc0); PIN(acc1); SBAR()
      WRD(0); WRD(1); WMM(0); WRD(2); WMM(1); WRD(3); WMM(2); WMM(3);
      STAMP(11);
      lds_wait(cOC, 4);
#pragma unroll
      for (int k = 0; k < 4; ++k) *(LAS f32x4*)(lds + L_OC + (wave ^ 1) * 4096 + k * 1024 + lo16) = (f32x4){acc1[4 * k], acc1[4 * k + 1], acc1[4 * k + 2], acc1[4 * k + 3]};
      lds_barrier(cBB, 16);
#pragma unroll
      for (int k = 0; k < 4; ++k) { const f32x4 v = *(LAS f32x4*)(lds + L_OC + wave * 4096 + k * 1024 + lo16);
        acc0[4 * k] += v[0]; acc0[4 * k + 1] += v[1]; acc0[4 * k + 2] += v[2]; acc0[4 * k + 3] += v[3]; }
      STAMP(12);
      const unsigned oo = ((64 * c + 32 * th + 4 * hi) * 896 + 448 + 64 * j + 32 * chh + r32) * 4;
#pragma unroll
      for (int r = 0; r < 16; ++r) __builtin_amdgcn_raw_buffer_store_b32(pk(0.f, acc0[r]), rs_out, oo, ((r & 3) + 8 * (r >> 2)) * 3584, OAUX);
    }
    STAMP(7);
    STAMP_OUT();
  } else {
    cf lam, coef, l16, l64; f32x16 ur = zero16, ui = zero16; unsigned xd[8];
    const float pd = bf2f(ud);
    {
      const CAS Params* kp = KARGS();
      const u32x4 rxa = mkrs((const char*)kp->xa + (size_t)c * 57344 + wa * 14336, 14336);
      const u32x4 rbr = mkrs((const char*)kp->bre + (size_t)j * 57344 + wa * 14336, 14336);
      const u32x4 rbi = mkrs((const char*)kp->bim + (size_t)j * 57344 + wa * 14336, 14336);
      const unsigned vo = (ve ^ 64) + 7168, lb = wa * 2048;
      int k = k0;
#define LDCH(slot) do { NEXTK(); const unsigned k_ = k * 128; DMA(rxa, ve, k_, lb, (slot)); DMA(rxa, vo, k_, lb, (slot) + 1024); DMA(rbr, ve, k_, lb, (slot) + 8192); \
      DMA(rbr, vo, k_, lb, (slot) + 9216); DMA(rbi, ve, k_, lb, (slot) + 16384); DMA(rbi, vo, k_, lb, (slot) + 17408); } while (0)
      LDCH(L_R1); LDCH(L_R1 + CH);
      {
        const float pa = bf2f(ua), pb = bf2f(ub), dt = rbf(expf(bf2f(uc)));
        const float er = expf(dt * pa); float sn, cs; sincosf(dt * pb, &sn, &cs);
        lam = {er * cs, er * sn};
        const float nr = lam.r - 1.f, ni = lam.i, den = pa * pa + pb * pb;
        coef = {2.f * (nr * pa + ni * pb) / den, 2.f * (ni * pa - nr * pb) / den};
        l16 = cmul(lam, lam); l16 = cmul(l16, l16); l16 = cmul(l16, l16); l16 = cmul(l16, l16);
        l64 = cmul(l16, l16); l64 = cmul(l64, l64);
        PIN(coef.r); PIN(coef.i); PIN(l64.r); PIN(l64.i);
      }
      const int trow = 32 * th + 16 * ((r32 >> 2) & 1) + (r32 & 3) + 4 * (r32 >> 3);
      const unsigned xa_off = trow * 128, xa_sw = (trow >> 1) & 7, b_off = 8192 + (32 * chh + r32) * 128;
#define G1(s_) do { bf16x8 xf_[4], rf_[4], if_[4]; \
      _Pragma("unroll") for (int ks = 0; ks < 4; ++ks) { xf_[ks] = ldsf(lds + (s_) + xa_off + (((2 * ks + hi) ^ xa_sw) << 4)); \
        rf_[ks] = ldsf(lds + (s_) + b_off + (((2 * ks + hi) ^ xsw) << 4)); if_[ks] = ldsf(lds + (s_) + b_off + 8192 + (((2 * ks + hi) ^ xsw) << 4)); } \
      SBAR(); _Pragma("unroll") for (int ks = 0; ks < 4; ++ks) { ur = MFMA(xf_[ks], rf_[ks], ur); ui = MFMA(xf_[ks], if_[ks], ui); } PIN(ur); PIN(ui); } while (0)
      STAMP(12);
      VMW(6); lds_barrier(cAB, 4); G1(L_R1);
      lds_wait_<1>(cBB, 4);
      { const u32x2 t2 = *(LAS u32x2*)(lds + L_TAG);
        tag = ((u64)(unsigned)__builtin_amdgcn_readfirstlane(t2[1]) << 32) | (unsigned)__builtin_amdgcn_readfirstlane(t2[0]); }
      __builtin_amdgcn_s_setprio(APRIO);
      STAMP(9);
      LDCH(0); VMW(APACE); LDCH(CH); VMW(APACE); LDCH(2 * CH); VMW(APACE); LDCH(3 * CH);
      STAMP(13);
      VMW(24); lds_barrier(cAB, 8); LDCH(L_R1); G1(L_R1 + CH);
      STAMP(14);
      VMW(18); lds_barrier(cAB, 12); G1(0); G1(CH);
      STAMP(15);
      VMW(6); lds_barrier(cAB, 16); G1(2 * CH); G1(3 * CH);
      VMW(0); lds_barrier(cAB, 20); G1(L_R1);
    }
#pragma unroll
    for (int r = 0; r < 8; ++r) {
      const int q0 = 32 * th + 4 * hi + ((2 * r) & 3) + 8 * (r >> 1), cq = (32 * chh + r32) >> 3;
      const unsigned x0 = *(LAS u16*)(lds + L_R1 + q0 * 128 + ((cq ^ ((q0 >> 1) & 7)) << 4) + (r32 & 7) * 2);
      const unsigned x1 = *(LAS u16*)(lds + L_R1 + (q0 + 1) * 128 + ((cq ^ ((q0 >> 1) & 7)) << 4) + (r32 & 7) * 2);
      xd[r] = pk(bf2f(x0) * pd, bf2f(x1) * pd); }
    STAMP(1);
    cf e = {ur[0], ui[0]};
#pragma unroll
    for (int r = 1; r < 16; ++r) e = cfma(lam, e, (cf){ur[r], ui[r]});
    const int seg = 2 * th + hi;
    *(LAS f32x2*)(lds + L_EX + (seg * 64 + 32 * chh + r32) * 8) = (f32x2){e.r, e.i};
    lds_barrier(cAB, 24);
    cf g0 = {0.f, 0.f}, lseg = {1.f, 0.f};
    {
      cf E = {0.f, 0.f};
#pragma unroll
      for (int s = 0; s < 4; ++s) {
        const f32x2 v = *(LAS f32x2*)(lds + L_EX + (s * 64 + 32 * chh + r32) * 8);
        if (s == seg) g0 = E;
        if (s < seg) lseg = cmul(lseg, l16);
        E = cfma(l16, E, (cf){v[0], v[1]});
      }
      if (th == 0 && hi == 0 && c < 31)
        BST(((u32x4){f2u(E.r), (unsigned)tag, f2u(E.i), (unsigned)tag}), rs_ws, r32 * 16, WS_E + ((2 * j + chh) * 32 + 31 - c) * 512);
    }
    STAMP(2);
    cf G = g0;
    if (c > 0) {
      const cf l128 = cmul(l64, l64);
      const __amdgpu_buffer_rsrc_t re = __builtin_amdgcn_make_buffer_rsrc(ws + WS_E + (2 * j + chh) * 16384, 0, 16384, 0x00020000);
      const unsigned eo = (32 - c + hi) * 512 + r32 * 16, want = (c - hi + 1) >> 1;
      u32x4 ev[16];
#pragma unroll 1
      for (unsigned s = 0; s < SPIN_CAP; ++s) {
        unsigned got = 0;
#pragma unroll
        for (int m = 0; m < 8; ++m) ev[m] = BLD(re, eo, m * 1024);
        if (c > 16) {
#pragma unroll
          for (int m = 8; m < 16; ++m) ev[m] = BLD(re, eo, m * 1024);
          SBAR();
#pragma unroll
          for (int m = 8; m < 16; ++m) got += ((ev[m][1] ^ (unsigned)tag) | (ev[m][3] ^ (unsigned)tag)) == 0;
        }
#pragma unroll
        for (int m = 0; m < 8; ++m) got += ((ev[m][1] ^ (unsigned)tag) | (ev[m][3] ^ (unsigned)tag)) == 0;
        if (wall(got == want)) break;
        __builtin_amdgcn_s_sleep(4);
      }
      STAMP(3);
      cf H = {0.f, 0.f};
      if (c > 16) {
#pragma unroll
        for (int m = 15; m >= 8; --m) H = cfma(l128, H, (cf){u2f(ev[m][0]), u2f(ev[m][2])});
      }
#pragma unroll
      for (int m = 7; m >= 0; --m) H = cfma(l128, H, (cf){u2f(ev[m][0]), u2f(ev[m][2])});
      H = cfma(l64, (cf){swap_hi(H.r), swap_hi(H.i)}, (cf){swap_lo(H.r), swap_lo(H.i)});
      G = cfma(lseg, H, g0);
    }
    STAMP(4);
    {
      const lptr ht = lds + L_HT + wa * 4096;
#pragma unroll
      for (int r = 0; r < 16; ++r) { G = cfma(lam, G, (cf){ur[r], ui[r]}); ur[r] = coef.r * G.r - coef.i * G.i; ui[r] = -(coef.r * G.i + coef.i * G.r); }
      *(LAS bf16x8*)(ht + r32 * 64 + hi * 32) = pack8(ur, 0); *(LAS bf16x8*)(ht + r32 * 64 + hi * 32 + 16) = pack8(ur, 1);
      *(LAS bf16x8*)(ht + 2048 + r32 * 64 + hi * 32) = pack8(ui, 0); *(LAS bf16x8*)(ht + 2048 + r32 * 64 + hi * 32 + 16) = pack8(ui, 1);
      asm volatile("s_waitcnt lgkmcnt(0)" ::: "memory");
#pragma unroll
      for (int k = 0; k < 4; ++k) {
        const bf16x8 f = ldstr2(ht + (k >> 1) * 2048 + (16 * (k & 1) + 8 * (g4 >> 1) + tq) * 64 + (g4 & 1) * 32 + tp * 8, 256);
        BST(U4(f), rs_ws, lo16, WS_H + ((2 * c + th) * 56 + (k >> 1) * 28 + 4 * j + 2 * chh + (k & 1)) * 1024);
      }
      publish4(ws, WS_FH + (c * 8 + j) * 128, tag, lane);
    }
    STAMP(5);
    const CAS Params* kp = KARGS();
    const unsigned cst = wa < 3 ? wa * 28672u : L_R1;
    {
      const u32x4 rc = mkrs((const char*)(th ? kp->cim : kp->cre) + (size_t)j * 57344, 57344);
      const unsigned vh = (lane >> 2) * 896 + (((lane & 3) ^ ((lane >> 4) & 3)) << 4) + 384 + chh * 64, vo = ve ^ 64;
      int n2 = 6 * cl;
#pragma unroll
      for (int n = 0; n < 24; ++n) {
        DMA(rc, (n & 1) ? vo : ve, (n2 & 7) * 7168 + (n2 >> 3) * 128 + chh * 512, cst + n2 * 1024, 0); if (n >= CPACE) VMW(CPACE);
        n2 = n2 == 23 ? 0 : n2 + 1; }
#pragma unroll
      for (int n = 0; n < 4; ++n) DMA(rc, vh, n * 14336, cst, 24576 + n * 1024);
    }
    VMW(0);
    bf16x8 cfr[14][2];
#pragma unroll
    for (int f = 0; f < 14; ++f)
#pragma unroll
      for (int n = 0; n < 2; ++n) { const int row = 32 * (n ^ chh) + r32;
        cfr[f][n] = f < 12 ? ldsf(lds + cst + (f >> 2) * 8192 + row * 128 + (((2 * (f & 3) + hi) ^ xsw) << 4))
                           : ldsf(lds + cst + 24576 + row * 64 + (((2 * (f & 1) + hi) ^ ((r32 >> 2) & 3)) << 4)); }
    lds_signal(cCR);
    STAMP(10);
    f32x16 acc[4] = {zero16, zero16, zero16, zero16};
#pragma unroll
    for (int r = 0; r < 8; ++r) { acc[0][2 * r] = u2f(xd[r] << 16); acc[0][2 * r + 1] = u2f(xd[r] & 0xffff0000u); }
    PIN(acc[0]);
    poll(ws, WS_FH + (c * 8 + umin(lane & 7, 6)) * 128, tag);
    STAMP(6);
    {
      const unsigned hb = WS_H + (2 * c * 56 + 14 * wa) * 1024, h0 = hb + th * 57344, h1 = hb + (th ^ 1) * 57344;
      const unsigned fa = chh * 2048, fb = chh ? 0u - 12288u : 0u;
      u32x4 hf[14][2];
#define LDH(f) do { hf[f][0] = BLD(rs_ws, lo16, h0 + (f) * 1024 + ((f) < 12 ? fa : fb)); hf[f][1] = BLD(rs_ws, lo16, h1 + (f) * 1024 + ((f) < 12 ? fa : fb)); } while (0)
#define MMH(f) do { acc[0] = MFMA(BF(hf[f][0]), cfr[f][0], acc[0]); acc[1] = MFMA(BF(hf[f][0]), cfr[f][1], acc[1]); \
                    acc[2] = MFMA(BF(hf[f][1]), cfr[f][0], acc[2]); acc[3] = MFMA(BF(hf[f][1]), cfr[f][1], acc[3]); \
                    PIN(acc[0]); PIN(acc[1]); PIN(acc[2]); PIN(acc[3]); } while (0)
#pragma unroll
      for (int f = 0; f < 5; ++f) LDH(f);
      SBAR();
      __builtin_amdgcn_s_setprio(GPRIO);
#pragma unroll
      for (int f = 0; f < 9; ++f) { MMH(f); SBAR(); LDH(f + 5); SBAR(); }
      lds_wait(cCR, 4);
      {
        const u32x4 rw = mkrs((const char*)kp->wo + j * 128 + wa * 7168, 401408 - j * 128 - wa * 7168);
        const unsigned vw = lrow * 896 + (((lane & 7) ^ ((lrow & 2) << 1)) << 4);
        int m2 = 4 * cl;
#pragma unroll
        for (int m = 0; m < 14; ++m) { DMA(rw, vw, m2 * 28672, wa * 1024 + m2 * 4096, L_WO); m2 = m2 == 13 ? 0 : m2 + 1; }
        if (wa == 0 && lane < 32) *(LAS u32x4*)(lds + L_WO + 57344 + lo16) = (u32x4){0, 0, 0, 0};
      }
      asm volatile("s_waitcnt vmcnt(14)" ::: "memory");
      SBAR();
#pragma unroll
      for (int f = 9; f < 14; ++f) MMH(f);
    }
    STAMP(11);
    poll(ws, WS_STARTED + ((b & 7) + 8 * ((loc & ~7) + (lane & 7))) * 128, tag);
    VMW(0);
    lds_signal(cWO);
    rs_write(lds + L_G2, wa, lane, acc[1], acc[2], acc[3]);
    lds_barrier(cAB, 28);
    f32x16 y = rs_read(lds + L_G2, wa, lane, acc[0]);
    const __amdgpu_buffer_rsrc_t rs_out = __builtin_amdgcn_make_buffer_rsrc(kp->out, 0, 2048 * 896 * 4, 0x00020000);
    const unsigned oo = ((64 * c + 32 * th + 4 * hi) * 896 + 64 * j + 32 * chh + r32) * 4;
#pragma unroll
    for (int r = 0; r < 16; ++r) __builtin_amdgcn_raw_buffer_store_b32(f2u(y[r]), rs_out, oo, ((r & 3) + 8 * (r >> 2)) * 3584, OAUX);
    STAMP(7);
    STAMP_OUT();
  }
}

extern "C" void kernel_launch(void* const* d_in, const int* in_sizes, int n_in, void* d_out, int out_size, void* d_ws, size_t ws_size,
                              hipStream_t stream) {
  Params p;
  p.xa = (const u16*)d_in[0]; p.lre = (const u16*)d_in[1]; p.lim = (const u16*)d_in[2]; p.bre = (const u16*)d_in[3]; p.bim = (const u16*)d_in[4];
  p.cre = (const u16*)d_in[5]; p.cim = (const u16*)d_in[6]; p.ldt = (const u16*)d_in[7]; p.dsk = (const u16*)d_in[8];
  p.xb = (const u16*)d_in[9]; p.wq = (const u16*)d_in[10]; p.wk = (const u16*)d_in[11]; p.wv = (const u16*)d_in[12]; p.wo = (const u16*)d_in[13];
  p.out = (float*)d_out; p.ws = (char*)d_ws;
  fused_kernel<<<256, 512, 0, stream>>>(p);
}
```

```cpp
#include <hip/hip_runtime.h>
#include <stdint.h>

#define DI __device__ __forceinline__
#define GAS __attribute__((address_space(1)))
#define LAS __attribute__((address_space(3)))
#define CAS __attribute__((address_space(4)))
typedef short bf16x8 __attribute__((ext_vector_type(8)));
typedef short s16x4 __attribute__((ext_vector_type(4)));
typedef float f32x16 __attribute__((ext_vector_type(16)));
typedef float f32x4 __attribute__((ext_vector_type(4)));
typedef float f32x2 __attribute__((ext_vector_type(2)));
typedef unsigned u32x4 __attribute__((ext_vector_type(4)));
typedef unsigned u32x2 __attribute__((ext_vector_type(2)));
typedef __bf16 bf16x2_t __attribute__((ext_vector_type(2)));
typedef unsigned long long u64;
typedef unsigned short u16;
typedef LAS char* lptr;
#define RLX_AGENT __ATOMIC_RELAXED, __HIP_MEMORY_SCOPE_AGENT
#define MFMA(a, b, c) __builtin_amdgcn_mfma_f32_32x32x16_bf16((a), (b), (c), 0, 0, 0)
#define SBAR() __builtin_amdgcn_sched_barrier(0)
#define PIN(x) asm volatile("" : "+v"(x))
#define MEMBAR() asm volatile("" ::: "memory")
#define SC1 16
#define APRIO 1
#define GPRIO 1
#define PSLEEP 2
#define APACE 63
#define OAUX 17
#define CPACE 6

constexpr unsigned WS_EPOCH = 0, WS_FKV = 4096, WS_FH = 8192, WS_E = 0x10000, WS_STARTED = 0x48000,
                   WS_KV = 0x50000, WS_O = 0x100000, WS_H = 0x300000, WS_END = 0x680000, WS_DBG = 0x700000;
constexpr unsigned SK = 15360, K_W = 8192, K_WQ = 107520;
constexpr unsigned CH = 24576;
constexpr unsigned L_QS = 99328, L_R1 = 107520, L_HT = 136192, L_WO = 0, L_OC = 57856, L_G2 = 107520, L_MISC = 159744, L_EX = L_MISC + 128, L_TOTAL = 163840;
constexpr unsigned cBB = L_MISC, cAB = L_MISC + 4, cCR = L_MISC + 8, cWO = L_MISC + 12, cOC = L_MISC + 16, cPB = L_MISC + 20, L_TAG = L_MISC + 64;
constexpr unsigned SPIN_CAP = 1u << 18;

struct Params {
  const u16 *xb, *wq, *wk, *wv; char* ws; const u16 *lre, *lim, *ldt, *dsk, *xa, *bre, *bim, *cre, *cim, *wo;
  float* out;
};

DI float u2f(unsigned v) { return __builtin_bit_cast(float, v); }
DI unsigned f2u(float v) { return __builtin_bit_cast(unsigned, v); }
DI unsigned umin(unsigned a, unsigned b) { return a < b ? a : b; }
DI unsigned umax(unsigned a, unsigned b) { return a > b ? a : b; }
DI bool wall(bool x) { return __builtin_amdgcn_ballot_w64(!x) == 0; }
DI float bf2f(unsigned v) { return u2f(v << 16); }
DI unsigned pk(float a, float b) { f32x2 v = {a, b}; return __builtin_bit_cast(unsigned, __builtin_convertvector(v, bf16x2_t)); }
DI float rbf(float a) { return u2f(pk(0.f, a)); }
DI bf16x8 pack8(const f32x16& x, int s) {
  u32x4 p = {pk(x[8 * s], x[8 * s + 1]), pk(x[8 * s + 2], x[8 * s + 3]), pk(x[8 * s + 4], x[8 * s + 5]), pk(x[8 * s + 6], x[8 * s + 7])};
  return __builtin_bit_cast(bf16x8, p);
}
DI u32x4 mkrs(const void* base, unsigned bytes) { const u64 a = (u64)base; return (u32x4){(unsigned)a, (unsigned)(a >> 32) & 0xffffu, bytes, 0x20000u}; }
#define DMA(rs, voff, soff, ldsb, IMM) asm volatile("s_add_u32 m0, %3, %4\n\ts_nop 0\n\tbuffer_load_dwordx4 %0, %1, %2 offen lds" \
                                                    :: "v"(voff), "s"(rs), "s"(soff), "s"(ldsb), "i"(IMM) : "memory", "scc")
DI bf16x8 ldsf(lptr p) { return *(LAS bf16x8*)p; }
DI s16x4 ldstr(lptr p) { return __builtin_amdgcn_ds_read_tr16_b64_v4i16((LAS s16x4*)p); }
DI bf16x8 ldstr2(lptr p, int second) { s16x4 lo = ldstr(p), hi = ldstr(p + second); return __builtin_shufflevector(lo, hi, 0, 1, 2, 3, 4, 5, 6, 7); }
DI void lds_signal(unsigned c) {
  asm volatile("s_waitcnt lgkmcnt(0)\n\ts_mov_b64 exec, 1\n\tds_add_u32 %0, %1\n\ts_mov_b64 exec, -1" :: "v"(c), "v"(1u) : "memory");
}
template <int SLEEP> DI void lds_wait_(unsigned c, unsigned target) {
  unsigned v, s, n;
  asm volatile("s_mov_b32 %2, 0x400000\n1:\n\tds_read_b32 %0, %3\n\ts_waitcnt lgkmcnt(0)\n\tv_readfirstlane_b32 %1, %0\n\ts_cmp_ge_u32 %1, %4\n\ts_cbranch_scc1 2f\n\t"
               "s_sleep %5\n\ts_sub_u32 %2, %2, 1\n\ts_cbranch_scc0 1b\n2:" : "=&v"(v), "=&s"(s), "=&s"(n) : "v"(c), "i"(target), "i"(SLEEP) : "memory", "scc");
}
#define lds_wait(c, t) lds_wait_<0>(c, t)
#define lds_barrier(c, t) do { lds_signal(c); lds_wait(c, t); } while (0)
DI void poll(const char* ws, unsigned off, u64 tag) {
#pragma unroll 1
  for (unsigned s = 0; s < SPIN_CAP; ++s) {
    u64 v = __hip_atomic_load((GAS u64*)(ws + off), RLX_AGENT);
    if (wall(v == tag)) break;
    __builtin_amdgcn_s_sleep(PSLEEP);
  }
  MEMBAR();
}
DI void publish4(char* ws, unsigned off, u64 tag, int lane) {
  unsigned old;
  asm volatile("s_waitcnt vmcnt(0)\n\ts_mov_b64 exec, 1\n\tds_add_rtn_u32 %0, %1, %2\n\ts_waitcnt lgkmcnt(0)\n\ts_mov_b64 exec, -1" : "=&v"(old) : "v"(cPB), "v"(1u) : "memory");
  if (__builtin_amdgcn_readfirstlane(old) == 3 && lane == 0) __hip_atomic_store((GAS u64*)(ws + off), tag, RLX_AGENT);
}
DI float swap_lo(float x) {
  auto r = __builtin_amdgcn_permlane32_swap(f2u(x), f2u(x), false, false); return u2f(r[0]); }
DI float swap_hi(float x) {
  auto r = __builtin_amdgcn_permlane32_swap(f2u(x), f2u(x), false, false); return u2f(r[1]); }
struct cf { float r, i; };
DI cf cmul(cf a, cf b) { return {a.r * b.r - a.i * b.i, a.r * b.i + a.i * b.r}; }
DI cf cfma(cf a, cf b, cf c) { return {__builtin_fmaf(a.r, b.r, __builtin_fmaf(-a.i, b.i, c.r)), __builtin_fmaf(a.r, b.i, __builtin_fmaf(a.i, b.r, c.i))}; }

#define STAMP(k) do {} while (0)
#define STAMP_OUT() do {} while (0)

struct KV { bf16x8 k0, k1, v0, v1; };

DI void rs_write(lptr base, int w, int lane, const f32x16& t1, const f32x16& t2, const f32x16& t3) {
#define RSW(T, I) _Pragma("unroll") for (int q = 0; q < 4; ++q) *(LAS f32x4*)(base + ((w ^ I) * 3 + I - 1) * 4096 + q * 1024 + lane * 16) = (f32x4){T[4 * q], T[4 * q + 1], T[4 * q + 2], T[4 * q + 3]}
  RSW(t1, 1); RSW(t2, 2); RSW(t3, 3);
}
DI f32x16 rs_read(lptr base, int w, int lane, f32x16 a) {
  f32x4 v[12];
#pragma unroll
  for (int s = 0; s < 12; ++s) v[s] = *(LAS f32x4*)(base + w * 12288 + s * 1024 + lane * 16);
  SBAR();
#pragma unroll
  for (int s = 0; s < 12; ++s) { const int q = s & 3; a[4 * q] += v[s][0]; a[4 * q + 1] += v[s][1]; a[4 * q + 2] += v[s][2]; a[4 * q + 3] += v[s][3]; }
  return a;
}

#define VMW_(n) asm volatile("s_waitcnt vmcnt(" #n ")" ::: "memory")
#define VMW(n) VMW_(n)
#define BAR() asm volatile("s_waitcnt lgkmcnt(0)\n\ts_barrier" ::: "memory")
#define BLD(R, V, S) __builtin_amdgcn_raw_buffer_load_b128(R, V, S, SC1)
#define BST(D, R, V, S) __builtin_amdgcn_raw_buffer_store_b128(D, R, (V) + (S), 0, SC1)
#define BF(x) __builtin_bit_cast(bf16x8, x)
#define U4(x) __builtin_bit_cast(u32x4, x)
#define EX(x) __builtin_amdgcn_exp2f(x)
#define KARGS() ({ const CAS Params* kp_ = (const CAS Params*)__builtin_amdgcn_kernarg_segment_ptr(); asm volatile("" : "+s"(kp_)); kp_; })
#define NEXTK() do { k += step; k = k >= 7 ? k - 7 : k; } while (0)

#if defined(__HIP_DEVICE_COMPILE__)
#define NO_PK __attribute__((target("no-packed-fp32-ops")))
#else
#define NO_PK
#endif
__global__ void __launch_bounds__(512, 2) NO_PK fused_kernel(Params p) {
  __shared__ __attribute__((aligned(128))) char smem[L_TOTAL];
  const lptr lds = (lptr)smem;
  const int tid = threadIdx.x, lane = tid & 63, wave = __builtin_amdgcn_readfirstlane(tid >> 6);
  const int b = blockIdx.x, loc = b >> 3, cl = loc >> 3, c = (b & 7) * 4 + cl, j = loc & 7;
  const int lrow = lane >> 3;
  const unsigned ve = lrow * 896 + (((lane & 7) ^ (lrow >> 1)) << 4);
  const int step = cl + 1, k0 = j == 7 ? 0 : j;
  const bool isB = wave < 4;
  const int wa = wave & 3;
  char* const ws = p.ws;
  STAMP(0);
  unsigned ua = 0, ub = 0, uc = 0, ud = 0;
  if (wave == 7) {
    u64 pc; unsigned len;
    asm volatile("s_getpc_b64 %0\n\ts_mov_b32 %1, 30000" : "=s"(pc), "=s"(len));
    const u32x4 rc = mkrs((const void*)pc, len);
#pragma unroll
    for (int i = 0; i < 6; ++i) asm volatile("s_mov_b32 m0, %3\n\ts_nop 0\n\tbuffer_load_dword %0, %1, %2 offen lds" :: "v"(lane * 128), "s"(rc), "s"(i * 8192), "s"(L_MISC + 2304) : "memory");
  }
  {
    const u32x4 rx = mkrs((const char*)p.xb + (size_t)c * 57344 + wave * 7168, 7168);
    const unsigned vx = ve ^ ((wave & 1) << 6), lb = wave * 1024;
    int k = k0;
    if (j == 7) {
      const u32x4 rw = mkrs(isB ? p.wk : p.wv, 25088);
      const unsigned vw = wa * 1024 + lane * 16, lw = K_W + (isB ? 0 : 3584) + wa * 1024; const bool half = wa < 3 || lane < 32;
#pragma unroll
      for (int i = 0; i < 7; ++i) { NEXTK(); DMA(rx, vx, k * 128, lb, i * SK); if (half) DMA(rw, vw, k * 3584, lw, i * SK); }
    } else {
      const int iw = wave * 64 + lane, wrow = (iw * 9363) >> 16;
      const unsigned vwq = wrow * 896 + (iw - wrow * 7) * 16;
      const u32x4 rw = mkrs((const char*)p.wq + j * 112, 401408 - j * 112);
      if (wave < 7) {
#pragma unroll
        for (int i = 0; i < 7; ++i) { NEXTK(); DMA(rx, vx, k * 128, lb, i * SK); DMA(rw, vwq, k * 57344, lb, i * SK + K_W); }
      } else {
#pragma unroll
        for (int i = 0; i < 7; ++i) { NEXTK(); DMA(rx, vx, k * 128, lb, i * SK); }
      }
    }
  }
  STAMP(8);
  if (tid < 16) ((LAS unsigned*)(lds + L_MISC))[tid] = 0;
  const int r32 = lane & 31, hi = lane >> 5;
  const int g4 = lane >> 4, tq = (lane & 15) >> 2, tp = lane & 3;
  const unsigned xsw = (r32 >> 1) & 7, lo16 = lane * 16;
  const int th = wa >> 1, chh = wa & 1;
  u64 tag;
  const __amdgpu_buffer_rsrc_t rs_ws = __builtin_amdgcn_make_buffer_rsrc(ws, 0, WS_END, 0x00020000);
  const f32x16 zero16 = {};
  const unsigned o_own = WS_O + (((2 * c + th) * 16 + 2 * j + chh) * 2) * 1024;
  const unsigned xq_off = (32 * th + r32) * 128;
  const unsigned wq_off = (8 * (g4 >> 1) + tq) * 112 + (28 * chh + 16 * (g4 & 1) + 4 * tp) * 2;
  f32x16 q = zero16;
#define MARK() do { const u32x4 ones = {~0u, ~0u, ~0u, ~0u};       \
    BST(ones, rs_ws, lo16, o_own); BST(ones, rs_ws, lo16 + 1024, o_own); } while (0)
#define LDEPOCH() asm volatile("s_load_dwordx2 %0, %1, 0x0\n\ts_waitcnt lgkmcnt(0)" : "=s"(tag) : "s"(ws) : "memory"); tag = (tag | 1) + 2
#define QRD(B, X, W) _Pragma("unroll") for (int ks = 0; ks < 4; ++ks) { B##x[ks] = ldsf(lds + (X) + xq_off + (((2 * ks + hi) ^ xsw) << 4)); B##w[ks] = ldstr2(lds + (W) + wq_off + ks * 1792, 448); } SBAR()
#define QMM(B) _Pragma("unroll") for (int ks = 0; ks < 4; ++ks) q = MFMA(B##w[ks], B##x[ks], q); PIN(q); SBAR()
  bf16x8 fax[4], faw[4], fbx[4], fbw[4];

  if (j == 7) {
    LDEPOCH();
    VMW(6); BAR();
    if (!isB) {
      const unsigned xk_off = (32 * chh + r32) * 128, wk_off = K_W + th * 3584 + (8 * (g4 >> 1) + tq) * 56 + (16 * (g4 & 1) + 4 * tp) * 2;
      f32x16 a = zero16;
#define KRD(B, i) _Pragma("unroll") for (int ks = 0; ks < 4; ++ks) { B##x[ks] = ldsf(lds + (i) * SK + xk_off + (((2 * ks + hi) ^ xsw) << 4)); B##w[ks] = ldstr2(lds + (i) * SK + wk_off + ks * 896, 224); } SBAR()
#define KMM(B) if (th) { _Pragma("unroll") for (int ks = 0; ks < 4; ++ks) a = MFMA(B##x[ks], B##w[ks], a); } else { _Pragma("unroll") for (int ks = 0; ks < 4; ++ks) a = MFMA(B##w[ks], B##x[ks], a); } PIN(a); SBAR()
      KRD(fa, 0); KRD(fb, 1); KMM(fa); KRD(fa, 2); KMM(fb); KRD(fb, 3); KMM(fa); KMM(fb);
      VMW(0); BAR();
      KRD(fa, 4); KRD(fb, 5); KMM(fa); KRD(fa, 6); KMM(fb); KMM(fa);
      STAMP(1);
      if (th) {
#pragma unroll
        for (int r = 0; r < 16; ++r) a[r] = r32 < 28 ? a[r] : (r32 == 28 ? 1.f : 0.f);
      } else if (hi) { a[12] = 1.f; a[13] = 1.f; a[14] = 1.f; a[15] = 0.f; }
      const unsigned ko = WS_KV + (2 * c + chh) * 4096 + th * 2048;
      BST(U4(pack8(a, 0)), rs_ws, lo16, ko); BST(U4(pack8(a, 1)), rs_ws, lo16 + 1024, ko);
      lds_signal(cCR);
      publish4(ws, WS_FKV + c * 128, tag, lane);
      STAMP(2);
      if (c == 0 && wa == 0) {
        __builtin_amdgcn_s_sleep(127);
#pragma unroll 1
        for (int k = 0; k < 4; ++k) poll(ws, WS_STARTED + (k * 64 + lane) * 128, tag);
        if (lane == 0) __hip_atomic_store((GAS u64*)(ws + WS_EPOCH), tag, RLX_AGENT);
      }
      STAMP_OUT();
      return;
    }
    VMW(0); BAR();
    const int i0 = wa * 128 + lane, r0 = (i0 * 9363) >> 16, r1 = ((i0 + 64) * 9363) >> 16;
    const unsigned vq0 = r0 * 896 + (i0 - r0 * 7) * 16, vq1 = r1 * 896 + (i0 + 64 - r1 * 7) * 16, lb = wave * 2048;
    const u32x4 rw = mkrs((const char*)p.wq + 784, 401408 - 784);
    int k = k0;
#pragma unroll
    for (int i = 0; i < 7; ++i) { NEXTK(); DMA(rw, vq0, k * 57344, lb, K_WQ + i * 7168); if (wave < 3) DMA(rw, vq1, k * 57344, lb, K_WQ + i * 7168 + 1024); }
    MARK();
    VMW(0);
    lds_barrier(cOC, 4);
    QRD(fa, 0, K_WQ); QRD(fb, SK, K_WQ + 7168); QMM(fa); QRD(fa, 2 * SK, K_WQ + 2 * 7168); QMM(fb); QRD(fb, 3 * SK, K_WQ + 3 * 7168); QMM(fa);
    QRD(fa, 4 * SK, K_WQ + 4 * 7168); QMM(fb); QRD(fb, 5 * SK, K_WQ + 5 * 7168); QMM(fa); QRD(fa, 6 * SK, K_WQ + 6 * 7168); QMM(fb); QMM(fa);
    lds_wait(cCR, 4);
    STAMP(1);
  } else {
    if (isB) { MARK(); LDEPOCH(); }
    if (wave < 7) VMW(6); else VMW(3);
    BAR();
    if (isB) { QRD(fa, 0, K_W); QRD(fb, SK, SK + K_W); QMM(fa); QRD(fa, 2 * SK, 2 * SK + K_W); QMM(fb); QRD(fb, 3 * SK, 3 * SK + K_W); QMM(fa); QMM(fb); }
    else { const CAS Params* kp = KARGS(); const unsigned co = (64 * j + 32 * chh + r32) * 2;
      asm volatile("s_nop 4\n\tglobal_load_ushort %0, %4, %5\n\tglobal_load_ushort %1, %4, %6\n\tglobal_load_ushort %2, %4, %7\n\tglobal_load_ushort %3, %4, %8"
                   : "=&v"(ua), "=&v"(ub), "=&v"(uc), "=&v"(ud) : "v"(co), "s"(kp->lre), "s"(kp->lim), "s"(kp->ldt), "s"(kp->dsk) : "memory"); }
    asm volatile("s_waitcnt vmcnt(0) lgkmcnt(0)\n\ts_barrier" : "+v"(ua), "+v"(ub), "+v"(uc), "+v"(ud) :: "memory");
    if (isB) { QRD(fa, 4 * SK, 4 * SK + K_W); QRD(fb, 5 * SK, 5 * SK + K_W); QMM(fa); QRD(fa, 6 * SK, 6 * SK + K_W); QMM(fb); QMM(fa); STAMP(1); }
  }

  if (isB) {
    if (tid == 0) *(LAS u64*)(lds + L_TAG) = tag;
    lds_barrier(cBB, 4);
    if (tid == 0) __hip_atomic_store((GAS u64*)(ws + WS_STARTED + b * 128), tag, RLX_AGENT);
    const float qs = 0.18898223650461361f * 1.4426950408889634f;
#pragma unroll
    for (int r = 0; r < 16; ++r) q[r] *= qs;
    if (hi) { q[12] = 0.f; q[13] = 0.f; q[14] = 0.f; q[15] = 0.f; }
    const bf16x8 qo0 = pack8(q, 0), qo1 = pack8(q, 1);
    *(LAS bf16x8*)(lds + L_QS + wave * 2048 + lo16) = qo0;
    *(LAS bf16x8*)(lds + L_QS + wave * 2048 + 1024 + lo16) = qo1;
    lds_barrier(cBB, 8);
    bf16x8 Q0[4], Q1[4];
    Q0[0] = qo0; Q1[0] = qo1;
#pragma unroll
    for (int i = 1; i < 4; ++i) { Q0[i] = ldsf(lds + L_QS + (wave ^ i) * 2048 + lo16); Q1[i] = ldsf(lds + L_QS + (wave ^ i) * 2048 + 1024 + lo16); }
    STAMP(2);
    poll(ws, WS_FKV + (wave * 8 + (lane & 7)) * 128, tag);
    STAMP(3);
    const __amdgpu_buffer_rsrc_t rs_kv = __builtin_amdgcn_make_buffer_rsrc(ws + WS_KV + wave * 65536, 0, 65536, 0x00020000);
#define LDKV(B, T) do { const unsigned so_ = (T) < 16 ? (((T) + loc) & 15) * 4096 : 65536; \
                        B.k0 = BF(BLD(rs_kv, lo16, so_)); B.k1 = BF(BLD(rs_kv, lo16 + 1024, so_)); \
                        B.v0 = BF(BLD(rs_kv, lo16 + 2048, so_)); B.v1 = BF(BLD(rs_kv, lo16 + 3072, so_)); } while (0)
    KV A0, A1, A2, A3;
    LDKV(A0, 0); LDKV(A1, 1); LDKV(A2, 2);
    SBAR();
    A3.k0 = A3.k1 = A3.v0 = A3.v1 = (bf16x8){};
    f32x16 O[4] = {zero16, zero16, zero16, zero16};
    f32x16 Sa, Sb = zero16; u32x4 Pa, Pb = {};
    Sa = MFMA(A0.k0, Q0[0], zero16); Sa = MFMA(A0.k1, Q1[0], Sa);
    STAMP(9);
#define UNIT(S, Sn, KN, QN, VP, OP, VC, OC) do { \
      Pb[2] = pk(Sn[12], Sn[13]); Pb[3] = pk(Sn[14], Sn[15]); \
      Sn = MFMA(KN.k0, Q0[QN], zero16); S[0] = EX(S[0]); S[1] = EX(S[1]); S[2] = EX(S[2]); S[3] = EX(S[3]); SBAR(); \
      Sn = MFMA(KN.k1, Q1[QN], Sn); Pa[0] = pk(S[0], S[1]); Pa[1] = pk(S[2], S[3]); S[4] = EX(S[4]); S[5] = EX(S[5]); S[6] = EX(S[6]); S[7] = EX(S[7]); SBAR(); \
      OP = MFMA(VP.v1, BF(Pb), OP); Pa[2] = pk(S[4], S[5]); Pa[3] = pk(S[6], S[7]); S[8] = EX(S[8]); S[9] = EX(S[9]); S[10] = EX(S[10]); S[11] = EX(S[11]); SBAR(); \
      OC = MFMA(VC.v0, BF(Pa), OC); Pb[0] = pk(S[8], S[9]); Pb[1] = pk(S[10], S[11]); S[12] = EX(S[12]); S[13] = EX(S[13]); S[14] = EX(S[14]); S[15] = EX(S[15]); SBAR(); \
    } while (0)
#define TILE(CUR, NXT, PRV, T) do { \
      UNIT(Sa, Sb, CUR, 1, PRV, O[3], CUR, O[0]); \
      LDKV(PRV, (T) + 3); SBAR(); \
      UNIT(Sb, Sa, CUR, 2, CUR, O[0], CUR, O[1]); \
      UNIT(Sa, Sb, CUR, 3, CUR, O[1], CUR, O[2]); \
      UNIT(Sb, Sa, NXT, 0, CUR, O[2], CUR, O[3]); } while (0)
#pragma unroll 1
    for (int t = 0; t < 16; t += 4) {
      TILE(A0, A1, A3, t); TILE(A1, A2, A0, t + 1); TILE(A2, A3, A1, t + 2); TILE(A3, A0, A2, t + 3);
    }
    Pb[2] = pk(Sb[12], Sb[13]); Pb[3] = pk(Sb[14], Sb[15]);
    O[3] = MFMA(A3.v1, BF(Pb), O[3]);
    PIN(O[3]);
    STAMP(4);
    lds_wait(cCR, 4);
    rs_write(lds + L_OC, wave, lane, O[1], O[2], O[3]);
    lds_barrier(cBB, 12);
    STAMP(10);
    f32x16 o = rs_read(lds + L_OC, wave, lane, O[0]);
    {
      float l = swap_hi(o[12]);
      float am = 0.f;
#pragma unroll
      for (int r = 0; r < 16; ++r) am = __builtin_fmaxf(am, __builtin_fabsf(o[r]));
      const bool ok = (l >= 0x1p-64f) && (l <= 0x1p64f) && (am <= 0x1p120f);
      if (!wall(ok)) {
        const __amdgpu_buffer_rsrc_t rk = __builtin_amdgcn_make_buffer_rsrc(ws + WS_KV, 0, 262144, 0x00020000);
        poll(ws, WS_FKV + r32 * 128, tag);
        float m = -__builtin_inff();
#pragma unroll 1
        for (int t = 0; t < 64; ++t) {
          f32x16 s = MFMA(BF(BLD(rk, lo16, t * 4096)), qo0, zero16); s = MFMA(BF(BLD(rk, lo16 + 1024, t * 4096)), qo1, s);
#pragma unroll
          for (int r = 0; r < 16; ++r) m = __builtin_fmaxf(m, s[r]);
        }
        m = __builtin_fmaxf(swap_lo(m), swap_hi(m));
        o = zero16;
#pragma unroll 1
        for (int t = 0; t < 64; ++t) {
          f32x16 s = MFMA(BF(BLD(rk, lo16, t * 4096)), qo0, zero16); s = MFMA(BF(BLD(rk, lo16 + 1024, t * 4096)), qo1, s);
#pragma unroll
          for (int r = 0; r < 16; ++r) s[r] = EX(s[r] - m);
          o = MFMA(BF(BLD(rk, lo16 + 2048, t * 4096)), pack8(s, 0), o); o = MFMA(BF(BLD(rk, lo16 + 3072, t * 4096)), pack8(s, 1), o);
        }
        l = swap_hi(o[12]);
      }
      const float inv = 1.f / l;
#pragma unroll
      for (int r = 0; r < 16; ++r) o[r] *= inv;
      if (hi) { o[12] = 0.f; o[13] = 0.f; o[14] = 0.f; o[15] = 0.f; }
      u32x4 f0 = U4(pack8(o, 0)), f1 = U4(pack8(o, 1));
#pragma unroll
      for (int k = 0; k < 4; ++k) { f0[k] = umin(f0[k], 0xfffffffeu); f1[k] = umin(f1[k], 0xfffffffeu); }
      BST(f0, rs_ws, lo16, o_own); BST(f1, rs_ws, lo16 + 1024, o_own);
    }
    lds_signal(cOC);
    STAMP(5);
    if (j == 7) { STAMP_OUT(); return; }
    {
      const CAS Params* kp = KARGS();
      const __amdgpu_buffer_rsrc_t rs_out = __builtin_amdgcn_make_buffer_rsrc(kp->out, 0, 2048 * 896 * 4, 0x00020000);
      lds_wait(cWO, 4);
      u32x4 of[16];
      const unsigned ob = WS_O + (((2 * c + th) * 16 + 8 * chh) * 2) * 1024;
#pragma unroll 1
      for (unsigned s = 0; s < SPIN_CAP; ++s) {
        unsigned mx = 0;
#pragma unroll
        for (int k = 0; k < 16; ++k) of[k] = BLD(rs_ws, lo16 + (k & 3) * 1024, ob + (k >> 2) * 4096);
        SBAR();
#pragma unroll
        for (int k = 0; k < 16; ++k) mx = umax(umax(mx, umax(of[k][0], of[k][1])), umax(of[k][2], of[k][3]));
        if (wall(mx != ~0u)) break;
        __builtin_amdgcn_s_sleep(2);
      }
      STAMP(6);
      f32x16 acc0 = zero16, acc1 = zero16;
      const int rowb = 224 * chh + 4 * (g4 >> 1) + tq;
      const unsigned wb = (L_WO + rowb * 128 + ((32 * (g4 & 1) + 8 * tp) ^ ((tq & 2) << 5))) ^ (chh << 6);
      bf16x8 w0[2][4], w1[2][4];
#define WRD(kk) _Pragma("unroll") for (int k4 = 0; k4 < 4; ++k4) { const int k = 4 * (kk) + k4; const unsigned a_ = (28 * (k >> 1) + 16 * (k & 1)) * 128; \
          w0[(kk) & 1][k4] = ldstr2(lds + wb + a_, 1024); w1[(kk) & 1][k4] = ldstr2(lds + (wb ^ 64) + a_, 1024); } SBAR()
#define WMM(kk) _Pragma("unroll") for (int k4 = 0; k4 < 4; ++k4) { acc1 = MFMA(BF(of[4 * (kk) + k4]), w1[(kk) & 1][k4], acc1); acc0 = MFMA(BF(of[4 * (kk) + k4]), w0[(kk) & 1][k4], acc0); } PIN(acc0); PIN(acc1); SBAR()
      WRD(0); WRD(1); WMM(0); WRD(2); WMM(1); WRD(3); WMM(2); WMM(3);
      STAMP(11);
      lds_wait(cOC, 4);
#pragma unroll
      for (int k = 0; k < 4; ++k) *(LAS f32x4*)(lds + L_OC + (wave ^ 1) * 4096 + k * 1024 + lo16) = (f32x4){acc1[4 * k], acc1[4 * k + 1], acc1[4 * k + 2], acc1[4 * k + 3]};
      lds_barrier(cBB, 16);
#pragma unroll
      for (int k = 0; k < 4; ++k) { const f32x4 v = *(LAS f32x4*)(lds + L_OC + wave * 4096 + k * 1024 + lo16);
        acc0[4 * k] += v[0]; acc0[4 * k + 1] += v[1]; acc0[4 * k + 2] += v[2]; acc0[4 * k + 3] += v[3]; }
      STAMP(12);
      const unsigned oo = ((64 * c + 32 * th + 4 * hi) * 896 + 448 + 64 * j + 32 * chh + r32) * 4;
#pragma unroll
      for (int r = 0; r < 16; ++r) __builtin_amdgcn_raw_buffer_store_b32(pk(0.f, acc0[r]), rs_out, oo, ((r & 3) + 8 * (r >> 2)) * 3584, OAUX);
    }
    STAMP(7);
    STAMP_OUT();
  } else {
    cf lam, coef, l16, l64; f32x16 ur = zero16, ui = zero16; unsigned xd[8];
    const float pd = bf2f(ud);
    {
      const CAS Params* kp = KARGS();
      const u32x4 rxa = mkrs((const char*)kp->xa + (size_t)c * 57344 + wa * 14336, 14336);
      const u32x4 rbr = mkrs((const char*)kp->bre + (size_t)j * 57344 + wa * 14336, 14336);
      const u32x4 rbi = mkrs((const char*)kp->bim + (size_t)j * 57344 + wa * 14336, 14336);
      const unsigned vo = (ve ^ 64) + 7168, lb = wa * 2048;
      int k = k0;
#define LDCH(slot) do { NEXTK(); const unsigned k_ = k * 128; DMA(rxa, ve, k_, lb, (slot)); DMA(rxa, vo, k_, lb, (slot) + 1024); DMA(rbr, ve, k_, lb, (slot) + 8192); \
      DMA(rbr, vo, k_, lb, (slot) + 9216); DMA(rbi, ve, k_, lb, (slot) + 16384); DMA(rbi, vo, k_, lb, (slot) + 17408); } while (0)
      LDCH(L_R1); LDCH(L_R1 + CH);
      {
        const float pa = bf2f(ua), pb = bf2f(ub), dt = rbf(expf(bf2f(uc)));
        const float er = expf(dt * pa); float sn, cs; sincosf(dt * pb, &sn, &cs);
        lam = {er * cs, er * sn};
        const float nr = lam.r - 1.f, ni = lam.i, den = pa * pa + pb * pb;
        coef = {2.f * (nr * pa + ni * pb) / den, 2.f * (ni * pa - nr * pb) / den};
        l16 = cmul(lam, lam); l16 = cmul(l16, l16); l16 = cmul(l16, l16); l16 = cmul(l16, l16);
        l64 = cmul(l16, l16); l64 = cmul(l64, l64);
        PIN(coef.r); PIN(coef.i); PIN(l64.r); PIN(l64.i);
      }
      const int trow = 32 * th + 16 * ((r32 >> 2) & 1) + (r32 & 3) + 4 * (r32 >> 3);
      const unsigned xa_off = trow * 128, xa_sw = (trow >> 1) & 7, b_off = 8192 + (32 * chh + r32) * 128;
#define G1(s_) do { bf16x8 xf_[4], rf_[4], if_[4]; \
      _Pragma("unroll") for (int ks = 0; ks < 4; ++ks) { xf_[ks] = ldsf(lds + (s_) + xa_off + (((2 * ks + hi) ^ xa_sw) << 4)); \
        rf_[ks] = ldsf(lds + (s_) + b_off + (((2 * ks + hi) ^ xsw) << 4)); if_[ks] = ldsf(lds + (s_) + b_off + 8192 + (((2 * ks + hi) ^ xsw) << 4)); } \
      SBAR(); _Pragma("unroll") for (int ks = 0; ks < 4; ++ks) { ur = MFMA(xf_[ks], rf_[ks], ur); ui = MFMA(xf_[ks], if_[ks], ui); } PIN(ur); PIN(ui); } while (0)
      STAMP(12);
      VMW(6); lds_barrier(cAB, 4); G1(L_R1);
      lds_wait_<1>(cBB, 4);
      { const u32x2 t2 = *(LAS u32x2*)(lds + L_TAG);
        tag = ((u64)(unsigned)__builtin_amdgcn_readfirstlane(t2[1]) << 32) | (unsigned)__builtin_amdgcn_readfirstlane(t2[0]); }
      __builtin_amdgcn_s_setprio(APRIO);
      STAMP(9);
      LDCH(0); VMW(APACE); LDCH(CH); VMW(APACE); LDCH(2 * CH); VMW(APACE); LDCH(3 * CH);
      STAMP(13);
      VMW(24); lds_barrier(cAB, 8); LDCH(L_R1); G1(L_R1 + CH);
      STAMP(14);
      VMW(18); lds_barrier(cAB, 12); G1(0); G1(CH);
      STAMP(15);
      VMW(6); lds_barrier(cAB, 16); G1(2 * CH); G1(3 * CH);
      VMW(0); lds_barrier(cAB, 20); G1(L_R1);
    }
#pragma unroll
    for (int r = 0; r < 8; ++r) {
      const int q0 = 32 * th + 4 * hi + ((2 * r) & 3) + 8 * (r >> 1), cq = (32 * chh + r32) >> 3;
      const unsigned x0 = *(LAS u16*)(lds + L_R1 + q0 * 128 + ((cq ^ ((q0 >> 1) & 7)) << 4) + (r32 & 7) * 2);
      const unsigned x1 = *(LAS u16*)(lds + L_R1 + (q0 + 1) * 128 + ((cq ^ ((q0 >> 1) & 7)) << 4) + (r32 & 7) * 2);
      xd[r] = pk(bf2f(x0) * pd, bf2f(x1) * pd); }
    STAMP(1);
    cf e = {ur[0], ui[0]};
#pragma unroll
    for (int r = 1; r < 16; ++r) e = cfma(lam, e, (cf){ur[r], ui[r]});
    const int seg = 2 * th + hi;
    *(LAS f32x2*)(lds + L_EX + (seg * 64 + 32 * chh + r32) * 8) = (f32x2){e.r, e.i};
    lds_barrier(cAB, 24);
    cf g0 = {0.f, 0.f}, lseg = {1.f, 0.f};
    {
      cf E = {0.f, 0.f};
#pragma unroll
      for (int s = 0; s < 4; ++s) {
        const f32x2 v = *(LAS f32x2*)(lds + L_EX + (s * 64 + 32 * chh + r32) * 8);
        if (s == seg) g0 = E;
        if (s < seg) lseg = cmul(lseg, l16);
        E = cfma(l16, E, (cf){v[0], v[1]});
      }
      if (th == 0 && hi == 0 && c < 31)
        BST(((u32x4){f2u(E.r), (unsigned)tag, f2u(E.i), (unsigned)tag}), rs_ws, r32 * 16, WS_E + ((2 * j + chh) * 32 + 31 - c) * 512);
    }
    STAMP(2);
    cf G = g0;
    if (c > 0) {
      const cf l128 = cmul(l64, l64);
      const __amdgpu_buffer_rsrc_t re = __builtin_amdgcn_make_buffer_rsrc(ws + WS_E + (2 * j + chh) * 16384, 0, 16384, 0x00020000);
      const unsigned eo = (32 - c + hi) * 512 + r32 * 16, want = (c - hi + 1) >> 1;
      u32x4 ev[16];
#pragma unroll 1
      for (unsigned s = 0; s < SPIN_CAP; ++s) {
        unsigned got = 0;
#pragma unroll
        for (int m = 0; m < 8; ++m) ev[m] = BLD(re, eo, m * 1024);
        if (c > 16) {
#pragma unroll
          for (int m = 8; m < 16; ++m) ev[m] = BLD(re, eo, m * 1024);
          SBAR();
#pragma unroll
          for (int m = 8; m < 16; ++m) got += ((ev[m][1] ^ (unsigned)tag) | (ev[m][3] ^ (unsigned)tag)) == 0;
        }
#pragma unroll
        for (int m = 0; m < 8; ++m) got += ((ev[m][1] ^ (unsigned)tag) | (ev[m][3] ^ (unsigned)tag)) == 0;
        if (wall(got == want)) break;
        __builtin_amdgcn_s_sleep(4);
      }
      STAMP(3);
      cf H = {0.f, 0.f};
      if (c > 16) {
#pragma unroll
        for (int m = 15; m >= 8; --m) H = cfma(l128, H, (cf){u2f(ev[m][0]), u2f(ev[m][2])});
      }
#pragma unroll
      for (int m = 7; m >= 0; --m) H = cfma(l128, H, (cf){u2f(ev[m][0]), u2f(ev[m][2])});
      H = cfma(l64, (cf){swap_hi(H.r), swap_hi(H.i)}, (cf){swap_lo(H.r), swap_lo(H.i)});
      G = cfma(lseg, H, g0);
    }
    STAMP(4);
    {
      const lptr ht = lds + L_HT + wa * 4096;
#pragma unroll
      for (int r = 0; r < 16; ++r) { G = cfma(lam, G, (cf){ur[r], ui[r]}); ur[r] = coef.r * G.r - coef.i * G.i; ui[r] = -(coef.r * G.i + coef.i * G.r); }
      *(LAS bf16x8*)(ht + r32 * 64 + hi * 32) = pack8(ur, 0); *(LAS bf16x8*)(ht + r32 * 64 + hi * 32 + 16) = pack8(ur, 1);
      *(LAS bf16x8*)(ht + 2048 + r32 * 64 + hi * 32) = pack8(ui, 0); *(LAS bf16x8*)(ht + 2048 + r32 * 64 + hi * 32 + 16) = pack8(ui, 1);
      asm volatile("s_waitcnt lgkmcnt(0)" ::: "memory");
#pragma unroll
      for (int k = 0; k < 4; ++k) {
        const bf16x8 f = ldstr2(ht + (k >> 1) * 2048 + (16 * (k & 1) + 8 * (g4 >> 1) + tq) * 64 + (g4 & 1) * 32 + tp * 8, 256);
        BST(U4(f), rs_ws, lo16, WS_H + ((2 * c + th) * 56 + (k >> 1) * 28 + 4 * j + 2 * chh + (k & 1)) * 1024);
      }
      publish4(ws, WS_FH + (c * 8 + j) * 128, tag, lane);
    }
    STAMP(5);
    const CAS Params* kp = KARGS();
    const unsigned cst = wa < 3 ? wa * 28672u : L_R1;
    {
      const u32x4 rc = mkrs((const char*)(th ? kp->cim : kp->cre) + (size_t)j * 57344, 57344);
      const unsigned vh = (lane >> 2) * 896 + (((lane & 3) ^ ((lane >> 4) & 3)) << 4) + 384 + chh * 64, vo = ve ^ 64;
      int n2 = 6 * cl;
#pragma unroll
      for (int n = 0; n < 24; ++n) {
        DMA(rc, (n & 1) ? vo : ve, (n2 & 7) * 7168 + (n2 >> 3) * 128 + chh * 512, cst + n2 * 1024, 0); if (n >= CPACE) VMW(CPACE);
        n2 = n2 == 23 ? 0 : n2 + 1; }
#pragma unroll
      for (int n = 0; n < 4; ++n) DMA(rc, vh, n * 14336, cst, 24576 + n * 1024);
    }
    VMW(0);
    bf16x8 cfr[14][2];
#pragma unroll
    for (int f = 0; f < 14; ++f)
#pragma unroll
      for (int n = 0; n < 2; ++n) { const int row = 32 * (n ^ chh) + r32;
        cfr[f][n] = f < 12 ? ldsf(lds + cst + (f >> 2) * 8192 + row * 128 + (((2 * (f & 3) + hi) ^ xsw) << 4))
                           : ldsf(lds + cst + 24576 + row * 64 + (((2 * (f & 1) + hi) ^ ((r32 >> 2) & 3)) << 4)); }
    lds_signal(cCR);
    STAMP(10);
    f32x16 acc[4] = {zero16, zero16, zero16, zero16};
#pragma unroll
    for (int r = 0; r < 8; ++r) { acc[0][2 * r] = u2f(xd[r] << 16); acc[0][2 * r + 1] = u2f(xd[r] & 0xffff0000u); }
    PIN(acc[0]);
    poll(ws, WS_FH + (c * 8 + umin(lane & 7, 6)) * 128, tag);
    STAMP(6);
    {
      const unsigned hb = WS_H + (2 * c * 56 + 14 * wa) * 1024, h0 = hb + th * 57344, h1 = hb + (th ^ 1) * 57344;
      const unsigned fa = chh * 2048, fb = chh ? 0u - 12288u : 0u;
      u32x4 hf[14][2];
#define LDH(f) do { hf[f][0] = BLD(rs_ws, lo16, h0 + (f) * 1024 + ((f) < 12 ? fa : fb)); hf[f][1] = BLD(rs_ws, lo16, h1 + (f) * 1024 + ((f) < 12 ? fa : fb)); } while (0)
#define MMH(f) do { acc[0] = MFMA(BF(hf[f][0]), cfr[f][0], acc[0]); acc[1] = MFMA(BF(hf[f][0]), cfr[f][1], acc[1]); \
                    acc[2] = MFMA(BF(hf[f][1]), cfr[f][0], acc[2]); acc[3] = MFMA(BF(hf[f][1]), cfr[f][1], acc[3]); \
                    PIN(acc[0]); PIN(acc[1]); PIN(acc[2]); PIN(acc[3]); } while (0)
#pragma unroll
      for (int f = 0; f < 5; ++f) LDH(f);
      SBAR();
      __builtin_amdgcn_s_setprio(GPRIO);
#pragma unroll
      for (int f = 0; f < 9; ++f) { MMH(f); SBAR(); LDH(f + 5); SBAR(); }
      lds_wait(cCR, 4);
      {
        const u32x4 rw = mkrs((const char*)kp->wo + j * 128 + wa * 7168, 401408 - j * 128 - wa * 7168);
        const unsigned vw = lrow * 896 + (((lane & 7) ^ ((lrow & 2) << 1)) << 4);
        int m2 = 4 * cl;
#pragma unroll
        for (int m = 0; m < 14; ++m) { DMA(rw, vw, m2 * 28672, wa * 1024 + m2 * 4096, L_WO); m2 = m2 == 13 ? 0 : m2 + 1; }
        if (wa == 0 && lane < 32) *(LAS u32x4*)(lds + L_WO + 57344 + lo16) = (u32x4){0, 0, 0, 0};
      }
      asm volatile("s_waitcnt vmcnt(14)" ::: "memory");
      SBAR();
#pragma unroll
      for (int f = 9; f < 14; ++f) MMH(f);
    }
    STAMP(11);
    poll(ws, WS_STARTED + ((b & 7) + 8 * ((loc & ~7) + (lane & 7))) * 128, tag);
    VMW(0);
    lds_signal(cWO);
    rs_write(lds + L_G2, wa, lane, acc[1], acc[2], acc[3]);
    lds_barrier(cAB, 28);
    f32x16 y = rs_read(lds + L_G2, wa, lane, acc[0]);
    const __amdgpu_buffer_rsrc_t rs_out = __builtin_amdgcn_make_buffer_rsrc(kp->out, 0, 2048 * 896 * 4, 0x00020000);
    const unsigned oo = ((64 * c + 32 * th + 4 * hi) * 896 + 64 * j + 32 * chh + r32) * 4;
#pragma unroll
    for (int r = 0; r < 16; ++r) __builtin_amdgcn_raw_buffer_store_b32(f2u(y[r]), rs_out, oo, ((r & 3) + 8 * (r >> 2)) * 3584, OAUX);
    STAMP(7);
    STAMP_OUT();
  }
}

extern "C" void kernel_launch(void* const* d_in, const int* in_sizes, int n_in, void* d_out, int out_size, void* d_ws, size_t ws_size,
                              hipStream_t stream) {
  Params p;
  p.xa = (const u16*)d_in[0]; p.lre = (const u16*)d_in[1]; p.lim = (const u16*)d_in[2]; p.bre = (const u16*)d_in[3]; p.bim = (const u16*)d_in[4];
  p.cre = (const u16*)d_in[5]; p.cim = (const u16*)d_in[6]; p.ldt = (const u16*)d_in[7]; p.dsk = (const u16*)d_in[8];
  p.xb = (const u16*)d_in[9]; p.wq = (const u16*)d_in[10]; p.wk = (const u16*)d_in[11]; p.wv = (const u16*)d_in[12]; p.wo = (const u16*)d_in[13];
  p.out = (float*)d_out; p.ws = (char*)d_ws;
  fused_kernel<<<256, 512, 0, stream>>>(p);
}
```

```cpp
#include <hip/hip_runtime.h>
#include <stdint.h>

#define DI __device__ __forceinline__
#define GAS __attribute__((address_space(1)))
#define LAS __attribute__((address_space(3)))
#define CAS __attribute__((address_space(4)))
typedef short bf16x8 __attribute__((ext_vector_type(8)));
typedef short s16x4 __attribute__((ext_vector_type(4)));
typedef float f32x16 __attribute__((ext_vector_type(16)));
typedef float f32x4 __attribute__((ext_vector_type(4)));
typedef float f32x2 __attribute__((ext_vector_type(2)));
typedef unsigned u32x4 __attribute__((ext_vector_type(4)));
typedef unsigned u32x2 __attribute__((ext_vector_type(2)));
typedef __bf16 bf16x2_t __attribute__((ext_vector_type(2)));
typedef unsigned long long u64;
typedef unsigned short u16;
typedef LAS char* lptr;
#define RLX_AGENT __ATOMIC_RELAXED, __HIP_MEMORY_SCOPE_AGENT
#define MFMA(a, b, c) __builtin_amdgcn_mfma_f32_32x32x16_bf16((a), (b), (c), 0, 0, 0)
#define SBAR() __builtin_amdgcn_sched_barrier(0)
#define PIN(x) asm volatile("" : "+v"(x))
#define MEMBAR() asm volatile("" ::: "memory")
#define SC1 16
#define APRIO 1
#define GPRIO 1
#define PSLEEP 2
#define APACE 63
#define OAUX 17
#define CPACE 6

constexpr unsigned WS_EPOCH = 0, WS_FKV = 4096, WS_FH = 8192, WS_E = 0x10000, WS_STARTED = 0x48000,
                   WS_KV = 0x50000, WS_O = 0x100000, WS_H = 0x300000, WS_END = 0x680000, WS_DBG = 0x700000;
constexpr unsigned SK = 15360, K_W = 8192, K_WQ = 107520;
constexpr unsigned CH = 24576;
constexpr unsigned L_QS = 99328, L_R1 = 107520, L_HT = 136192, L_WO = 0, L_OC = 57856, L_G2 = 107520, L_MISC = 159744, L_EX = L_MISC + 128, L_TOTAL = 163840;
constexpr unsigned cBB = L_MISC, cAB = L_MISC + 4, cCR = L_MISC + 8, cWO = L_MISC + 12, cOC = L_MISC + 16, cPB = L_MISC + 20, L_TAG = L_MISC + 64;
constexpr unsigned SPIN_CAP = 1u << 18;

struct Params {
  const u16 *xb, *wq, *wk, *wv; char* ws; const u16 *lre, *lim, *ldt, *dsk, *xa, *bre, *bim, *cre, *cim, *wo;
  float* out;
};

DI float u2f(unsigned v) { return __builtin_bit_cast(float, v); }
DI unsigned f2u(float v) { return __builtin_bit_cast(unsigned, v); }
DI unsigned umin(unsigned a, unsigned b) { return a < b ? a : b; }
DI unsigned umax(unsigned a, unsigned b) { return a > b ? a : b; }
DI bool wall(bool x) { return __builtin_amdgcn_ballot_w64(!x) == 0; }
DI float bf2f(unsigned v) { return u2f(v << 16); }
DI unsigned pk(float a, float b) { f32x2 v = {a, b}; return __builtin_bit_cast(unsigned, __builtin_convertvector(v, bf16x2_t)); }
DI float rbf(float a) { return u2f(pk(0.f, a)); }
DI bf16x8 pack8(const f32x16& x, int s) {
  u32x4 p = {pk(x[8 * s], x[8 * s + 1]), pk(x[8 * s + 2], x[8 * s + 3]), pk(x[8 * s + 4], x[8 * s + 5]), pk(x[8 * s + 6], x[8 * s + 7])};
  return __builtin_bit_cast(bf16x8, p);
}
DI u32x4 mkrs(const void* base, unsigned bytes) { const u64 a = (u64)base; return (u32x4){(unsigned)a, (unsigned)(a >> 32) & 0xffffu, bytes, 0x20000u}; }
#define DMA(rs, voff, soff, ldsb, IMM) asm volatile("s_add_u32 m0, %3, %4\n\ts_nop 0\n\tbuffer_load_dwordx4 %0, %1, %2 offen lds" \
                                                    :: "v"(voff), "s"(rs), "s"(soff), "s"(ldsb), "i"(IMM) : "memory", "scc")
DI bf16x8 ldsf(lptr p) { return *(LAS bf16x8*)p; }
DI s16x4 ldstr(lptr p) { return __builtin_amdgcn_ds_read_tr16_b64_v4i16((LAS s16x4*)p); }
DI bf16x8 ldstr2(lptr p, int second) { s16x4 lo = ldstr(p), hi = ldstr(p + second); return __builtin_shufflevector(lo, hi, 0, 1, 2, 3, 4, 5, 6, 7); }
DI void lds_signal(unsigned c) {
  asm volatile("s_waitcnt lgkmcnt(0)\n\ts_mov_b64 exec, 1\n\tds_add_u32 %0, %1\n\ts_mov_b64 exec, -1" :: "v"(c), "v"(1u) : "memory");
}
template <int SLEEP> DI void lds_wait_(unsigned c, unsigned target) {
  unsigned v, s, n;
  asm volatile("s_mov_b32 %2, 0x400000\n1:\n\tds_read_b32 %0, %3\n\ts_waitcnt lgkmcnt(0)\n\tv_readfirstlane_b32 %1, %0\n\ts_cmp_ge_u32 %1, %4\n\ts_cbranch_scc1 2f\n\t"
               "s_sleep %5\n\ts_sub_u32 %2, %2, 1\n\ts_cbranch_scc0 1b\n2:" : "=&v"(v), "=&s"(s), "=&s"(n) : "v"(c), "i"(target), "i"(SLEEP) : "memory", "scc");
}
#define lds_wait(c, t) lds_wait_<0>(c, t)
#define lds_barrier(c, t) do { lds_signal(c); lds_wait(c, t); } while (0)
DI void poll(const char* ws, unsigned off, u64 tag) {
#pragma unroll 1
  for (unsigned s = 0; s < SPIN_CAP; ++s) {
    u64 v = __hip_atomic_load((GAS u64*)(ws + off), RLX_AGENT);
    if (wall(v == tag)) break;
    __builtin_amdgcn_s_sleep(PSLEEP);
  }
  MEMBAR();
}
DI void publish4(char* ws, unsigned off, u64 tag, int lane) {
  unsigned old;
  asm volatile("s_waitcnt vmcnt(0)\n\ts_mov_b64 exec, 1\n\tds_add_rtn_u32 %0, %1, %2\n\ts_waitcnt lgkmcnt(0)\n\ts_mov_b64 exec, -1" : "=&v"(old) : "v"(cPB), "v"(1u) : "memory");
  if (__builtin_amdgcn_readfirstlane(old) == 3 && lane == 0) __hip_atomic_store((GAS u64*)(ws + off), tag, RLX_AGENT);
}
DI float swap_lo(float x) {
  auto r = __builtin_amdgcn_permlane32_swap(f2u(x), f2u(x), false, false); return u2f(r[0]); }
DI float swap_hi(float x) {
  auto r = __builtin_amdgcn_permlane32_swap(f2u(x), f2u(x), false, false); return u2f(r[1]); }
struct cf { float r, i; };
DI cf cmul(cf a, cf b) { return {a.r * b.r - a.i * b.i, a.r * b.i + a.i * b.r}; }
DI cf cfma(cf a, cf b, cf c) { return {__builtin_fmaf(a.r, b.r, __builtin_fmaf(-a.i, b.i, c.r)), __builtin_fmaf(a.r, b.i, __builtin_fmaf(a.i, b.r, c.i))}; }

#define STAMP(k) do {} while (0)
#define STAMP_OUT() do {} while (0)

struct KV { bf16x8 k0, k1, v0, v1; };

DI void rs_write(lptr base, int w, int lane, const f32x16& t1, const f32x16& t2, const f32x16& t3) {
#define RSW(T, I) _Pragma("unroll") for (int q = 0; q < 4; ++q) *(LAS f32x4*)(base + ((w ^ I) * 3 + I - 1) * 4096 + q * 1024 + lane * 16) = (f32x4){T[4 * q], T[4 * q + 1], T[4 * q + 2], T[4 * q + 3]}
  RSW(t1, 1); RSW(t2, 2); RSW(t3, 3);
}
DI f32x16 rs_read(lptr base, int w, int lane, f32x16 a) {
  f32x4 v[12];
#pragma unroll
  for (int s = 0; s < 12; ++s) v[s] = *(LAS f32x4*)(base + w * 12288 + s * 1024 + lane * 16);
  SBAR();
#pragma unroll
  for (int s = 0; s < 12; ++s) { const int q = s & 3; a[4 * q] += v[s][0]; a[4 * q + 1] += v[s][1]; a[4 * q + 2] += v[s][2]; a[4 * q + 3] += v[s][3]; }
  return a;
}

#define VMW_(n) asm volatile("s_waitcnt vmcnt(" #n ")" ::: "memory")
#define VMW(n) VMW_(n)
#define BAR() asm volatile("s_waitcnt lgkmcnt(0)\n\ts_barrier" ::: "memory")
#define BLD(R, V, S) __builtin_amdgcn_raw_buffer_load_b128(R, V, S, SC1)
#define BST(D, R, V, S) __builtin_amdgcn_raw_buffer_store_b128(D, R, (V) + (S), 0, SC1)
#define BF(x) __builtin_bit_cast(bf16x8, x)
#define U4(x) __builtin_bit_cast(u32x4, x)
#define EX(x) __builtin_amdgcn_exp2f(x)
#define KARGS() ({ const CAS Params* kp_ = (const CAS Params*)__builtin_amdgcn_kernarg_segment_ptr(); asm volatile("" : "+s"(kp_)); kp_; })
#define NEXTK() do { k += step; k = k >= 7 ? k - 7 : k; } while (0)

#if defined(__HIP_DEVICE_COMPILE__)
#define NO_PK __attribute__((target("no-packed-fp32-ops")))
#else
#define NO_PK
#endif
__global__ void __launch_bounds__(512, 2) NO_PK fused_kernel(Params p) {
  __shared__ __attribute__((aligned(128))) char smem[L_TOTAL];
  const lptr lds = (lptr)smem;
  const int tid = threadIdx.x, lane = tid & 63, wave = __builtin_amdgcn_readfirstlane(tid >> 6);
  const int b = blockIdx.x, loc = b >> 3, cl = loc >> 3, c = (b & 7) * 4 + cl, j = loc & 7;
  const int lrow = lane >> 3;
  const unsigned ve = lrow * 896 + (((lane & 7) ^ (lrow >> 1)) << 4);
  const int step = cl + 1, k0 = j == 7 ? 0 : j;
  const bool isB = wave < 4;
  const int wa = wave & 3;
  char* const ws = p.ws;
  STAMP(0);
  unsigned ua = 0, ub = 0, uc = 0, ud = 0;
  {
    const u32x4 rx = mkrs((const char*)p.xb + (size_t)c * 57344 + wave * 7168, 7168);
    const unsigned vx = ve ^ ((wave & 1) << 6), lb = wave * 1024;
    int k = k0;
    if (j == 7) {
      const u32x4 rw = mkrs(isB ? p.wk : p.wv, 25088);
      const unsigned vw = wa * 1024 + lane * 16, lw = K_W + (isB ? 0 : 3584) + wa * 1024; const bool half = wa < 3 || lane < 32;
#pragma unroll
      for (int i = 0; i < 7; ++i) { NEXTK(); DMA(rx, vx, k * 128, lb, i * SK); if (half) DMA(rw, vw, k * 3584, lw, i * SK); }
    } else {
      const int iw = wave * 64 + lane, wrow = (iw * 9363) >> 16;
      const unsigned vwq = wrow * 896 + (iw - wrow * 7) * 16;
      const u32x4 rw = mkrs((const char*)p.wq + j * 112, 401408 - j * 112);
      if (wave < 7) {
#pragma unroll
        for (int i = 0; i < 7; ++i) { NEXTK(); DMA(rx, vx, k * 128, lb, i * SK); DMA(rw, vwq, k * 57344, lb, i * SK + K_W); }
      } else {
#pragma unroll
        for (int i = 0; i < 7; ++i) { NEXTK(); DMA(rx, vx, k * 128, lb, i * SK); }
      }
    }
  }
  STAMP(8);
  if (tid < 16) ((LAS unsigned*)(lds + L_MISC))[tid] = 0;
  const int r32 = lane & 31, hi = lane >> 5;
  const int g4 = lane >> 4, tq = (lane & 15) >> 2, tp = lane & 3;
  const unsigned xsw = (r32 >> 1) & 7, lo16 = lane * 16;
  const int th = wa >> 1, chh = wa & 1;
  u64 tag;
  const __amdgpu_buffer_rsrc_t rs_ws = __builtin_amdgcn_make_buffer_rsrc(ws, 0, WS_END, 0x00020000);
  const f32x16 zero16 = {};
  const unsigned o_own = WS_O + (((2 * c + th) * 16 + 2 * j + chh) * 2) * 1024;
  const unsigned xq_off = (32 * th + r32) * 128;
  const unsigned wq_off = (8 * (g4 >> 1) + tq) * 112 + (28 * chh + 16 * (g4 & 1) + 4 * tp) * 2;
  f32x16 q = zero16;
#define MARK() do { const u32x4 ones = {~0u, ~0u, ~0u, ~0u};       \
    BST(ones, rs_ws, lo16, o_own); BST(ones, rs_ws, lo16 + 1024, o_own); } while (0)
#define LDEPOCH() asm volatile("s_load_dwordx2 %0, %1, 0x0\n\ts_waitcnt lgkmcnt(0)" : "=s"(tag) : "s"(ws) : "memory"); tag = (tag | 1) + 2
#define QRD(B, X, W) _Pragma("unroll") for (int ks = 0; ks < 4; ++ks) { B##x[ks] = ldsf(lds + (X) + xq_off + (((2 * ks + hi) ^ xsw) << 4)); B##w[ks] = ldstr2(lds + (W) + wq_off + ks * 1792, 448); } SBAR()
#define QMM(B) _Pragma("unroll") for (int ks = 0; ks < 4; ++ks) q = MFMA(B##w[ks], B##x[ks], q); PIN(q); SBAR()
  bf16x8 fax[4], faw[4], fbx[4], fbw[4];

  if (j == 7) {
    LDEPOCH();
    VMW(6); BAR();
    if (!isB) {
      const unsigned xk_off = (32 * chh + r32) * 128, wk_off = K_W + th * 3584 + (8 * (g4 >> 1) + tq) * 56 + (16 * (g4 & 1) + 4 * tp) * 2;
      f32x16 a = zero16;
#define KRD(B, i) _Pragma("unroll") for (int ks = 0; ks < 4; ++ks) { B##x[ks] = ldsf(lds + (i) * SK + xk_off + (((2 * ks + hi) ^ xsw) << 4)); B##w[ks] = ldstr2(lds + (i) * SK + wk_off + ks * 896, 224); } SBAR()
#define KMM(B) if (th) { _Pragma("unroll") for (int ks = 0; ks < 4; ++ks) a = MFMA(B##x[ks], B##w[ks], a); } else { _Pragma("unroll") for (int ks = 0; ks < 4; ++ks) a = MFMA(B##w[ks], B##x[ks], a); } PIN(a); SBAR()
      KRD(fa, 0); KRD(fb, 1); KMM(fa); KRD(fa, 2); KMM(fb); KRD(fb, 3); KMM(fa); KMM(fb);
      VMW(0); BAR();
      KRD(fa, 4); KRD(fb, 5); KMM(fa); KRD(fa, 6); KMM(fb); KMM(fa);
      STAMP(1);
      if (th) {
#pragma unroll
        for (int r = 0; r < 16; ++r) a[r] = r32 < 28 ? a[r] : (r32 == 28 ? 1.f : 0.f);
      } else if (hi) { a[12] = 1.f; a[13] = 1.f; a[14] = 1.f; a[15] = 0.f; }
      const unsigned ko = WS_KV + (2 * c + chh) * 4096 + th * 2048;
      BST(U4(pack8(a, 0)), rs_ws, lo16, ko); BST(U4(pack8(a, 1)), rs_ws, lo16 + 1024, ko);
      lds_signal(cCR);
      publish4(ws, WS_FKV + c * 128, tag, lane);
      STAMP(2);
      if (c == 0 && wa == 0) {
        __builtin_amdgcn_s_sleep(127);
#pragma unroll 1
        for (int k = 0; k < 4; ++k) poll(ws, WS_STARTED + (k * 64 + lane) * 128, tag);
        if (lane == 0) __hip_atomic_store((GAS u64*)(ws + WS_EPOCH), tag, RLX_AGENT);
      }
      STAMP_OUT();
      return;
    }
    VMW(0); BAR();
    const int i0 = wa * 128 + lane, r0 = (i0 * 9363) >> 16, r1 = ((i0 + 64) * 9363) >> 16;
    const unsigned vq0 = r0 * 896 + (i0 - r0 * 7) * 16, vq1 = r1 * 896 + (i0 + 64 - r1 * 7) * 16, lb = wave * 2048;
    const u32x4 rw = mkrs((const char*)p.wq + 784, 401408 - 784);
    int k = k0;
#pragma unroll
    for (int i = 0; i < 7; ++i) { NEXTK(); DMA(rw, vq0, k * 57344, lb, K_WQ + i * 7168); if (wave < 3) DMA(rw, vq1, k * 57344, lb, K_WQ + i * 7168 + 1024); }
    MARK();
    VMW(0);
    lds_barrier(cOC, 4);
    QRD(fa, 0, K_WQ); QRD(fb, SK, K_WQ + 7168); QMM(fa); QRD(fa, 2 * SK, K_WQ + 2 * 7168); QMM(fb); QRD(fb, 3 * SK, K_WQ + 3 * 7168); QMM(fa);
    QRD(fa, 4 * SK, K_WQ + 4 * 7168); QMM(fb); QRD(fb, 5 * SK, K_WQ + 5 * 7168); QMM(fa); QRD(fa, 6 * SK, K_WQ + 6 * 7168); QMM(fb); QMM(fa);
    lds_wait(cCR, 4);
    STAMP(1);
  } else {
    if (isB) { MARK(); LDEPOCH(); }
    if (wave < 7) VMW(6); else VMW(3);
    BAR();
    if (isB) { QRD(fa, 0, K_W); QRD(fb, SK, SK + K_W); QMM(fa); QRD(fa, 2 * SK, 2 * SK + K_W); QMM(fb); QRD(fb, 3 * SK, 3 * SK + K_W); QMM(fa); QMM(fb); }
    else { const CAS Params* kp = KARGS(); const unsigned co = (64 * j + 32 * chh + r32) * 2;
      asm volatile("s_nop 4\n\tglobal_load_ushort %0, %4, %5\n\tglobal_load_ushort %1, %4, %6\n\tglobal_load_ushort %2, %4, %7\n\tglobal_load_ushort %3, %4, %8"
                   : "=&v"(ua), "=&v"(ub), "=&v"(uc), "=&v"(ud) : "v"(co), "s"(kp->lre), "s"(kp->lim), "s"(kp->ldt), "s"(kp->dsk) : "memory"); }
    asm volatile("s_waitcnt vmcnt(0) lgkmcnt(0)\n\ts_barrier" : "+v"(ua), "+v"(ub), "+v"(uc), "+v"(ud) :: "memory");
    if (isB) { QRD(fa, 4 * SK, 4 * SK + K_W); QRD(fb, 5 * SK, 5 * SK + K_W); QMM(fa); QRD(fa, 6 * SK, 6 * SK + K_W); QMM(fb); QMM(fa); STAMP(1); }
  }

  if (isB) {
    if (tid == 0) *(LAS u64*)(lds + L_TAG) = tag;
    lds_barrier(cBB, 4);
    if (tid == 0) __hip_atomic_store((GAS u64*)(ws + WS_STARTED + b * 128), tag, RLX_AGENT);
    const float qs = 0.18898223650461361f * 1.4426950408889634f;
#pragma unroll
    for (int r = 0; r < 16; ++r) q[r] *= qs;
    if (hi) { q[12] = 0.f; q[13] = 0.f; q[14] = 0.f; q[15] = 0.f; }
    const bf16x8 qo0 = pack8(q, 0), qo1 = pack8(q, 1);
    *(LAS bf16x8*)(lds + L_QS + wave * 2048 + lo16) = qo0;
    *(LAS bf16x8*)(lds + L_QS + wave * 2048 + 1024 + lo16) = qo1;
    lds_barrier(cBB, 8);
    bf16x8 Q0[4], Q1[4];
    Q0[0] = qo0; Q1[0] = qo1;
#pragma unroll
    for (int i = 1; i < 4; ++i) { Q0[i] = ldsf(lds + L_QS + (wave ^ i) * 2048 + lo16); Q1[i] = ldsf(lds + L_QS + (wave ^ i) * 2048 + 1024 + lo16); }
    STAMP(2);
    poll(ws, WS_FKV + (wave * 8 + (lane & 7)) * 128, tag);
    STAMP(3);
    const __amdgpu_buffer_rsrc_t rs_kv = __builtin_amdgcn_make_buffer_rsrc(ws + WS_KV + wave * 65536, 0, 65536, 0x00020000);
#define LDKV(B, T) do { const unsigned so_ = (T) < 16 ? (((T) + loc) & 15) * 4096 : 65536; \
                        B.k0 = BF(BLD(rs_kv, lo16, so_)); B.k1 = BF(BLD(rs_kv, lo16 + 1024, so_)); \
                        B.v0 = BF(BLD(rs_kv, lo16 + 2048, so_)); B.v1 = BF(BLD(rs_kv, lo16 + 3072, so_)); } while (0)
    KV A0, A1, A2, A3;
    LDKV(A0, 0); LDKV(A1, 1); LDKV(A2, 2);
    SBAR();
    A3.k0 = A3.k1 = A3.v0 = A3.v1 = (bf16x8){};
    f32x16 O[4] = {zero16, zero16, zero16, zero16};
    f32x16 Sa, Sb = zero16; u32x4 Pa, Pb = {};
    Sa = MFMA(A0.k0, Q0[0], zero16); Sa = MFMA(A0.k1, Q1[0], Sa);
    STAMP(9);
#define UNIT(S, Sn, KN, QN, VP, OP, VC, OC) do { \
      Pb[2] = pk(Sn[12], Sn[13]); Pb[3] = pk(Sn[14], Sn[15]); \
      Sn = MFMA(KN.k0, Q0[QN], zero16); S[0] = EX(S[0]); S[1] = EX(S[1]); S[2] = EX(S[2]); S[3] = EX(S[3]); SBAR(); \
      Sn = MFMA(KN.k1, Q1[QN], Sn); Pa[0] = pk(S[0], S[1]); Pa[1] = pk(S[2], S[3]); S[4] = EX(S[4]); S[5] = EX(S[5]); S[6] = EX(S[6]); S[7] = EX(S[7]); SBAR(); \
      OP = MFMA(VP.v1, BF(Pb), OP); Pa[2] = pk(S[4], S[5]); Pa[3] = pk(S[6], S[7]); S[8] = EX(S[8]); S[9] = EX(S[9]); S[10] = EX(S[10]); S[11] = EX(S[11]); SBAR(); \
      OC = MFMA(VC.v0, BF(Pa), OC); Pb[0] = pk(S[8], S[9]); Pb[1] = pk(S[10], S[11]); S[12] = EX(S[12]); S[13] = EX(S[13]); S[14] = EX(S[14]); S[15] = EX(S[15]); SBAR(); \
    } while (0)
#define TILE(CUR, NXT, PRV, T) do { \
      UNIT(Sa, Sb, CUR, 1, PRV, O[3], CUR, O[0]); \
      LDKV(PRV, (T) + 3); SBAR(); \
      UNIT(Sb, Sa, CUR, 2, CUR, O[0], CUR, O[1]); \
      UNIT(Sa, Sb, CUR, 3, CUR, O[1], CUR, O[2]); \
      UNIT(Sb, Sa, NXT, 0, CUR, O[2], CUR, O[3]); } while (0)
#pragma unroll 1
    for (int t = 0; t < 16; t += 4) {
      TILE(A0, A1, A3, t); TILE(A1, A2, A0, t + 1); TILE(A2, A3, A1, t + 2); TILE(A3, A0, A2, t + 3);
    }
    Pb[2] = pk(Sb[12], Sb[13]); Pb[3] = pk(Sb[14], Sb[15]);
    O[3] = MFMA(A3.v1, BF(Pb), O[3]);
    PIN(O[3]);
    STAMP(4);
    lds_wait(cCR, 4);
    rs_write(lds + L_OC, wave, lane, O[1], O[2], O[3]);
    lds_barrier(cBB, 12);
    STAMP(10);
    f32x16 o = rs_read(lds + L_OC, wave, lane, O[0]);
    {
      float l = swap_hi(o[12]);
      float am = 0.f;
#pragma unroll
      for (int r = 0; r < 16; ++r) am = __builtin_fmaxf(am, __builtin_fabsf(o[r]));
      const bool ok = (l >= 0x1p-64f) && (l <= 0x1p64f) && (am <= 0x1p120f);
      if (!wall(ok)) {
        const __amdgpu_buffer_rsrc_t rk = __builtin_amdgcn_make_buffer_rsrc(ws + WS_KV, 0, 262144, 0x00020000);
        poll(ws, WS_FKV + r32 * 128, tag);
        float m = -__builtin_inff();
#pragma unroll 1
        for (int t = 0; t < 64; ++t) {
          f32x16 s = MFMA(BF(BLD(rk, lo16, t * 4096)), qo0, zero16); s = MFMA(BF(BLD(rk, lo16 + 1024, t * 4096)), qo1, s);
#pragma unroll
          for (int r = 0; r < 16; ++r) m = __builtin_fmaxf(m, s[r]);
        }
        m = __builtin_fmaxf(swap_lo(m), swap_hi(m));
        o = zero16;
#pragma unroll 1
        for (int t = 0; t < 64; ++t) {
          f32x16 s = MFMA(BF(BLD(rk, lo16, t * 4096)), qo0, zero16); s = MFMA(BF(BLD(rk, lo16 + 1024, t * 4096)), qo1, s);
#pragma unroll
          for (int r = 0; r < 16; ++r) s[r] = EX(s[r] - m);
          o = MFMA(BF(BLD(rk, lo16 + 2048, t * 4096)), pack8(s, 0), o); o = MFMA(BF(BLD(rk, lo16 + 3072, t * 4096)), pack8(s, 1), o);
        }
        l = swap_hi(o[12]);
      }
      const float inv = 1.f / l;
#pragma unroll
      for (int r = 0; r < 16; ++r) o[r] *= inv;
      if (hi) { o[12] = 0.f; o[13] = 0.f; o[14] = 0.f; o[15] = 0.f; }
      u32x4 f0 = U4(pack8(o, 0)), f1 = U4(pack8(o, 1));
#pragma unroll
      for (int k = 0; k < 4; ++k) { f0[k] = umin(f0[k], 0xfffffffeu); f1[k] = umin(f1[k], 0xfffffffeu); }
      BST(f0, rs_ws, lo16, o_own); BST(f1, rs_ws, lo16 + 1024, o_own);
    }
    lds_signal(cOC);
    STAMP(5);
    if (j == 7) { STAMP_OUT(); return; }
    {
      const CAS Params* kp = KARGS();
      const __amdgpu_buffer_rsrc_t rs_out = __builtin_amdgcn_make_buffer_rsrc(kp->out, 0, 2048 * 896 * 4, 0x00020000);
      lds_wait(cWO, 4);
      u32x4 of[16];
      const unsigned ob = WS_O + (((2 * c + th) * 16 + 8 * chh) * 2) * 1024;
#pragma unroll 1
      for (unsigned s = 0; s < SPIN_CAP; ++s) {
        unsigned mx = 0;
#pragma unroll
        for (int k = 0; k < 16; ++k) of[k] = BLD(rs_ws, lo16 + (k & 3) * 1024, ob + (k >> 2) * 4096);
        SBAR();
#pragma unroll
        for (int k = 0; k < 16; ++k) mx = umax(umax(mx, umax(of[k][0], of[k][1])), umax(of[k][2], of[k][3]));
        if (wall(mx != ~0u)) break;
        __builtin_amdgcn_s_sleep(2);
      }
      STAMP(6);
      f32x16 acc0 = zero16, acc1 = zero16;
      const int rowb = 224 * chh + 4 * (g4 >> 1) + tq;
      const unsigned wb = (L_WO + rowb * 128 + ((32 * (g4 & 1) + 8 * tp) ^ ((tq & 2) << 5))) ^ (chh << 6);
      bf16x8 w0[2][4], w1[2][4];
#define WRD(kk) _Pragma("unroll") for (int k4 = 0; k4 < 4; ++k4) { const int k = 4 * (kk) + k4; const unsigned a_ = (28 * (k >> 1) + 16 * (k & 1)) * 128; \
          w0[(kk) & 1][k4] = ldstr2(lds + wb + a_, 1024); w1[(kk) & 1][k4] = ldstr2(lds + (wb ^ 64) + a_, 1024); } SBAR()
#define WMM(kk) _Pragma("unroll") for (int k4 = 0; k4 < 4; ++k4) { acc1 = MFMA(BF(of[4 * (kk) + k4]), w1[(kk) & 1][k4], acc1); acc0 = MFMA(BF(of[4 * (kk) + k4]), w0[(kk) & 1][k4], acc0); } PIN(acc0); PIN(acc1); SBAR()
      WRD(0); WRD(1); WMM(0); WRD(2); WMM(1); WRD(3); WMM(2); WMM(3);
      STAMP(11);
      lds_wait(cOC, 4);
#pragma unroll
      for (int k = 0; k < 4; ++k) *(LAS f32x4*)(lds + L_OC + (wave ^ 1) * 4096 + k * 1024 + lo16) = (f32x4){acc1[4 * k], acc1[4 * k + 1], acc1[4 * k + 2], acc1[4 * k + 3]};
      lds_barrier(cBB, 16);
#pragma unroll
      for (int k = 0; k < 4; ++k) { const f32x4 v = *(LAS f32x4*)(lds + L_OC + wave * 4096 + k * 1024 + lo16);
        acc0[4 * k] += v[0]; acc0[4 * k + 1] += v[1]; acc0[4 * k + 2] += v[2]; acc0[4 * k + 3] += v[3]; }
      STAMP(12);
      const unsigned oo = ((64 * c + 32 * th + 4 * hi) * 896 + 448 + 64 * j + 32 * chh + r32) * 4;
#pragma unroll
      for (int r = 0; r < 16; ++r) __builtin_amdgcn_raw_buffer_store_b32(pk(0.f, acc0[r]), rs_out, oo, ((r & 3) + 8 * (r >> 2)) * 3584, OAUX);
    }
    STAMP(7);
    STAMP_OUT();
  } else {
    cf lam, coef, l16, l64; f32x16 ur = zero16, ui = zero16; unsigned xd[8];
    const float pd = bf2f(ud);
    {
      const CAS Params* kp = KARGS();
      const u32x4 rxa = mkrs((const char*)kp->xa + (size_t)c * 57344 + wa * 14336, 14336);
      const u32x4 rbr = mkrs((const char*)kp->bre + (size_t)j * 57344 + wa * 14336, 14336);
      const u32x4 rbi = mkrs((const char*)kp->bim + (size_t)j * 57344 + wa * 14336, 14336);
      const unsigned vo = (ve ^ 64) + 7168, lb = wa * 2048;
      int k = k0;
#define LDCH(slot) do { NEXTK(); const unsigned k_ = k * 128; DMA(rxa, ve, k_, lb, (slot)); DMA(rxa, vo, k_, lb, (slot) + 1024); DMA(rbr, ve, k_, lb, (slot) + 8192); \
      DMA(rbr, vo, k_, lb, (slot) + 9216); DMA(rbi, ve, k_, lb, (slot) + 16384); DMA(rbi, vo, k_, lb, (slot) + 17408); } while (0)
      LDCH(L_R1); LDCH(L_R1 + CH);
      {
        const float pa = bf2f(ua), pb = bf2f(ub), dt = rbf(expf(bf2f(uc)));
        const float er = expf(dt * pa); float sn, cs; sincosf(dt * pb, &sn, &cs);
        lam = {er * cs, er * sn};
        const float nr = lam.r - 1.f, ni = lam.i, den = pa * pa + pb * pb;
        coef = {2.f * (nr * pa + ni * pb) / den, 2.f * (ni * pa - nr * pb) / den};
        l16 = cmul(lam, lam); l16 = cmul(l16, l16); l16 = cmul(l16, l16); l16 = cmul(l16, l16);
        l64 = cmul(l16, l16); l64 = cmul(l64, l64);
        PIN(coef.r); PIN(coef.i); PIN(l64.r); PIN(l64.i);
      }
      const int trow = 32 * th + 16 * ((r32 >> 2) & 1) + (r32 & 3) + 4 * (r32 >> 3);
      const unsigned xa_off = trow * 128, xa_sw = (trow >> 1) & 7, b_off = 8192 + (32 * chh + r32) * 128;
#define G1(s_) do { bf16x8 xf_[4], rf_[4], if_[4]; \
      _Pragma("unroll") for (int ks = 0; ks < 4; ++ks) { xf_[ks] = ldsf(lds + (s_) + xa_off + (((2 * ks + hi) ^ xa_sw) << 4)); \
        rf_[ks] = ldsf(lds + (s_) + b_off + (((2 * ks + hi) ^ xsw) << 4)); if_[ks] = ldsf(lds + (s_) + b_off + 8192 + (((2 * ks + hi) ^ xsw) << 4)); } \
      SBAR(); _Pragma("unroll") for (int ks = 0; ks < 4; ++ks) { ur = MFMA(xf_[ks], rf_[ks], ur); ui = MFMA(xf_[ks], if_[ks], ui); } PIN(ur); PIN(ui); } while (0)
      STAMP(12);
      VMW(6); lds_barrier(cAB, 4); G1(L_R1);
      lds_wait_<1>(cBB, 4);
      { const u32x2 t2 = *(LAS u32x2*)(lds + L_TAG);
        tag = ((u64)(unsigned)__builtin_amdgcn_readfirstlane(t2[1]) << 32) | (unsigned)__builtin_amdgcn_readfirstlane(t2[0]); }
      __builtin_amdgcn_s_setprio(APRIO);
      STAMP(9);
      LDCH(0); VMW(APACE); LDCH(CH); VMW(APACE); LDCH(2 * CH); VMW(APACE); LDCH(3 * CH);
      STAMP(13);
      VMW(24); lds_barrier(cAB, 8); LDCH(L_R1); G1(L_R1 + CH);
      STAMP(14);
      VMW(18); lds_barrier(cAB, 12); G1(0); G1(CH);
      STAMP(15);
      VMW(6); lds_barrier(cAB, 16); G1(2 * CH); G1(3 * CH);
      VMW(0); lds_barrier(cAB, 20); G1(L_R1);
    }
#pragma unroll
    for (int r = 0; r < 8; ++r) {
      const int q0 = 32 * th + 4 * hi + ((2 * r) & 3) + 8 * (r >> 1), cq = (32 * chh + r32) >> 3;
      const unsigned x0 = *(LAS u16*)(lds + L_R1 + q0 * 128 + ((cq ^ ((q0 >> 1) & 7)) << 4) + (r32 & 7) * 2);
      const unsigned x1 = *(LAS u16*)(lds + L_R1 + (q0 + 1) * 128 + ((cq ^ ((q0 >> 1) & 7)) << 4) + (r32 & 7) * 2);
      xd[r] = pk(bf2f(x0) * pd, bf2f(x1) * pd); }
    STAMP(1);
    cf e = {ur[0], ui[0]};
#pragma unroll
    for (int r = 1; r < 16; ++r) e = cfma(lam, e, (cf){ur[r], ui[r]});
    const int seg = 2 * th + hi;
    *(LAS f32x2*)(lds + L_EX + (seg * 64 + 32 * chh + r32) * 8) = (f32x2){e.r, e.i};
    lds_barrier(cAB, 24);
    cf g0 = {0.f, 0.f}, lseg = {1.f, 0.f};
    {
      cf E = {0.f, 0.f};
#pragma unroll
      for (int s = 0; s < 4; ++s) {
        const f32x2 v = *(LAS f32x2*)(lds + L_EX + (s * 64 + 32 * chh + r32) * 8);
        if (s == seg) g0 = E;
        if (s < seg) lseg = cmul(lseg, l16);
        E = cfma(l16, E, (cf){v[0], v[1]});
      }
      if (th == 0 && hi == 0 && c < 31)
        BST(((u32x4){f2u(E.r), (unsigned)tag, f2u(E.i), (unsigned)tag}), rs_ws, r32 * 16, WS_E + ((2 * j + chh) * 32 + 31 - c) * 512);
    }
    STAMP(2);
    cf G = g0;
    if (c > 0) {
      const cf l128 = cmul(l64, l64);
      const __amdgpu_buffer_rsrc_t re = __builtin_amdgcn_make_buffer_rsrc(ws + WS_E + (2 * j + chh) * 16384, 0, 16384, 0x00020000);
      const unsigned eo = (32 - c + hi) * 512 + r32 * 16, want = (c - hi + 1) >> 1;
      u32x4 ev[16];
#pragma unroll 1
      for (unsigned s = 0; s < SPIN_CAP; ++s) {
        unsigned got = 0;
#pragma unroll
        for (int m = 0; m < 8; ++m) ev[m] = BLD(re, eo, m * 1024);
        if (c > 16) {
#pragma unroll
          for (int m = 8; m < 16; ++m) ev[m] = BLD(re, eo, m * 1024);
          SBAR();
#pragma unroll
          for (int m = 8; m < 16; ++m) got += ((ev[m][1] ^ (unsigned)tag) | (ev[m][3] ^ (unsigned)tag)) == 0;
        }
#pragma unroll
        for (int m = 0; m < 8; ++m) got += ((ev[m][1] ^ (unsigned)tag) | (ev[m][3] ^ (unsigned)tag)) == 0;
        if (wall(got == want)) break;
        __builtin_amdgcn_s_sleep(4);
      }
      STAMP(3);
      cf H = {0.f, 0.f};
      if (c > 16) {
#pragma unroll
        for (int m = 15; m >= 8; --m) H = cfma(l128, H, (cf){u2f(ev[m][0]), u2f(ev[m][2])});
      }
#pragma unroll
      for (int m = 7; m >= 0; --m) H = cfma(l128, H, (cf){u2f(ev[m][0]), u2f(ev[m][2])});
      H = cfma(l64, (cf){swap_hi(H.r), swap_hi(H.i)}, (cf){swap_lo(H.r), swap_lo(H.i)});
      G = cfma(lseg, H, g0);
    }
    STAMP(4);
    {
      const lptr ht = lds + L_HT + wa * 4096;
#pragma unroll
      for (int r = 0; r < 16; ++r) { G = cfma(lam, G, (cf){ur[r], ui[r]}); ur[r] = coef.r * G.r - coef.i * G.i; ui[r] = -(coef.r * G.i + coef.i * G.r); }
      *(LAS bf16x8*)(ht + r32 * 64 + hi * 32) = pack8(ur, 0); *(LAS bf16x8*)(ht + r32 * 64 + hi * 32 + 16) = pack8(ur, 1);
      *(LAS bf16x8*)(ht + 2048 + r32 * 64 + hi * 32) = pack8(ui, 0); *(LAS bf16x8*)(ht + 2048 + r32 * 64 + hi * 32 + 16) = pack8(ui, 1);
      asm volatile("s_waitcnt lgkmcnt(0)" ::: "memory");
#pragma unroll
      for (int k = 0; k < 4; ++k) {
        const bf16x8 f = ldstr2(ht + (k >> 1) * 2048 + (16 * (k & 1) + 8 * (g4 >> 1) + tq) * 64 + (g4 & 1) * 32 + tp * 8, 256);
        BST(U4(f), rs_ws, lo16, WS_H + ((2 * c + th) * 56 + (k >> 1) * 28 + 4 * j + 2 * chh + (k & 1)) * 1024);
      }
      publish4(ws, WS_FH + (c * 8 + j) * 128, tag, lane);
    }
    STAMP(5);
    const CAS Params* kp = KARGS();
    const unsigned cst = wa < 3 ? wa * 28672u : L_R1;
    {
      const u32x4 rc = mkrs((const char*)(th ? kp->cim : kp->cre) + (size_t)j * 57344, 57344);
      const unsigned vh = (lane >> 2) * 896 + (((lane & 3) ^ ((lane >> 4) & 3)) << 4) + 384 + chh * 64, vo = ve ^ 64;
      int n2 = 6 * cl;
#pragma unroll
      for (int n = 0; n < 24; ++n) {
        DMA(rc, (n & 1) ? vo : ve, (n2 & 7) * 7168 + (n2 >> 3) * 128 + chh * 512, cst + n2 * 1024, 0); if (n >= CPACE) VMW(CPACE);
        n2 = n2 == 23 ? 0 : n2 + 1; }
#pragma unroll
      for (int n = 0; n < 4; ++n) DMA(rc, vh, n * 14336, cst, 24576 + n * 1024);
    }
    VMW(0);
    bf16x8 cfr[14][2];
#pragma unroll
    for (int f = 0; f < 14; ++f)
#pragma unroll
      for (int n = 0; n < 2; ++n) { const int row = 32 * (n ^ chh) + r32;
        cfr[f][n] = f < 12 ? ldsf(lds + cst + (f >> 2) * 8192 + row * 128 + (((2 * (f & 3) + hi) ^ xsw) << 4))
                           : ldsf(lds + cst + 24576 + row * 64 + (((2 * (f & 1) + hi) ^ ((r32 >> 2) & 3)) << 4)); }
    lds_signal(cCR);
    STAMP(10);
    f32x16 acc[4] = {zero16, zero16, zero16, zero16};
#pragma unroll
    for (int r = 0; r < 8; ++r) { acc[0][2 * r] = u2f(xd[r] << 16); acc[0][2 * r + 1] = u2f(xd[r] & 0xffff0000u); }
    PIN(acc[0]);
    poll(ws, WS_FH + (c * 8 + umin(lane & 7, 6)) * 128, tag);
    STAMP(6);
    {
      const unsigned hb = WS_H + (2 * c * 56 + 14 * wa) * 1024, h0 = hb + th * 57344, h1 = hb + (th ^ 1) * 57344;
      const unsigned fa = chh * 2048, fb = chh ? 0u - 12288u : 0u;
      u32x4 hf[14][2];
#define LDH(f) do { hf[f][0] = BLD(rs_ws, lo16, h0 + (f) * 1024 + ((f) < 12 ? fa : fb)); hf[f][1] = BLD(rs_ws, lo16, h1 + (f) * 1024 + ((f) < 12 ? fa : fb)); } while (0)
#define MMH(f) do { acc[0] = MFMA(BF(hf[f][0]), cfr[f][0], acc[0]); acc[1] = MFMA(BF(hf[f][0]), cfr[f][1], acc[1]); \
                    acc[2] = MFMA(BF(hf[f][1]), cfr[f][0], acc[2]); acc[3] = MFMA(BF(hf[f][1]), cfr[f][1], acc[3]); \
                    PIN(acc[0]); PIN(acc[1]); PIN(acc[2]); PIN(acc[3]); } while (0)
#pragma unroll
      for (int f = 0; f < 5; ++f) LDH(f);
      SBAR();
      __builtin_amdgcn_s_setprio(GPRIO);
#pragma unroll
      for (int f = 0; f < 9; ++f) { MMH(f); SBAR(); LDH(f + 5); SBAR(); }
      lds_wait(cCR, 4);
      {
        const u32x4 rw = mkrs((const char*)kp->wo + j * 128 + wa * 7168, 401408 - j * 128 - wa * 7168);
        const unsigned vw = lrow * 896 + (((lane & 7) ^ ((lrow & 2) << 1)) << 4);
        int m2 = 4 * cl;
#pragma unroll
        for (int m = 0; m < 14; ++m) { DMA(rw, vw, m2 * 28672, wa * 1024 + m2 * 4096, L_WO); m2 = m2 == 13 ? 0 : m2 + 1; }
        if (wa == 0 && lane < 32) *(LAS u32x4*)(lds + L_WO + 57344 + lo16) = (u32x4){0, 0, 0, 0};
      }
      asm volatile("s_waitcnt vmcnt(14)" ::: "memory");
      SBAR();
#pragma unroll
      for (int f = 9; f < 14; ++f) MMH(f);
    }
    STAMP(11);
    poll(ws, WS_STARTED + ((b & 7) + 8 * ((loc & ~7) + (lane & 7))) * 128, tag);
    VMW(0);
    lds_signal(cWO);
    rs_write(lds + L_G2, wa, lane, acc[1], acc[2], acc[3]);
    lds_barrier(cAB, 28);
    f32x16 y = rs_read(lds + L_G2, wa, lane, acc[0]);
    const __amdgpu_buffer_rsrc_t rs_out = __builtin_amdgcn_make_buffer_rsrc(kp->out, 0, 2048 * 896 * 4, 0x00020000);
    const unsigned oo = ((64 * c + 32 * th + 4 * hi) * 896 + 64 * j + 32 * chh + r32) * 4;
#pragma unroll
    for (int r = 0; r < 16; ++r) __builtin_amdgcn_raw_buffer_store_b32(f2u(y[r]), rs_out, oo, ((r & 3) + 8 * (r >> 2)) * 3584, OAUX);
    STAMP(7);
    STAMP_OUT();
  }
}

__global__ void hold_kernel(int ticks) { const unsigned long long t0 = __builtin_amdgcn_s_memrealtime(); while (__builtin_amdgcn_s_memrealtime() - t0 < (unsigned)ticks) __builtin_amdgcn_s_sleep(1); }
__global__ void __launch_bounds__(512, 2) hold_big(int ticks) { __shared__ char big[163840]; const unsigned long long t0 = __builtin_amdgcn_s_memrealtime(); while (__builtin_amdgcn_s_memrealtime() - t0 < (unsigned)ticks) __builtin_amdgcn_s_sleep(1); if (ticks == 12345) ((volatile char*)big)[threadIdx.x] = 1; }
extern "C" void kernel_launch(void* const* d_in, const int* in_sizes, int n_in, void* d_out, int out_size, void* d_ws, size_t ws_size,
                              hipStream_t stream) {
  Params p;
  p.xa = (const u16*)d_in[0]; p.lre = (const u16*)d_in[1]; p.lim = (const u16*)d_in[2]; p.bre = (const u16*)d_in[3]; p.bim = (const u16*)d_in[4];
  p.cre = (const u16*)d_in[5]; p.cim = (const u16*)d_in[6]; p.ldt = (const u16*)d_in[7]; p.dsk = (const u16*)d_in[8];
  p.xb = (const u16*)d_in[9]; p.wq = (const u16*)d_in[10]; p.wk = (const u16*)d_in[11]; p.wv = (const u16*)d_in[12]; p.wo = (const u16*)d_in[13];
  p.out = (float*)d_out; p.ws = (char*)d_ws;
  hold_kernel<<<1, 64, 0, stream>>>(0);
  fused_kernel<<<256, 512, 0, stream>>>(p);
}
```

```cpp
#include <hip/hip_runtime.h>
#include <stdint.h>

#define DI __device__ __forceinline__
#define GAS __attribute__((address_space(1)))
#define LAS __attribute__((address_space(3)))
#define CAS __attribute__((address_space(4)))
typedef short bf16x8 __attribute__((ext_vector_type(8)));
typedef short s16x4 __attribute__((ext_vector_type(4)));
typedef float f32x16 __attribute__((ext_vector_type(16)));
typedef float f32x4 __attribute__((ext_vector_type(4)));
typedef float f32x2 __attribute__((ext_vector_type(2)));
typedef unsigned u32x4 __attribute__((ext_vector_type(4)));
typedef unsigned u32x2 __attribute__((ext_vector_type(2)));
typedef __bf16 bf16x2_t __attribute__((ext_vector_type(2)));
typedef unsigned long long u64;
typedef unsigned short u16;
typedef LAS char* lptr;
#define RLX_AGENT __ATOMIC_RELAXED, __HIP_MEMORY_SCOPE_AGENT
#define MFMA(a, b, c) __builtin_amdgcn_mfma_f32_32x32x16_bf16((a), (b), (c), 0, 0, 0)
#define SBAR() __builtin_amdgcn_sched_barrier(0)
#define PIN(x) asm volatile("" : "+v"(x))
#define MEMBAR() asm volatile("" ::: "memory")
#define SC1 16
#define APRIO 1
#define BPRIO 0
#define ASLEEP 1
#define EPRIO 1
#define KILLA 0
#define GOSLEEP 4
#define PRET 0
#define OSLEEP 2
#define HPRE 14
#define HPRIO 1
#define P2PRIO 1
#define GPRIO 1
#define PSLEEP 2
#define APACE 63
#define OAUX 17
#define CPACE 63

constexpr unsigned WS_EPOCH = 0, WS_FKV = 4096, WS_FH = 8192, WS_E = 0x10000, WS_STARTED = 0x48000,
                   WS_KV = 0x50000, WS_O = 0x100000, WS_H = 0x300000, WS_END = 0x680000, WS_DBG = 0x700000;
constexpr unsigned SK = 15360, K_W = 8192, K_WQ = 107520;
constexpr unsigned CH = 24576;
constexpr unsigned L_QS = 99328, L_R1 = 107520, L_HT = 65536, L_WO = 0, L_OC = 57856, L_G2 = 107520, L_MISC = 159744, L_EX = L_MISC + 128, L_TOTAL = 163840;
constexpr unsigned cBB = L_MISC, cAB = L_MISC + 4, cCR = L_MISC + 8, cWO = L_MISC + 12, cOC = L_MISC + 16, cPB = L_MISC + 20, cPR = L_MISC + 24, cP4 = L_MISC + 32, cGO = L_MISC + 36, L_TAG = L_MISC + 64;
constexpr unsigned SPIN_CAP = 1u << 18;

struct Params {
  const u16 *xb, *wq, *wk, *wv; char* ws; const u16 *lre, *lim, *ldt, *dsk, *xa, *bre, *bim, *cre, *cim, *wo;
  float* out;
};

DI float u2f(unsigned v) { return __builtin_bit_cast(float, v); }
DI unsigned f2u(float v) { return __builtin_bit_cast(unsigned, v); }
DI unsigned umin(unsigned a, unsigned b) { return a < b ? a : b; }
DI unsigned umax(unsigned a, unsigned b) { return a > b ? a : b; }
DI bool wall(bool x) { return __builtin_amdgcn_ballot_w64(!x) == 0; }
DI float bf2f(unsigned v) { return u2f(v << 16); }
DI unsigned pk(float a, float b) { f32x2 v = {a, b}; return __builtin_bit_cast(unsigned, __builtin_convertvector(v, bf16x2_t)); }
DI float rbf(float a) { return u2f(pk(0.f, a)); }
DI bf16x8 pack8(const f32x16& x, int s) {
  u32x4 p = {pk(x[8 * s], x[8 * s + 1]), pk(x[8 * s + 2], x[8 * s + 3]), pk(x[8 * s + 4], x[8 * s + 5]), pk(x[8 * s + 6], x[8 * s + 7])};
  return __builtin_bit_cast(bf16x8, p);
}
DI u32x4 mkrs(const void* base, unsigned bytes) { const u64 a = (u64)base; return (u32x4){(unsigned)a, (unsigned)(a >> 32) & 0xffffu, bytes, 0x20000u}; }
#define DMA(rs, voff, soff, ldsb, IMM) asm volatile("s_add_u32 m0, %3, %4\n\ts_nop 0\n\tbuffer_load_dwordx4 %0, %1, %2 offen lds" \
                                                    :: "v"(voff), "s"(rs), "s"(soff), "s"(ldsb), "i"(IMM) : "memory", "scc")
DI bf16x8 ldsf(lptr p) { return *(LAS bf16x8*)p; }
DI s16x4 ldstr(lptr p) { return __builtin_amdgcn_ds_read_tr16_b64_v4i16((LAS s16x4*)p); }
DI bf16x8 ldstr2(lptr p, int second) { s16x4 lo = ldstr(p), hi = ldstr(p + second); return __builtin_shufflevector(lo, hi, 0, 1, 2, 3, 4, 5, 6, 7); }
DI void lds_signal(unsigned c) {
  asm volatile("s_waitcnt lgkmcnt(0)\n\ts_mov_b64 exec, 1\n\tds_add_u32 %0, %1\n\ts_mov_b64 exec, -1" :: "v"(c), "v"(1u) : "memory");
}
template <int SLEEP> DI void lds_wait_(unsigned c, unsigned target) {
  unsigned v, s, n;
  asm volatile("s_mov_b32 %2, 0x400000\n1:\n\tds_read_b32 %0, %3\n\ts_waitcnt lgkmcnt(0)\n\tv_readfirstlane_b32 %1, %0\n\ts_cmp_ge_u32 %1, %4\n\ts_cbranch_scc1 2f\n\t"
               "s_sleep %5\n\ts_sub_u32 %2, %2, 1\n\ts_cbranch_scc0 1b\n2:" : "=&v"(v), "=&s"(s), "=&s"(n) : "v"(c), "i"(target), "i"(SLEEP) : "memory", "scc");
}
#define lds_wait(c, t) lds_wait_<0>(c, t)
#define lds_barrier(c, t) do { lds_signal(c); lds_wait(c, t); } while (0)
#define lds_barrierA(c, t) do { lds_signal(c); lds_wait_<ASLEEP>(c, t); } while (0)
DI void poll(const char* ws, unsigned off, u64 tag) {
#pragma unroll 1
  for (unsigned s = 0; s < SPIN_CAP; ++s) {
    u64 v = __hip_atomic_load((GAS u64*)(ws + off), RLX_AGENT);
    if (wall(v == tag)) break;
    __builtin_amdgcn_s_sleep(PSLEEP);
  }
  MEMBAR();
}
DI void publish4(char* ws, unsigned off, u64 tag, int lane) {
  unsigned old;
  asm volatile("s_waitcnt vmcnt(0)\n\ts_mov_b64 exec, 1\n\tds_add_rtn_u32 %0, %1, %2\n\ts_waitcnt lgkmcnt(0)\n\ts_mov_b64 exec, -1" : "=&v"(old) : "v"(cPB), "v"(1u) : "memory");
  if (__builtin_amdgcn_readfirstlane(old) == 3 && lane == 0) __hip_atomic_store((GAS u64*)(ws + off), tag, RLX_AGENT);
}
DI float swap_lo(float x) {
  auto r = __builtin_amdgcn_permlane32_swap(f2u(x), f2u(x), false, false); return u2f(r[0]); }
DI float swap_hi(float x) {
  auto r = __builtin_amdgcn_permlane32_swap(f2u(x), f2u(x), false, false); return u2f(r[1]); }
struct cf { float r, i; };
DI cf cmul(cf a, cf b) { return {a.r * b.r - a.i * b.i, a.r * b.i + a.i * b.r}; }
DI cf cfma(cf a, cf b, cf c) { return {__builtin_fmaf(a.r, b.r, __builtin_fmaf(-a.i, b.i, c.r)), __builtin_fmaf(a.r, b.i, __builtin_fmaf(a.i, b.r, c.i))}; }

#define STAMP(k) do {} while (0)
#define STAMP_OUT() do {} while (0)

struct KV { bf16x8 k0, k1, v0, v1; };

DI void rs_write(lptr base, int w, int lane, const f32x16& t1, const f32x16& t2, const f32x16& t3) {
#define RSW(T, I) _Pragma("unroll") for (int q = 0; q < 4; ++q) *(LAS f32x4*)(base + ((w ^ I) * 3 + I - 1) * 4096 + q * 1024 + lane * 16) = (f32x4){T[4 * q], T[4 * q + 1], T[4 * q + 2], T[4 * q + 3]}
  RSW(t1, 1); RSW(t2, 2); RSW(t3, 3);
}
DI f32x16 rs_read(lptr base, int w, int lane, f32x16 a) {
  f32x4 v[12];
#pragma unroll
  for (int s = 0; s < 12; ++s) v[s] = *(LAS f32x4*)(base + w * 12288 + s * 1024 + lane * 16);
  SBAR();
#pragma unroll
  for (int s = 0; s < 12; ++s) { const int q = s & 3; a[4 * q] += v[s][0]; a[4 * q + 1] += v[s][1]; a[4 * q + 2] += v[s][2]; a[4 * q + 3] += v[s][3]; }
  return a;
}

#define VMW_(n) asm volatile("s_waitcnt vmcnt(" #n ")" ::: "memory")
#define VMW(n) VMW_(n)
#define BAR() asm volatile("s_waitcnt lgkmcnt(0)\n\ts_barrier" ::: "memory")
#define BLD(R, V, S) __builtin_amdgcn_raw_buffer_load_b128(R, V, S, SC1)
#define BST(D, R, V, S) __builtin_amdgcn_raw_buffer_store_b128(D, R, (V) + (S), 0, SC1)
#define BF(x) __builtin_bit_cast(bf16x8, x)
#define U4(x) __builtin_bit_cast(u32x4, x)
#define EX(x) __builtin_amdgcn_exp2f(x)
#define KARGS() ({ const CAS Params* kp_ = (const CAS Params*)__builtin_amdgcn_kernarg_segment_ptr(); asm volatile("" : "+s"(kp_)); kp_; })
#define NEXTK() do { k += step; k = k >= 7 ? k - 7 : k; } while (0)

#if defined(__HIP_DEVICE_COMPILE__)
#define NO_PK __attribute__((target("no-packed-fp32-ops")))
#else
#define NO_PK
#endif
__global__ void __launch_bounds__(512, 2) NO_PK fused_kernel(Params p) {
  __shared__ __attribute__((aligned(128))) char smem[L_TOTAL];
  const lptr lds = (lptr)smem;
  const int tid = threadIdx.x, lane = tid & 63, wave = __builtin_amdgcn_readfirstlane(tid >> 6);
  const int b = blockIdx.x, loc = b >> 3, cl = loc >> 3, c = (b & 7) * 4 + cl, j = loc & 7;
  const int lrow = lane >> 3;
  const unsigned ve = lrow * 896 + (((lane & 7) ^ (lrow >> 1)) << 4);
  const int step = cl + 1, k0 = j == 7 ? 0 : j;
  const bool isB = wave < 4;
  const int wa = wave & 3;
  char* const ws = p.ws;
  STAMP(0);
  unsigned ua = 0, ub = 0, uc = 0, ud = 0;
  u32x4 rxa, rbr, rbi; int ka = k0;
  const unsigned vo = (ve ^ 64) + 7168, lba = wa * 2048;
#define NEXTKA() do { ka += step; ka = ka >= 7 ? ka - 7 : ka; } while (0)
#define LDCH(slot) do { NEXTKA(); const unsigned k_ = ka * 128; DMA(rxa, ve, k_, lba, (slot)); DMA(rxa, vo, k_, lba, (slot) + 1024); \
      DMA(rbr, ve, k_, lba, (slot) + 8192); DMA(rbr, vo, k_, lba, (slot) + 9216); DMA(rbi, ve, k_, lba, (slot) + 16384); DMA(rbi, vo, k_, lba, (slot) + 17408); } while (0)
  {
    const u32x4 rx = mkrs((const char*)p.xb + (size_t)c * 57344 + wave * 7168, 7168);
    const unsigned vx = ve ^ ((wave & 1) << 6), lb = wave * 1024;
    int k = k0;
    if (j == 7) {
      const u32x4 rw = mkrs(isB ? p.wk : p.wv, 25088);
      const unsigned vw = wa * 1024 + lane * 16, lw = K_W + (isB ? 0 : 3584) + wa * 1024; const bool half = wa < 3 || lane < 32;
#pragma unroll
      for (int i = 0; i < 7; ++i) { NEXTK(); DMA(rx, vx, k * 128, lb, i * SK); if (half) DMA(rw, vw, k * 3584, lw, i * SK); }
    } else {
      const int iw = wave * 64 + lane, wrow = (iw * 9363) >> 16;
      const unsigned vwq = wrow * 896 + (iw - wrow * 7) * 16;
      const u32x4 rw = mkrs((const char*)p.wq + j * 112, 401408 - j * 112);
      if (wave == 0) { const unsigned co = (64 * j + lane) * 2;
        asm volatile("global_load_ushort %0, %4, %5\n\tglobal_load_ushort %1, %4, %6\n\tglobal_load_ushort %2, %4, %7\n\tglobal_load_ushort %3, %4, %8"
                     : "=&v"(ua), "=&v"(ub), "=&v"(uc), "=&v"(ud) : "v"(co), "s"(p.lre), "s"(p.lim), "s"(p.ldt), "s"(p.dsk) : "memory"); }
      if (wave < 7) {
#pragma unroll
        for (int i = 0; i < 7; ++i) { NEXTK(); DMA(rx, vx, k * 128, lb, i * SK); DMA(rw, vwq, k * 57344, lb, i * SK + K_W); }
      } else {
#pragma unroll
        for (int i = 0; i < 7; ++i) { NEXTK(); DMA(rx, vx, k * 128, lb, i * SK); }
      }
      rxa = mkrs((const char*)p.xa + (size_t)c * 57344 + wa * 14336, 14336);
      rbr = mkrs((const char*)p.bre + (size_t)j * 57344 + wa * 14336, 14336);
      rbi = mkrs((const char*)p.bim + (size_t)j * 57344 + wa * 14336, 14336);
    }
  }
  STAMP(8);
  if (tid < 16) ((LAS unsigned*)(lds + L_MISC))[tid] = 0;
  const int r32 = lane & 31, hi = lane >> 5;
  const int g4 = lane >> 4, tq = (lane & 15) >> 2, tp = lane & 3;
  const unsigned xsw = (r32 >> 1) & 7, lo16 = lane * 16;
  const int th = wa >> 1, chh = wa & 1;
  u64 tag;
  const __amdgpu_buffer_rsrc_t rs_ws = __builtin_amdgcn_make_buffer_rsrc(ws, 0, WS_END, 0x00020000);
  const f32x16 zero16 = {};
  const unsigned o_own = WS_O + (((2 * c + th) * 16 + 2 * j + chh) * 2) * 1024;
  const unsigned xq_off = (32 * th + r32) * 128;
  const unsigned wq_off = (8 * (g4 >> 1) + tq) * 112 + (28 * chh + 16 * (g4 & 1) + 4 * tp) * 2;
  f32x16 q = zero16;
#define MARK() do { const u32x4 ones = {~0u, ~0u, ~0u, ~0u};       \
    BST(ones, rs_ws, lo16, o_own); BST(ones, rs_ws, lo16 + 1024, o_own); } while (0)
#define LDEPOCH() asm volatile("s_load_dwordx2 %0, %1, 0x0\n\ts_waitcnt lgkmcnt(0)" : "=s"(tag) : "s"(ws) : "memory"); tag = (tag | 1) + 2
#define QRD(B, X, W) _Pragma("unroll") for (int ks = 0; ks < 4; ++ks) { B##x[ks] = ldsf(lds + (X) + xq_off + (((2 * ks + hi) ^ xsw) << 4)); B##w[ks] = ldstr2(lds + (W) + wq_off + ks * 1792, 448); } SBAR()
#define QMM(B) _Pragma("unroll") for (int ks = 0; ks < 4; ++ks) q = MFMA(B##w[ks], B##x[ks], q); PIN(q); SBAR()
  bf16x8 fax[4], faw[4], fbx[4], fbw[4];

  if (j == 7) {
    if (PRET && !isB) { const u32x4 z = {0, 0, 0, 0}; const unsigned ko = WS_KV + (2 * c + chh) * 4096 + th * 2048;
      BST(z, rs_ws, lo16, ko); BST(z, rs_ws, lo16 + 1024, ko); if (tid == 256) __hip_atomic_store((GAS u64*)(ws + WS_FKV + c * 128), 0ull, RLX_AGENT); }
    LDEPOCH();
    VMW(6); BAR();
    if (!isB) {
      const unsigned xk_off = (32 * chh + r32) * 128, wk_off = K_W + th * 3584 + (8 * (g4 >> 1) + tq) * 56 + (16 * (g4 & 1) + 4 * tp) * 2;
      f32x16 a = zero16;
#define KRD(B, i) _Pragma("unroll") for (int ks = 0; ks < 4; ++ks) { B##x[ks] = ldsf(lds + (i) * SK + xk_off + (((2 * ks + hi) ^ xsw) << 4)); B##w[ks] = ldstr2(lds + (i) * SK + wk_off + ks * 896, 224); } SBAR()
#define KMM(B) if (th) { _Pragma("unroll") for (int ks = 0; ks < 4; ++ks) a = MFMA(B##x[ks], B##w[ks], a); } else { _Pragma("unroll") for (int ks = 0; ks < 4; ++ks) a = MFMA(B##w[ks], B##x[ks], a); } PIN(a); SBAR()
      KRD(fa, 0); KRD(fb, 1); KMM(fa); KRD(fa, 2); KMM(fb); KRD(fb, 3); KMM(fa); KMM(fb);
      VMW(0); BAR();
      KRD(fa, 4); KRD(fb, 5); KMM(fa); KRD(fa, 6); KMM(fb); KMM(fa);
      STAMP(1);
      if (th) {
#pragma unroll
        for (int r = 0; r < 16; ++r) a[r] = r32 < 28 ? a[r] : (r32 == 28 ? 1.f : 0.f);
      } else if (hi) { a[12] = 1.f; a[13] = 1.f; a[14] = 1.f; a[15] = 0.f; }
      const unsigned ko = WS_KV + (2 * c + chh) * 4096 + th * 2048;
      BST(U4(pack8(a, 0)), rs_ws, lo16, ko); BST(U4(pack8(a, 1)), rs_ws, lo16 + 1024, ko);
      lds_signal(cCR);
      publish4(ws, WS_FKV + c * 128, tag, lane);
      STAMP(2);
      if (c == 0 && wa == 0) {
        __builtin_amdgcn_s_sleep(127);
#pragma unroll 1
        for (int k = 0; k < 4; ++k) poll(ws, WS_STARTED + (k * 64 + lane) * 128, tag);
        if (lane == 0) __hip_atomic_store((GAS u64*)(ws + WS_EPOCH), tag, RLX_AGENT);
      }
      STAMP_OUT();
      return;
    }
    VMW(0); BAR();
    const int i0 = wa * 128 + lane, r0 = (i0 * 9363) >> 16, r1 = ((i0 + 64) * 9363) >> 16;
    const unsigned vq0 = r0 * 896 + (i0 - r0 * 7) * 16, vq1 = r1 * 896 + (i0 + 64 - r1 * 7) * 16, lb = wave * 2048;
    const u32x4 rw = mkrs((const char*)p.wq + 784, 401408 - 784);
    int k = k0;
#pragma unroll
    for (int i = 0; i < 7; ++i) { NEXTK(); DMA(rw, vq0, k * 57344, lb, K_WQ + i * 7168); if (wave < 3) DMA(rw, vq1, k * 57344, lb, K_WQ + i * 7168 + 1024); }
    MARK();
    VMW(0);
    lds_barrier(cOC, 4);
    QRD(fa, 0, K_WQ); QRD(fb, SK, K_WQ + 7168); QMM(fa); QRD(fa, 2 * SK, K_WQ + 2 * 7168); QMM(fb); QRD(fb, 3 * SK, K_WQ + 3 * 7168); QMM(fa);
    QRD(fa, 4 * SK, K_WQ + 4 * 7168); QMM(fb); QRD(fb, 5 * SK, K_WQ + 5 * 7168); QMM(fa); QRD(fa, 6 * SK, K_WQ + 6 * 7168); QMM(fb); QMM(fa);
    lds_wait(cCR, 4);
    STAMP(1);
  } else {
    if (wave == 0) {
      asm volatile("s_waitcnt vmcnt(14)" : "+v"(ua), "+v"(ub), "+v"(uc), "+v"(ud) :: "memory");
      const float pa = bf2f(ua), pb = bf2f(ub), dt = rbf(expf(bf2f(uc)));
      const float er = expf(dt * pa); float sn, cs; sincosf(dt * pb, &sn, &cs);
      const float nr = er * cs - 1.f, ni = er * sn, den = pa * pa + pb * pb;
      const cf lam = {er * cs, -ni};
      cf l16 = cmul(lam, lam); l16 = cmul(l16, l16); l16 = cmul(l16, l16); l16 = cmul(l16, l16);
      cf l64 = cmul(l16, l16); l64 = cmul(l64, l64);
      *(LAS f32x4*)(lds + L_EX + lo16) = (f32x4){lam.r, lam.i, 2.f * (nr * pa + ni * pb) / den, 2.f * (ni * pa - nr * pb) / den};
      *(LAS f32x4*)(lds + L_EX + 1024 + lo16) = (f32x4){l16.r, l16.i, l64.r, l64.i};
      *(LAS unsigned*)(lds + L_EX + 2048 + lane * 4) = ud;
    }
    if (isB) { MARK(); LDEPOCH(); }
    if (wave < 7) VMW(6); else VMW(3);
    BAR();
    if (isB) { QRD(fa, 0, K_W); QRD(fb, SK, SK + K_W); QMM(fa); QRD(fa, 2 * SK, 2 * SK + K_W); QMM(fb); QRD(fb, 3 * SK, 3 * SK + K_W); QMM(fa); QMM(fb); VMW(0); }
    else { LDCH(L_R1); LDCH(L_R1 + CH); VMW(12); }
    BAR();
    if (isB) { QRD(fa, 4 * SK, 4 * SK + K_W); QRD(fb, 5 * SK, 5 * SK + K_W); QMM(fa); QRD(fa, 6 * SK, 6 * SK + K_W); QMM(fb); QMM(fa); STAMP(1); }
  }

  if (isB) {
    if (tid == 0) *(LAS u64*)(lds + L_TAG) = tag;
    lds_barrier(cBB, 4);
    if (tid == 0) __hip_atomic_store((GAS u64*)(ws + WS_STARTED + b * 128), tag, RLX_AGENT);
    const float qs = 0.18898223650461361f * 1.4426950408889634f;
#pragma unroll
    for (int r = 0; r < 16; ++r) q[r] *= qs;
    if (hi) { q[12] = 0.f; q[13] = 0.f; q[14] = 0.f; q[15] = 0.f; }
    const bf16x8 qo0 = pack8(q, 0), qo1 = pack8(q, 1);
    *(LAS bf16x8*)(lds + L_QS + wave * 2048 + lo16) = qo0;
    *(LAS bf16x8*)(lds + L_QS + wave * 2048 + 1024 + lo16) = qo1;
    lds_barrier(cBB, 8);
    bf16x8 Q0[4], Q1[4];
    Q0[3] = qo0; Q1[3] = qo1;
#pragma unroll
    for (int i = 0; i < 3; ++i) { Q0[i] = ldsf(lds + L_QS + (wave ^ (3 - i)) * 2048 + lo16); Q1[i] = ldsf(lds + L_QS + (wave ^ (3 - i)) * 2048 + 1024 + lo16); }
    KV A0, A1, A2, A3;
    A3.v1 = (bf16x8){};
    f32x16 O[4] = {zero16, zero16, zero16, zero16};
    f32x16 Sa, Sb = zero16; u32x4 Pa, Pb = {};
    PIN(O[0]); PIN(O[1]); PIN(O[2]); PIN(O[3]); PIN(Sb); PIN(Pb); PIN(A3.v1);
    STAMP(2);
    poll(ws, WS_FKV + (wave * 8 + (lane & 7)) * 128, tag);
    STAMP(3);
    const __amdgpu_buffer_rsrc_t rs_kv = __builtin_amdgcn_make_buffer_rsrc(ws + WS_KV + wave * 65536, 0, 65536, 0x00020000);
#define LDKV(B, T) do { const unsigned so_ = (((T) + loc) & 15) * 4096; \
                        B.k0 = BF(BLD(rs_kv, lo16, so_)); B.k1 = BF(BLD(rs_kv, lo16, so_ + 1024)); \
                        B.v0 = BF(BLD(rs_kv, lo16, so_ + 2048)); B.v1 = BF(BLD(rs_kv, lo16, so_ + 3072)); } while (0)
    __builtin_amdgcn_s_setprio(BPRIO);
    LDKV(A0, 0); LDKV(A1, 1); LDKV(A2, 2);
    SBAR();
    Sa = MFMA(A0.k0, Q0[0], zero16); Sa = MFMA(A0.k1, Q1[0], Sa);
    STAMP(9);
#define UNIT(S, Sn, KN, QN, VP, OP, VC, OC, X) do { \
      Pb[2] = pk(Sn[12], Sn[13]); Pb[3] = pk(Sn[14], Sn[15]); \
      if ((QN) >= 0) Sn = MFMA(KN.k0, Q0[(QN) & 3], zero16); S[0] = EX(S[0]); S[1] = EX(S[1]); S[2] = EX(S[2]); S[3] = EX(S[3]); SBAR(); \
      if ((QN) >= 0) Sn = MFMA(KN.k1, Q1[(QN) & 3], Sn); Pa[0] = pk(S[0], S[1]); Pa[1] = pk(S[2], S[3]); S[4] = EX(S[4]); S[5] = EX(S[5]); S[6] = EX(S[6]); S[7] = EX(S[7]); SBAR(); \
      X; SBAR(); \
      OP = MFMA(VP.v1, BF(Pb), OP); Pa[2] = pk(S[4], S[5]); Pa[3] = pk(S[6], S[7]); S[8] = EX(S[8]); S[9] = EX(S[9]); S[10] = EX(S[10]); S[11] = EX(S[11]); SBAR(); \
      OC = MFMA(VC.v0, BF(Pa), OC); Pb[0] = pk(S[8], S[9]); Pb[1] = pk(S[10], S[11]); S[12] = EX(S[12]); S[13] = EX(S[13]); S[14] = EX(S[14]); S[15] = EX(S[15]); SBAR(); \
    } while (0)
#define TILE(CUR, NXT, PRV, T, LD) do { \
      UNIT(Sa, Sb, CUR, 1, PRV, O[3], CUR, O[0], ); \
      if (LD) LDKV(PRV, (T) + 3); SBAR(); \
      UNIT(Sb, Sa, CUR, 2, CUR, O[0], CUR, O[1], ); \
      UNIT(Sa, Sb, CUR, 3, CUR, O[1], CUR, O[2], ); \
      UNIT(Sb, Sa, NXT, 0, CUR, O[2], CUR, O[3], ); } while (0)
#pragma unroll 1
    for (int t = 0; t < 12; t += 4) {
      TILE(A0, A1, A3, t, 1); TILE(A1, A2, A0, t + 1, 1); TILE(A2, A3, A1, t + 2, 1); TILE(A3, A0, A2, t + 3, 1);
    }
    TILE(A0, A1, A3, 12, 1); TILE(A1, A2, A0, 13, 0); TILE(A2, A3, A1, 14, 0);
    const lptr oc = lds + L_OC + lo16;
#define OUTP(T, I) _Pragma("unroll") for (int q4 = 0; q4 < 4; ++q4) *(LAS f32x4*)(oc + ((wave ^ I) * 3 + I - 1) * 4096 + q4 * 1024) = (f32x4){T[4 * q4], T[4 * q4 + 1], T[4 * q4 + 2], T[4 * q4 + 3]}
    UNIT(Sa, Sb, A3, 1, A2, O[3], A3, O[0], );
    UNIT(Sb, Sa, A3, 2, A3, O[0], A3, O[1], lds_wait(cCR, 4));
    UNIT(Sa, Sb, A3, 3, A3, O[1], A3, O[2], OUTP(O[0], 3));
    UNIT(Sb, Sa, A3, -1, A3, O[2], A3, O[3], OUTP(O[1], 2));
    Pb[2] = pk(Sb[12], Sb[13]); Pb[3] = pk(Sb[14], Sb[15]);
    O[3] = MFMA(A3.v1, BF(Pb), O[3]);
    PIN(O[3]);
    STAMP(4);
    __builtin_amdgcn_s_setprio(2);
    OUTP(O[2], 1);
    lds_barrier(cBB, 12);
    STAMP(10);
    unsigned lo16b = lo16; PIN(lo16b);
    const int lane = lo16b >> 4, r32 = lane & 31, hi = lane >> 5, g4 = lane >> 4, tq = (lane & 15) >> 2, tp = lane & 3;
    f32x16 o = rs_read(lds + L_OC, wave, lane, O[3]);
    {
      float l = swap_hi(o[12]);
      float am = 0.f;
#pragma unroll
      for (int r = 0; r < 16; r += 2) am = __builtin_fmaxf(__builtin_fmaxf(am, __builtin_fabsf(o[r])), __builtin_fabsf(o[r + 1]));
      const bool ok = (l >= 0x1p-64f) && (l <= 0x1p64f) && (am <= 0x1p120f);
      const bool slow = !wall(ok);
      if (slow) {
        const __amdgpu_buffer_rsrc_t rk = __builtin_amdgcn_make_buffer_rsrc(ws + WS_KV, 0, 262144, 0x00020000);
        poll(ws, WS_FKV + r32 * 128, tag);
        float m = -__builtin_inff();
#pragma unroll 1
        for (int t = 0; t < 64; ++t) {
          f32x16 s = MFMA(BF(BLD(rk, lo16, t * 4096)), qo0, zero16); s = MFMA(BF(BLD(rk, lo16 + 1024, t * 4096)), qo1, s);
#pragma unroll
          for (int r = 0; r < 16; ++r) m = __builtin_fmaxf(m, s[r]);
        }
        m = __builtin_fmaxf(swap_lo(m), swap_hi(m));
        o = zero16;
#pragma unroll 1
        for (int t = 0; t < 64; ++t) {
          f32x16 s = MFMA(BF(BLD(rk, lo16, t * 4096)), qo0, zero16); s = MFMA(BF(BLD(rk, lo16 + 1024, t * 4096)), qo1, s);
#pragma unroll
          for (int r = 0; r < 16; ++r) s[r] = EX(s[r] - m);
          o = MFMA(BF(BLD(rk, lo16 + 2048, t * 4096)), pack8(s, 0), o); o = MFMA(BF(BLD(rk, lo16 + 3072, t * 4096)), pack8(s, 1), o);
        }
        l = swap_hi(o[12]);
      }
      const float inv = __builtin_amdgcn_rcpf(l);
#pragma unroll
      for (int r = 0; r < 16; ++r) o[r] *= inv;
      if (hi) { o[12] = 0.f; o[13] = 0.f; o[14] = 0.f; o[15] = 0.f; }
      u32x4 f0 = U4(pack8(o, 0)), f1 = U4(pack8(o, 1));
      if (slow) {
#pragma unroll
        for (int k = 0; k < 4; ++k) { f0[k] = umin(f0[k], 0xfffffffeu); f1[k] = umin(f1[k], 0xfffffffeu); }
      }
      BST(f0, rs_ws, lo16, o_own); BST(f1, rs_ws, lo16 + 1024, o_own);
    }
    lds_signal(cOC);
    lds_signal(cGO + 4 * wave);
    STAMP(5);
    if (j == 7) { STAMP_OUT(); return; }
    {
      const CAS Params* kp = KARGS();
      const __amdgpu_buffer_rsrc_t rs_out = __builtin_amdgcn_make_buffer_rsrc(kp->out, 0, 2048 * 896 * 4, 0x00020000);
      lds_wait(cWO, 4);
      u32x4 of[16];
      const unsigned ob = WS_O + (((2 * c + th) * 16 + 8 * chh) * 2) * 1024;
#define LDO(k) of[k] = BLD(rs_ws, lo16 + ((k) & 3) * 1024, ob + ((k) >> 2) * 4096)
      const int rowb = 224 * chh + 4 * (g4 >> 1) + tq;
      const unsigned wb = (L_WO + rowb * 128 + ((32 * (g4 & 1) + 8 * tp) ^ ((tq & 2) << 5))) ^ (chh << 6);
      bf16x8 w0[16], w1[16];
#pragma unroll
      for (int k = 0; k < 16; ++k) w1[k] = ldstr2(lds + (wb ^ 64) + (28 * (k >> 1) + 16 * (k & 1)) * 128, 1024);
      SBAR();
      {
        unsigned have = 0;
#pragma unroll 1
        for (unsigned s = 0; s < SPIN_CAP; ++s) {
          const unsigned w = __builtin_amdgcn_raw_buffer_load_b32(rs_ws, (lane & 7) * 2048 + 2044, ob, SC1);
          const unsigned rdy = (s + 1 < SPIN_CAP ? (unsigned)__builtin_amdgcn_ballot_w64(w != ~0u) : ~0u) & 0xffu & ~have;
#pragma unroll
          for (int h = 0; h < 8; ++h) if (rdy >> h & 1) { LDO(2 * h); LDO(2 * h + 1); }
          have |= rdy;
          if (have == 0xffu) break;
          __builtin_amdgcn_s_sleep(OSLEEP);
        }
      }
      f32x16 acc0 = zero16, acc1 = zero16;
#define OBAD(h) (!wall(umax(umax(umax(umax(of[2 * (h)][0], of[2 * (h)][1]), umax(of[2 * (h)][2], of[2 * (h)][3])), umax(of[2 * (h) + 1][0], of[2 * (h) + 1][1])), umax(of[2 * (h) + 1][2], of[2 * (h) + 1][3])) != ~0u))
#define OHEAD(h) do { \
        if (OBAD(h)) { \
          _Pragma("unroll 1") for (unsigned s = 0; s < SPIN_CAP; ++s) { LDO(2 * h); LDO(2 * h + 1); if (!OBAD(h)) break; __builtin_amdgcn_s_sleep(OSLEEP); } \
        } \
        acc1 = MFMA(BF(of[2 * h]), w1[2 * h], acc1); acc1 = MFMA(BF(of[2 * h + 1]), w1[2 * h + 1], acc1); PIN(acc1); \
        w0[2 * h] = ldstr2(lds + wb + 28 * h * 128, 1024); w0[2 * h + 1] = ldstr2(lds + wb + (28 * h + 16) * 128, 1024); } while (0)
      OHEAD(0); STAMP(6); OHEAD(1); OHEAD(2); OHEAD(3); OHEAD(4); OHEAD(5); OHEAD(6); OHEAD(7);
      STAMP(11);
      lds_wait(cOC, 4);
#pragma unroll
      for (int k = 0; k < 4; ++k) acc0 = MFMA(BF(of[k]), w0[k], acc0);
      SBAR();
#pragma unroll
      for (int k = 0; k < 4; ++k) *(LAS f32x4*)(lds + L_OC + (wave ^ 1) * 4096 + k * 1024 + lo16) = (f32x4){acc1[4 * k], acc1[4 * k + 1], acc1[4 * k + 2], acc1[4 * k + 3]};
      SBAR();
#pragma unroll
      for (int k = 4; k < 8; ++k) acc0 = MFMA(BF(of[k]), w0[k], acc0);
      PIN(acc0);
      lds_signal(cPR + 4 * th);
#pragma unroll
      for (int k = 8; k < 16; ++k) acc0 = MFMA(BF(of[k]), w0[k], acc0);
      PIN(acc0);
      lds_wait(cPR + 4 * th, 2);
#pragma unroll
      for (int k = 0; k < 4; ++k) { const f32x4 v = *(LAS f32x4*)(lds + L_OC + wave * 4096 + k * 1024 + lo16);
        acc0[4 * k] += v[0]; acc0[4 * k + 1] += v[1]; acc0[4 * k + 2] += v[2]; acc0[4 * k + 3] += v[3]; }
      STAMP(12);
      const unsigned oo = ((64 * c + 32 * th + 4 * hi) * 896 + 448 + 64 * j + 32 * chh + r32) * 4;
#pragma unroll
      for (int r = 0; r < 16; ++r) __builtin_amdgcn_raw_buffer_store_b32(f2u(acc0[r]), rs_out, oo, ((r & 3) + 8 * (r >> 2)) * 3584, OAUX);
    }
    STAMP(7);
    STAMP_OUT();
  } else {
    if (KILLA == 5) { lds_signal(cCR); lds_signal(cWO); STAMP(7); STAMP_OUT(); return; }
    cf lam, coef, l16, l64; f32x16 ur = zero16, ui = zero16;
    f32x16 acc[4] = {zero16, zero16, zero16, zero16};
    const unsigned cs0 = wa * 16384u, cs1 = L_R1 + wa * 12288u;
    unsigned a_htw = L_HT + wa * 4096 + r32 * 64 + hi * 32, a_htr = L_HT + wa * 4096 + (8 * (g4 >> 1) + tq) * 64 + (g4 & 1) * 32 + tp * 8;
    unsigned a_hst = WS_H + ((2 * c + th) * 56 + 4 * j + 2 * chh) * 1024 + lo16;
    unsigned a_c[4], a_cn[2];
#pragma unroll
    for (int f = 0; f < 4; ++f) a_c[f] = cs0 + (32 * chh + r32) * 128 + (((2 * f + hi) ^ xsw) << 4);
#pragma unroll
    for (int f = 0; f < 2; ++f) a_cn[f] = cs1 + 8192 + (32 * chh + r32) * 64 + (((2 * f + hi) ^ ((r32 >> 2) & 3)) << 4);
    unsigned a_vh = (lane >> 2) * 896 + (((lane & 3) ^ ((lane >> 4) & 3)) << 4) + 384 + chh * 64, a_vo = ve ^ 64;
    unsigned a_vw = lrow * 896 + (((lane & 7) ^ ((lrow & 2) << 1)) << 4);
    unsigned a_fl = lane < 32 ? WS_FH + (c * 8 + umin(lane & 7, 6)) * 128 : WS_STARTED + ((b & 7) + 8 * ((loc & ~7) + (lane & 7))) * 128;
    unsigned a_el = (31 - umin(r32, c - 1)) * 512 + 4, a_eo = (32 - c + hi) * 512 + r32 * 16;
    {
      LDCH(0); LDCH(CH);
      { const f32x4 c0 = *(LAS f32x4*)(lds + L_EX + (32 * chh + r32) * 16), c1 = *(LAS f32x4*)(lds + L_EX + 1024 + (32 * chh + r32) * 16);
        lam = {c0[0], c0[1]}; coef = {c0[2], c0[3]}; l16 = {c1[0], c1[1]}; l64 = {c1[2], c1[3]}; ud = *(LAS unsigned*)(lds + L_EX + 2048 + (32 * chh + r32) * 4); }
      PIN(a_htw); PIN(a_htr); PIN(a_hst); PIN(a_c[0]); PIN(a_c[1]); PIN(a_c[2]); PIN(a_c[3]); PIN(a_cn[0]); PIN(a_cn[1]); PIN(a_vh); PIN(a_vw); PIN(a_fl); PIN(a_el); PIN(a_eo); PIN(a_vo);
      const int trow = 32 * th + 16 * ((r32 >> 2) & 1) + (r32 & 3) + 4 * (r32 >> 3);
      const unsigned xa_off = trow * 128, xa_sw = (trow >> 1) & 7, b_off = 8192 + (32 * chh + r32) * 128;
      bf16x8 gax[4], gar[4], gai[4], gbx[4], gbr[4], gbi[4];
#define GRD(B, s_) _Pragma("unroll") for (int ks = 0; ks < 4; ++ks) { B##x[ks] = ldsf(lds + (s_) + xa_off + (((2 * ks + hi) ^ xa_sw) << 4)); \
        B##r[ks] = ldsf(lds + (s_) + b_off + (((2 * ks + hi) ^ xsw) << 4)); B##i[ks] = ldsf(lds + (s_) + b_off + 8192 + (((2 * ks + hi) ^ xsw) << 4)); } SBAR()
#define GMM(B) _Pragma("unroll") for (int ks = 0; ks < 4; ++ks) { ur = MFMA(B##x[ks], B##r[ks], ur); ui = MFMA(B##x[ks], B##i[ks], ui); } PIN(ur); PIN(ui); SBAR()
      __builtin_amdgcn_s_setprio(APRIO);
      STAMP(12);
      VMW(12); lds_barrierA(cAB, 4); GRD(ga, L_R1); GRD(gb, L_R1 + CH); GMM(ga);
      lds_wait_<1>(cBB, 4);
      LDCH(2 * CH); GMM(gb); LDCH(3 * CH);
      VMW(12); lds_barrierA(cAB, 8);
      STAMP(13);
      GRD(ga, 0); GRD(gb, CH); LDCH(L_R1); GMM(ga);
      VMW(6); lds_barrierA(cAB, 12);
      STAMP(14);
      GRD(ga, 2 * CH); GMM(gb); GRD(gb, 3 * CH); GMM(ga);
      VMW(0); lds_barrierA(cAB, 16);
      STAMP(15);
      GRD(ga, L_R1); GMM(gb); GMM(ga);
      { const u32x2 t2 = *(LAS u32x2*)(lds + L_TAG);
        tag = ((u64)(unsigned)__builtin_amdgcn_readfirstlane(t2[1]) << 32) | (unsigned)__builtin_amdgcn_readfirstlane(t2[0]); }
    }
    {
      const int e = r32 - 8 * hi; const unsigned dv = ud << ((e & 1) * 16);
      const lptr xr = lds + L_R1 + (32 * th + r32) * 128;
#pragma unroll
      for (int s2 = 0; s2 < 2; ++s2) {
        const u32x4 dg = {(e >> 1) == 8 * s2 ? dv : 0u, (e >> 1) == 8 * s2 + 1 ? dv : 0u, (e >> 1) == 8 * s2 + 2 ? dv : 0u, (e >> 1) == 8 * s2 + 3 ? dv : 0u};
        acc[0] = MFMA(ldsf(xr + (((4 * chh + 2 * s2 + hi) ^ xsw) << 4)), BF(dg), acc[0]);
      }
      PIN(acc[0]);
    }
    STAMP(1);
    if (KILLA == 1) { VMW(0); lds_signal(cCR); lds_signal(cWO); STAMP(7); STAMP_OUT(); return; }
    __builtin_amdgcn_s_setprio(EPRIO);
    cf e = {0.f, 0.f};
#pragma unroll
    for (int r = 0; r < 16; ++r) {
      const float a = coef.r * ur[r] - coef.i * ui[r], b2 = -(coef.r * ui[r] + coef.i * ur[r]);
      ur[r] = a; ui[r] = b2; e = r ? cfma(lam, e, (cf){a, b2}) : (cf){a, b2};
    }
    const int seg = 2 * th + hi;
    *(LAS f32x2*)(lds + L_EX + (seg * 64 + 32 * chh + r32) * 8) = (f32x2){e.r, e.i};
    lds_barrierA(cAB, 20);
    cf g0 = {0.f, 0.f}, lseg = {1.f, 0.f};
    {
      cf E = {0.f, 0.f};
#pragma unroll
      for (int s = 0; s < 4; ++s) {
        const f32x2 v = *(LAS f32x2*)(lds + L_EX + (s * 64 + 32 * chh + r32) * 8);
        if (s == seg) g0 = E;
        if (s < seg) lseg = cmul(lseg, l16);
        E = cfma(l16, E, (cf){v[0], v[1]});
      }
      if (th == 0 && hi == 0 && c < 31)
        BST(((u32x4){f2u(E.r), (unsigned)tag, f2u(E.i), (unsigned)tag}), rs_ws, r32 * 16, WS_E + ((2 * j + chh) * 32 + 31 - c) * 512);
    }
    __builtin_amdgcn_s_setprio(0);
    const CAS Params* kp = KARGS();
    {
      const u32x4 rc = mkrs((const char*)(th ? kp->cim : kp->cre) + (size_t)j * 57344, 57344);
      int n2 = 6 * cl;
#pragma unroll
      for (int n = 0; n < 24; ++n) {
        DMA(rc, (n & 1) ? a_vo : ve, (n2 & 7) * 7168 + (n2 >> 3) * 128 + chh * 512, (n2 < 16 ? cs0 : cs1 - 16384) + n2 * 1024, 0); if (n >= CPACE) VMW(CPACE);
        n2 = n2 == 23 ? 0 : n2 + 1; }
#pragma unroll
      for (int n = 0; n < 4; ++n) DMA(rc, a_vh, n * 14336, cs1, 8192 + n * 1024);
    }
    if (KILLA == 2) { VMW(0); lds_signal(cCR); lds_signal(cWO); STAMP(7); STAMP_OUT(); return; }
    STAMP(2);
    cf G = g0;
    if (c > 0) {
      const cf l128 = cmul(l64, l64);
      const __amdgpu_buffer_rsrc_t re = __builtin_amdgcn_make_buffer_rsrc(ws + WS_E + (2 * j + chh) * 16384, 0, 16384, 0x00020000);
      const unsigned eo = a_eo, want = (c - hi + 1) >> 1;
      u32x4 ev[16];
#pragma unroll 1
      for (unsigned s = 0; s < SPIN_CAP; ++s) {
        const unsigned t1 = __builtin_amdgcn_raw_buffer_load_b32(re, a_el, 0, SC1);
        if (wall(t1 == (unsigned)tag)) break;
        __builtin_amdgcn_s_sleep(PSLEEP);
      }
      MEMBAR();
#pragma unroll 1
      for (unsigned s = 0; s < SPIN_CAP; ++s) {
        unsigned got = 0;
#pragma unroll
        for (int m = 0; m < 8; ++m) ev[m] = BLD(re, eo, m * 1024);
        if (c > 16) {
#pragma unroll
          for (int m = 8; m < 16; ++m) ev[m] = BLD(re, eo, m * 1024);
        }
        VMW(0);
        __builtin_amdgcn_s_setprio(HPRIO);
        if (c > 16) {
#pragma unroll
          for (int m = 8; m < 16; ++m) got += ((ev[m][1] ^ (unsigned)tag) | (ev[m][3] ^ (unsigned)tag)) == 0;
        }
#pragma unroll
        for (int m = 0; m < 8; ++m) got += ((ev[m][1] ^ (unsigned)tag) | (ev[m][3] ^ (unsigned)tag)) == 0;
        if (wall(got == want)) break;
        __builtin_amdgcn_s_sleep(4);
      }
      STAMP(3);
      cf H = {0.f, 0.f};
      if (c > 16) {
#pragma unroll
        for (int m = 15; m >= 8; --m) H = cfma(l128, H, (cf){u2f(ev[m][0]), u2f(ev[m][2])});
      }
#pragma unroll
      for (int m = 7; m >= 0; --m) H = cfma(l128, H, (cf){u2f(ev[m][0]), u2f(ev[m][2])});
      H = cfma(l64, (cf){swap_hi(H.r), swap_hi(H.i)}, (cf){swap_lo(H.r), swap_lo(H.i)});
      G = cfma(lseg, H, g0);
    }
    STAMP(4);
    __builtin_amdgcn_s_setprio(P2PRIO);
    {
#pragma unroll
      for (int r = 0; r < 16; ++r) { G = cfma(lam, G, (cf){ur[r], ui[r]}); ur[r] = G.r; ui[r] = G.i; }
      *(LAS bf16x8*)(lds + a_htw) = pack8(ur, 0); *(LAS bf16x8*)(lds + a_htw + 16) = pack8(ur, 1);
      *(LAS bf16x8*)(lds + a_htw + 2048) = pack8(ui, 0); *(LAS bf16x8*)(lds + a_htw + 2064) = pack8(ui, 1);
      unsigned a_hs2 = a_hst + 28 * 1024; PIN(a_hs2);
      __builtin_amdgcn_s_setprio(0);
      asm volatile("s_waitcnt lgkmcnt(0)" ::: "memory");
#pragma unroll
      for (int k = 0; k < 4; ++k) BST(U4(ldstr2(lds + a_htr + (k >> 1) * 2048 + (k & 1) * 1024, 256)), rs_ws, (k >> 1) ? a_hs2 : a_hst, (k & 1) * 1024);
      publish4(ws, WS_FH + (c * 8 + j) * 128, tag, lane);
    }
    if (KILLA == 3) { VMW(0); lds_signal(cCR); lds_signal(cWO); STAMP(7); STAMP_OUT(); return; }
    STAMP(5);
    bf16x8 cfr[14][2];
    const int cflip = chh ? -1 : 1;
#define LDC(f) do { if ((f) < 12) { const unsigned a_ = a_c[(f) & 3] + ((f) < 8 ? ((f) >> 2) * 8192 : cs1 - cs0); cfr[f][0] = ldsf(lds + a_); cfr[f][1] = ldsf(lds + a_ + cflip * 4096); } \
                    else { cfr[f][0] = ldsf(lds + a_cn[(f) & 1]); cfr[f][1] = ldsf(lds + a_cn[(f) & 1] + cflip * 2048); } } while (0)
#pragma unroll
    for (int f = 0; f < 8; ++f) LDC(f);
    lds_signal(cCR);
    STAMP(10);
    lds_wait_<ASLEEP>(cCR, 4);
    {
      const u32x4 rw = mkrs((const char*)kp->wo + j * 128 + wa * 7168, 401408 - j * 128 - wa * 7168);
      int m2 = 4 * cl;
#pragma unroll
      for (int m = 0; m < 14; ++m) { DMA(rw, a_vw, m2 * 28672, wa * 1024 + m2 * 4096, L_WO); m2 = m2 == 13 ? 0 : m2 + 1; }
      if (wa == 0 && lane < 32) *(LAS u32x4*)(lds + L_WO + 57344 + lo16) = (u32x4){0, 0, 0, 0};
    }
    poll(ws, a_fl, tag);
    VMW(0);
    lds_signal(cWO);
    if (KILLA == 4) { STAMP(7); STAMP_OUT(); return; }
    STAMP(6);
    {
      const unsigned hb = WS_H + (2 * c * 56 + 14 * wa) * 1024, h0 = hb + th * 57344, h1 = hb + (th ^ 1) * 57344;
      const unsigned fa = chh * 2048, fb = chh ? 0u - 12288u : 0u;
      u32x4 hf[14][2];
#define LDH(f) do { hf[f][0] = BLD(rs_ws, lo16, h0 + (f) * 1024 + ((f) < 12 ? fa : fb)); hf[f][1] = BLD(rs_ws, lo16, h1 + (f) * 1024 + ((f) < 12 ? fa : fb)); } while (0)
#define MMH(f) do { acc[0] = MFMA(BF(hf[f][0]), cfr[f][0], acc[0]); acc[1] = MFMA(BF(hf[f][0]), cfr[f][1], acc[1]); \
                    acc[2] = MFMA(BF(hf[f][1]), cfr[f][0], acc[2]); acc[3] = MFMA(BF(hf[f][1]), cfr[f][1], acc[3]); \
                    PIN(acc[0]); PIN(acc[1]); PIN(acc[2]); PIN(acc[3]); } while (0)
#pragma unroll
      for (int f = 0; f < HPRE; ++f) LDH(f);
      SBAR();
      lds_wait_<GOSLEEP>(cGO + 4 * wa, 1);
      STAMP(9);
      __builtin_amdgcn_s_setprio(GPRIO);
#pragma unroll
      for (int f = 0; f < 8; ++f) { MMH(f); SBAR(); if (f + HPRE < 14) LDH(f + HPRE); if (f >= 1 && f < 7) LDC(f + 7); SBAR(); }
      lds_signal(cP4);
#pragma unroll
      for (int f = 8; f < 14; ++f) MMH(f);
    }
    lds_wait(cP4, 4);
    STAMP(11);
    rs_write(lds + L_G2, wa, lane, acc[1], acc[2], acc[3]);
    lds_barrierA(cAB, 24);
    f32x16 y = rs_read(lds + L_G2, wa, lane, acc[0]);
    const __amdgpu_buffer_rsrc_t rs_out = __builtin_amdgcn_make_buffer_rsrc(kp->out, 0, 2048 * 896 * 4, 0x00020000);
    const unsigned oo = ((64 * c + 32 * th + 4 * hi) * 896 + 64 * j + 32 * chh + r32) * 4;
#pragma unroll
    for (int r = 0; r < 16; ++r) __builtin_amdgcn_raw_buffer_store_b32(f2u(y[r]), rs_out, oo, ((r & 3) + 8 * (r >> 2)) * 3584, OAUX);
    STAMP(7);
    STAMP_OUT();
  }
}

extern "C" void kernel_launch(void* const* d_in, const int* in_sizes, int n_in, void* d_out, int out_size, void* d_ws, size_t ws_size,
                              hipStream_t stream) {
  Params p;
  p.xa = (const u16*)d_in[0]; p.lre = (const u16*)d_in[1]; p.lim = (const u16*)d_in[2]; p.bre = (const u16*)d_in[3]; p.bim = (const u16*)d_in[4];
  p.cre = (const u16*)d_in[5]; p.cim = (const u16*)d_in[6]; p.ldt = (const u16*)d_in[7]; p.dsk = (const u16*)d_in[8];
  p.xb = (const u16*)d_in[9]; p.wq = (const u16*)d_in[10]; p.wk = (const u16*)d_in[11]; p.wv = (const u16*)d_in[12]; p.wo = (const u16*)d_in[13];
  p.out = (float*)d_out; p.ws = (char*)d_ws;
  fused_kernel<<<256, 512, 0, stream>>>(p);
}
```

```cpp
#include <hip/hip_runtime.h>
#include <stdint.h>

#define DI __device__ __forceinline__
#define GAS __attribute__((address_space(1)))
#define LAS __attribute__((address_space(3)))
#define CAS __attribute__((address_space(4)))
typedef short bf16x8 __attribute__((ext_vector_type(8)));
typedef short s16x4 __attribute__((ext_vector_type(4)));
typedef float f32x16 __attribute__((ext_vector_type(16)));
typedef float f32x4 __attribute__((ext_vector_type(4)));
typedef float f32x2 __attribute__((ext_vector_type(2)));
typedef unsigned u32x4 __attribute__((ext_vector_type(4)));
typedef unsigned u32x2 __attribute__((ext_vector_type(2)));
typedef __bf16 bf16x2_t __attribute__((ext_vector_type(2)));
typedef unsigned long long u64;
typedef unsigned short u16;
typedef LAS char* lptr;
#define RLX_AGENT __ATOMIC_RELAXED, __HIP_MEMORY_SCOPE_AGENT
#define MFMA(a, b, c) __builtin_amdgcn_mfma_f32_32x32x16_bf16((a), (b), (c), 0, 0, 0)
#define SBAR() __builtin_amdgcn_sched_barrier(0)
#define PIN(x) asm volatile("" : "+v"(x))
#define MEMBAR() asm volatile("" ::: "memory")
#define SC1 16
#define APRIO 1
#define BPRIO 0
#define ASLEEP 1
#define EPRIO 1
#define KILLA 0
#define GOSLEEP 4
#define PRET 0
#define OSLEEP 2
#define HPRE 14
#define HPRIO 1
#define P2PRIO 1
#define PPOS 0
#define GNOP 7
#define HOLD 1
#define GPRIO 1
#define PSLEEP 2
#define APACE 63
#define OAUX 17
#define CPACE 63

constexpr unsigned WS_EPOCH = 0, WS_FH = 8192, WS_E = 0x10000, WS_FP = 0x48000,
                   WS_KV = 0x50000, WS_O = 0x100000, WS_H = 0x300000, WS_Q = 0x680000, WS_END = 0x880000, WS_DBG = 0x900000;
constexpr unsigned SK = 16384, K_W = 8192;
constexpr unsigned CH = 24576, L_SA = 135168;
constexpr unsigned L_R1 = 107520, L_HT = 65536, L_WO = 0, L_OC = 57856, L_G2 = 107520, L_MISC = 159744, L_EX = L_MISC + 128, L_TOTAL = 163840;
constexpr unsigned cBB = L_MISC, cAB = L_MISC + 4, cCR = L_MISC + 8, cWO = L_MISC + 12, cOC = L_MISC + 16, cPB = L_MISC + 20, cPR = L_MISC + 24, cP4 = L_MISC + 32, cPQ = L_MISC + 52, cST = L_MISC + 56, cGO = L_MISC + 36, L_TAG = L_MISC + 64;
constexpr unsigned SPIN_CAP = 1u << 18;

struct Params {
  const u16 *xb, *wq, *wk, *wv; char* ws; const u16 *lre, *lim, *ldt, *dsk, *xa, *bre, *bim, *cre, *cim, *wo;
  float* out;
};

DI float u2f(unsigned v) { return __builtin_bit_cast(float, v); }
DI unsigned f2u(float v) { return __builtin_bit_cast(unsigned, v); }
DI unsigned umin(unsigned a, unsigned b) { return a < b ? a : b; }
DI unsigned umax(unsigned a, unsigned b) { return a > b ? a : b; }
DI bool wall(bool x) { return __builtin_amdgcn_ballot_w64(!x) == 0; }
DI float bf2f(unsigned v) { return u2f(v << 16); }
DI unsigned pk(float a, float b) { f32x2 v = {a, b}; return __builtin_bit_cast(unsigned, __builtin_convertvector(v, bf16x2_t)); }
DI float rbf(float a) { return u2f(pk(0.f, a)); }
DI bf16x8 pack8(const f32x16& x, int s) {
  u32x4 p = {pk(x[8 * s], x[8 * s + 1]), pk(x[8 * s + 2], x[8 * s + 3]), pk(x[8 * s + 4], x[8 * s + 5]), pk(x[8 * s + 6], x[8 * s + 7])};
  return __builtin_bit_cast(bf16x8, p);
}
DI u32x4 mkrs(const void* base, unsigned bytes) { const u64 a = (u64)base; return (u32x4){(unsigned)a, (unsigned)(a >> 32) & 0xffffu, bytes, 0x20000u}; }
#define DMA(rs, voff, soff, ldsb, IMM) asm volatile("s_add_u32 m0, %3, %4\n\ts_nop 0\n\tbuffer_load_dwordx4 %0, %1, %2 offen lds" \
                                                    :: "v"(voff), "s"(rs), "s"(soff), "s"(ldsb), "i"(IMM) : "memory", "scc")
DI bf16x8 ldsf(lptr p) { return *(LAS bf16x8*)p; }
DI s16x4 ldstr(lptr p) { return __builtin_amdgcn_ds_read_tr16_b64_v4i16((LAS s16x4*)p); }
DI bf16x8 ldstr2(lptr p, int second) { s16x4 lo = ldstr(p), hi = ldstr(p + second); return __builtin_shufflevector(lo, hi, 0, 1, 2, 3, 4, 5, 6, 7); }
DI void lds_signal(unsigned c) {
  asm volatile("s_waitcnt lgkmcnt(0)\n\ts_mov_b64 exec, 1\n\tds_add_u32 %0, %1\n\ts_mov_b64 exec, -1" :: "v"(c), "v"(1u) : "memory");
}
template <int SLEEP> DI void lds_wait_(unsigned c, unsigned target) {
  unsigned v, s, n;
  asm volatile("s_mov_b32 %2, 0x400000\n1:\n\tds_read_b32 %0, %3\n\ts_waitcnt lgkmcnt(0)\n\tv_readfirstlane_b32 %1, %0\n\ts_cmp_ge_u32 %1, %4\n\ts_cbranch_scc1 2f\n\t"
               "s_sleep %5\n\ts_sub_u32 %2, %2, 1\n\ts_cbranch_scc0 1b\n2:" : "=&v"(v), "=&s"(s), "=&s"(n) : "v"(c), "i"(target), "i"(SLEEP) : "memory", "scc");
}
#define lds_wait(c, t) lds_wait_<0>(c, t)
#define lds_barrier(c, t) do { lds_signal(c); lds_wait(c, t); } while (0)
#define lds_barrierA(c, t) do { lds_signal(c); lds_wait_<ASLEEP>(c, t); } while (0)
DI void poll(const char* ws, unsigned off, u64 tag) {
#pragma unroll 1
  for (unsigned s = 0; s < SPIN_CAP; ++s) {
    u64 v = __hip_atomic_load((GAS u64*)(ws + off), RLX_AGENT);
    if (wall(v == tag)) break;
    __builtin_amdgcn_s_sleep(PSLEEP);
  }
  MEMBAR();
}
DI void publish4(char* ws, unsigned off, u64 tag, int lane, unsigned cnt) {
  unsigned old;
  asm volatile("s_waitcnt vmcnt(0)\n\ts_mov_b64 exec, 1\n\tds_add_rtn_u32 %0, %1, %2\n\ts_waitcnt lgkmcnt(0)\n\ts_mov_b64 exec, -1" : "=&v"(old) : "v"(cnt), "v"(1u) : "memory");
  if (__builtin_amdgcn_readfirstlane(old) == 3 && lane == 0) __hip_atomic_store((GAS u64*)(ws + off), tag, RLX_AGENT);
}
DI float swap_lo(float x) {
  auto r = __builtin_amdgcn_permlane32_swap(f2u(x), f2u(x), false, false); return u2f(r[0]); }
DI float swap_hi(float x) {
  auto r = __builtin_amdgcn_permlane32_swap(f2u(x), f2u(x), false, false); return u2f(r[1]); }
struct cf { float r, i; };
DI cf cmul(cf a, cf b) { return {a.r * b.r - a.i * b.i, a.r * b.i + a.i * b.r}; }
DI cf cfma(cf a, cf b, cf c) { return {__builtin_fmaf(a.r, b.r, __builtin_fmaf(-a.i, b.i, c.r)), __builtin_fmaf(a.r, b.i, __builtin_fmaf(a.i, b.r, c.i))}; }

#define STAMP(k) do {} while (0)
#define STAMP_OUT() do {} while (0)

struct KV { bf16x8 k0, k1, v0, v1; };

DI void rs_write(lptr base, int w, int lane, const f32x16& t1, const f32x16& t2, const f32x16& t3) {
#define RSW(T, I) _Pragma("unroll") for (int q = 0; q < 4; ++q) *(LAS f32x4*)(base + ((w ^ I) * 3 + I - 1) * 4096 + q * 1024 + lane * 16) = (f32x4){T[4 * q], T[4 * q + 1], T[4 * q + 2], T[4 * q + 3]}
  RSW(t1, 1); RSW(t2, 2); RSW(t3, 3);
}
DI f32x16 rs_read(lptr base, int w, int lane, f32x16 a) {
  f32x4 v[12];
#pragma unroll
  for (int s = 0; s < 12; ++s) v[s] = *(LAS f32x4*)(base + w * 12288 + s * 1024 + lane * 16);
  SBAR();
#pragma unroll
  for (int s = 0; s < 12; ++s) { const int q = s & 3; a[4 * q] += v[s][0]; a[4 * q + 1] += v[s][1]; a[4 * q + 2] += v[s][2]; a[4 * q + 3] += v[s][3]; }
  return a;
}

#define VMW_(n) asm volatile("s_waitcnt vmcnt(" #n ")" ::: "memory")
#define VMW(n) VMW_(n)
#define BAR() asm volatile("s_waitcnt lgkmcnt(0)\n\ts_barrier" ::: "memory")
#define BLD(R, V, S) __builtin_amdgcn_raw_buffer_load_b128(R, V, S, SC1)
#define BST(D, R, V, S) __builtin_amdgcn_raw_buffer_store_b128(D, R, (V) + (S), 0, SC1)
#define BF(x) __builtin_bit_cast(bf16x8, x)
#define U4(x) __builtin_bit_cast(u32x4, x)
#define EX(x) __builtin_amdgcn_exp2f(x)
#define KARGS() ({ const CAS Params* kp_ = (const CAS Params*)__builtin_amdgcn_kernarg_segment_ptr(); asm volatile("" : "+s"(kp_)); kp_; })
#define NEXTK() do { k += step; k = k >= 7 ? k - 7 : k; } while (0)

#if defined(__HIP_DEVICE_COMPILE__)
#define NO_PK __attribute__((target("no-packed-fp32-ops")))
#else
#define NO_PK
#endif
__global__ void __launch_bounds__(512, 2) NO_PK fused_kernel(Params p) {
  __shared__ __attribute__((aligned(128))) char smem[L_TOTAL];
  const lptr lds = (lptr)smem;
  const int tid = threadIdx.x, lane = tid & 63, wave = __builtin_amdgcn_readfirstlane(tid >> 6);
  const int b = blockIdx.x, loc = b >> 3, cl = loc >> 3, c = (b & 7) * 4 + cl, j = loc & 7;
  const int lrow = lane >> 3;
  const unsigned ve = lrow * 896 + (((lane & 7) ^ (lrow >> 1)) << 4);
  const int step = cl + 1, k0 = j == 7 ? 0 : j;
  const bool isB = wave < 4;
  const int wa = wave & 3;
  char* const ws = p.ws;
  STAMP(0);
  unsigned ua = 0, ub = 0, uc = 0, ud = 0;
  u32x4 rxa, rbr, rbi; int ka = k0;
  const unsigned vo = (ve ^ 64) + 7168, lba = wa * 2048;
#define NEXTKA() do { ka += step; ka = ka >= 7 ? ka - 7 : ka; } while (0)
#define LDCH(slot) do { NEXTKA(); const unsigned k_ = ka * 128; DMA(rxa, ve, k_, lba, (slot)); DMA(rxa, vo, k_, lba, (slot) + 1024); \
      DMA(rbr, ve, k_, lba, (slot) + 8192); DMA(rbr, vo, k_, lba, (slot) + 9216); DMA(rbi, ve, k_, lba, (slot) + 16384); DMA(rbi, vo, k_, lba, (slot) + 17408); } while (0)
  {
    const u32x4 rx = mkrs((const char*)p.xb + (size_t)c * 57344 + wave * 7168, 7168);
    const unsigned vx = ve ^ ((wave & 1) << 6), lb = wave * 1024;
    int k = k0;
    if (j == 7) {
      const u32x4 rw = mkrs(isB ? p.wk : p.wv, 25088);
      const unsigned vw = wa * 1024 + lane * 16, lw = K_W + (isB ? 0 : 3584) + wa * 1024; const bool half = wa < 3 || lane < 32;
#pragma unroll
      for (int i = 0; i < 7; ++i) { NEXTK(); DMA(rx, vx, k * 128, lb, i * SK); if (half) DMA(rw, vw, k * 3584, lw, i * SK); }
    } else {
      const u32x4 rw = mkrs((const char*)p.wq + j * 128 + wave * 7168, 401408 - j * 128 - wave * 7168);
      const unsigned vw = lrow * 896 + (((lane & 7) ^ ((lrow & 2) << 1)) << 4);
      if (wave == 0) { const unsigned co = (64 * j + lane) * 2;
        asm volatile("global_load_ushort %0, %4, %5\n\tglobal_load_ushort %1, %4, %6\n\tglobal_load_ushort %2, %4, %7\n\tglobal_load_ushort %3, %4, %8"
                     : "=&v"(ua), "=&v"(ub), "=&v"(uc), "=&v"(ud) : "v"(co), "s"(p.lre), "s"(p.lim), "s"(p.ldt), "s"(p.dsk) : "memory"); }
#pragma unroll
      for (int i = 0; i < 7; ++i) { NEXTK(); DMA(rx, vx, k * 128, lb, i * SK); DMA(rw, vw, k * 57344, lb, i * SK + K_W); }
      rxa = mkrs((const char*)p.xa + (size_t)c * 57344 + wa * 14336, 14336);
      rbr = mkrs((const char*)p.bre + (size_t)j * 57344 + wa * 14336, 14336);
      rbi = mkrs((const char*)p.bim + (size_t)j * 57344 + wa * 14336, 14336);
    }
  }
  STAMP(8);
  if (tid < 16) ((LAS unsigned*)(lds + L_MISC))[tid] = 0;
  const int r32 = lane & 31, hi = lane >> 5;
  const int g4 = lane >> 4, tq = (lane & 15) >> 2, tp = lane & 3;
  const unsigned xsw = (r32 >> 1) & 7, lo16 = lane * 16;
  const int th = wa >> 1, chh = wa & 1;
  u64 tag;
  const __amdgpu_buffer_rsrc_t rs_ws = __builtin_amdgcn_make_buffer_rsrc(ws, 0, WS_END, 0x00020000);
  const f32x16 zero16 = {};
  const unsigned o_own = WS_O + (((2 * c + th) * 16 + 2 * j + chh) * 2) * 1024;
#define LDEPOCH() asm volatile("s_load_dwordx2 %0, %1, 0x0\n\ts_waitcnt lgkmcnt(0)" : "=s"(tag) : "s"(ws) : "memory"); tag = (tag | 1) + 2

  if (isB) {
    if (wave == 0 && j < 7) {
      asm volatile("s_waitcnt vmcnt(14)" : "+v"(ua), "+v"(ub), "+v"(uc), "+v"(ud) :: "memory");
      const float pa = bf2f(ua), pb = bf2f(ub), dt = rbf(expf(bf2f(uc)));
      const float er = expf(dt * pa); float sn, cs; sincosf(dt * pb, &sn, &cs);
      const float nr = er * cs - 1.f, ni = er * sn, den = pa * pa + pb * pb;
      const cf lam = {er * cs, -ni};
      cf l16 = cmul(lam, lam); l16 = cmul(l16, l16); l16 = cmul(l16, l16); l16 = cmul(l16, l16);
      cf l64 = cmul(l16, l16); l64 = cmul(l64, l64);
      *(LAS f32x4*)(lds + L_EX + lo16) = (f32x4){lam.r, lam.i, 2.f * (nr * pa + ni * pb) / den, 2.f * (ni * pa - nr * pb) / den};
      *(LAS f32x4*)(lds + L_EX + 1024 + lo16) = (f32x4){l16.r, l16.i, l64.r, l64.i};
      *(LAS unsigned*)(lds + L_EX + 2048 + lane * 4) = ud;
    }
    { const u32x4 ones = {~0u, ~0u, ~0u, ~0u};
      BST(ones, rs_ws, lo16, o_own); BST(ones, rs_ws, lo16 + 1024, o_own); }
    LDEPOCH();
    if (tid == 0) *(LAS u64*)(lds + L_TAG) = tag;
    {
      const unsigned x_off = (32 * th + r32) * 128;
      const int wrow = 8 * (g4 >> 1) + tq;
      bf16x8 fax[4], faw[4], fbx[4], fbw[4];
      f32x16 q = zero16;
#define PRD(B, i, P) _Pragma("unroll") for (int ks = 0; ks < 4; ++ks) { B##x[ks] = ldsf(lds + (i) * SK + x_off + (((2 * ks + hi) ^ xsw) << 4)); B##w[ks] = ldstr2(lds + (i) * SK + w_off + ks * 16 * (P), 4 * (P)); } SBAR()
#define PMM(B, SW) _Pragma("unroll") for (int ks = 0; ks < 4; ++ks) q = (SW) ? MFMA(B##x[ks], B##w[ks], q) : MFMA(B##w[ks], B##x[ks], q); PIN(q); SBAR()
#define PROJ(P, SW) do { VMW(6); BAR(); PRD(fa, 0, P); PRD(fb, 1, P); PMM(fa, SW); PRD(fa, 2, P); PMM(fb, SW); PRD(fb, 3, P); PMM(fa, SW); \
        VMW(0); BAR(); PRD(fa, 4, P); PMM(fb, SW); PRD(fb, 5, P); PMM(fa, SW); PRD(fa, 6, P); lds_signal(cBB); PMM(fb, SW); PMM(fa, SW); } while (0)
      if (j < 7) {
        const unsigned w_off = K_W + wrow * 128 + ((64 * chh + 32 * (g4 & 1) + 8 * tp) ^ ((tq & 2) << 5));
        PROJ(128, false);
        STAMP(1);
        const float qs = 0.18898223650461361f * 1.4426950408889634f;
#pragma unroll
        for (int g = 0; g < 4; ++g)
          __builtin_amdgcn_raw_buffer_store_b64((u32x2){pk(q[4 * g] * qs, q[4 * g + 1] * qs), pk(q[4 * g + 2] * qs, q[4 * g + 3] * qs)}, rs_ws, lane * 8 + g * 512,
                                                WS_Q + ((2 * c + th) * 112 + 16 * j + 8 * chh) * 256, SC1);
      } else {
        const unsigned w_off = K_W + chh * 3584 + wrow * 56 + (16 * (g4 & 1) + 4 * tp) * 2;
        if (chh) { PROJ(56, true);
#pragma unroll
          for (int r = 0; r < 16; ++r) q[r] = r32 < 28 ? q[r] : (r32 == 28 ? 1.f : 0.f);
        } else { PROJ(56, false); if (hi) { q[12] = 0.f; q[13] = 0.f; q[14] = 0.f; q[15] = 0.f; } }
        STAMP(1);
        const unsigned ko = WS_KV + (2 * c + th) * 4096 + chh * 2048;
        BST(U4(pack8(q, 0)), rs_ws, lo16, ko); BST(U4(pack8(q, 1)), rs_ws, lo16 + 1024, ko);
      }
      publish4(ws, WS_FP + (c * 8 + j) * 128, tag, lane, cPQ);
    }
    KV A0, A1, A2, A3;
    A3.v1 = (bf16x8){};
    f32x16 O[4] = {zero16, zero16, zero16, zero16};
    f32x16 Sa, Sb = zero16; u32x4 Pa, Pb = {};
    PIN(O[0]); PIN(O[1]); PIN(O[2]); PIN(O[3]); PIN(Sb); PIN(Pb); PIN(A3.v1);
    STAMP(2);
    poll(ws, WS_FP + (lane < 8 ? (wave * 8 + lane) * 8 + 7 : c * 8 + ((56 * j + (lane & 1) * 55) >> 6)) * 128, tag);
    if (HOLD == 4) lds_signal(cST);
    STAMP(3);
    const __amdgpu_buffer_rsrc_t rs_kv = __builtin_amdgcn_make_buffer_rsrc(ws + WS_KV + wave * 65536, 0, 65536, 0x00020000);
#define LDKV(B, T) do { const unsigned so_ = (((T) + loc) & 15) * 4096; \
                        B.k0 = BF(BLD(rs_kv, lo16, so_)); B.k1 = BF(BLD(rs_kv, lo16, so_ + 1024)); \
                        B.v0 = BF(BLD(rs_kv, lo16, so_ + 2048)); B.v1 = BF(BLD(rs_kv, lo16, so_ + 3072)); } while (0)
    __builtin_amdgcn_s_setprio(BPRIO);
    bf16x8 Q0[4], Q1[4];
    u32x2 qp[4][4];
#define LDQ(i) _Pragma("unroll") for (int u = 0; u < 4; ++u) qp[i][u] = __builtin_amdgcn_raw_buffer_load_b64(rs_ws, lane * 8 + u * 512, \
                 WS_Q + ((2 * c + ((wave ^ (3 - (i))) >> 1)) * 112 + 7 * (2 * j + ((wave ^ (3 - (i))) & 1))) * 256, SC1)
    LDQ(0); LDKV(A0, 0); LDQ(1); LDQ(2); LDQ(3); LDKV(A1, 1); LDKV(A2, 2);
    if (HOLD == 3) lds_signal(cST);
    SBAR();
#pragma unroll
    for (int i = 0; i < 4; ++i) { Q0[i] = BF(((u32x4){qp[i][0][0], qp[i][0][1], qp[i][1][0], qp[i][1][1]}));
      Q1[i] = BF(((u32x4){qp[i][2][0], qp[i][2][1], hi ? 0u : qp[i][3][0], hi ? 0u : qp[i][3][1]})); }
    Sa = MFMA(A0.k0, Q0[0], zero16); Sa = MFMA(A0.k1, Q1[0], Sa);
    STAMP(9);
#define UNIT(S, Sn, KN, QN, VP, OP, VC, OC, X) do { \
      Pb[2] = pk(Sn[12], Sn[13]); Pb[3] = pk(Sn[14], Sn[15]); \
      if ((QN) >= 0) Sn = MFMA(KN.k0, Q0[(QN) & 3], zero16); S[0] = EX(S[0]); S[1] = EX(S[1]); S[2] = EX(S[2]); S[3] = EX(S[3]); SBAR(); \
      if ((QN) >= 0) Sn = MFMA(KN.k1, Q1[(QN) & 3], Sn); Pa[0] = pk(S[0], S[1]); Pa[1] = pk(S[2], S[3]); S[4] = EX(S[4]); S[5] = EX(S[5]); S[6] = EX(S[6]); S[7] = EX(S[7]); SBAR(); \
      X; SBAR(); \
      OP = MFMA(VP.v1, BF(Pb), OP); Pa[2] = pk(S[4], S[5]); Pa[3] = pk(S[6], S[7]); S[8] = EX(S[8]); S[9] = EX(S[9]); S[10] = EX(S[10]); S[11] = EX(S[11]); SBAR(); \
      OC = MFMA(VC.v0, BF(Pa), OC); Pb[0] = pk(S[8], S[9]); Pb[1] = pk(S[10], S[11]); S[12] = EX(S[12]); S[13] = EX(S[13]); S[14] = EX(S[14]); S[15] = EX(S[15]); SBAR(); \
    } while (0)
#define TILE(CUR, NXT, PRV, T, LD) do { \
      UNIT(Sa, Sb, CUR, 1, PRV, O[3], CUR, O[0], ); \
      if (LD) LDKV(PRV, (T) + 3); SBAR(); \
      UNIT(Sb, Sa, CUR, 2, CUR, O[0], CUR, O[1], ); \
      UNIT(Sa, Sb, CUR, 3, CUR, O[1], CUR, O[2], ); \
      UNIT(Sb, Sa, NXT, 0, CUR, O[2], CUR, O[3], ); } while (0)
#pragma unroll 1
    for (int t = 0; t < 12; t += 4) {
      TILE(A0, A1, A3, t, 1); TILE(A1, A2, A0, t + 1, 1); TILE(A2, A3, A1, t + 2, 1); TILE(A3, A0, A2, t + 3, 1);
    }
    TILE(A0, A1, A3, 12, 1);
    const lptr oc = lds + L_OC + lo16;
#define OUTP(T, I) _Pragma("unroll") for (int q4 = 0; q4 < 4; ++q4) *(LAS f32x4*)(oc + ((wave ^ I) * 3 + I - 1) * 4096 + q4 * 1024) = (f32x4){T[4 * q4], T[4 * q4 + 1], T[4 * q4 + 2], T[4 * q4 + 3]}
    f32x4 pv[12];
    UNIT(Sa, Sb, A1, 1, A0, O[3], A1, O[0], ); UNIT(Sb, Sa, A1, 2, A1, O[0], A1, O[1], ); UNIT(Sa, Sb, A2, 0, A1, O[1], A1, O[2], );
    UNIT(Sb, Sa, A2, 1, A1, O[2], A2, O[0], ); UNIT(Sa, Sb, A2, 2, A2, O[0], A2, O[1], ); UNIT(Sb, Sa, A3, 0, A2, O[1], A2, O[2], );
    UNIT(Sa, Sb, A3, 1, A2, O[2], A3, O[0], ); UNIT(Sb, Sa, A3, 2, A3, O[0], A3, O[1], if (j < 7) lds_wait(cCR, 4));
    UNIT(Sa, Sb, A1, 3, A3, O[1], A3, O[2], OUTP(O[0], 3));
    UNIT(Sb, Sa, A2, 3, A3, O[2], A1, O[3], OUTP(O[1], 2));
    UNIT(Sa, Sb, A3, 3, A1, O[3], A2, O[3], OUTP(O[2], 1); lds_signal(cBB));
    UNIT(Sb, Sa, A3, -1, A2, O[3], A3, O[3], lds_wait(cBB, 8); _Pragma("unroll") for (int s2 = 0; s2 < 12; ++s2) pv[s2] = *(LAS f32x4*)(oc + wave * 12288 + s2 * 1024));
    Pb[2] = pk(Sb[12], Sb[13]); Pb[3] = pk(Sb[14], Sb[15]);
    O[3] = MFMA(A3.v1, BF(Pb), O[3]);
    STAMP(4);
    __builtin_amdgcn_s_setprio(2);
#pragma unroll
    for (int s2 = 0; s2 < 4; ++s2) pv[s2] = pv[s2] + pv[s2 + 4] + pv[s2 + 8];
    STAMP(10);
    unsigned lo16b = lo16; PIN(lo16b);
    const int lane = lo16b >> 4, r32 = lane & 31, hi = lane >> 5, g4 = lane >> 4, tq = (lane & 15) >> 2, tp = lane & 3;
    f32x16 o = O[3];
#pragma unroll
    for (int r = 0; r < 16; ++r) o[r] += pv[r >> 2][r & 3];
    {
      float l = swap_hi(o[12]);
      float am = 0.f;
#pragma unroll
      for (int r = 0; r < 16; r += 2) am = __builtin_fmaxf(__builtin_fmaxf(am, __builtin_fabsf(o[r])), __builtin_fabsf(o[r + 1]));
      const bool ok = (l >= 0x1p-64f) && (l <= 0x1p64f) && (am <= 0x1p120f);
      const bool slow = !wall(ok);
      if (slow) {
        const __amdgpu_buffer_rsrc_t rk = __builtin_amdgcn_make_buffer_rsrc(ws + WS_KV, 0, 262144, 0x00020000);
        poll(ws, WS_FP + (r32 * 8 + 7) * 128, tag);
        float m = -__builtin_inff();
#pragma unroll 1
        for (int t = 0; t < 64; ++t) {
          f32x16 s = MFMA(BF(BLD(rk, lo16, t * 4096)), Q0[3], zero16); s = MFMA(BF(BLD(rk, lo16 + 1024, t * 4096)), Q1[3], s);
#pragma unroll
          for (int r = 0; r < 16; ++r) m = __builtin_fmaxf(m, s[r]);
        }
        m = __builtin_fmaxf(swap_lo(m), swap_hi(m));
        o = zero16;
#pragma unroll 1
        for (int t = 0; t < 64; ++t) {
          f32x16 s = MFMA(BF(BLD(rk, lo16, t * 4096)), Q0[3], zero16); s = MFMA(BF(BLD(rk, lo16 + 1024, t * 4096)), Q1[3], s);
#pragma unroll
          for (int r = 0; r < 16; ++r) s[r] = EX(s[r] - m);
          o = MFMA(BF(BLD(rk, lo16 + 2048, t * 4096)), pack8(s, 0), o); o = MFMA(BF(BLD(rk, lo16 + 3072, t * 4096)), pack8(s, 1), o);
        }
        l = swap_hi(o[12]);
      }
      const float inv = __builtin_amdgcn_rcpf(l);
#pragma unroll
      for (int r = 0; r < 16; ++r) o[r] *= inv;
      if (hi) { o[12] = 0.f; o[13] = 0.f; o[14] = 0.f; o[15] = 0.f; }
      u32x4 f0 = U4(pack8(o, 0)), f1 = U4(pack8(o, 1));
      if (slow) {
#pragma unroll
        for (int k = 0; k < 4; ++k) { f0[k] = umin(f0[k], 0xfffffffeu); f1[k] = umin(f1[k], 0xfffffffeu); }
      }
      BST(f0, rs_ws, lo16, o_own); BST(f1, rs_ws, lo16 + 1024, o_own);
    }
    lds_signal(cOC);
    lds_signal(cGO + 4 * wave);
    STAMP(5);
    if (j == 7) { STAMP_OUT(); return; }
    {
      const CAS Params* kp = KARGS();
      const __amdgpu_buffer_rsrc_t rs_out = __builtin_amdgcn_make_buffer_rsrc(kp->out, 0, 2048 * 896 * 4, 0x00020000);
      lds_wait(cWO, 4);
      u32x4 of[16];
      const unsigned ob = WS_O + (((2 * c + th) * 16 + 8 * chh) * 2) * 1024;
#define LDO(k) of[k] = BLD(rs_ws, lo16 + ((k) & 3) * 1024, ob + ((k) >> 2) * 4096)
      const int rowb = 224 * chh + 4 * (g4 >> 1) + tq;
      const unsigned wb = (L_WO + rowb * 128 + ((32 * (g4 & 1) + 8 * tp) ^ ((tq & 2) << 5))) ^ (chh << 6);
      bf16x8 w0[16], w1[16];
#pragma unroll
      for (int k = 0; k < 16; ++k) w1[k] = ldstr2(lds + (wb ^ 64) + (28 * (k >> 1) + 16 * (k & 1)) * 128, 1024);
      SBAR();
      {
        unsigned have = 0;
#pragma unroll 1
        for (unsigned s = 0; s < SPIN_CAP; ++s) {
          const unsigned w = __builtin_amdgcn_raw_buffer_load_b32(rs_ws, (lane & 7) * 2048 + 2044, ob, SC1);
          const unsigned rdy = (s + 1 < SPIN_CAP ? (unsigned)__builtin_amdgcn_ballot_w64(w != ~0u) : ~0u) & 0xffu & ~have;
#pragma unroll
          for (int h = 0; h < 8; ++h) if (rdy >> h & 1) { LDO(2 * h); LDO(2 * h + 1); }
          have |= rdy;
          if (have == 0xffu) break;
          __builtin_amdgcn_s_sleep(OSLEEP);
        }
      }
      f32x16 acc0 = zero16, acc1 = zero16;
#define OBAD(h) (!wall(umax(umax(umax(umax(of[2 * (h)][0], of[2 * (h)][1]), umax(of[2 * (h)][2], of[2 * (h)][3])), umax(of[2 * (h) + 1][0], of[2 * (h) + 1][1])), umax(of[2 * (h) + 1][2], of[2 * (h) + 1][3])) != ~0u))
#define OHEAD(h) do { \
        if (OBAD(h)) { \
          _Pragma("unroll 1") for (unsigned s = 0; s < SPIN_CAP; ++s) { LDO(2 * h); LDO(2 * h + 1); if (!OBAD(h)) break; __builtin_amdgcn_s_sleep(OSLEEP); } \
        } \
        acc1 = MFMA(BF(of[2 * h]), w1[2 * h], acc1); acc1 = MFMA(BF(of[2 * h + 1]), w1[2 * h + 1], acc1); PIN(acc1); \
        w0[2 * h] = ldstr2(lds + wb + 28 * h * 128, 1024); w0[2 * h + 1] = ldstr2(lds + wb + (28 * h + 16) * 128, 1024); } while (0)
      OHEAD(0); STAMP(6); OHEAD(1); OHEAD(2); OHEAD(3); OHEAD(4); OHEAD(5); OHEAD(6); OHEAD(7);
      STAMP(11);
      lds_wait(cOC, 4);
#pragma unroll
      for (int k = 0; k < 4; ++k) acc0 = MFMA(BF(of[k]), w0[k], acc0);
      SBAR();
#pragma unroll
      for (int k = 0; k < 4; ++k) *(LAS f32x4*)(lds + L_OC + (wave ^ 1) * 4096 + k * 1024 + lo16) = (f32x4){acc1[4 * k], acc1[4 * k + 1], acc1[4 * k + 2], acc1[4 * k + 3]};
      SBAR();
#pragma unroll
      for (int k = 4; k < 8; ++k) acc0 = MFMA(BF(of[k]), w0[k], acc0);
      PIN(acc0);
      lds_signal(cPR + 4 * th);
#pragma unroll
      for (int k = 8; k < 16; ++k) acc0 = MFMA(BF(of[k]), w0[k], acc0);
      PIN(acc0);
      lds_wait(cPR + 4 * th, 2);
#pragma unroll
      for (int k = 0; k < 4; ++k) { const f32x4 v = *(LAS f32x4*)(lds + L_OC + wave * 4096 + k * 1024 + lo16);
        acc0[4 * k] += v[0]; acc0[4 * k + 1] += v[1]; acc0[4 * k + 2] += v[2]; acc0[4 * k + 3] += v[3]; }
      STAMP(12);
      const unsigned oo = ((64 * c + 32 * th + 4 * hi) * 896 + 448 + 64 * j + 32 * chh + r32) * 4;
#pragma unroll
      for (int r = 0; r < 16; ++r) __builtin_amdgcn_raw_buffer_store_b32(f2u(acc0[r]), rs_out, oo, ((r & 3) + 8 * (r >> 2)) * 3584, OAUX);
    }
    STAMP(7);
    STAMP_OUT();
  } else {
    if (j == 7) {
      VMW(6); BAR(); VMW(0); BAR();
      if (c == 0 && wa == 0) {
        LDEPOCH();
        __builtin_amdgcn_s_sleep(127);
#pragma unroll 1
        for (int k = 0; k < 4; ++k) poll(ws, WS_FP + (k * 64 + lane) * 128, tag);
        if (lane == 0) __hip_atomic_store((GAS u64*)(ws + WS_EPOCH), tag, RLX_AGENT);
      }
      STAMP_OUT();
      return;
    }
    VMW(6); BAR(); LDCH(L_SA); VMW(6); BAR();
    if (KILLA == 5) { lds_signal(cCR); lds_signal(cWO); STAMP(7); STAMP_OUT(); return; }
    cf lam, coef, l16, l64; f32x16 ur = zero16, ui = zero16;
    f32x16 acc[4] = {zero16, zero16, zero16, zero16};
    const unsigned cs0 = wa * 16384u, cs1 = L_R1 + wa * 12288u;
    unsigned a_htw = L_HT + wa * 4096 + r32 * 64 + hi * 32, a_htr = L_HT + wa * 4096 + (8 * (g4 >> 1) + tq) * 64 + (g4 & 1) * 32 + tp * 8;
    unsigned a_hst = WS_H + ((2 * c + th) * 56 + 4 * j + 2 * chh) * 1024 + lo16;
    unsigned a_c[4], a_cn[2];
#pragma unroll
    for (int f = 0; f < 4; ++f) a_c[f] = cs0 + (32 * chh + r32) * 128 + (((2 * f + hi) ^ xsw) << 4);
#pragma unroll
    for (int f = 0; f < 2; ++f) a_cn[f] = cs1 + 8192 + (32 * chh + r32) * 64 + (((2 * f + hi) ^ ((r32 >> 2) & 3)) << 4);
    unsigned a_vh = (lane >> 2) * 896 + (((lane & 3) ^ ((lane >> 4) & 3)) << 4) + 384 + chh * 64, a_vo = ve ^ 64;
    unsigned a_vw = lrow * 896 + (((lane & 7) ^ ((lrow & 2) << 1)) << 4);
    unsigned a_fl = lane < 32 ? WS_FH + (c * 8 + umin(lane & 7, 6)) * 128 : WS_FP + (c * 8 + (lane & 7)) * 128;
    unsigned a_el = (31 - umin(r32, c - 1)) * 512 + 4, a_eo = (32 - c + hi) * 512 + r32 * 16;
    {
      if (HOLD == 2) lds_wait_<1>(cPQ, 4);
      LDCH(0); LDCH(CH);
      { const f32x4 c0 = *(LAS f32x4*)(lds + L_EX + (32 * chh + r32) * 16), c1 = *(LAS f32x4*)(lds + L_EX + 1024 + (32 * chh + r32) * 16);
        lam = {c0[0], c0[1]}; coef = {c0[2], c0[3]}; l16 = {c1[0], c1[1]}; l64 = {c1[2], c1[3]}; ud = *(LAS unsigned*)(lds + L_EX + 2048 + (32 * chh + r32) * 4); }
      PIN(a_htw); PIN(a_htr); PIN(a_hst); PIN(a_c[0]); PIN(a_c[1]); PIN(a_c[2]); PIN(a_c[3]); PIN(a_cn[0]); PIN(a_cn[1]); PIN(a_vh); PIN(a_vw); PIN(a_fl); PIN(a_el); PIN(a_eo); PIN(a_vo);
      const int trow = 32 * th + 16 * ((r32 >> 2) & 1) + (r32 & 3) + 4 * (r32 >> 3);
      const unsigned xa_off = trow * 128, xa_sw = (trow >> 1) & 7, b_off = 8192 + (32 * chh + r32) * 128;
      bf16x8 gax[4], gar[4], gai[4], gbx[4], gbr[4], gbi[4];
#define GRD(B, s_) _Pragma("unroll") for (int ks = 0; ks < 4; ++ks) { B##x[ks] = ldsf(lds + (s_) + xa_off + (((2 * ks + hi) ^ xa_sw) << 4)); \
        B##r[ks] = ldsf(lds + (s_) + b_off + (((2 * ks + hi) ^ xsw) << 4)); B##i[ks] = ldsf(lds + (s_) + b_off + 8192 + (((2 * ks + hi) ^ xsw) << 4)); } SBAR()
#define GMM(B) _Pragma("unroll") for (int ks = 0; ks < 4; ++ks) { ur = MFMA(B##x[ks], B##r[ks], ur); ui = MFMA(B##x[ks], B##i[ks], ui); } PIN(ur); PIN(ui); SBAR()
#define GAP() do { SBAR(); asm volatile("s_nop %0" :: "i"(GNOP)); SBAR(); } while (0)
#define GMS(B) _Pragma("unroll") for (int ks = 0; ks < 4; ++ks) { ur = MFMA(B##x[ks], B##r[ks], ur); GAP(); ui = MFMA(B##x[ks], B##i[ks], ui); GAP(); } PIN(ur); PIN(ui); SBAR()
      if (PPOS == 0) __builtin_amdgcn_s_setprio(APRIO);
      STAMP(12);
      if (HOLD == 0 || HOLD == 2) {
        lds_wait_<1>(cBB, 4);
        LDCH(2 * CH); LDCH(3 * CH); LDCH(4 * CH);
        VMW(18); lds_barrierA(cAB, 4); GRD(ga, L_SA); GRD(gb, 0); GMM(ga); GRD(ga, CH); GMM(gb);
      } else {
        if (HOLD == 1) { lds_wait_<1>(cPQ, 4); LDCH(2 * CH); LDCH(3 * CH); LDCH(4 * CH); VMW(18); } else VMW(0);
        lds_barrierA(cAB, 4); GRD(ga, L_SA); GRD(gb, 0); GMM(ga); GRD(ga, CH); GMM(gb);
        if (HOLD >= 3) { lds_wait_<1>(cST, 4); LDCH(2 * CH); LDCH(3 * CH); LDCH(4 * CH); }
      }
      VMW(6); lds_barrierA(cAB, 8);
      STAMP(13);
      if (PPOS == 1) __builtin_amdgcn_s_setprio(APRIO);
      GRD(gb, 2 * CH); LDCH(L_SA); GMM(ga); GRD(ga, 3 * CH); GMS(gb);
      VMW(6); lds_barrierA(cAB, 12);
      STAMP(14);
      GRD(gb, 4 * CH); GMS(ga);
      VMW(0); lds_barrierA(cAB, 16);
      STAMP(15);
      GRD(ga, L_SA); GMS(gb); GMS(ga);
      { const u32x2 t2 = *(LAS u32x2*)(lds + L_TAG);
        tag = ((u64)(unsigned)__builtin_amdgcn_readfirstlane(t2[1]) << 32) | (unsigned)__builtin_amdgcn_readfirstlane(t2[0]); }
    }
    {
      const int e = r32 - 8 * hi; const unsigned dv = ud << ((e & 1) * 16);
      const lptr xr = lds + L_SA + (32 * th + r32) * 128;
#pragma unroll
      for (int s2 = 0; s2 < 2; ++s2) {
        const u32x4 dg = {(e >> 1) == 8 * s2 ? dv : 0u, (e >> 1) == 8 * s2 + 1 ? dv : 0u, (e >> 1) == 8 * s2 + 2 ? dv : 0u, (e >> 1) == 8 * s2 + 3 ? dv : 0u};
        acc[0] = MFMA(ldsf(xr + (((4 * chh + 2 * s2 + hi) ^ xsw) << 4)), BF(dg), acc[0]);
      }
      PIN(acc[0]);
    }
    STAMP(1);
    if (KILLA == 1) { VMW(0); lds_signal(cCR); lds_signal(cWO); STAMP(7); STAMP_OUT(); return; }
    __builtin_amdgcn_s_setprio(EPRIO);
    cf e = {0.f, 0.f};
#pragma unroll
    for (int r = 0; r < 16; ++r) {
      const float a = coef.r * ur[r] - coef.i * ui[r], b2 = -(coef.r * ui[r] + coef.i * ur[r]);
      ur[r] = a; ui[r] = b2; e = r ? cfma(lam, e, (cf){a, b2}) : (cf){a, b2};
    }
    const int seg = 2 * th + hi;
    *(LAS f32x2*)(lds + L_EX + (seg * 64 + 32 * chh + r32) * 8) = (f32x2){e.r, e.i};
    lds_barrierA(cAB, 20);
    cf g0 = {0.f, 0.f}, lseg = {1.f, 0.f};
    {
      cf E = {0.f, 0.f};
#pragma unroll
      for (int s = 0; s < 4; ++s) {
        const f32x2 v = *(LAS f32x2*)(lds + L_EX + (s * 64 + 32 * chh + r32) * 8);
        if (s == seg) g0 = E;
        if (s < seg) lseg = cmul(lseg, l16);
        E = cfma(l16, E, (cf){v[0], v[1]});
      }
      if (th == 0 && hi == 0 && c < 31)
        BST(((u32x4){f2u(E.r), (unsigned)tag, f2u(E.i), (unsigned)tag}), rs_ws, r32 * 16, WS_E + ((2 * j + chh) * 32 + 31 - c) * 512);
    }
    __builtin_amdgcn_s_setprio(0);
    const CAS Params* kp = KARGS();
    {
      const u32x4 rc = mkrs((const char*)(th ? kp->cim : kp->cre) + (size_t)j * 57344, 57344);
      int n2 = 6 * cl;
#pragma unroll
      for (int n = 0; n < 24; ++n) {
        DMA(rc, (n & 1) ? a_vo : ve, (n2 & 7) * 7168 + (n2 >> 3) * 128 + chh * 512, (n2 < 16 ? cs0 : cs1 - 16384) + n2 * 1024, 0); if (n >= CPACE) VMW(CPACE);
        n2 = n2 == 23 ? 0 : n2 + 1; }
#pragma unroll
      for (int n = 0; n < 4; ++n) DMA(rc, a_vh, n * 14336, cs1, 8192 + n * 1024);
    }
    if (KILLA == 2) { VMW(0); lds_signal(cCR); lds_signal(cWO); STAMP(7); STAMP_OUT(); return; }
    STAMP(2);
    cf G = g0;
    if (c > 0) {
      const cf l128 = cmul(l64, l64);
      const __amdgpu_buffer_rsrc_t re = __builtin_amdgcn_make_buffer_rsrc(ws + WS_E + (2 * j + chh) * 16384, 0, 16384, 0x00020000);
      const unsigned eo = a_eo, want = (c - hi + 1) >> 1;
      u32x4 ev[16];
#pragma unroll 1
      for (unsigned s = 0; s < SPIN_CAP; ++s) {
        const unsigned t1 = __builtin_amdgcn_raw_buffer_load_b32(re, a_el, 0, SC1);
        if (wall(t1 == (unsigned)tag)) break;
        __builtin_amdgcn_s_sleep(PSLEEP);
      }
      MEMBAR();
#pragma unroll 1
      for (unsigned s = 0; s < SPIN_CAP; ++s) {
        unsigned got = 0;
#pragma unroll
        for (int m = 0; m < 8; ++m) ev[m] = BLD(re, eo, m * 1024);
        if (c > 16) {
#pragma unroll
          for (int m = 8; m < 16; ++m) ev[m] = BLD(re, eo, m * 1024);
        }
        VMW(0);
        __builtin_amdgcn_s_setprio(HPRIO);
        if (c > 16) {
#pragma unroll
          for (int m = 8; m < 16; ++m) got += ((ev[m][1] ^ (unsigned)tag) | (ev[m][3] ^ (unsigned)tag)) == 0;
        }
#pragma unroll
        for (int m = 0; m < 8; ++m) got += ((ev[m][1] ^ (unsigned)tag) | (ev[m][3] ^ (unsigned)tag)) == 0;
        if (wall(got == want)) break;
        __builtin_amdgcn_s_sleep(4);
      }
      STAMP(3);
      cf H = {0.f, 0.f};
      if (c > 16) {
#pragma unroll
        for (int m = 15; m >= 8; --m) H = cfma(l128, H, (cf){u2f(ev[m][0]), u2f(ev[m][2])});
      }
#pragma unroll
      for (int m = 7; m >= 0; --m) H = cfma(l128, H, (cf){u2f(ev[m][0]), u2f(ev[m][2])});
      H = cfma(l64, (cf){swap_hi(H.r), swap_hi(H.i)}, (cf){swap_lo(H.r), swap_lo(H.i)});
      G = cfma(lseg, H, g0);
    }
    STAMP(4);
    __builtin_amdgcn_s_setprio(P2PRIO);
    {
#pragma unroll
      for (int r = 0; r < 16; ++r) { G = cfma(lam, G, (cf){ur[r], ui[r]}); ur[r] = G.r; ui[r] = G.i; }
      *(LAS bf16x8*)(lds + a_htw) = pack8(ur, 0); *(LAS bf16x8*)(lds + a_htw + 16) = pack8(ur, 1);
      *(LAS bf16x8*)(lds + a_htw + 2048) = pack8(ui, 0); *(LAS bf16x8*)(lds + a_htw + 2064) = pack8(ui, 1);
      unsigned a_hs2 = a_hst + 28 * 1024; PIN(a_hs2);
      __builtin_amdgcn_s_setprio(0);
      asm volatile("s_waitcnt lgkmcnt(0)" ::: "memory");
#pragma unroll
      for (int k = 0; k < 4; ++k) BST(U4(ldstr2(lds + a_htr + (k >> 1) * 2048 + (k & 1) * 1024, 256)), rs_ws, (k >> 1) ? a_hs2 : a_hst, (k & 1) * 1024);
      publish4(ws, WS_FH + (c * 8 + j) * 128, tag, lane, cPB);
    }
    if (KILLA == 3) { VMW(0); lds_signal(cCR); lds_signal(cWO); STAMP(7); STAMP_OUT(); return; }
    STAMP(5);
    bf16x8 cfr[14][2];
    const int cflip = chh ? -1 : 1;
#define LDC(f) do { if ((f) < 12) { const unsigned a_ = a_c[(f) & 3] + ((f) < 8 ? ((f) >> 2) * 8192 : cs1 - cs0); cfr[f][0] = ldsf(lds + a_); cfr[f][1] = ldsf(lds + a_ + cflip * 4096); } \
                    else { cfr[f][0] = ldsf(lds + a_cn[(f) & 1]); cfr[f][1] = ldsf(lds + a_cn[(f) & 1] + cflip * 2048); } } while (0)
#pragma unroll
    for (int f = 0; f < 8; ++f) LDC(f);
    lds_signal(cCR);
    STAMP(10);
    lds_wait_<ASLEEP>(cCR, 4);
    {
      const u32x4 rw = mkrs((const char*)kp->wo + j * 128 + wa * 7168, 401408 - j * 128 - wa * 7168);
      int m2 = 4 * cl;
#pragma unroll
      for (int m = 0; m < 14; ++m) { DMA(rw, a_vw, m2 * 28672, wa * 1024 + m2 * 4096, L_WO); m2 = m2 == 13 ? 0 : m2 + 1; }
      if (wa == 0 && lane < 32) *(LAS u32x4*)(lds + L_WO + 57344 + lo16) = (u32x4){0, 0, 0, 0};
    }
    poll(ws, a_fl, tag);
    VMW(0);
    lds_signal(cWO);
    if (KILLA == 4) { STAMP(7); STAMP_OUT(); return; }
    STAMP(6);
    {
      const unsigned hb = WS_H + (2 * c * 56 + 14 * wa) * 1024, h0 = hb + th * 57344, h1 = hb + (th ^ 1) * 57344;
      const unsigned fa = chh * 2048, fb = chh ? 0u - 12288u : 0u;
      u32x4 hf[14][2];
#define LDH(f) do { hf[f][0] = BLD(rs_ws, lo16, h0 + (f) * 1024 + ((f) < 12 ? fa : fb)); hf[f][1] = BLD(rs_ws, lo16, h1 + (f) * 1024 + ((f) < 12 ? fa : fb)); } while (0)
#define MMH(f) do { acc[0] = MFMA(BF(hf[f][0]), cfr[f][0], acc[0]); acc[1] = MFMA(BF(hf[f][0]), cfr[f][1], acc[1]); \
                    acc[2] = MFMA(BF(hf[f][1]), cfr[f][0], acc[2]); acc[3] = MFMA(BF(hf[f][1]), cfr[f][1], acc[3]); \
                    PIN(acc[0]); PIN(acc[1]); PIN(acc[2]); PIN(acc[3]); } while (0)
#pragma unroll
      for (int f = 0; f < HPRE; ++f) LDH(f);
      SBAR();
      lds_wait_<GOSLEEP>(cGO + 4 * wa, 1);
      STAMP(9);
      __builtin_amdgcn_s_setprio(GPRIO);
#pragma unroll
      for (int f = 0; f < 8; ++f) { MMH(f); SBAR(); if (f + HPRE < 14) LDH(f + HPRE); if (f >= 1 && f < 7) LDC(f + 7); SBAR(); }
      lds_signal(cP4);
#pragma unroll
      for (int f = 8; f < 14; ++f) MMH(f);
    }
    lds_wait(cP4, 4);
    STAMP(11);
    rs_write(lds + L_G2, wa, lane, acc[1], acc[2], acc[3]);
    lds_barrierA(cAB, 24);
    f32x16 y = rs_read(lds + L_G2, wa, lane, acc[0]);
    const __amdgpu_buffer_rsrc_t rs_out = __builtin_amdgcn_make_buffer_rsrc(kp->out, 0, 2048 * 896 * 4, 0x00020000);
    const unsigned oo = ((64 * c + 32 * th + 4 * hi) * 896 + 64 * j + 32 * chh + r32) * 4;
#pragma unroll
    for (int r = 0; r < 16; ++r) __builtin_amdgcn_raw_buffer_store_b32(f2u(y[r]), rs_out, oo, ((r & 3) + 8 * (r >> 2)) * 3584, OAUX);
    STAMP(7);
    STAMP_OUT();
  }
}

extern "C" void kernel_launch(void* const* d_in, const int* in_sizes, int n_in, void* d_out, int out_size, void* d_ws, size_t ws_size,
                              hipStream_t stream) {
  Params p;
  p.xa = (const u16*)d_in[0]; p.lre = (const u16*)d_in[1]; p.lim = (const u16*)d_in[2]; p.bre = (const u16*)d_in[3]; p.bim = (const u16*)d_in[4];
  p.cre = (const u16*)d_in[5]; p.cim = (const u16*)d_in[6]; p.ldt = (const u16*)d_in[7]; p.dsk = (const u16*)d_in[8];
  p.xb = (const u16*)d_in[9]; p.wq = (const u16*)d_in[10]; p.wk = (const u16*)d_in[11]; p.wv = (const u16*)d_in[12]; p.wo = (const u16*)d_in[13];
  p.out = (float*)d_out; p.ws = (char*)d_ws;
  fused_kernel<<<256, 512, 0, stream>>>(p);
}
```

```cpp
#include <hip/hip_runtime.h>
#include <stdint.h>

#define DI __device__ __forceinline__
#define GAS __attribute__((address_space(1)))
#define LAS __attribute__((address_space(3)))
#define CAS __attribute__((address_space(4)))
typedef short bf16x8 __attribute__((ext_vector_type(8)));
typedef short s16x4 __attribute__((ext_vector_type(4)));
typedef float f32x16 __attribute__((ext_vector_type(16)));
typedef float f32x4 __attribute__((ext_vector_type(4)));
typedef float f32x2 __attribute__((ext_vector_type(2)));
typedef unsigned u32x4 __attribute__((ext_vector_type(4)));
typedef unsigned u32x2 __attribute__((ext_vector_type(2)));
typedef __bf16 bf16x2_t __attribute__((ext_vector_type(2)));
typedef unsigned long long u64;
typedef unsigned short u16;
typedef LAS char* lptr;
#define RLX_AGENT __ATOMIC_RELAXED, __HIP_MEMORY_SCOPE_AGENT
#define MFMA(a, b, c) __builtin_amdgcn_mfma_f32_32x32x16_bf16((a), (b), (c), 0, 0, 0)
#define SBAR() __builtin_amdgcn_sched_barrier(0)
#define PIN(x) asm volatile("" : "+v"(x))
#define MEMBAR() asm volatile("" ::: "memory")
#define SC1 16

constexpr unsigned WS_EPOCH = 0, WS_FH = 8192, WS_E = 0x10000, WS_FP = 0x48000,
                   WS_KV = 0x50000, WS_O = 0x100000, WS_H = 0x300000, WS_Q = 0x680000, WS_END = 0x880000, WS_DBG = 0x900000;
constexpr unsigned SK = 16384, K_W = 8192;
constexpr unsigned CH = 24576, L_SA = 135168;
constexpr unsigned L_R1 = 107520, L_HT = 65536, L_WO = 0, L_OC = 57856, L_G2 = 107520, L_MISC = 159744, L_EX = L_MISC + 128, L_TOTAL = 163840;
constexpr unsigned cBB = L_MISC, cAB = L_MISC + 4, cCR = L_MISC + 8, cWO = L_MISC + 12, cOC = L_MISC + 16, cPB = L_MISC + 20, cPR = L_MISC + 24, cP4 = L_MISC + 32, cPQ = L_MISC + 52, cGO = L_MISC + 36, L_TAG = L_MISC + 64;
constexpr unsigned SPIN_CAP = 1u << 18;

struct Params {
  const u16 *xb, *wq, *wk, *wv; char* ws; const u16 *lre, *lim, *ldt, *dsk, *xa, *bre, *bim, *cre, *cim, *wo;
  float* out;
};

DI float u2f(unsigned v) { return __builtin_bit_cast(float, v); }
DI unsigned f2u(float v) { return __builtin_bit_cast(unsigned, v); }
DI unsigned umin(unsigned a, unsigned b) { return a < b ? a : b; }
DI unsigned umax(unsigned a, unsigned b) { return a > b ? a : b; }
DI bool wall(bool x) { return __builtin_amdgcn_ballot_w64(!x) == 0; }
DI float bf2f(unsigned v) { return u2f(v << 16); }
DI unsigned pk(float a, float b) { f32x2 v = {a, b}; return __builtin_bit_cast(unsigned, __builtin_convertvector(v, bf16x2_t)); }
DI float rbf(float a) { return u2f(pk(0.f, a)); }
DI bf16x8 pack8(const f32x16& x, int s) {
  u32x4 p = {pk(x[8 * s], x[8 * s + 1]), pk(x[8 * s + 2], x[8 * s + 3]), pk(x[8 * s + 4], x[8 * s + 5]), pk(x[8 * s + 6], x[8 * s + 7])};
  return __builtin_bit_cast(bf16x8, p);
}
DI u32x4 mkrs(const void* base, unsigned bytes) { const u64 a = (u64)base; return (u32x4){(unsigned)a, (unsigned)(a >> 32) & 0xffffu, bytes, 0x20000u}; }
#define DMA(rs, voff, soff, ldsb, IMM) asm volatile("s_add_u32 m0, %3, %4\n\ts_nop 0\n\tbuffer_load_dwordx4 %0, %1, %2 offen lds" \
                                                    :: "v"(voff), "s"(rs), "s"(soff), "s"(ldsb), "i"(IMM) : "memory", "scc")
DI bf16x8 ldsf(lptr p) { return *(LAS bf16x8*)p; }
DI s16x4 ldstr(lptr p) { return __builtin_amdgcn_ds_read_tr16_b64_v4i16((LAS s16x4*)p); }
DI bf16x8 ldstr2(lptr p, int second) { s16x4 lo = ldstr(p), hi = ldstr(p + second); return __builtin_shufflevector(lo, hi, 0, 1, 2, 3, 4, 5, 6, 7); }
DI void lds_signal(unsigned c) {
  asm volatile("s_waitcnt lgkmcnt(0)\n\ts_mov_b64 exec, 1\n\tds_add_u32 %0, %1\n\ts_mov_b64 exec, -1" :: "v"(c), "v"(1u) : "memory");
}
template <int SLEEP> DI void lds_wait_(unsigned c, unsigned target) {
  unsigned v, s, n;
  asm volatile("s_mov_b32 %2, 0x400000\n1:\n\tds_read_b32 %0, %3\n\ts_waitcnt lgkmcnt(0)\n\tv_readfirstlane_b32 %1, %0\n\ts_cmp_ge_u32 %1, %4\n\ts_cbranch_scc1 2f\n\t"
               "s_sleep %5\n\ts_sub_u32 %2, %2, 1\n\ts_cbranch_scc0 1b\n2:" : "=&v"(v), "=&s"(s), "=&s"(n) : "v"(c), "i"(target), "i"(SLEEP) : "memory", "scc");
}
#define lds_wait(c, t) lds_wait_<0>(c, t)
#define lds_barrier(c, t) do { lds_signal(c); lds_wait(c, t); } while (0)
#define lds_barrierA(c, t) do { lds_signal(c); lds_wait_<1>(c, t); } while (0)
DI void poll(const char* ws, unsigned off, u64 tag) {
#pragma unroll 1
  for (unsigned s = 0; s < SPIN_CAP; ++s) {
    u64 v = __hip_atomic_load((GAS u64*)(ws + off), RLX_AGENT);
    if (wall(v == tag)) break;
    __builtin_amdgcn_s_sleep(2);
  }
  MEMBAR();
}
DI void publish4(char* ws, unsigned off, u64 tag, int lane, unsigned cnt) {
  unsigned old;
  asm volatile("s_waitcnt vmcnt(0)\n\ts_mov_b64 exec, 1\n\tds_add_rtn_u32 %0, %1, %2\n\ts_waitcnt lgkmcnt(0)\n\ts_mov_b64 exec, -1" : "=&v"(old) : "v"(cnt), "v"(1u) : "memory");
  if (__builtin_amdgcn_readfirstlane(old) == 3 && lane == 0) __hip_atomic_store((GAS u64*)(ws + off), tag, RLX_AGENT);
}
DI float swap_lo(float x) {
  auto r = __builtin_amdgcn_permlane32_swap(f2u(x), f2u(x), false, false); return u2f(r[0]); }
DI float swap_hi(float x) {
  auto r = __builtin_amdgcn_permlane32_swap(f2u(x), f2u(x), false, false); return u2f(r[1]); }
struct cf { float r, i; };
DI cf cmul(cf a, cf b) { return {a.r * b.r - a.i * b.i, a.r * b.i + a.i * b.r}; }
DI cf cfma(cf a, cf b, cf c) { return {__builtin_fmaf(a.r, b.r, __builtin_fmaf(-a.i, b.i, c.r)), __builtin_fmaf(a.r, b.i, __builtin_fmaf(a.i, b.r, c.i))}; }

#define STAMP(k) do {} while (0)
#define STAMP_OUT() do {} while (0)

struct KV { bf16x8 k0, k1, v0, v1; };

DI void rs_write(lptr base, int w, int lane, const f32x16& t1, const f32x16& t2, const f32x16& t3) {
#define RSW(T, I) _Pragma("unroll") for (int q = 0; q < 4; ++q) *(LAS f32x4*)(base + ((w ^ I) * 3 + I - 1) * 4096 + q * 1024 + lane * 16) = (f32x4){T[4 * q], T[4 * q + 1], T[4 * q + 2], T[4 * q + 3]}
  RSW(t1, 1); RSW(t2, 2); RSW(t3, 3);
}
DI f32x16 rs_read(lptr base, int w, int lane, f32x16 a) {
  f32x4 v[12];
#pragma unroll
  for (int s = 0; s < 12; ++s) v[s] = *(LAS f32x4*)(base + w * 12288 + s * 1024 + lane * 16);
  SBAR();
#pragma unroll
  for (int s = 0; s < 12; ++s) { const int q = s & 3; a[4 * q] += v[s][0]; a[4 * q + 1] += v[s][1]; a[4 * q + 2] += v[s][2]; a[4 * q + 3] += v[s][3]; }
  return a;
}

#define VMW_(n) asm volatile("s_waitcnt vmcnt(" #n ")" ::: "memory")
#define VMW(n) VMW_(n)
#define BAR() asm volatile("s_waitcnt lgkmcnt(0)\n\ts_barrier" ::: "memory")
#define BLD(R, V, S) __builtin_amdgcn_raw_buffer_load_b128(R, V, S, SC1)
#define BST(D, R, V, S) __builtin_amdgcn_raw_buffer_store_b128(D, R, (V) + (S), 0, SC1)
#define BF(x) __builtin_bit_cast(bf16x8, x)
#define U4(x) __builtin_bit_cast(u32x4, x)
#define EX(x) __builtin_amdgcn_exp2f(x)
#define KARGS() ({ const CAS Params* kp_ = (const CAS Params*)__builtin_amdgcn_kernarg_segment_ptr(); asm volatile("" : "+s"(kp_)); kp_; })
#define NEXTK() do { k += step; k = k >= 7 ? k - 7 : k; } while (0)

#if defined(__HIP_DEVICE_COMPILE__)
#define NO_PK __attribute__((target("no-packed-fp32-ops")))
#else
#define NO_PK
#endif
__global__ void __launch_bounds__(512, 2) NO_PK fused_kernel(Params p) {
  __shared__ __attribute__((aligned(128))) char smem[L_TOTAL];
  const lptr lds = (lptr)smem;
  const int tid = threadIdx.x, lane = tid & 63, wave = __builtin_amdgcn_readfirstlane(tid >> 6);
  const int b = blockIdx.x, loc = b >> 3, cl = loc >> 3, c = (b & 7) * 4 + cl, j = loc & 7;
  const int lrow = lane >> 3;
  const unsigned ve = lrow * 896 + (((lane & 7) ^ (lrow >> 1)) << 4);
  const int step = cl + 1, k0 = j == 7 ? 0 : j;
  const bool isB = wave < 4;
  const int wa = wave & 3;
  char* const ws = p.ws;
  STAMP(0);
  unsigned ua = 0, ub = 0, uc = 0, ud = 0;
  u32x4 rxa, rbr, rbi; int ka = k0;
  const unsigned vo = (ve ^ 64) + 7168, lba = wa * 2048;
#define NEXTKA() do { ka += step; ka = ka >= 7 ? ka - 7 : ka; } while (0)
#define LDCH(slot) do { NEXTKA(); const unsigned k_ = ka * 128; DMA(rxa, ve, k_, lba, (slot)); DMA(rxa, vo, k_, lba, (slot) + 1024); \
      DMA(rbr, ve, k_, lba, (slot) + 8192); DMA(rbr, vo, k_, lba, (slot) + 9216); DMA(rbi, ve, k_, lba, (slot) + 16384); DMA(rbi, vo, k_, lba, (slot) + 17408); } while (0)
  {
    const u32x4 rx = mkrs((const char*)p.xb + (size_t)c * 57344 + wave * 7168, 7168);
    const unsigned vx = ve ^ ((wave & 1) << 6), lb = wave * 1024;
    int k = k0;
    if (j == 7) {
      const u32x4 rw = mkrs(isB ? p.wk : p.wv, 25088);
      const unsigned vw = wa * 1024 + lane * 16, lw = K_W + (isB ? 0 : 3584) + wa * 1024; const bool half = wa < 3 || lane < 32;
#pragma unroll
      for (int i = 0; i < 7; ++i) { NEXTK(); DMA(rx, vx, k * 128, lb, i * SK); if (half) DMA(rw, vw, k * 3584, lw, i * SK); }
    } else {
      const u32x4 rw = mkrs((const char*)p.wq + j * 128 + wave * 7168, 401408 - j * 128 - wave * 7168);
      const unsigned vw = lrow * 896 + (((lane & 7) ^ ((lrow & 2) << 1)) << 4);
      if (wave == 0) { const unsigned co = (64 * j + lane) * 2;
        asm volatile("global_load_ushort %0, %4, %5\n\tglobal_load_ushort %1, %4, %6\n\tglobal_load_ushort %2, %4, %7\n\tglobal_load_ushort %3, %4, %8"
                     : "=&v"(ua), "=&v"(ub), "=&v"(uc), "=&v"(ud) : "v"(co), "s"(p.lre), "s"(p.lim), "s"(p.ldt), "s"(p.dsk) : "memory"); }
#pragma unroll
      for (int i = 0; i < 7; ++i) { NEXTK(); DMA(rx, vx, k * 128, lb, i * SK); DMA(rw, vw, k * 57344, lb, i * SK + K_W); }
      rxa = mkrs((const char*)p.xa + (size_t)c * 57344 + wa * 14336, 14336);
      rbr = mkrs((const char*)p.bre + (size_t)j * 57344 + wa * 14336, 14336);
      rbi = mkrs((const char*)p.bim + (size_t)j * 57344 + wa * 14336, 14336);
    }
  }
  STAMP(8);
  if (tid < 16) ((LAS unsigned*)(lds + L_MISC))[tid] = 0;
  const int r32 = lane & 31, hi = lane >> 5;
  const int g4 = lane >> 4, tq = (lane & 15) >> 2, tp = lane & 3;
  const unsigned xsw = (r32 >> 1) & 7, lo16 = lane * 16;
  const int th = wa >> 1, chh = wa & 1;
  u64 tag;
  const __amdgpu_buffer_rsrc_t rs_ws = __builtin_amdgcn_make_buffer_rsrc(ws, 0, WS_END, 0x00020000);
  const f32x16 zero16 = {};
  const unsigned o_own = WS_O + (((2 * c + th) * 16 + 2 * j + chh) * 2) * 1024;
#define LDEPOCH() asm volatile("s_load_dwordx2 %0, %1, 0x0\n\ts_waitcnt lgkmcnt(0)" : "=s"(tag) : "s"(ws) : "memory"); tag = (tag | 1) + 2

  if (isB) {
    if (wave == 0 && j < 7) {
      asm volatile("s_waitcnt vmcnt(14)" : "+v"(ua), "+v"(ub), "+v"(uc), "+v"(ud) :: "memory");
      const float pa = bf2f(ua), pb = bf2f(ub), dt = rbf(expf(bf2f(uc)));
      const float er = expf(dt * pa); float sn, cs; sincosf(dt * pb, &sn, &cs);
      const float nr = er * cs - 1.f, ni = er * sn, den = pa * pa + pb * pb;
      const cf lam = {er * cs, -ni};
      cf l16 = cmul(lam, lam); l16 = cmul(l16, l16); l16 = cmul(l16, l16); l16 = cmul(l16, l16);
      cf l64 = cmul(l16, l16); l64 = cmul(l64, l64);
      *(LAS f32x4*)(lds + L_EX + lo16) = (f32x4){lam.r, lam.i, 2.f * (nr * pa + ni * pb) / den, 2.f * (ni * pa - nr * pb) / den};
      *(LAS f32x4*)(lds + L_EX + 1024 + lo16) = (f32x4){l16.r, l16.i, l64.r, l64.i};
      *(LAS unsigned*)(lds + L_EX + 2048 + lane * 4) = ud;
    }
    { const u32x4 ones = {~0u, ~0u, ~0u, ~0u};
      BST(ones, rs_ws, lo16, o_own); BST(ones, rs_ws, lo16 + 1024, o_own); }
    LDEPOCH();
    if (tid == 0) *(LAS u64*)(lds + L_TAG) = tag;
    {
      const unsigned x_off = (32 * th + r32) * 128;
      const int wrow = 8 * (g4 >> 1) + tq;
      bf16x8 fax[4], faw[4], fbx[4], fbw[4];
      f32x16 q = zero16;
#define PRD(B, i, P) _Pragma("unroll") for (int ks = 0; ks < 4; ++ks) { B##x[ks] = ldsf(lds + (i) * SK + x_off + (((2 * ks + hi) ^ xsw) << 4)); B##w[ks] = ldstr2(lds + (i) * SK + w_off + ks * 16 * (P), 4 * (P)); } SBAR()
#define PMM(B, SW) _Pragma("unroll") for (int ks = 0; ks < 4; ++ks) q = (SW) ? MFMA(B##x[ks], B##w[ks], q) : MFMA(B##w[ks], B##x[ks], q); PIN(q); SBAR()
#define PROJ(P, SW) do { VMW(6); BAR(); PRD(fa, 0, P); PRD(fb, 1, P); PMM(fa, SW); PRD(fa, 2, P); PMM(fb, SW); PRD(fb, 3, P); PMM(fa, SW); \
        VMW(0); BAR(); PRD(fa, 4, P); PMM(fb, SW); PRD(fb, 5, P); PMM(fa, SW); PRD(fa, 6, P); PMM(fb, SW); PMM(fa, SW); } while (0)
      if (j < 7) {
        const unsigned w_off = K_W + wrow * 128 + ((64 * chh + 32 * (g4 & 1) + 8 * tp) ^ ((tq & 2) << 5));
        PROJ(128, false);
        STAMP(1);
        const float qs = 0.18898223650461361f * 1.4426950408889634f;
#pragma unroll
        for (int g = 0; g < 4; ++g)
          __builtin_amdgcn_raw_buffer_store_b64((u32x2){pk(q[4 * g] * qs, q[4 * g + 1] * qs), pk(q[4 * g + 2] * qs, q[4 * g + 3] * qs)}, rs_ws, lane * 8 + g * 512,
                                                WS_Q + ((2 * c + th) * 112 + 16 * j + 8 * chh) * 256, SC1);
      } else {
        const unsigned w_off = K_W + chh * 3584 + wrow * 56 + (16 * (g4 & 1) + 4 * tp) * 2;
        if (chh) { PROJ(56, true);
#pragma unroll
          for (int r = 0; r < 16; ++r) q[r] = r32 < 28 ? q[r] : (r32 == 28 ? 1.f : 0.f);
        } else { PROJ(56, false); if (hi) { q[12] = 0.f; q[13] = 0.f; q[14] = 0.f; q[15] = 0.f; } }
        STAMP(1);
        const unsigned ko = WS_KV + (2 * c + th) * 4096 + chh * 2048;
        BST(U4(pack8(q, 0)), rs_ws, lo16, ko); BST(U4(pack8(q, 1)), rs_ws, lo16 + 1024, ko);
      }
      publish4(ws, WS_FP + (c * 8 + j) * 128, tag, lane, cPQ);
    }
    KV A0, A1, A2, A3;
    A3.v1 = (bf16x8){};
    f32x16 O[4] = {zero16, zero16, zero16, zero16};
    f32x16 Sa, Sb = zero16; u32x4 Pa, Pb = {};
    PIN(O[0]); PIN(O[1]); PIN(O[2]); PIN(O[3]); PIN(Sb); PIN(Pb); PIN(A3.v1);
    STAMP(2);
    poll(ws, WS_FP + (lane < 8 ? (wave * 8 + lane) * 8 + 7 : c * 8 + ((56 * j + (lane & 1) * 55) >> 6)) * 128, tag);
    STAMP(3);
    const __amdgpu_buffer_rsrc_t rs_kv = __builtin_amdgcn_make_buffer_rsrc(ws + WS_KV + wave * 65536, 0, 65536, 0x00020000);
#define LDKV(B, T) do { const unsigned so_ = (((T) + loc) & 15) * 4096; \
                        B.k0 = BF(BLD(rs_kv, lo16, so_)); B.k1 = BF(BLD(rs_kv, lo16, so_ + 1024)); \
                        B.v0 = BF(BLD(rs_kv, lo16, so_ + 2048)); B.v1 = BF(BLD(rs_kv, lo16, so_ + 3072)); } while (0)
    bf16x8 Q0[4], Q1[4];
    u32x2 qp[4][4];
#define LDQ(i) _Pragma("unroll") for (int u = 0; u < 4; ++u) qp[i][u] = __builtin_amdgcn_raw_buffer_load_b64(rs_ws, lane * 8 + u * 512, \
                 WS_Q + ((2 * c + ((wave ^ (3 - (i))) >> 1)) * 112 + 7 * (2 * j + ((wave ^ (3 - (i))) & 1))) * 256, SC1)
    LDQ(0); LDKV(A0, 0); LDQ(1); LDQ(2); LDQ(3); LDKV(A1, 1); LDKV(A2, 2);
    SBAR();
#pragma unroll
    for (int i = 0; i < 4; ++i) { Q0[i] = BF(((u32x4){qp[i][0][0], qp[i][0][1], qp[i][1][0], qp[i][1][1]}));
      Q1[i] = BF(((u32x4){qp[i][2][0], qp[i][2][1], hi ? 0u : qp[i][3][0], hi ? 0u : qp[i][3][1]})); }
    Sa = MFMA(A0.k0, Q0[0], zero16); Sa = MFMA(A0.k1, Q1[0], Sa);
    STAMP(9);
#define UNIT(S, Sn, KN, QN, VP, OP, VC, OC, X) do { \
      Pb[2] = pk(Sn[12], Sn[13]); Pb[3] = pk(Sn[14], Sn[15]); \
      if ((QN) >= 0) Sn = MFMA(KN.k0, Q0[(QN) & 3], zero16); S[0] = EX(S[0]); S[1] = EX(S[1]); S[2] = EX(S[2]); S[3] = EX(S[3]); SBAR(); \
      if ((QN) >= 0) Sn = MFMA(KN.k1, Q1[(QN) & 3], Sn); Pa[0] = pk(S[0], S[1]); Pa[1] = pk(S[2], S[3]); S[4] = EX(S[4]); S[5] = EX(S[5]); S[6] = EX(S[6]); S[7] = EX(S[7]); SBAR(); \
      X; SBAR(); \
      OP = MFMA(VP.v1, BF(Pb), OP); Pa[2] = pk(S[4], S[5]); Pa[3] = pk(S[6], S[7]); S[8] = EX(S[8]); S[9] = EX(S[9]); S[10] = EX(S[10]); S[11] = EX(S[11]); SBAR(); \
      OC = MFMA(VC.v0, BF(Pa), OC); Pb[0] = pk(S[8], S[9]); Pb[1] = pk(S[10], S[11]); S[12] = EX(S[12]); S[13] = EX(S[13]); S[14] = EX(S[14]); S[15] = EX(S[15]); SBAR(); \
    } while (0)
#define TILE(CUR, NXT, PRV, T, LD) do { \
      UNIT(Sa, Sb, CUR, 1, PRV, O[3], CUR, O[0], ); \
      if (LD) LDKV(PRV, (T) + 3); SBAR(); \
      UNIT(Sb, Sa, CUR, 2, CUR, O[0], CUR, O[1], ); \
      UNIT(Sa, Sb, CUR, 3, CUR, O[1], CUR, O[2], ); \
      UNIT(Sb, Sa, NXT, 0, CUR, O[2], CUR, O[3], ); } while (0)
#pragma unroll 1
    for (int t = 0; t < 12; t += 4) {
      TILE(A0, A1, A3, t, 1); TILE(A1, A2, A0, t + 1, 1); TILE(A2, A3, A1, t + 2, 1); TILE(A3, A0, A2, t + 3, 1);
    }
    TILE(A0, A1, A3, 12, 1);
    const lptr oc = lds + L_OC + lo16;
#define OUTP(T, I) _Pragma("unroll") for (int q4 = 0; q4 < 4; ++q4) *(LAS f32x4*)(oc + ((wave ^ I) * 3 + I - 1) * 4096 + q4 * 1024) = (f32x4){T[4 * q4], T[4 * q4 + 1], T[4 * q4 + 2], T[4 * q4 + 3]}
    f32x4 pv[12];
    UNIT(Sa, Sb, A1, 1, A0, O[3], A1, O[0], ); UNIT(Sb, Sa, A1, 2, A1, O[0], A1, O[1], ); UNIT(Sa, Sb, A2, 0, A1, O[1], A1, O[2], );
    UNIT(Sb, Sa, A2, 1, A1, O[2], A2, O[0], ); UNIT(Sa, Sb, A2, 2, A2, O[0], A2, O[1], ); UNIT(Sb, Sa, A3, 0, A2, O[1], A2, O[2], );
    UNIT(Sa, Sb, A3, 1, A2, O[2], A3, O[0], ); UNIT(Sb, Sa, A3, 2, A3, O[0], A3, O[1], lds_wait(j < 7 ? cCR : cPQ, 4));
    UNIT(Sa, Sb, A1, 3, A3, O[1], A3, O[2], OUTP(O[0], 3));
    UNIT(Sb, Sa, A2, 3, A3, O[2], A1, O[3], OUTP(O[1], 2));
    UNIT(Sa, Sb, A3, 3, A1, O[3], A2, O[3], OUTP(O[2], 1); lds_signal(cBB));
    UNIT(Sb, Sa, A3, -1, A2, O[3], A3, O[3], lds_wait(cBB, 4); _Pragma("unroll") for (int s2 = 0; s2 < 12; ++s2) pv[s2] = *(LAS f32x4*)(oc + wave * 12288 + s2 * 1024));
    Pb[2] = pk(Sb[12], Sb[13]); Pb[3] = pk(Sb[14], Sb[15]);
    O[3] = MFMA(A3.v1, BF(Pb), O[3]);
    STAMP(4);
    __builtin_amdgcn_s_setprio(2);
#pragma unroll
    for (int s2 = 0; s2 < 4; ++s2) pv[s2] = pv[s2] + pv[s2 + 4] + pv[s2 + 8];
    STAMP(10);
    unsigned lo16b = lo16; PIN(lo16b);
    const int lane = lo16b >> 4, r32 = lane & 31, hi = lane >> 5, g4 = lane >> 4, tq = (lane & 15) >> 2, tp = lane & 3;
    f32x16 o = O[3];
#pragma unroll
    for (int r = 0; r < 16; ++r) o[r] += pv[r >> 2][r & 3];
    {
      float l = swap_hi(o[12]);
      float am = 0.f;
#pragma unroll
      for (int r = 0; r < 16; r += 2) am = __builtin_fmaxf(__builtin_fmaxf(am, __builtin_fabsf(o[r])), __builtin_fabsf(o[r + 1]));
      const bool ok = (l >= 0x1p-90f) && (l <= 0x1p110f) && (am <= 0x1p126f);
      const bool slow = !wall(ok);
      if (slow) {
        const __amdgpu_buffer_rsrc_t rk = __builtin_amdgcn_make_buffer_rsrc(ws + WS_KV, 0, 262144, 0x00020000);
        poll(ws, WS_FP + (r32 * 8 + 7) * 128, tag);
        float m = -__builtin_inff();
#pragma unroll 1
        for (int t = 0; t < 64; ++t) {
          f32x16 s = MFMA(BF(BLD(rk, lo16, t * 4096)), Q0[3], zero16); s = MFMA(BF(BLD(rk, lo16 + 1024, t * 4096)), Q1[3], s);
#pragma unroll
          for (int r = 0; r < 16; ++r) m = __builtin_fmaxf(m, s[r]);
        }
        m = __builtin_fmaxf(swap_lo(m), swap_hi(m));
        o = zero16;
#pragma unroll 1
        for (int t = 0; t < 64; ++t) {
          f32x16 s = MFMA(BF(BLD(rk, lo16, t * 4096)), Q0[3], zero16); s = MFMA(BF(BLD(rk, lo16 + 1024, t * 4096)), Q1[3], s);
#pragma unroll
          for (int r = 0; r < 16; ++r) s[r] = EX(s[r] - m);
          o = MFMA(BF(BLD(rk, lo16 + 2048, t * 4096)), pack8(s, 0), o); o = MFMA(BF(BLD(rk, lo16 + 3072, t * 4096)), pack8(s, 1), o);
        }
        l = swap_hi(o[12]);
      }
      const float inv = __builtin_amdgcn_rcpf(l);
#pragma unroll
      for (int r = 0; r < 16; ++r) o[r] *= inv;
      if (hi) { o[12] = 0.f; o[13] = 0.f; o[14] = 0.f; o[15] = 0.f; }
      u32x4 f0 = U4(pack8(o, 0)), f1 = U4(pack8(o, 1));
      if (slow) {
#pragma unroll
        for (int k = 0; k < 4; ++k) { f0[k] = umin(f0[k], 0xfffffffeu); f1[k] = umin(f1[k], 0xfffffffeu); }
      }
      BST(f0, rs_ws, lo16, o_own); BST(f1, rs_ws, lo16 + 1024, o_own);
    }
    lds_signal(cOC);
    lds_signal(cGO + 4 * wave);
    STAMP(5);
    if (j == 7) { STAMP_OUT(); return; }
    {
      const CAS Params* kp = KARGS();
      const __amdgpu_buffer_rsrc_t rs_out = __builtin_amdgcn_make_buffer_rsrc(kp->out, 0, 2048 * 896 * 4, 0x00020000);
      lds_wait(cWO, 4);
      u32x4 of[16];
      const unsigned ob = WS_O + (((2 * c + th) * 16 + 8 * chh) * 2) * 1024;
#define LDO(k) of[k] = BLD(rs_ws, lo16 + ((k) & 3) * 1024, ob + ((k) >> 2) * 4096)
      const int rowb = 224 * chh + 4 * (g4 >> 1) + tq;
      const unsigned wb = (L_WO + rowb * 128 + ((32 * (g4 & 1) + 8 * tp) ^ ((tq & 2) << 5))) ^ (chh << 6);
      bf16x8 w0[16], w1[16];
#pragma unroll
      for (int k = 0; k < 16; ++k) w1[k] = ldstr2(lds + (wb ^ 64) + (28 * (k >> 1) + 16 * (k & 1)) * 128, 1024);
      SBAR();
      {
        unsigned have = 0;
#pragma unroll 1
        for (unsigned s = 0; s < SPIN_CAP; ++s) {
          const unsigned w = __builtin_amdgcn_raw_buffer_load_b32(rs_ws, (lane & 7) * 2048 + 2044, ob, SC1);
          const unsigned rdy = (s + 1 < SPIN_CAP ? (unsigned)__builtin_amdgcn_ballot_w64(w != ~0u) : ~0u) & 0xffu & ~have;
#pragma unroll
          for (int h = 0; h < 8; ++h) if (rdy >> h & 1) { LDO(2 * h); LDO(2 * h + 1); }
          have |= rdy;
          if (have == 0xffu) break;
          __builtin_amdgcn_s_sleep(2);
        }
      }
      f32x16 acc0 = zero16, acc1 = zero16;
#define OBAD(h) (!wall(umax(umax(umax(umax(of[2 * (h)][0], of[2 * (h)][1]), umax(of[2 * (h)][2], of[2 * (h)][3])), umax(of[2 * (h) + 1][0], of[2 * (h) + 1][1])), umax(of[2 * (h) + 1][2], of[2 * (h) + 1][3])) != ~0u))
#define OHEAD(h) do { \
        if (OBAD(h)) { \
          _Pragma("unroll 1") for (unsigned s = 0; s < SPIN_CAP; ++s) { LDO(2 * h); LDO(2 * h + 1); if (!OBAD(h)) break; __builtin_amdgcn_s_sleep(2); } \
        } \
        acc1 = MFMA(BF(of[2 * h]), w1[2 * h], acc1); acc1 = MFMA(BF(of[2 * h + 1]), w1[2 * h + 1], acc1); PIN(acc1); \
        w0[2 * h] = ldstr2(lds + wb + 28 * h * 128, 1024); w0[2 * h + 1] = ldstr2(lds + wb + (28 * h + 16) * 128, 1024); } while (0)
      OHEAD(0); STAMP(6); OHEAD(1); OHEAD(2); OHEAD(3); OHEAD(4); OHEAD(5); OHEAD(6); OHEAD(7);
      STAMP(11);
      lds_wait(cOC, 4);
#pragma unroll
      for (int k = 0; k < 4; ++k) acc0 = MFMA(BF(of[k]), w0[k], acc0);
      SBAR();
#pragma unroll
      for (int k = 0; k < 4; ++k) *(LAS f32x4*)(lds + L_OC + (wave ^ 1) * 4096 + k * 1024 + lo16) = (f32x4){acc1[4 * k], acc1[4 * k + 1], acc1[4 * k + 2], acc1[4 * k + 3]};
      SBAR();
#pragma unroll
      for (int k = 4; k < 8; ++k) acc0 = MFMA(BF(of[k]), w0[k], acc0);
      PIN(acc0);
      lds_signal(cPR + 4 * th);
#pragma unroll
      for (int k = 8; k < 16; ++k) acc0 = MFMA(BF(of[k]), w0[k], acc0);
      PIN(acc0);
      lds_wait(cPR + 4 * th, 2);
#pragma unroll
      for (int k = 0; k < 4; ++k) { const f32x4 v = *(LAS f32x4*)(lds + L_OC + wave * 4096 + k * 1024 + lo16);
        acc0[4 * k] += v[0]; acc0[4 * k + 1] += v[1]; acc0[4 * k + 2] += v[2]; acc0[4 * k + 3] += v[3]; }
      STAMP(12);
      const unsigned oo = ((64 * c + 32 * th + 4 * hi) * 896 + 448 + 64 * j + 32 * chh + r32) * 4;
#pragma unroll
      for (int r = 0; r < 16; ++r) __builtin_amdgcn_raw_buffer_store_b32(f2u(acc0[r]), rs_out, oo, ((r & 3) + 8 * (r >> 2)) * 3584, 17);
    }
    STAMP(7);
    STAMP_OUT();
  } else {
    if (j == 7) {
      VMW(6); BAR(); VMW(0); BAR();
      if (c == 0 && wa == 0) {
        LDEPOCH();
        __builtin_amdgcn_s_sleep(127);
#pragma unroll 1
        for (int k = 0; k < 4; ++k) poll(ws, WS_FP + (k * 64 + lane) * 128, tag);
        if (lane == 0) __hip_atomic_store((GAS u64*)(ws + WS_EPOCH), tag, RLX_AGENT);
      }
      STAMP_OUT();
      return;
    }
    VMW(6); BAR(); LDCH(L_SA); VMW(6); BAR();
    cf lam, coef, l16, l64; f32x16 ur = zero16, ui = zero16;
    f32x16 acc[4] = {zero16, zero16, zero16, zero16};
    const unsigned cs0 = wa * 16384u, cs1 = L_R1 + wa * 12288u;
    unsigned a_htw = L_HT + wa * 4096 + r32 * 64 + hi * 32, a_htr = L_HT + wa * 4096 + (8 * (g4 >> 1) + tq) * 64 + (g4 & 1) * 32 + tp * 8;
    unsigned a_hst = WS_H + ((2 * c + th) * 56 + 4 * j + 2 * chh) * 1024 + lo16;
    unsigned a_c[4], a_cn[2];
#pragma unroll
    for (int f = 0; f < 4; ++f) a_c[f] = cs0 + (32 * chh + r32) * 128 + (((2 * f + hi) ^ xsw) << 4);
#pragma unroll
    for (int f = 0; f < 2; ++f) a_cn[f] = cs1 + 8192 + (32 * chh + r32) * 64 + (((2 * f + hi) ^ ((r32 >> 2) & 3)) << 4);
    unsigned a_vh = (lane >> 2) * 896 + (((lane & 3) ^ ((lane >> 4) & 3)) << 4) + 384 + chh * 64, a_vo = ve ^ 64;
    unsigned a_vw = lrow * 896 + (((lane & 7) ^ ((lrow & 2) << 1)) << 4);
    unsigned a_fl = lane < 32 ? WS_FH + (c * 8 + umin(lane & 7, 6)) * 128 : WS_FP + (c * 8 + (lane & 7)) * 128;
    unsigned a_el = (31 - umin(r32, c - 1)) * 512 + 4, a_eo = (32 - c + hi) * 512 + r32 * 16;
    {
      LDCH(0); LDCH(CH);
      { const f32x4 c0 = *(LAS f32x4*)(lds + L_EX + (32 * chh + r32) * 16), c1 = *(LAS f32x4*)(lds + L_EX + 1024 + (32 * chh + r32) * 16);
        lam = {c0[0], c0[1]}; coef = {c0[2], c0[3]}; l16 = {c1[0], c1[1]}; l64 = {c1[2], c1[3]}; ud = *(LAS unsigned*)(lds + L_EX + 2048 + (32 * chh + r32) * 4); }
      PIN(a_htw); PIN(a_htr); PIN(a_hst); PIN(a_c[0]); PIN(a_c[1]); PIN(a_c[2]); PIN(a_c[3]); PIN(a_cn[0]); PIN(a_cn[1]); PIN(a_vh); PIN(a_vw); PIN(a_fl); PIN(a_el); PIN(a_eo); PIN(a_vo);
      const int trow = 32 * th + 16 * ((r32 >> 2) & 1) + (r32 & 3) + 4 * (r32 >> 3);
      const unsigned xa_off = trow * 128, xa_sw = (trow >> 1) & 7, b_off = 8192 + (32 * chh + r32) * 128;
      bf16x8 gax[4], gar[4], gai[4], gbx[4], gbr[4], gbi[4];
#define GRD(B, s_) _Pragma("unroll") for (int ks = 0; ks < 4; ++ks) { B##x[ks] = ldsf(lds + (s_) + xa_off + (((2 * ks + hi) ^ xa_sw) << 4)); \
        B##r[ks] = ldsf(lds + (s_) + b_off + (((2 * ks + hi) ^ xsw) << 4)); B##i[ks] = ldsf(lds + (s_) + b_off + 8192 + (((2 * ks + hi) ^ xsw) << 4)); } SBAR()
#define GMM(B) _Pragma("unroll") for (int ks = 0; ks < 4; ++ks) { ur = MFMA(B##x[ks], B##r[ks], ur); ui = MFMA(B##x[ks], B##i[ks], ui); } PIN(ur); PIN(ui); SBAR()
#define GAP() do { SBAR(); asm volatile("s_nop %0" :: "i"(7)); SBAR(); } while (0)
#define GMS(B) _Pragma("unroll") for (int ks = 0; ks < 4; ++ks) { ur = MFMA(B##x[ks], B##r[ks], ur); GAP(); ui = MFMA(B##x[ks], B##i[ks], ui); GAP(); } PIN(ur); PIN(ui); SBAR()
      __builtin_amdgcn_s_setprio(1);
      STAMP(12);
      lds_wait_<1>(cPQ, 4);
      LDCH(2 * CH); LDCH(3 * CH); LDCH(4 * CH);
      VMW(18); lds_barrierA(cAB, 4); GRD(ga, L_SA); GRD(gb, 0); GMM(ga); GRD(ga, CH); GMM(gb);
      VMW(6); lds_barrierA(cAB, 8);
      STAMP(13);
      GRD(gb, 2 * CH); LDCH(L_SA); GMM(ga); GRD(ga, 3 * CH); GMS(gb);
      VMW(6); lds_barrierA(cAB, 12);
      STAMP(14);
      GRD(gb, 4 * CH); GMS(ga);
      VMW(0); lds_barrierA(cAB, 16);
      STAMP(15);
      GRD(ga, L_SA); GMS(gb); GMS(ga);
      { const u32x2 t2 = *(LAS u32x2*)(lds + L_TAG);
        tag = ((u64)(unsigned)__builtin_amdgcn_readfirstlane(t2[1]) << 32) | (unsigned)__builtin_amdgcn_readfirstlane(t2[0]); }
    }
    {
      const int e = r32 - 8 * hi; const unsigned dv = ud << ((e & 1) * 16);
      const lptr xr = lds + L_SA + (32 * th + r32) * 128;
#pragma unroll
      for (int s2 = 0; s2 < 2; ++s2) {
        const u32x4 dg = {(e >> 1) == 8 * s2 ? dv : 0u, (e >> 1) == 8 * s2 + 1 ? dv : 0u, (e >> 1) == 8 * s2 + 2 ? dv : 0u, (e >> 1) == 8 * s2 + 3 ? dv : 0u};
        acc[0] = MFMA(ldsf(xr + (((4 * chh + 2 * s2 + hi) ^ xsw) << 4)), BF(dg), acc[0]);
      }
      PIN(acc[0]);
    }
    STAMP(1);
    cf e = {0.f, 0.f};
#pragma unroll
    for (int r = 0; r < 16; ++r) {
      const float a = coef.r * ur[r] - coef.i * ui[r], b2 = -(coef.r * ui[r] + coef.i * ur[r]);
      ur[r] = a; ui[r] = b2; e = r ? cfma(lam, e, (cf){a, b2}) : (cf){a, b2};
    }
    const int seg = 2 * th + hi;
    *(LAS f32x2*)(lds + L_EX + (seg * 64 + 32 * chh + r32) * 8) = (f32x2){e.r, e.i};
    lds_barrierA(cAB, 20);
    cf g0 = {0.f, 0.f}, lseg = {1.f, 0.f};
    {
      cf E = {0.f, 0.f};
#pragma unroll
      for (int s = 0; s < 4; ++s) {
        const f32x2 v = *(LAS f32x2*)(lds + L_EX + (s * 64 + 32 * chh + r32) * 8);
        if (s == seg) g0 = E;
        if (s < seg) lseg = cmul(lseg, l16);
        E = cfma(l16, E, (cf){v[0], v[1]});
      }
      if (th == 0 && hi == 0 && c < 31)
        BST(((u32x4){f2u(E.r), (unsigned)tag, f2u(E.i), (unsigned)tag}), rs_ws, r32 * 16, WS_E + ((2 * j + chh) * 32 + 31 - c) * 512);
    }
    __builtin_amdgcn_s_setprio(0);
    const CAS Params* kp = KARGS();
    {
      const u32x4 rc = mkrs((const char*)(th ? kp->cim : kp->cre) + (size_t)j * 57344, 57344);
      int n2 = 6 * cl;
#pragma unroll
      for (int n = 0; n < 24; ++n) {
        DMA(rc, (n & 1) ? a_vo : ve, (n2 & 7) * 7168 + (n2 >> 3) * 128 + chh * 512, (n2 < 16 ? cs0 : cs1 - 16384) + n2 * 1024, 0);
        n2 = n2 == 23 ? 0 : n2 + 1; }
#pragma unroll
      for (int n = 0; n < 4; ++n) DMA(rc, a_vh, n * 14336, cs1, 8192 + n * 1024);
    }
    STAMP(2);
    cf G = g0;
    if (c > 0) {
      const cf l128 = cmul(l64, l64);
      const __amdgpu_buffer_rsrc_t re = __builtin_amdgcn_make_buffer_rsrc(ws + WS_E + (2 * j + chh) * 16384, 0, 16384, 0x00020000);
      const unsigned eo = a_eo, want = (c - hi + 1) >> 1;
      u32x4 ev[16];
#pragma unroll 1
      for (unsigned s = 0; s < SPIN_CAP; ++s) {
        const unsigned t1 = __builtin_amdgcn_raw_buffer_load_b32(re, a_el, 0, SC1);
        if (wall(t1 == (unsigned)tag)) break;
        __builtin_amdgcn_s_sleep(2);
      }
      MEMBAR();
#pragma unroll 1
      for (unsigned s = 0; s < SPIN_CAP; ++s) {
        unsigned got = 0;
#pragma unroll
        for (int m = 0; m < 8; ++m) ev[m] = BLD(re, eo, m * 1024);
        if (c > 16) {
#pragma unroll
          for (int m = 8; m < 16; ++m) ev[m] = BLD(re, eo, m * 1024);
        }
        VMW(0);
        __builtin_amdgcn_s_setprio(1);
        if (c > 16) {
#pragma unroll
          for (int m = 8; m < 16; ++m) got += ((ev[m][1] ^ (unsigned)tag) | (ev[m][3] ^ (unsigned)tag)) == 0;
        }
#pragma unroll
        for (int m = 0; m < 8; ++m) got += ((ev[m][1] ^ (unsigned)tag) | (ev[m][3] ^ (unsigned)tag)) == 0;
        if (wall(got == want)) break;
        __builtin_amdgcn_s_sleep(4);
      }
      STAMP(3);
      cf H = {0.f, 0.f};
      if (c > 16) {
#pragma unroll
        for (int m = 15; m >= 8; --m) H = cfma(l128, H, (cf){u2f(ev[m][0]), u2f(ev[m][2])});
      }
#pragma unroll
      for (int m = 7; m >= 0; --m) H = cfma(l128, H, (cf){u2f(ev[m][0]), u2f(ev[m][2])});
      H = cfma(l64, (cf){swap_hi(H.r), swap_hi(H.i)}, (cf){swap_lo(H.r), swap_lo(H.i)});
      G = cfma(lseg, H, g0);
    }
    STAMP(4);
    __builtin_amdgcn_s_setprio(1);
    {
#pragma unroll
      for (int r = 0; r < 16; ++r) { G = cfma(lam, G, (cf){ur[r], ui[r]}); ur[r] = G.r; ui[r] = G.i; }
      *(LAS bf16x8*)(lds + a_htw) = pack8(ur, 0); *(LAS bf16x8*)(lds + a_htw + 16) = pack8(ur, 1);
      *(LAS bf16x8*)(lds + a_htw + 2048) = pack8(ui, 0); *(LAS bf16x8*)(lds + a_htw + 2064) = pack8(ui, 1);
      unsigned a_hs2 = a_hst + 28 * 1024; PIN(a_hs2);
      __builtin_amdgcn_s_setprio(0);
      asm volatile("s_waitcnt lgkmcnt(0)" ::: "memory");
#pragma unroll
      for (int k = 0; k < 4; ++k) BST(U4(ldstr2(lds + a_htr + (k >> 1) * 2048 + (k & 1) * 1024, 256)), rs_ws, (k >> 1) ? a_hs2 : a_hst, (k & 1) * 1024);
      publish4(ws, WS_FH + (c * 8 + j) * 128, tag, lane, cPB);
    }
    STAMP(5);
    bf16x8 cfr[14][2];
    const int cflip = chh ? -1 : 1;
#define LDC(f) do { if ((f) < 12) { const unsigned a_ = a_c[(f) & 3] + ((f) < 8 ? ((f) >> 2) * 8192 : cs1 - cs0); cfr[f][0] = ldsf(lds + a_); cfr[f][1] = ldsf(lds + a_ + cflip * 4096); } \
                    else { cfr[f][0] = ldsf(lds + a_cn[(f) & 1]); cfr[f][1] = ldsf(lds + a_cn[(f) & 1] + cflip * 2048); } } while (0)
#pragma unroll
    for (int f = 0; f < 8; ++f) LDC(f);
    lds_signal(cCR);
    STAMP(10);
    lds_wait_<1>(cCR, 4);
    {
      const u32x4 rw = mkrs((const char*)kp->wo + j * 128 + wa * 7168, 401408 - j * 128 - wa * 7168);
      int m2 = 4 * cl;
#pragma unroll
      for (int m = 0; m < 14; ++m) { DMA(rw, a_vw, m2 * 28672, wa * 1024 + m2 * 4096, L_WO); m2 = m2 == 13 ? 0 : m2 + 1; }
      if (wa == 0 && lane < 32) *(LAS u32x4*)(lds + L_WO + 57344 + lo16) = (u32x4){0, 0, 0, 0};
    }
    poll(ws, a_fl, tag);
    VMW(0);
    lds_signal(cWO);
    STAMP(6);
    {
      const unsigned hb = WS_H + (2 * c * 56 + 14 * wa) * 1024, h0 = hb + th * 57344, h1 = hb + (th ^ 1) * 57344;
      const unsigned fa = chh * 2048, fb = chh ? 0u - 12288u : 0u;
      u32x4 hf[14][2];
#define LDH(f) do { hf[f][0] = BLD(rs_ws, lo16, h0 + (f) * 1024 + ((f) < 12 ? fa : fb)); hf[f][1] = BLD(rs_ws, lo16, h1 + (f) * 1024 + ((f) < 12 ? fa : fb)); } while (0)
#define MMH(f) do { acc[0] = MFMA(BF(hf[f][0]), cfr[f][0], acc[0]); acc[1] = MFMA(BF(hf[f][0]), cfr[f][1], acc[1]); \
                    acc[2] = MFMA(BF(hf[f][1]), cfr[f][0], acc[2]); acc[3] = MFMA(BF(hf[f][1]), cfr[f][1], acc[3]); \
                    PIN(acc[0]); PIN(acc[1]); PIN(acc[2]); PIN(acc[3]); } while (0)
#pragma unroll
      for (int f = 0; f < 14; ++f) LDH(f);
      SBAR();
      lds_wait_<4>(cGO + 4 * wa, 1);
      STAMP(9);
      __builtin_amdgcn_s_setprio(1);
#pragma unroll
      for (int f = 0; f < 8; ++f) { MMH(f); SBAR(); if (f >= 1 && f < 7) LDC(f + 7); SBAR(); }
      lds_signal(cP4);
#pragma unroll
      for (int f = 8; f < 14; ++f) MMH(f);
    }
    lds_wait(cP4, 4);
    STAMP(11);
    rs_write(lds + L_G2, wa, lane, acc[1], acc[2], acc[3]);
    lds_barrierA(cAB, 24);
    f32x16 y = rs_read(lds + L_G2, wa, lane, acc[0]);
    const __amdgpu_buffer_rsrc_t rs_out = __builtin_amdgcn_make_buffer_rsrc(kp->out, 0, 2048 * 896 * 4, 0x00020000);
    const unsigned oo = ((64 * c + 32 * th + 4 * hi) * 896 + 64 * j + 32 * chh + r32) * 4;
#pragma unroll
    for (int r = 0; r < 16; ++r) __builtin_amdgcn_raw_buffer_store_b32(f2u(y[r]), rs_out, oo, ((r & 3) + 8 * (r >> 2)) * 3584, 17);
    STAMP(7);
    STAMP_OUT();
  }
}

extern "C" void kernel_launch(void* const* d_in, const int* in_sizes, int n_in, void* d_out, int out_size, void* d_ws, size_t ws_size,
                              hipStream_t stream) {
  Params p;
  p.xa = (const u16*)d_in[0]; p.lre = (const u16*)d_in[1]; p.lim = (const u16*)d_in[2]; p.bre = (const u16*)d_in[3]; p.bim = (const u16*)d_in[4];
  p.cre = (const u16*)d_in[5]; p.cim = (const u16*)d_in[6]; p.ldt = (const u16*)d_in[7]; p.dsk = (const u16*)d_in[8];
  p.xb = (const u16*)d_in[9]; p.wq = (const u16*)d_in[10]; p.wk = (const u16*)d_in[11]; p.wv = (const u16*)d_in[12]; p.wo = (const u16*)d_in[13];
  p.out = (float*)d_out; p.ws = (char*)d_ws;
  fused_kernel<<<256, 512, 0, stream>>>(p);
}
```

```cpp
#include <hip/hip_runtime.h>
#include <stdint.h>

#define DI __device__ __forceinline__
#define GAS __attribute__((address_space(1)))
#define LAS __attribute__((address_space(3)))
#define CAS __attribute__((address_space(4)))
typedef short bf16x8 __attribute__((ext_vector_type(8)));
typedef short s16x4 __attribute__((ext_vector_type(4)));
typedef float f32x16 __attribute__((ext_vector_type(16)));
typedef float f32x4 __attribute__((ext_vector_type(4)));
typedef float f32x2 __attribute__((ext_vector_type(2)));
typedef unsigned u32x4 __attribute__((ext_vector_type(4)));
typedef unsigned u32x2 __attribute__((ext_vector_type(2)));
typedef __bf16 bf16x2_t __attribute__((ext_vector_type(2)));
typedef unsigned long long u64;
typedef unsigned short u16;
typedef LAS char* lptr;
#define RLX_AGENT __ATOMIC_RELAXED, __HIP_MEMORY_SCOPE_AGENT
#define MFMA(a, b, c) __builtin_amdgcn_mfma_f32_32x32x16_bf16((a), (b), (c), 0, 0, 0)
#define SBAR() __builtin_amdgcn_sched_barrier(0)
#define PIN(x) asm volatile("" : "+v"(x))
#define MEMBAR() asm volatile("" ::: "memory")
#define SC1 16

constexpr unsigned WS_EPOCH = 0, WS_FH = 8192, WS_E = 0x10000, WS_FP = 0x48000,
                   WS_KV = 0x50000, WS_O = 0x100000, WS_H = 0x300000, WS_Q = 0x680000, WS_END = 0x880000;
constexpr unsigned SK = 16384, K_W = 8192;
constexpr unsigned CH = 24576, L_SA = 135168;
constexpr unsigned L_R1 = 107520, L_HT = 65536, L_WO = 0, L_OC = 57856, L_G2 = 107520, L_MISC = 159744, L_EX = L_MISC + 128, L_TOTAL = 163840;
constexpr unsigned cBB = L_MISC, cAB = L_MISC + 4, cCR = L_MISC + 8, cWO = L_MISC + 12, cOC = L_MISC + 16, cPB = L_MISC + 20, cPR = L_MISC + 24, cP4 = L_MISC + 32, cPQ = L_MISC + 52, cGO = L_MISC + 36, L_TAG = L_MISC + 64;
constexpr unsigned SPIN_CAP = 1u << 18;

struct Params {
  const u16 *xb, *wq, *wk, *wv; char* ws; const u16 *lre, *lim, *ldt, *dsk, *xa, *bre, *bim, *cre, *cim, *wo;
  float* out;
};

DI float u2f(unsigned v) { return __builtin_bit_cast(float, v); }
DI unsigned f2u(float v) { return __builtin_bit_cast(unsigned, v); }
DI unsigned umin(unsigned a, unsigned b) { return a < b ? a : b; }
DI unsigned umax(unsigned a, unsigned b) { return a > b ? a : b; }
DI bool wall(bool x) { return __builtin_amdgcn_ballot_w64(!x) == 0; }
DI float bf2f(unsigned v) { return u2f(v << 16); }
DI unsigned pk(float a, float b) { f32x2 v = {a, b}; return __builtin_bit_cast(unsigned, __builtin_convertvector(v, bf16x2_t)); }
DI float rbf(float a) { return u2f(pk(0.f, a)); }
DI bf16x8 pack8(const f32x16& x, int s) {
  u32x4 p = {pk(x[8 * s], x[8 * s + 1]), pk(x[8 * s + 2], x[8 * s + 3]), pk(x[8 * s + 4], x[8 * s + 5]), pk(x[8 * s + 6], x[8 * s + 7])};
  return __builtin_bit_cast(bf16x8, p);
}
DI u32x4 mkrs(const void* base, unsigned bytes) { const u64 a = (u64)base; return (u32x4){(unsigned)a, (unsigned)(a >> 32) & 0xffffu, bytes, 0x20000u}; }
#define DMA(rs, voff, soff, ldsb, IMM) asm volatile("s_add_u32 m0, %3, %4\n\ts_nop 0\n\tbuffer_load_dwordx4 %0, %1, %2 offen lds" \
                                                    :: "v"(voff), "s"(rs), "s"(soff), "s"(ldsb), "i"(IMM) : "memory", "scc")
DI bf16x8 ldsf(lptr p) { return *(LAS bf16x8*)p; }
DI s16x4 ldstr(lptr p) { return __builtin_amdgcn_ds_read_tr16_b64_v4i16((LAS s16x4*)p); }
DI bf16x8 ldstr2(lptr p, int second) { s16x4 lo = ldstr(p), hi = ldstr(p + second); return __builtin_shufflevector(lo, hi, 0, 1, 2, 3, 4, 5, 6, 7); }
DI void lds_signal(unsigned c) {
  asm volatile("s_waitcnt lgkmcnt(0)\n\ts_mov_b64 exec, 1\n\tds_add_u32 %0, %1\n\ts_mov_b64 exec, -1" :: "v"(c), "v"(1u) : "memory");
}
template <int SLEEP> DI void lds_wait_(unsigned c, unsigned target) {
  unsigned v, s, n;
  asm volatile("s_mov_b32 %2, 0x400000\n1:\n\tds_read_b32 %0, %3\n\ts_waitcnt lgkmcnt(0)\n\tv_readfirstlane_b32 %1, %0\n\ts_cmp_ge_u32 %1, %4\n\ts_cbranch_scc1 2f\n\t"
               "s_sleep %5\n\ts_sub_u32 %2, %2, 1\n\ts_cbranch_scc0 1b\n2:" : "=&v"(v), "=&s"(s), "=&s"(n) : "v"(c), "i"(target), "i"(SLEEP) : "memory", "scc");
}
#define lds_wait(c, t) lds_wait_<0>(c, t)
#define lds_barrier(c, t) do { lds_signal(c); lds_wait(c, t); } while (0)
#define lds_barrierA(c, t) do { lds_signal(c); lds_wait_<1>(c, t); } while (0)
DI void poll(const char* ws, unsigned off, u64 tag) {
#pragma unroll 1
  for (unsigned s = 0; s < SPIN_CAP; ++s) {
    u64 v = __hip_atomic_load((GAS u64*)(ws + off), RLX_AGENT);
    if (wall(v == tag)) break;
    __builtin_amdgcn_s_sleep(2);
  }
  MEMBAR();
}
DI void publish4(char* ws, unsigned off, u64 tag, int lane, unsigned cnt) {
  unsigned old;
  asm volatile("s_waitcnt vmcnt(0)\n\ts_mov_b64 exec, 1\n\tds_add_rtn_u32 %0, %1, %2\n\ts_waitcnt lgkmcnt(0)\n\ts_mov_b64 exec, -1" : "=&v"(old) : "v"(cnt), "v"(1u) : "memory");
  if (__builtin_amdgcn_readfirstlane(old) == 3 && lane == 0) __hip_atomic_store((GAS u64*)(ws + off), tag, RLX_AGENT);
}
DI float swap_lo(float x) {
  auto r = __builtin_amdgcn_permlane32_swap(f2u(x), f2u(x), false, false); return u2f(r[0]); }
DI float swap_hi(float x) {
  auto r = __builtin_amdgcn_permlane32_swap(f2u(x), f2u(x), false, false); return u2f(r[1]); }
struct cf { float r, i; };
DI cf cmul(cf a, cf b) { return {a.r * b.r - a.i * b.i, a.r * b.i + a.i * b.r}; }
DI cf cfma(cf a, cf b, cf c) { return {__builtin_fmaf(a.r, b.r, __builtin_fmaf(-a.i, b.i, c.r)), __builtin_fmaf(a.r, b.i, __builtin_fmaf(a.i, b.r, c.i))}; }

struct KV { bf16x8 k0, k1, v0, v1; };

DI void rs_write(lptr base, int w, int lane, const f32x16& t1, const f32x16& t2, const f32x16& t3) {
#define RSW(T, I) _Pragma("unroll") for (int q = 0; q < 4; ++q) *(LAS f32x4*)(base + ((w ^ I) * 3 + I - 1) * 4096 + q * 1024 + lane * 16) = (f32x4){T[4 * q], T[4 * q + 1], T[4 * q + 2], T[4 * q + 3]}
  RSW(t1, 1); RSW(t2, 2); RSW(t3, 3);
}
DI f32x16 rs_read(lptr base, int w, int lane, f32x16 a) {
  f32x4 v[12];
#pragma unroll
  for (int s = 0; s < 12; ++s) v[s] = *(LAS f32x4*)(base + w * 12288 + s * 1024 + lane * 16);
  SBAR();
#pragma unroll
  for (int s = 0; s < 12; ++s) { const int q = s & 3; a[4 * q] += v[s][0]; a[4 * q + 1] += v[s][1]; a[4 * q + 2] += v[s][2]; a[4 * q + 3] += v[s][3]; }
  return a;
}

#define VMW_(n) asm volatile("s_waitcnt vmcnt(" #n ")" ::: "memory")
#define VMW(n) VMW_(n)
#define BAR() asm volatile("s_waitcnt lgkmcnt(0)\n\ts_barrier" ::: "memory")
#define BLD(R, V, S) __builtin_amdgcn_raw_buffer_load_b128(R, V, S, SC1)
#define BST(D, R, V, S) __builtin_amdgcn_raw_buffer_store_b128(D, R, (V) + (S), 0, SC1)
#define BF(x) __builtin_bit_cast(bf16x8, x)
#define U4(x) __builtin_bit_cast(u32x4, x)
#define EX(x) __builtin_amdgcn_exp2f(x)
#define KARGS() ({ const CAS Params* kp_ = (const CAS Params*)__builtin_amdgcn_kernarg_segment_ptr(); asm volatile("" : "+s"(kp_)); kp_; })
#define NEXTK() do { k += step; k = k >= 7 ? k - 7 : k; } while (0)

#if defined(__HIP_DEVICE_COMPILE__)
#define NO_PK __attribute__((target("no-packed-fp32-ops")))
#else
#define NO_PK
#endif
__global__ void __launch_bounds__(512, 2) NO_PK fused_kernel(Params p) {
  __shared__ __attribute__((aligned(128))) char smem[L_TOTAL];
  const lptr lds = (lptr)smem;
  const int tid = threadIdx.x, lane = tid & 63, wave = __builtin_amdgcn_readfirstlane(tid >> 6);
  const int b = blockIdx.x, loc = b >> 3, cl = loc >> 3, c = (b & 7) * 4 + cl, j = loc & 7;
  const int lrow = lane >> 3;
  const unsigned ve = lrow * 896 + (((lane & 7) ^ (lrow >> 1)) << 4);
  const int step = cl + 1, k0 = j == 7 ? 0 : j;
  const bool isB = wave < 4;
  const int wa = wave & 3;
  char* const ws = p.ws;
  unsigned ua = 0, ub = 0, uc = 0, ud = 0;
  u32x4 rxa, rbr, rbi; int ka = k0;
  const unsigned vo = (ve ^ 64) + 7168, lba = wa * 2048;
#define NEXTKA() do { ka += step; ka = ka >= 7 ? ka - 7 : ka; } while (0)
#define LDCH(slot) do { NEXTKA(); const unsigned k_ = ka * 128; DMA(rxa, ve, k_, lba, (slot)); DMA(rxa, vo, k_, lba, (slot) + 1024); \
      DMA(rbr, ve, k_, lba, (slot) + 8192); DMA(rbr, vo, k_, lba, (slot) + 9216); DMA(rbi, ve, k_, lba, (slot) + 16384); DMA(rbi, vo, k_, lba, (slot) + 17408); } while (0)
  {
    const u32x4 rx = mkrs((const char*)p.xb + (size_t)c * 57344 + wave * 7168, 7168);
    const unsigned vx = ve ^ ((wave & 1) << 6), lb = wave * 1024;
    int k = k0;
    if (j == 7) {
      const u32x4 rw = mkrs(isB ? p.wk : p.wv, 25088);
      const unsigned vw = wa * 1024 + lane * 16, lw = K_W + (isB ? 0 : 3584) + wa * 1024; const bool half = wa < 3 || lane < 32;
#pragma unroll
      for (int i = 0; i < 7; ++i) { NEXTK(); DMA(rx, vx, k * 128, lb, i * SK); if (half) DMA(rw, vw, k * 3584, lw, i * SK); }
    } else {
      const u32x4 rw = mkrs((const char*)p.wq + j * 128 + wave * 7168, 401408 - j * 128 - wave * 7168);
      const unsigned vw = lrow * 896 + (((lane & 7) ^ ((lrow & 2) << 1)) << 4);
      if (wave == 0) { const unsigned co = (64 * j + lane) * 2;
        asm volatile("global_load_ushort %0, %4, %5\n\tglobal_load_ushort %1, %4, %6\n\tglobal_load_ushort %2, %4, %7\n\tglobal_load_ushort %3, %4, %8"
                     : "=&v"(ua), "=&v"(ub), "=&v"(uc), "=&v"(ud) : "v"(co), "s"(p.lre), "s"(p.lim), "s"(p.ldt), "s"(p.dsk) : "memory"); }
#pragma unroll
      for (int i = 0; i < 7; ++i) { NEXTK(); DMA(rx, vx, k * 128, lb, i * SK); DMA(rw, vw, k * 57344, lb, i * SK + K_W); }
      rxa = mkrs((const char*)p.xa + (size_t)c * 57344 + wa * 14336, 14336);
      rbr = mkrs((const char*)p.bre + (size_t)j * 57344 + wa * 14336, 14336);
      rbi = mkrs((const char*)p.bim + (size_t)j * 57344 + wa * 14336, 14336);
    }
  }
  if (tid < 16) ((LAS unsigned*)(lds + L_MISC))[tid] = 0;
  const int r32 = lane & 31, hi = lane >> 5;
  const int g4 = lane >> 4, tq = (lane & 15) >> 2, tp = lane & 3;
  const unsigned xsw = (r32 >> 1) & 7, lo16 = lane * 16;
  const int th = wa >> 1, chh = wa & 1;
  u64 tag;
  const __amdgpu_buffer_rsrc_t rs_ws = __builtin_amdgcn_make_buffer_rsrc(ws, 0, WS_END, 0x00020000);
  const f32x16 zero16 = {};
  const unsigned o_own = WS_O + (((2 * c + th) * 16 + 2 * j + chh) * 2) * 1024;
#define LDEPOCH() asm volatile("s_load_dwordx2 %0, %1, 0x0\n\ts_waitcnt lgkmcnt(0)" : "=s"(tag) : "s"(ws) : "memory"); tag = (tag | 1) + 2

  if (isB) {
    if (wave == 0 && j < 7) {
      asm volatile("s_waitcnt vmcnt(14)" : "+v"(ua), "+v"(ub), "+v"(uc), "+v"(ud) :: "memory");
      const float pa = bf2f(ua), pb = bf2f(ub), dt = rbf(expf(bf2f(uc)));
      const float er = expf(dt * pa); float sn, cs; sincosf(dt * pb, &sn, &cs);
      const float nr = er * cs - 1.f, ni = er * sn, den = pa * pa + pb * pb;
      const cf lam = {er * cs, -ni};
      cf l16 = cmul(lam, lam); l16 = cmul(l16, l16); l16 = cmul(l16, l16); l16 = cmul(l16, l16);
      cf l64 = cmul(l16, l16); l64 = cmul(l64, l64);
      *(LAS f32x4*)(lds + L_EX + lo16) = (f32x4){lam.r, lam.i, 2.f * (nr * pa + ni * pb) / den, 2.f * (ni * pa - nr * pb) / den};
      *(LAS f32x4*)(lds + L_EX + 1024 + lo16) = (f32x4){l16.r, l16.i, l64.r, l64.i};
      *(LAS unsigned*)(lds + L_EX + 2048 + lane * 4) = ud;
    }
    { const u32x4 ones = {~0u, ~0u, ~0u, ~0u};
      BST(ones, rs_ws, lo16, o_own); BST(ones, rs_ws, lo16 + 1024, o_own); }
    LDEPOCH();
    if (tid == 0) *(LAS u64*)(lds + L_TAG) = tag;
    {
      const unsigned x_off = (32 * th + r32) * 128;
      const int wrow = 8 * (g4 >> 1) + tq;
      bf16x8 fax[4], faw[4], fbx[4], fbw[4];
      f32x16 q = zero16;
#define PRD(B, i, P) _Pragma("unroll") for (int ks = 0; ks < 4; ++ks) { B##x[ks] = ldsf(lds + (i) * SK + x_off + (((2 * ks + hi) ^ xsw) << 4)); B##w[ks] = ldstr2(lds + (i) * SK + w_off + ks * 16 * (P), 4 * (P)); } SBAR()
#define PMM(B, SW) _Pragma("unroll") for (int ks = 0; ks < 4; ++ks) q = (SW) ? MFMA(B##x[ks], B##w[ks], q) : MFMA(B##w[ks], B##x[ks], q); PIN(q); SBAR()
#define PROJ(P, SW) do { VMW(6); BAR(); PRD(fa, 0, P); PRD(fb, 1, P); PMM(fa, SW); PRD(fa, 2, P); PMM(fb, SW); PRD(fb, 3, P); PMM(fa, SW); \
        VMW(0); BAR(); PRD(fa, 4, P); PMM(fb, SW); PRD(fb, 5, P); PMM(fa, SW); PRD(fa, 6, P); PMM(fb, SW); PMM(fa, SW); } while (0)
      if (j < 7) {
        const unsigned w_off = K_W + wrow * 128 + ((64 * chh + 32 * (g4 & 1) + 8 * tp) ^ ((tq & 2) << 5));
        PROJ(128, false);
        const float qs = 0.18898223650461361f * 1.4426950408889634f;
#pragma unroll
        for (int g = 0; g < 4; ++g)
          __builtin_amdgcn_raw_buffer_store_b64((u32x2){pk(q[4 * g] * qs, q[4 * g + 1] * qs), pk(q[4 * g + 2] * qs, q[4 * g + 3] * qs)}, rs_ws, lane * 8 + g * 512,
                                                WS_Q + ((2 * c + th) * 112 + 16 * j + 8 * chh) * 256, SC1);
      } else {
        const unsigned w_off = K_W + chh * 3584 + wrow * 56 + (16 * (g4 & 1) + 4 * tp) * 2;
        if (chh) { PROJ(56, true);
#pragma unroll
          for (int r = 0; r < 16; ++r) q[r] = r32 < 28 ? q[r] : (r32 == 28 ? 1.f : 0.f);
        } else { PROJ(56, false); if (hi) { q[12] = 0.f; q[13] = 0.f; q[14] = 0.f; q[15] = 0.f; } }
        const unsigned ko = WS_KV + (2 * c + th) * 4096 + chh * 2048;
        BST(U4(pack8(q, 0)), rs_ws, lo16, ko); BST(U4(pack8(q, 1)), rs_ws, lo16 + 1024, ko);
      }
      publish4(ws, WS_FP + (c * 8 + j) * 128, tag, lane, cPQ);
    }
    KV A0, A1, A2, A3;
    A3.v1 = (bf16x8){};
    f32x16 O[4] = {zero16, zero16, zero16, zero16};
    f32x16 Sa, Sb = zero16; u32x4 Pa, Pb = {};
    PIN(O[0]); PIN(O[1]); PIN(O[2]); PIN(O[3]); PIN(Sb); PIN(Pb); PIN(A3.v1);
    poll(ws, WS_FP + (lane < 8 ? (wave * 8 + lane) * 8 + 7 : c * 8 + ((56 * j + (lane & 1) * 55) >> 6)) * 128, tag);
    const __amdgpu_buffer_rsrc_t rs_kv = __builtin_amdgcn_make_buffer_rsrc(ws + WS_KV + wave * 65536, 0, 65536, 0x00020000);
#define LDKV(B, T) do { const unsigned so_ = (((T) + loc) & 15) * 4096; \
                        B.k0 = BF(BLD(rs_kv, lo16, so_)); B.k1 = BF(BLD(rs_kv, lo16, so_ + 1024)); \
                        B.v0 = BF(BLD(rs_kv, lo16, so_ + 2048)); B.v1 = BF(BLD(rs_kv, lo16, so_ + 3072)); } while (0)
    bf16x8 Q0[4], Q1[4];
    u32x2 qp[4][4];
#define LDQ(i) _Pragma("unroll") for (int u = 0; u < 4; ++u) qp[i][u] = __builtin_amdgcn_raw_buffer_load_b64(rs_ws, lane * 8 + u * 512, \
                 WS_Q + ((2 * c + ((wave ^ (3 - (i))) >> 1)) * 112 + 7 * (2 * j + ((wave ^ (3 - (i))) & 1))) * 256, SC1)
    LDQ(0); LDKV(A0, 0); LDQ(1); LDQ(2); LDQ(3); LDKV(A1, 1); LDKV(A2, 2);
    SBAR();
#pragma unroll
    for (int i = 0; i < 4; ++i) { Q0[i] = BF(((u32x4){qp[i][0][0], qp[i][0][1], qp[i][1][0], qp[i][1][1]}));
      Q1[i] = BF(((u32x4){qp[i][2][0], qp[i][2][1], hi ? 0u : qp[i][3][0], hi ? 0u : qp[i][3][1]})); }
    Sa = MFMA(A0.k0, Q0[0], zero16); Sa = MFMA(A0.k1, Q1[0], Sa);
#define UNIT(S, Sn, KN, QN, VP, OP, VC, OC, X) do { \
      Pb[2] = pk(Sn[12], Sn[13]); Pb[3] = pk(Sn[14], Sn[15]); \
      if ((QN) >= 0) Sn = MFMA(KN.k0, Q0[(QN) & 3], zero16); S[0] = EX(S[0]); S[1] = EX(S[1]); S[2] = EX(S[2]); S[3] = EX(S[3]); SBAR(); \
      if ((QN) >= 0) Sn = MFMA(KN.k1, Q1[(QN) & 3], Sn); Pa[0] = pk(S[0], S[1]); Pa[1] = pk(S[2], S[3]); S[4] = EX(S[4]); S[5] = EX(S[5]); S[6] = EX(S[6]); S[7] = EX(S[7]); SBAR(); \
      X; SBAR(); \
      OP = MFMA(VP.v1, BF(Pb), OP); Pa[2] = pk(S[4], S[5]); Pa[3] = pk(S[6], S[7]); S[8] = EX(S[8]); S[9] = EX(S[9]); S[10] = EX(S[10]); S[11] = EX(S[11]); SBAR(); \
      OC = MFMA(VC.v0, BF(Pa), OC); Pb[0] = pk(S[8], S[9]); Pb[1] = pk(S[10], S[11]); S[12] = EX(S[12]); S[13] = EX(S[13]); S[14] = EX(S[14]); S[15] = EX(S[15]); SBAR(); \
    } while (0)
#define TILE(CUR, NXT, PRV, T, LD) do { \
      UNIT(Sa, Sb, CUR, 1, PRV, O[3], CUR, O[0], ); \
      if (LD) LDKV(PRV, (T) + 3); SBAR(); \
      UNIT(Sb, Sa, CUR, 2, CUR, O[0], CUR, O[1], ); \
      UNIT(Sa, Sb, CUR, 3, CUR, O[1], CUR, O[2], ); \
      UNIT(Sb, Sa, NXT, 0, CUR, O[2], CUR, O[3], ); } while (0)
#pragma unroll 1
    for (int t = 0; t < 12; t += 4) {
      TILE(A0, A1, A3, t, 1); TILE(A1, A2, A0, t + 1, 1); TILE(A2, A3, A1, t + 2, 1); TILE(A3, A0, A2, t + 3, 1);
    }
    TILE(A0, A1, A3, 12, 1);
    const lptr oc = lds + L_OC + lo16;
#define OUTP(T, I) _Pragma("unroll") for (int q4 = 0; q4 < 4; ++q4) *(LAS f32x4*)(oc + ((wave ^ I) * 3 + I - 1) * 4096 + q4 * 1024) = (f32x4){T[4 * q4], T[4 * q4 + 1], T[4 * q4 + 2], T[4 * q4 + 3]}
    f32x4 pv[12];
    UNIT(Sa, Sb, A1, 1, A0, O[3], A1, O[0], ); UNIT(Sb, Sa, A1, 2, A1, O[0], A1, O[1], ); UNIT(Sa, Sb, A2, 0, A1, O[1], A1, O[2], );
    UNIT(Sb, Sa, A2, 1, A1, O[2], A2, O[0], ); UNIT(Sa, Sb, A2, 2, A2, O[0], A2, O[1], ); UNIT(Sb, Sa, A3, 0, A2, O[1], A2, O[2], );
    UNIT(Sa, Sb, A3, 1, A2, O[2], A3, O[0], ); UNIT(Sb, Sa, A3, 2, A3, O[0], A3, O[1], lds_wait(j < 7 ? cCR : cPQ, 4));
    UNIT(Sa, Sb, A1, 3, A3, O[1], A3, O[2], OUTP(O[0], 3));
    UNIT(Sb, Sa, A2, 3, A3, O[2], A1, O[3], OUTP(O[1], 2));
    UNIT(Sa, Sb, A3, 3, A1, O[3], A2, O[3], OUTP(O[2], 1); lds_signal(cBB));
    UNIT(Sb, Sa, A3, -1, A2, O[3], A3, O[3], lds_wait(cBB, 4); _Pragma("unroll") for (int s2 = 0; s2 < 12; ++s2) pv[s2] = *(LAS f32x4*)(oc + wave * 12288 + s2 * 1024));
    Pb[2] = pk(Sb[12], Sb[13]); Pb[3] = pk(Sb[14], Sb[15]);
    O[3] = MFMA(A3.v1, BF(Pb), O[3]);
    __builtin_amdgcn_s_setprio(2);
#pragma unroll
    for (int s2 = 0; s2 < 4; ++s2) pv[s2] = pv[s2] + pv[s2 + 4] + pv[s2 + 8];
    unsigned lo16b = lo16; PIN(lo16b);
    const int lane = lo16b >> 4, r32 = lane & 31, hi = lane >> 5, g4 = lane >> 4, tq = (lane & 15) >> 2, tp = lane & 3;
    f32x16 o = O[3];
#pragma unroll
    for (int r = 0; r < 16; ++r) o[r] += pv[r >> 2][r & 3];
    {
      float l = swap_hi(o[12]);
      float am = 0.f;
#pragma unroll
      for (int r = 0; r < 16; r += 2) am = __builtin_fmaxf(__builtin_fmaxf(am, __builtin_fabsf(o[r])), __builtin_fabsf(o[r + 1]));
      const bool ok = (l >= 0x1p-90f) && (l <= 0x1p110f) && (am <= 0x1p126f);
      const bool slow = !wall(ok);
      if (slow) {
        const __amdgpu_buffer_rsrc_t rk = __builtin_amdgcn_make_buffer_rsrc(ws + WS_KV, 0, 262144, 0x00020000);
        poll(ws, WS_FP + (r32 * 8 + 7) * 128, tag);
        float m = -__builtin_inff();
#pragma unroll 1
        for (int t = 0; t < 64; ++t) {
          f32x16 s = MFMA(BF(BLD(rk, lo16, t * 4096)), Q0[3], zero16); s = MFMA(BF(BLD(rk, lo16 + 1024, t * 4096)), Q1[3], s);
#pragma unroll
          for (int r = 0; r < 16; ++r) m = __builtin_fmaxf(m, s[r]);
        }
        m = __builtin_fmaxf(swap_lo(m), swap_hi(m));
        o = zero16;
#pragma unroll 1
        for (int t = 0; t < 64; ++t) {
          f32x16 s = MFMA(BF(BLD(rk, lo16, t * 4096)), Q0[3], zero16); s = MFMA(BF(BLD(rk, lo16 + 1024, t * 4096)), Q1[3], s);
#pragma unroll
          for (int r = 0; r < 16; ++r) s[r] = EX(s[r] - m);
          o = MFMA(BF(BLD(rk, lo16 + 2048, t * 4096)), pack8(s, 0), o); o = MFMA(BF(BLD(rk, lo16 + 3072, t * 4096)), pack8(s, 1), o);
        }
        l = swap_hi(o[12]);
      }
      const float inv = __builtin_amdgcn_rcpf(l);
#pragma unroll
      for (int r = 0; r < 16; ++r) o[r] *= inv;
      if (hi) { o[12] = 0.f; o[13] = 0.f; o[14] = 0.f; o[15] = 0.f; }
      u32x4 f0 = U4(pack8(o, 0)), f1 = U4(pack8(o, 1));
      if (slow) {
#pragma unroll
        for (int k = 0; k < 4; ++k) { f0[k] = umin(f0[k], 0xfffffffeu); f1[k] = umin(f1[k], 0xfffffffeu); }
      }
      BST(f0, rs_ws, lo16, o_own); BST(f1, rs_ws, lo16 + 1024, o_own);
    }
    lds_signal(cOC);
    lds_signal(cGO + 4 * wave);
    if (j == 7) return;
    {
      const CAS Params* kp = KARGS();
      const __amdgpu_buffer_rsrc_t rs_out = __builtin_amdgcn_make_buffer_rsrc(kp->out, 0, 2048 * 896 * 4, 0x00020000);
      lds_wait(cWO, 4);
      u32x4 of[16];
      const unsigned ob = WS_O + (((2 * c + th) * 16 + 8 * chh) * 2) * 1024;
#define LDO(k) of[k] = BLD(rs_ws, lo16 + ((k) & 3) * 1024, ob + ((k) >> 2) * 4096)
      const int rowb = 224 * chh + 4 * (g4 >> 1) + tq;
      const unsigned wb = (L_WO + rowb * 128 + ((32 * (g4 & 1) + 8 * tp) ^ ((tq & 2) << 5))) ^ (chh << 6);
      bf16x8 w0[16], w1[16];
#pragma unroll
      for (int k = 0; k < 16; ++k) w1[k] = ldstr2(lds + (wb ^ 64) + (28 * (k >> 1) + 16 * (k & 1)) * 128, 1024);
      SBAR();
      {
        unsigned have = 0;
#pragma unroll 1
        for (unsigned s = 0; s < SPIN_CAP; ++s) {
          const unsigned w = __builtin_amdgcn_raw_buffer_load_b32(rs_ws, (lane & 7) * 2048 + 2044, ob, SC1);
          const unsigned rdy = (s + 1 < SPIN_CAP ? (unsigned)__builtin_amdgcn_ballot_w64(w != ~0u) : ~0u) & 0xffu & ~have;
#pragma unroll
          for (int h = 0; h < 8; ++h) if (rdy >> h & 1) { LDO(2 * h); LDO(2 * h + 1); }
          have |= rdy;
          if (have == 0xffu) break;
          __builtin_amdgcn_s_sleep(2);
        }
      }
      f32x16 acc0 = zero16, acc1 = zero16;
#define OBAD(h) (!wall(umax(umax(umax(umax(of[2 * (h)][0], of[2 * (h)][1]), umax(of[2 * (h)][2], of[2 * (h)][3])), umax(of[2 * (h) + 1][0], of[2 * (h) + 1][1])), umax(of[2 * (h) + 1][2], of[2 * (h) + 1][3])) != ~0u))
#define OHEAD(h) do { \
        if (OBAD(h)) { \
          _Pragma("unroll 1") for (unsigned s = 0; s < SPIN_CAP; ++s) { LDO(2 * h); LDO(2 * h + 1); if (!OBAD(h)) break; __builtin_amdgcn_s_sleep(2); } \
        } \
        acc1 = MFMA(BF(of[2 * h]), w1[2 * h], acc1); acc1 = MFMA(BF(of[2 * h + 1]), w1[2 * h + 1], acc1); PIN(acc1); \
        w0[2 * h] = ldstr2(lds + wb + 28 * h * 128, 1024); w0[2 * h + 1] = ldstr2(lds + wb + (28 * h + 16) * 128, 1024); } while (0)
      OHEAD(0); OHEAD(1); OHEAD(2); OHEAD(3); OHEAD(4); OHEAD(5); OHEAD(6); OHEAD(7);
      lds_wait(cOC, 4);
#pragma unroll
      for (int k = 0; k < 4; ++k) acc0 = MFMA(BF(of[k]), w0[k], acc0);
      SBAR();
      const lptr tw = lds + L_OC + hi * 512 + r32 * 4;
#pragma unroll
      for (int r = 0; r < 16; ++r) *(LAS float*)(tw + (wave ^ 1) * 4096 + ((r & 3) + 8 * (r >> 2)) * 128) = acc1[r];
      SBAR();
#pragma unroll
      for (int k = 4; k < 8; ++k) acc0 = MFMA(BF(of[k]), w0[k], acc0);
      PIN(acc0);
      lds_signal(cPR + 4 * th);
#pragma unroll
      for (int k = 8; k < 16; ++k) acc0 = MFMA(BF(of[k]), w0[k], acc0);
      PIN(acc0);
      lds_wait(cPR + 4 * th, 2);
      f32x4 pa[4];
#pragma unroll
      for (int k = 0; k < 4; ++k) pa[k] = *(LAS f32x4*)(lds + L_OC + wave * 4096 + k * 1024 + lo16);
#pragma unroll
      for (int r = 0; r < 16; ++r) *(LAS float*)(tw + 16384 + wave * 4096 + ((r & 3) + 8 * (r >> 2)) * 128) = acc0[r];
      MEMBAR();
      const unsigned oo = ((64 * c + 32 * th + (lane >> 3)) * 896 + 448 + 64 * j + 32 * chh + (lane & 7) * 4) * 4;
#pragma unroll
      for (int k = 0; k < 4; ++k)
        __builtin_amdgcn_raw_buffer_store_b128(U4(pa[k] + *(LAS f32x4*)(lds + L_OC + 16384 + wave * 4096 + k * 1024 + lo16)), rs_out, oo + k * 28672, 0, 17);
    }
  } else {
    if (j == 7) {
      VMW(6); BAR(); VMW(0); BAR();
      if (c == 0 && wa == 0) {
        LDEPOCH();
        __builtin_amdgcn_s_sleep(127);
#pragma unroll 1
        for (int k = 0; k < 4; ++k) poll(ws, WS_FP + (k * 64 + lane) * 128, tag);
        if (lane == 0) __hip_atomic_store((GAS u64*)(ws + WS_EPOCH), tag, RLX_AGENT);
      }
      return;
    }
    VMW(6); BAR(); LDCH(L_SA); VMW(6); BAR();
    cf lam, coef, l16, l64; f32x16 ur = zero16, ui = zero16;
    f32x16 acc[4] = {zero16, zero16, zero16, zero16};
    const unsigned cs0 = wa * 16384u, cs1 = L_R1 + wa * 12288u;
    unsigned a_htw = L_HT + wa * 4096 + r32 * 64 + hi * 32, a_htr = L_HT + wa * 4096 + (8 * (g4 >> 1) + tq) * 64 + (g4 & 1) * 32 + tp * 8;
    unsigned a_hst = WS_H + ((2 * c + th) * 56 + 4 * j + 2 * chh) * 1024 + lo16;
    unsigned a_c[4], a_cn[2];
#pragma unroll
    for (int f = 0; f < 4; ++f) a_c[f] = cs0 + (32 * chh + r32) * 128 + (((2 * f + hi) ^ xsw) << 4);
#pragma unroll
    for (int f = 0; f < 2; ++f) a_cn[f] = cs1 + 8192 + (32 * chh + r32) * 64 + (((2 * f + hi) ^ ((r32 >> 2) & 3)) << 4);
    unsigned a_vh = (lane >> 2) * 896 + (((lane & 3) ^ ((lane >> 4) & 3)) << 4) + 384 + chh * 64, a_vo = ve ^ 64;
    unsigned a_vw = lrow * 896 + (((lane & 7) ^ ((lrow & 2) << 1)) << 4);
    unsigned a_fl = lane < 32 ? WS_FH + (c * 8 + umin(lane & 7, 6)) * 128 : WS_FP + (c * 8 + (lane & 7)) * 128;
    unsigned a_el = (31 - umin(r32, c - 1)) * 512 + 4, a_eo = (32 - c + hi) * 512 + r32 * 16;
    {
      LDCH(0); LDCH(CH);
      { const f32x4 c0 = *(LAS f32x4*)(lds + L_EX + (32 * chh + r32) * 16), c1 = *(LAS f32x4*)(lds + L_EX + 1024 + (32 * chh + r32) * 16);
        lam = {c0[0], c0[1]}; coef = {c0[2], c0[3]}; l16 = {c1[0], c1[1]}; l64 = {c1[2], c1[3]}; ud = *(LAS unsigned*)(lds + L_EX + 2048 + (32 * chh + r32) * 4); }
      PIN(a_htw); PIN(a_htr); PIN(a_hst); PIN(a_c[0]); PIN(a_c[1]); PIN(a_c[2]); PIN(a_c[3]); PIN(a_cn[0]); PIN(a_cn[1]); PIN(a_vh); PIN(a_vw); PIN(a_fl); PIN(a_el); PIN(a_eo); PIN(a_vo);
      const int trow = 32 * th + 16 * ((r32 >> 2) & 1) + (r32 & 3) + 4 * (r32 >> 3);
      const unsigned xa_off = trow * 128, xa_sw = (trow >> 1) & 7, b_off = 8192 + (32 * chh + r32) * 128;
      bf16x8 gax[4], gar[4], gai[4], gbx[4], gbr[4], gbi[4];
#define GRD(B, s_) _Pragma("unroll") for (int ks = 0; ks < 4; ++ks) { B##x[ks] = ldsf(lds + (s_) + xa_off + (((2 * ks + hi) ^ xa_sw) << 4)); \
        B##r[ks] = ldsf(lds + (s_) + b_off + (((2 * ks + hi) ^ xsw) << 4)); B##i[ks] = ldsf(lds + (s_) + b_off + 8192 + (((2 * ks + hi) ^ xsw) << 4)); } SBAR()
#define GMM(B) _Pragma("unroll") for (int ks = 0; ks < 4; ++ks) { ur = MFMA(B##x[ks], B##r[ks], ur); ui = MFMA(B##x[ks], B##i[ks], ui); } PIN(ur); PIN(ui); SBAR()
#define GAP() do { SBAR(); asm volatile("s_nop %0" :: "i"(7)); SBAR(); } while (0)
#define GMS(B) _Pragma("unroll") for (int ks = 0; ks < 4; ++ks) { ur = MFMA(B##x[ks], B##r[ks], ur); GAP(); ui = MFMA(B##x[ks], B##i[ks], ui); GAP(); } PIN(ur); PIN(ui); SBAR()
      __builtin_amdgcn_s_setprio(1);
      lds_wait_<1>(cPQ, 4);
      LDCH(2 * CH); LDCH(3 * CH); LDCH(4 * CH);
      VMW(18); lds_barrierA(cAB, 4); GRD(ga, L_SA); GRD(gb, 0); GMM(ga); GRD(ga, CH); GMM(gb);
      VMW(6); lds_barrierA(cAB, 8);
      GRD(gb, 2 * CH); LDCH(L_SA); GMM(ga); GRD(ga, 3 * CH); GMS(gb);
      VMW(6); lds_barrierA(cAB, 12);
      GRD(gb, 4 * CH); GMS(ga);
      VMW(0); lds_barrierA(cAB, 16);
      GRD(ga, L_SA); GMS(gb); GMS(ga);
      { const u32x2 t2 = *(LAS u32x2*)(lds + L_TAG);
        tag = ((u64)(unsigned)__builtin_amdgcn_readfirstlane(t2[1]) << 32) | (unsigned)__builtin_amdgcn_readfirstlane(t2[0]); }
    }
    {
      const int e = r32 - 8 * hi; const unsigned dv = ud << ((e & 1) * 16);
      const lptr xr = lds + L_SA + (32 * th + r32) * 128;
#pragma unroll
      for (int s2 = 0; s2 < 2; ++s2) {
        const u32x4 dg = {(e >> 1) == 8 * s2 ? dv : 0u, (e >> 1) == 8 * s2 + 1 ? dv : 0u, (e >> 1) == 8 * s2 + 2 ? dv : 0u, (e >> 1) == 8 * s2 + 3 ? dv : 0u};
        acc[0] = MFMA(ldsf(xr + (((4 * chh + 2 * s2 + hi) ^ xsw) << 4)), BF(dg), acc[0]);
      }
      PIN(acc[0]);
    }
    cf e = {0.f, 0.f};
#pragma unroll
    for (int r = 0; r < 16; ++r) {
      const float a = coef.r * ur[r] - coef.i * ui[r], b2 = -(coef.r * ui[r] + coef.i * ur[r]);
      ur[r] = a; ui[r] = b2; e = r ? cfma(lam, e, (cf){a, b2}) : (cf){a, b2};
    }
    const int seg = 2 * th + hi;
    *(LAS f32x2*)(lds + L_EX + (seg * 64 + 32 * chh + r32) * 8) = (f32x2){e.r, e.i};
    lds_barrierA(cAB, 20);
    cf g0 = {0.f, 0.f}, lseg = {1.f, 0.f};
    {
      cf E = {0.f, 0.f};
#pragma unroll
      for (int s = 0; s < 4; ++s) {
        const f32x2 v = *(LAS f32x2*)(lds + L_EX + (s * 64 + 32 * chh + r32) * 8);
        if (s == seg) g0 = E;
        if (s < seg) lseg = cmul(lseg, l16);
        E = cfma(l16, E, (cf){v[0], v[1]});
      }
      if (th == 0 && hi == 0 && c < 31)
        BST(((u32x4){f2u(E.r), (unsigned)tag, f2u(E.i), (unsigned)tag}), rs_ws, r32 * 16, WS_E + ((2 * j + chh) * 32 + 31 - c) * 512);
    }
    __builtin_amdgcn_s_setprio(0);
    const CAS Params* kp = KARGS();
    {
      const u32x4 rc = mkrs((const char*)(th ? kp->cim : kp->cre) + (size_t)j * 57344, 57344);
      int n2 = 6 * cl;
#pragma unroll
      for (int n = 0; n < 24; ++n) {
        DMA(rc, (n & 1) ? a_vo : ve, (n2 & 7) * 7168 + (n2 >> 3) * 128 + chh * 512, (n2 < 16 ? cs0 : cs1 - 16384) + n2 * 1024, 0);
        n2 = n2 == 23 ? 0 : n2 + 1; }
#pragma unroll
      for (int n = 0; n < 4; ++n) DMA(rc, a_vh, n * 14336, cs1, 8192 + n * 1024);
    }
    cf G = g0;
    if (c > 0) {
      const cf l128 = cmul(l64, l64);
      const __amdgpu_buffer_rsrc_t re = __builtin_amdgcn_make_buffer_rsrc(ws + WS_E + (2 * j + chh) * 16384, 0, 16384, 0x00020000);
      const unsigned eo = a_eo, want = (c - hi + 1) >> 1;
      u32x4 ev[16];
#pragma unroll 1
      for (unsigned s = 0; s < SPIN_CAP; ++s) {
        const unsigned t1 = __builtin_amdgcn_raw_buffer_load_b32(re, a_el, 0, SC1);
        if (wall(t1 == (unsigned)tag)) break;
        __builtin_amdgcn_s_sleep(2);
      }
      MEMBAR();
#pragma unroll 1
      for (unsigned s = 0; s < SPIN_CAP; ++s) {
        unsigned got = 0;
#pragma unroll
        for (int m = 0; m < 8; ++m) ev[m] = BLD(re, eo, m * 1024);
        if (c > 16) {
#pragma unroll
          for (int m = 8; m < 16; ++m) ev[m] = BLD(re, eo, m * 1024);
        }
        VMW(0);
        __builtin_amdgcn_s_setprio(1);
        if (c > 16) {
#pragma unroll
          for (int m = 8; m < 16; ++m) got += ((ev[m][1] ^ (unsigned)tag) | (ev[m][3] ^ (unsigned)tag)) == 0;
        }
#pragma unroll
        for (int m = 0; m < 8; ++m) got += ((ev[m][1] ^ (unsigned)tag) | (ev[m][3] ^ (unsigned)tag)) == 0;
        if (wall(got == want)) break;
        __builtin_amdgcn_s_sleep(4);
      }
      cf H = {0.f, 0.f};
      if (c > 16) {
#pragma unroll
        for (int m = 15; m >= 8; --m) H = cfma(l128, H, (cf){u2f(ev[m][0]), u2f(ev[m][2])});
      }
#pragma unroll
      for (int m = 7; m >= 0; --m) H = cfma(l128, H, (cf){u2f(ev[m][0]), u2f(ev[m][2])});
      H = cfma(l64, (cf){swap_hi(H.r), swap_hi(H.i)}, (cf){swap_lo(H.r), swap_lo(H.i)});
      G = cfma(lseg, H, g0);
    }
    __builtin_amdgcn_s_setprio(1);
    {
#pragma unroll
      for (int r = 0; r < 16; ++r) { G = cfma(lam, G, (cf){ur[r], ui[r]}); ur[r] = G.r; ui[r] = G.i; }
      *(LAS bf16x8*)(lds + a_htw) = pack8(ur, 0); *(LAS bf16x8*)(lds + a_htw + 16) = pack8(ur, 1);
      *(LAS bf16x8*)(lds + a_htw + 2048) = pack8(ui, 0); *(LAS bf16x8*)(lds + a_htw + 2064) = pack8(ui, 1);
      unsigned a_hs2 = a_hst + 28 * 1024; PIN(a_hs2);
      __builtin_amdgcn_s_setprio(0);
      asm volatile("s_waitcnt lgkmcnt(0)" ::: "memory");
#pragma unroll
      for (int k = 0; k < 4; ++k) BST(U4(ldstr2(lds + a_htr + (k >> 1) * 2048 + (k & 1) * 1024, 256)), rs_ws, (k >> 1) ? a_hs2 : a_hst, (k & 1) * 1024);
      publish4(ws, WS_FH + (c * 8 + j) * 128, tag, lane, cPB);
    }
    bf16x8 cfr[14][2];
    const int cflip = chh ? -1 : 1;
#define LDC(f) do { if ((f) < 12) { const unsigned a_ = a_c[(f) & 3] + ((f) < 8 ? ((f) >> 2) * 8192 : cs1 - cs0); cfr[f][0] = ldsf(lds + a_); cfr[f][1] = ldsf(lds + a_ + cflip * 4096); } \
                    else { cfr[f][0] = ldsf(lds + a_cn[(f) & 1]); cfr[f][1] = ldsf(lds + a_cn[(f) & 1] + cflip * 2048); } } while (0)
#pragma unroll
    for (int f = 0; f < 8; ++f) LDC(f);
    lds_signal(cCR);
    lds_wait_<1>(cCR, 4);
    {
      const u32x4 rw = mkrs((const char*)kp->wo + j * 128 + wa * 7168, 401408 - j * 128 - wa * 7168);
      int m2 = 4 * cl;
#pragma unroll
      for (int m = 0; m < 14; ++m) { DMA(rw, a_vw, m2 * 28672, wa * 1024 + m2 * 4096, L_WO); m2 = m2 == 13 ? 0 : m2 + 1; }
      if (wa == 0 && lane < 32) *(LAS u32x4*)(lds + L_WO + 57344 + lo16) = (u32x4){0, 0, 0, 0};
    }
    poll(ws, a_fl, tag);
    VMW(0);
    lds_signal(cWO);
    {
      const unsigned hb = WS_H + (2 * c * 56 + 14 * wa) * 1024, h0 = hb + th * 57344, h1 = hb + (th ^ 1) * 57344;
      const unsigned fa = chh * 2048, fb = chh ? 0u - 12288u : 0u;
      u32x4 hf[14][2];
#define LDH(f) do { hf[f][0] = BLD(rs_ws, lo16, h0 + (f) * 1024 + ((f) < 12 ? fa : fb)); hf[f][1] = BLD(rs_ws, lo16, h1 + (f) * 1024 + ((f) < 12 ? fa : fb)); } while (0)
#define MMH(f) do { acc[0] = MFMA(BF(hf[f][0]), cfr[f][0], acc[0]); acc[1] = MFMA(BF(hf[f][0]), cfr[f][1], acc[1]); \
                    acc[2] = MFMA(BF(hf[f][1]), cfr[f][0], acc[2]); acc[3] = MFMA(BF(hf[f][1]), cfr[f][1], acc[3]); \
                    PIN(acc[0]); PIN(acc[1]); PIN(acc[2]); PIN(acc[3]); } while (0)
#pragma unroll
      for (int f = 0; f < 14; ++f) LDH(f);
      SBAR();
      lds_wait_<4>(cGO + 4 * wa, 1);
      __builtin_amdgcn_s_setprio(1);
#pragma unroll
      for (int f = 0; f < 8; ++f) { MMH(f); SBAR(); if (f >= 1 && f < 7) LDC(f + 7); SBAR(); }
      lds_signal(cP4);
#pragma unroll
      for (int f = 8; f < 14; ++f) MMH(f);
    }
    lds_wait(cP4, 4);
    rs_write(lds + L_G2, wa, lane, acc[1], acc[2], acc[3]);
    lds_barrierA(cAB, 24);
    f32x16 y = rs_read(lds + L_G2, wa, lane, acc[0]);
    const __amdgpu_buffer_rsrc_t rs_out = __builtin_amdgcn_make_buffer_rsrc(kp->out, 0, 2048 * 896 * 4, 0x00020000);
    MEMBAR();
#pragma unroll
    for (int r = 0; r < 16; ++r) *(LAS float*)(lds + L_G2 + wa * 12288 + hi * 512 + r32 * 4 + ((r & 3) + 8 * (r >> 2)) * 128) = y[r];
    MEMBAR();
    const unsigned oo = ((64 * c + 32 * th + (lane >> 3)) * 896 + 64 * j + 32 * chh + (lane & 7) * 4) * 4;
#pragma unroll
    for (int k = 0; k < 4; ++k) __builtin_amdgcn_raw_buffer_store_b128(*(LAS u32x4*)(lds + L_G2 + wa * 12288 + k * 1024 + lo16), rs_out, oo + k * 28672, 0, 17);
  }
}

extern "C" void kernel_launch(void* const* d_in, const int* in_sizes, int n_in, void* d_out, int out_size, void* d_ws, size_t ws_size,
                              hipStream_t stream) {
  Params p;
  p.xa = (const u16*)d_in[0]; p.lre = (const u16*)d_in[1]; p.lim = (const u16*)d_in[2]; p.bre = (const u16*)d_in[3]; p.bim = (const u16*)d_in[4];
  p.cre = (const u16*)d_in[5]; p.cim = (const u16*)d_in[6]; p.ldt = (const u16*)d_in[7]; p.dsk = (const u16*)d_in[8];
  p.xb = (const u16*)d_in[9]; p.wq = (const u16*)d_in[10]; p.wk = (const u16*)d_in[11]; p.wv = (const u16*)d_in[12]; p.wo = (const u16*)d_in[13];
  p.out = (float*)d_out; p.ws = (char*)d_ws;
  fused_kernel<<<256, 512, 0, stream>>>(p);
}
```

```cpp
#include <hip/hip_runtime.h>
#include <stdint.h>

#define DI __device__ __forceinline__
#define GAS __attribute__((address_space(1)))
#define LAS __attribute__((address_space(3)))
#define CAS __attribute__((address_space(4)))
typedef short bf16x8 __attribute__((ext_vector_type(8)));
typedef short s16x4 __attribute__((ext_vector_type(4)));
typedef float f32x16 __attribute__((ext_vector_type(16)));
typedef float f32x4 __attribute__((ext_vector_type(4)));
typedef float f32x2 __attribute__((ext_vector_type(2)));
typedef unsigned u32x4 __attribute__((ext_vector_type(4)));
typedef unsigned u32x2 __attribute__((ext_vector_type(2)));
typedef __bf16 bf16x2_t __attribute__((ext_vector_type(2)));
typedef unsigned long long u64;
typedef unsigned short u16;
typedef LAS char* lptr;
#define RLX_AGENT __ATOMIC_RELAXED, __HIP_MEMORY_SCOPE_AGENT
#define MFMA(a, b, c) __builtin_amdgcn_mfma_f32_32x32x16_bf16((a), (b), (c), 0, 0, 0)
#define SBAR() __builtin_amdgcn_sched_barrier(0)
#define PIN(x) asm volatile("" : "+v"(x))
#define MEMBAR() asm volatile("" ::: "memory")
#define SC1 16

constexpr unsigned WS_EPOCH = 0, WS_FH = 8192, WS_E = 0x10000, WS_FP = 0x48000,
                   WS_KV = 0x50000, WS_O = 0x100000, WS_H = 0x300000, WS_Q = 0x680000, WS_END = 0x880000, WS_DBG = 0x900000;
constexpr unsigned SK = 16384, K_W = 8192;
constexpr unsigned CH = 24576, L_SA = 135168;
constexpr unsigned L_R1 = 107520, L_HT = 65536, L_WO = 0, L_OC = 57856, L_G2 = 107520, L_MISC = 159744, L_EX = L_MISC + 128, L_TOTAL = 163840;
constexpr unsigned cBB = L_MISC, cAB = L_MISC + 4, cCR = L_MISC + 8, cWO = L_MISC + 12, cOC = L_MISC + 16, cPB = L_MISC + 20, cPR = L_MISC + 24, cP4 = L_MISC + 32, cPQ = L_MISC + 52, cGO = L_MISC + 36, L_TAG = L_MISC + 64;
constexpr unsigned SPIN_CAP = 1u << 18;

struct Params {
  const u16 *xb, *wq, *wk, *wv; char* ws; const u16 *lre, *lim, *ldt, *dsk, *xa, *bre, *bim, *cre, *cim, *wo;
  float* out;
};

DI float u2f(unsigned v) { return __builtin_bit_cast(float, v); }
DI unsigned f2u(float v) { return __builtin_bit_cast(unsigned, v); }
DI unsigned umin(unsigned a, unsigned b) { return a < b ? a : b; }
DI unsigned umax(unsigned a, unsigned b) { return a > b ? a : b; }
DI bool wall(bool x) { return __builtin_amdgcn_ballot_w64(!x) == 0; }
DI float bf2f(unsigned v) { return u2f(v << 16); }
DI unsigned pk(float a, float b) { f32x2 v = {a, b}; return __builtin_bit_cast(unsigned, __builtin_convertvector(v, bf16x2_t)); }
DI float rbf(float a) { return u2f(pk(0.f, a)); }
DI bf16x8 pack8(const f32x16& x, int s) {
  u32x4 p = {pk(x[8 * s], x[8 * s + 1]), pk(x[8 * s + 2], x[8 * s + 3]), pk(x[8 * s + 4], x[8 * s + 5]), pk(x[8 * s + 6], x[8 * s + 7])};
  return __builtin_bit_cast(bf16x8, p);
}
DI u32x4 mkrs(const void* base, unsigned bytes) { const u64 a = (u64)base; return (u32x4){(unsigned)a, (unsigned)(a >> 32) & 0xffffu, bytes, 0x20000u}; }
#define DMA(rs, voff, soff, ldsb, IMM) asm volatile("s_add_u32 m0, %3, %4\n\ts_nop 0\n\tbuffer_load_dwordx4 %0, %1, %2 offen lds" \
                                                    :: "v"(voff), "s"(rs), "s"(soff), "s"(ldsb), "i"(IMM) : "memory", "scc")
#define DMAX(rs, voff, soff, ldsb, IMM) asm volatile("s_add_u32 m0, %3, %4\n\ts_nop 0\n\tbuffer_load_dwordx4 %0, %1, %2 offen nt lds" \
                                                     :: "v"(voff), "s"(rs), "s"(soff), "s"(ldsb), "i"(IMM) : "memory", "scc")
DI bf16x8 ldsf(lptr p) { return *(LAS bf16x8*)p; }
DI s16x4 ldstr(lptr p) { return __builtin_amdgcn_ds_read_tr16_b64_v4i16((LAS s16x4*)p); }
DI bf16x8 ldstr2(lptr p, int second) { s16x4 lo = ldstr(p), hi = ldstr(p + second); return __builtin_shufflevector(lo, hi, 0, 1, 2, 3, 4, 5, 6, 7); }
DI void lds_signal(unsigned c) {
  asm volatile("s_waitcnt lgkmcnt(0)\n\ts_mov_b64 exec, 1\n\tds_add_u32 %0, %1\n\ts_mov_b64 exec, -1" :: "v"(c), "v"(1u) : "memory");
}
template <int SLEEP> DI void lds_wait_(unsigned c, unsigned target) {
  unsigned v, s, n;
  asm volatile("s_mov_b32 %2, 0x400000\n1:\n\tds_read_b32 %0, %3\n\ts_waitcnt lgkmcnt(0)\n\tv_readfirstlane_b32 %1, %0\n\ts_cmp_ge_u32 %1, %4\n\ts_cbranch_scc1 2f\n\t"
               "s_sleep %5\n\ts_sub_u32 %2, %2, 1\n\ts_cbranch_scc0 1b\n2:" : "=&v"(v), "=&s"(s), "=&s"(n) : "v"(c), "i"(target), "i"(SLEEP) : "memory", "scc");
}
#define lds_wait(c, t) lds_wait_<0>(c, t)
#define lds_barrier(c, t) do { lds_signal(c); lds_wait(c, t); } while (0)
#define lds_barrierA(c, t) do { lds_signal(c); lds_wait_<1>(c, t); } while (0)
DI void poll(const char* ws, unsigned off, u64 tag) {
#pragma unroll 1
  for (unsigned s = 0; s < SPIN_CAP; ++s) {
    u64 v = __hip_atomic_load((GAS u64*)(ws + off), RLX_AGENT);
    if (wall(v == tag)) break;
    __builtin_amdgcn_s_sleep(2);
  }
  MEMBAR();
}
DI void publish4(char* ws, unsigned off, u64 tag, int lane, unsigned cnt) {
  unsigned old;
  asm volatile("s_waitcnt vmcnt(0)\n\ts_mov_b64 exec, 1\n\tds_add_rtn_u32 %0, %1, %2\n\ts_waitcnt lgkmcnt(0)\n\ts_mov_b64 exec, -1" : "=&v"(old) : "v"(cnt), "v"(1u) : "memory");
  if (__builtin_amdgcn_readfirstlane(old) == 3 && lane == 0) __hip_atomic_store((GAS u64*)(ws + off), tag, RLX_AGENT);
}
DI float swap_lo(float x) {
  auto r = __builtin_amdgcn_permlane32_swap(f2u(x), f2u(x), false, false); return u2f(r[0]); }
DI float swap_hi(float x) {
  auto r = __builtin_amdgcn_permlane32_swap(f2u(x), f2u(x), false, false); return u2f(r[1]); }
struct cf { float r, i; };
DI cf cmul(cf a, cf b) { return {a.r * b.r - a.i * b.i, a.r * b.i + a.i * b.r}; }
DI cf cfma(cf a, cf b, cf c) { return {__builtin_fmaf(a.r, b.r, __builtin_fmaf(-a.i, b.i, c.r)), __builtin_fmaf(a.r, b.i, __builtin_fmaf(a.i, b.r, c.i))}; }

#define STAMP(k) do {} while (0)
#define STAMP_OUT() do {} while (0)

struct KV { bf16x8 k0, k1, v0, v1; };

DI void rs_write(lptr base, int w, int lane, const f32x16& t1, const f32x16& t2, const f32x16& t3) {
#define RSW(T, I) _Pragma("unroll") for (int q = 0; q < 4; ++q) *(LAS f32x4*)(base + ((w ^ I) * 3 + I - 1) * 4096 + q * 1024 + lane * 16) = (f32x4){T[4 * q], T[4 * q + 1], T[4 * q + 2], T[4 * q + 3]}
  RSW(t1, 1); RSW(t2, 2); RSW(t3, 3);
}
DI f32x16 rs_read(lptr base, int w, int lane, f32x16 a) {
  f32x4 v[12];
#pragma unroll
  for (int s = 0; s < 12; ++s) v[s] = *(LAS f32x4*)(base + w * 12288 + s * 1024 + lane * 16);
  SBAR();
#pragma unroll
  for (int s = 0; s < 12; ++s) { const int q = s & 3; a[4 * q] += v[s][0]; a[4 * q + 1] += v[s][1]; a[4 * q + 2] += v[s][2]; a[4 * q + 3] += v[s][3]; }
  return a;
}

#define VMW_(n) asm volatile("s_waitcnt vmcnt(" #n ")" ::: "memory")
#define VMW(n) VMW_(n)
#define BAR() asm volatile("s_waitcnt lgkmcnt(0)\n\ts_barrier" ::: "memory")
#define BLD(R, V, S) __builtin_amdgcn_raw_buffer_load_b128(R, V, S, SC1)
#define BST(D, R, V, S) __builtin_amdgcn_raw_buffer_store_b128(D, R, (V) + (S), 0, SC1)
#define BF(x) __builtin_bit_cast(bf16x8, x)
#define U4(x) __builtin_bit_cast(u32x4, x)
#define EX(x) __builtin_amdgcn_exp2f(x)
#define KARGS() ({ const CAS Params* kp_ = (const CAS Params*)__builtin_amdgcn_kernarg_segment_ptr(); asm volatile("" : "+s"(kp_)); kp_; })
#define NEXTK() do { k += step; k = k >= 7 ? k - 7 : k; } while (0)

#if defined(__HIP_DEVICE_COMPILE__)
#define NO_PK __attribute__((target("no-packed-fp32-ops")))
#else
#define NO_PK
#endif
__global__ void __launch_bounds__(512, 2) NO_PK fused_kernel(Params p) {
  __shared__ __attribute__((aligned(128))) char smem[L_TOTAL];
  const lptr lds = (lptr)smem;
  const int tid = threadIdx.x, lane = tid & 63, wave = __builtin_amdgcn_readfirstlane(tid >> 6);
  const int b = blockIdx.x, loc = b >> 3, cl = loc >> 3, c = (b & 7) * 4 + cl, j = loc & 7;
  const int lrow = lane >> 3;
  const unsigned ve = lrow * 896 + (((lane & 7) ^ (lrow >> 1)) << 4);
  const int step = 1 + (cl + (b & 7) * 4) % 6, k0 = j == 7 ? 0 : j;
  const bool isB = wave < 4;
  const int wa = wave & 3;
  char* const ws = p.ws;
  STAMP(0);
  unsigned ua = 0, ub = 0, uc = 0, ud = 0;
  u32x4 rxa, rbr, rbi; int ka = k0;
  const unsigned vo = (ve ^ 64) + 7168, lba = wa * 2048;
#define NEXTKA() do { ka += step; ka = ka >= 7 ? ka - 7 : ka; } while (0)
#define LDCH(slot) do { NEXTKA(); const unsigned k_ = ka * 128; DMAX(rxa, ve, k_, lba, (slot)); DMAX(rxa, vo, k_, lba, (slot) + 1024); \
      DMA(rbr, ve, k_, lba, (slot) + 8192); DMA(rbr, vo, k_, lba, (slot) + 9216); DMA(rbi, ve, k_, lba, (slot) + 16384); DMA(rbi, vo, k_, lba, (slot) + 17408); } while (0)
  {
    const u32x4 rx = mkrs((const char*)p.xb + (size_t)c * 57344 + wave * 7168, 7168);
    const unsigned vx = ve ^ ((wave & 1) << 6), lb = wave * 1024;
    int k = k0;
    if (j == 7) {
      const u32x4 rw = mkrs(isB ? p.wk : p.wv, 25088);
      const unsigned vw = wa * 1024 + lane * 16, lw = K_W + (isB ? 0 : 3584) + wa * 1024; const bool half = wa < 3 || lane < 32;
#pragma unroll
      for (int i = 0; i < 7; ++i) { NEXTK(); DMAX(rx, vx, k * 128, lb, i * SK); if (half) DMA(rw, vw, k * 3584, lw, i * SK); }
    } else {
      const u32x4 rw = mkrs((const char*)p.wq + j * 128 + wave * 7168, 401408 - j * 128 - wave * 7168);
      const unsigned vw = lrow * 896 + (((lane & 7) ^ ((lrow & 2) << 1)) << 4);
      if (wave == 0) { const unsigned co = (64 * j + lane) * 2;
        asm volatile("global_load_ushort %0, %4, %5\n\tglobal_load_ushort %1, %4, %6\n\tglobal_load_ushort %2, %4, %7\n\tglobal_load_ushort %3, %4, %8"
                     : "=&v"(ua), "=&v"(ub), "=&v"(uc), "=&v"(ud) : "v"(co), "s"(p.lre), "s"(p.lim), "s"(p.ldt), "s"(p.dsk) : "memory"); }
#pragma unroll
      for (int i = 0; i < 7; ++i) { NEXTK(); DMAX(rx, vx, k * 128, lb, i * SK); DMA(rw, vw, k * 57344, lb, i * SK + K_W); }
      rxa = mkrs((const char*)p.xa + (size_t)c * 57344 + wa * 14336, 14336);
      rbr = mkrs((const char*)p.bre + (size_t)j * 57344 + wa * 14336, 14336);
      rbi = mkrs((const char*)p.bim + (size_t)j * 57344 + wa * 14336, 14336);
    }
  }
  STAMP(8);
  if (tid < 16) ((LAS unsigned*)(lds + L_MISC))[tid] = 0;
  const int r32 = lane & 31, hi = lane >> 5;
  const int g4 = lane >> 4, tq = (lane & 15) >> 2, tp = lane & 3;
  const unsigned xsw = (r32 >> 1) & 7, lo16 = lane * 16;
  const int th = wa >> 1, chh = wa & 1;
  u64 tag;
  const __amdgpu_buffer_rsrc_t rs_ws = __builtin_amdgcn_make_buffer_rsrc(ws, 0, WS_END, 0x00020000);
  const f32x16 zero16 = {};
  const unsigned o_own = WS_O + (((2 * c + th) * 16 + 2 * j + chh) * 2) * 1024;
#define MARK() do { const u32x4 ones = {~0u, ~0u, ~0u, ~0u};       \
    BST(ones, rs_ws, lo16, o_own); BST(ones, rs_ws, lo16 + 1024, o_own); } while (0)
#define LDEPOCH() asm volatile("s_load_dwordx2 %0, %1, 0x0\n\ts_waitcnt lgkmcnt(0)" : "=s"(tag) : "s"(ws) : "memory"); tag = (tag | 1) + 2

  if (isB) {
    if (wave == 0 && j < 7) {
      asm volatile("s_waitcnt vmcnt(14)" : "+v"(ua), "+v"(ub), "+v"(uc), "+v"(ud) :: "memory");
      const float pa = bf2f(ua), pb = bf2f(ub), dt = rbf(expf(bf2f(uc)));
      const float er = expf(dt * pa); float sn, cs; sincosf(dt * pb, &sn, &cs);
      const float nr = er * cs - 1.f, ni = er * sn, den = pa * pa + pb * pb;
      const cf lam = {er * cs, -ni};
      cf l16 = cmul(lam, lam); l16 = cmul(l16, l16); l16 = cmul(l16, l16); l16 = cmul(l16, l16);
      cf l64 = cmul(l16, l16); l64 = cmul(l64, l64);
      *(LAS f32x4*)(lds + L_EX + lo16) = (f32x4){lam.r, lam.i, 2.f * (nr * pa + ni * pb) / den, 2.f * (ni * pa - nr * pb) / den};
      *(LAS f32x4*)(lds + L_EX + 1024 + lo16) = (f32x4){l16.r, l16.i, l64.r, l64.i};
      *(LAS unsigned*)(lds + L_EX + 2048 + lane * 4) = ud;
    }
    LDEPOCH();
    if (tid == 0) *(LAS u64*)(lds + L_TAG) = tag;
    {
      const unsigned x_off = (32 * th + r32) * 128;
      const int wrow = 8 * (g4 >> 1) + tq;
      bf16x8 fax[4], faw[4], fbx[4], fbw[4];
      f32x16 q = zero16;
#define PRD(B, i, P) _Pragma("unroll") for (int ks = 0; ks < 4; ++ks) { B##x[ks] = ldsf(lds + (i) * SK + x_off + (((2 * ks + hi) ^ xsw) << 4)); B##w[ks] = ldstr2(lds + (i) * SK + w_off + ks * 16 * (P), 4 * (P)); } SBAR()
#define PMM(B, SW) _Pragma("unroll") for (int ks = 0; ks < 4; ++ks) q = (SW) ? MFMA(B##x[ks], B##w[ks], q) : MFMA(B##w[ks], B##x[ks], q); PIN(q); SBAR()
#define PROJ(P, SW) do { VMW(6); BAR(); PRD(fa, 0, P); PRD(fb, 1, P); PMM(fa, SW); PRD(fa, 2, P); PMM(fb, SW); PRD(fb, 3, P); PMM(fa, SW); \
        VMW(0); BAR(); PRD(fa, 4, P); PMM(fb, SW); PRD(fb, 5, P); PMM(fa, SW); PRD(fa, 6, P); PMM(fb, SW); PMM(fa, SW); } while (0)
      if (j < 7) {
        const unsigned w_off = K_W + wrow * 128 + ((64 * chh + 32 * (g4 & 1) + 8 * tp) ^ ((tq & 2) << 5));
        PROJ(128, false);
        STAMP(1);
        const float qs = 0.18898223650461361f * 1.4426950408889634f;
#pragma unroll
        for (int g = 0; g < 4; ++g)
          __builtin_amdgcn_raw_buffer_store_b64((u32x2){pk(q[4 * g] * qs, q[4 * g + 1] * qs), pk(q[4 * g + 2] * qs, q[4 * g + 3] * qs)}, rs_ws, lane * 8 + g * 512,
                                                WS_Q + ((2 * c + th) * 112 + 16 * j + 8 * chh) * 256, SC1);
      } else {
        const unsigned w_off = K_W + chh * 3584 + wrow * 56 + (16 * (g4 & 1) + 4 * tp) * 2;
        if (chh) { PROJ(56, true);
#pragma unroll
          for (int r = 0; r < 16; ++r) q[r] = r32 < 28 ? q[r] : (r32 == 28 ? 1.f : 0.f);
        } else { PROJ(56, false); if (hi) { q[12] = 0.f; q[13] = 0.f; q[14] = 0.f; q[15] = 0.f; } }
        STAMP(1);
        const unsigned ko = WS_KV + (2 * c + th) * 4096 + chh * 2048;
        BST(U4(pack8(q, 0)), rs_ws, lo16, ko); BST(U4(pack8(q, 1)), rs_ws, lo16 + 1024, ko);
      }
      publish4(ws, WS_FP + (c * 8 + j) * 128, tag, lane, cPQ);
    }
    KV A0, A1, A2, A3;
    A3.v1 = (bf16x8){};
    f32x16 O[4] = {zero16, zero16, zero16, zero16};
    f32x16 Sa, Sb = zero16; u32x4 Pa, Pb = {};
    PIN(O[0]); PIN(O[1]); PIN(O[2]); PIN(O[3]); PIN(Sb); PIN(Pb); PIN(A3.v1);
    STAMP(2);
    poll(ws, WS_FP + (lane < 8 ? (wave * 8 + lane) * 8 + 7 : c * 8 + ((56 * j + (lane & 1) * 55) >> 6)) * 128, tag);
    STAMP(3);
    const __amdgpu_buffer_rsrc_t rs_kv = __builtin_amdgcn_make_buffer_rsrc(ws + WS_KV + wave * 65536, 0, 65536, 0x00020000);
#define LDKV(B, T) do { const unsigned so_ = (((T) + loc) & 15) * 4096; \
                        B.k0 = BF(BLD(rs_kv, lo16, so_)); B.k1 = BF(BLD(rs_kv, lo16, so_ + 1024)); \
                        B.v0 = BF(BLD(rs_kv, lo16, so_ + 2048)); B.v1 = BF(BLD(rs_kv, lo16, so_ + 3072)); } while (0)
    bf16x8 Q0[4], Q1[4];
    u32x2 qp[4][4];
#define LDQ(i) _Pragma("unroll") for (int u = 0; u < 4; ++u) qp[i][u] = __builtin_amdgcn_raw_buffer_load_b64(rs_ws, lane * 8 + u * 512, \
                 WS_Q + ((2 * c + ((wave ^ (3 - (i))) >> 1)) * 112 + 7 * (2 * j + ((wave ^ (3 - (i))) & 1))) * 256, SC1)
    LDQ(0); LDKV(A0, 0); LDQ(1); LDQ(2); LDQ(3); LDKV(A1, 1); LDKV(A2, 2);
    SBAR();
#pragma unroll
    for (int i = 0; i < 4; ++i) { Q0[i] = BF(((u32x4){qp[i][0][0], qp[i][0][1], qp[i][1][0], qp[i][1][1]}));
      Q1[i] = BF(((u32x4){qp[i][2][0], qp[i][2][1], hi ? 0u : qp[i][3][0], hi ? 0u : qp[i][3][1]})); }
    Sa = MFMA(A0.k0, Q0[0], zero16); Sa = MFMA(A0.k1, Q1[0], Sa);
    STAMP(9);
#define UNIT(S, Sn, KN, QN, VP, OP, VC, OC, X) do { \
      Pb[2] = pk(Sn[12], Sn[13]); Pb[3] = pk(Sn[14], Sn[15]); \
      if ((QN) >= 0) Sn = MFMA(KN.k0, Q0[(QN) & 3], zero16); S[0] = EX(S[0]); S[1] = EX(S[1]); S[2] = EX(S[2]); S[3] = EX(S[3]); SBAR(); \
      if ((QN) >= 0) Sn = MFMA(KN.k1, Q1[(QN) & 3], Sn); Pa[0] = pk(S[0], S[1]); Pa[1] = pk(S[2], S[3]); S[4] = EX(S[4]); S[5] = EX(S[5]); S[6] = EX(S[6]); S[7] = EX(S[7]); SBAR(); \
      X; SBAR(); \
      OP = MFMA(VP.v1, BF(Pb), OP); Pa[2] = pk(S[4], S[5]); Pa[3] = pk(S[6], S[7]); S[8] = EX(S[8]); S[9] = EX(S[9]); S[10] = EX(S[10]); S[11] = EX(S[11]); SBAR(); \
      OC = MFMA(VC.v0, BF(Pa), OC); Pb[0] = pk(S[8], S[9]); Pb[1] = pk(S[10], S[11]); S[12] = EX(S[12]); S[13] = EX(S[13]); S[14] = EX(S[14]); S[15] = EX(S[15]); SBAR(); \
    } while (0)
#define TILE(CUR, NXT, PRV, T, LD) do { \
      UNIT(Sa, Sb, CUR, 1, PRV, O[3], CUR, O[0], ); \
      if (LD) LDKV(PRV, (T) + 3); SBAR(); \
      UNIT(Sb, Sa, CUR, 2, CUR, O[0], CUR, O[1], ); \
      UNIT(Sa, Sb, CUR, 3, CUR, O[1], CUR, O[2], ); \
      UNIT(Sb, Sa, NXT, 0, CUR, O[2], CUR, O[3], ); } while (0)
#pragma unroll 1
    for (int t = 0; t < 12; t += 4) {
      TILE(A0, A1, A3, t, 1); TILE(A1, A2, A0, t + 1, 1); TILE(A2, A3, A1, t + 2, 1); TILE(A3, A0, A2, t + 3, 1);
    }
    TILE(A0, A1, A3, 12, 1);
    const lptr oc = lds + L_OC + lo16;
#define OUTP(T, I) _Pragma("unroll") for (int q4 = 0; q4 < 4; ++q4) *(LAS f32x4*)(oc + ((wave ^ I) * 3 + I - 1) * 4096 + q4 * 1024) = (f32x4){T[4 * q4], T[4 * q4 + 1], T[4 * q4 + 2], T[4 * q4 + 3]}
    f32x4 pv[12];
    UNIT(Sa, Sb, A1, 1, A0, O[3], A1, O[0], ); UNIT(Sb, Sa, A1, 2, A1, O[0], A1, O[1], ); UNIT(Sa, Sb, A2, 0, A1, O[1], A1, O[2], );
    UNIT(Sb, Sa, A2, 1, A1, O[2], A2, O[0], ); UNIT(Sa, Sb, A2, 2, A2, O[0], A2, O[1], ); UNIT(Sb, Sa, A3, 0, A2, O[1], A2, O[2], );
    UNIT(Sa, Sb, A3, 1, A2, O[2], A3, O[0], ); UNIT(Sb, Sa, A3, 2, A3, O[0], A3, O[1], lds_wait(j < 7 ? cCR : cPB, 4));
    UNIT(Sa, Sb, A1, 3, A3, O[1], A3, O[2], OUTP(O[0], 3));
    UNIT(Sb, Sa, A2, 3, A3, O[2], A1, O[3], OUTP(O[1], 2));
    UNIT(Sa, Sb, A3, 3, A1, O[3], A2, O[3], OUTP(O[2], 1); lds_signal(cBB));
    UNIT(Sb, Sa, A3, -1, A2, O[3], A3, O[3], lds_wait(cBB, 4); _Pragma("unroll") for (int s2 = 0; s2 < 12; ++s2) pv[s2] = *(LAS f32x4*)(oc + wave * 12288 + s2 * 1024));
    Pb[2] = pk(Sb[12], Sb[13]); Pb[3] = pk(Sb[14], Sb[15]);
    O[3] = MFMA(A3.v1, BF(Pb), O[3]);
    STAMP(4);
    __builtin_amdgcn_s_setprio(2);
#pragma unroll
    for (int s2 = 0; s2 < 4; ++s2) pv[s2] = pv[s2] + pv[s2 + 4] + pv[s2 + 8];
    STAMP(10);
    unsigned lo16b = lo16; PIN(lo16b);
    const int lane = lo16b >> 4, r32 = lane & 31, hi = lane >> 5, g4 = lane >> 4, tq = (lane & 15) >> 2, tp = lane & 3;
    f32x16 o = O[3];
#pragma unroll
    for (int r = 0; r < 16; ++r) o[r] += pv[r >> 2][r & 3];
    {
      float l = swap_hi(o[12]);
      float am = 0.f;
#pragma unroll
      for (int r = 0; r < 16; r += 2) am = __builtin_fmaxf(__builtin_fmaxf(am, __builtin_fabsf(o[r])), __builtin_fabsf(o[r + 1]));
      const bool ok = (l >= 0x1p-90f) && (l <= 0x1p110f) && (am <= 0x1p126f);
      const bool slow = !wall(ok);
      if (slow) {
        const __amdgpu_buffer_rsrc_t rk = __builtin_amdgcn_make_buffer_rsrc(ws + WS_KV, 0, 262144, 0x00020000);
        poll(ws, WS_FP + (r32 * 8 + 7) * 128, tag);
        float m = -__builtin_inff();
#pragma unroll 1
        for (int t = 0; t < 64; ++t) {
          f32x16 s = MFMA(BF(BLD(rk, lo16, t * 4096)), Q0[3], zero16); s = MFMA(BF(BLD(rk, lo16 + 1024, t * 4096)), Q1[3], s);
#pragma unroll
          for (int r = 0; r < 16; ++r) m = __builtin_fmaxf(m, s[r]);
        }
        m = __builtin_fmaxf(swap_lo(m), swap_hi(m));
        o = zero16;
#pragma unroll 1
        for (int t = 0; t < 64; ++t) {
          f32x16 s = MFMA(BF(BLD(rk, lo16, t * 4096)), Q0[3], zero16); s = MFMA(BF(BLD(rk, lo16 + 1024, t * 4096)), Q1[3], s);
#pragma unroll
          for (int r = 0; r < 16; ++r) s[r] = EX(s[r] - m);
          o = MFMA(BF(BLD(rk, lo16 + 2048, t * 4096)), pack8(s, 0), o); o = MFMA(BF(BLD(rk, lo16 + 3072, t * 4096)), pack8(s, 1), o);
        }
        l = swap_hi(o[12]);
      }
      const float inv = __builtin_amdgcn_rcpf(l);
#pragma unroll
      for (int r = 0; r < 16; ++r) o[r] *= inv;
      if (hi) { o[12] = 0.f; o[13] = 0.f; o[14] = 0.f; o[15] = 0.f; }
      u32x4 f0 = U4(pack8(o, 0)), f1 = U4(pack8(o, 1));
      if (slow) {
#pragma unroll
        for (int k = 0; k < 4; ++k) { f0[k] = umin(f0[k], 0xfffffffeu); f1[k] = umin(f1[k], 0xfffffffeu); }
      }
      BST(f0, rs_ws, lo16, o_own); BST(f1, rs_ws, lo16 + 1024, o_own);
    }
    lds_signal(cOC);
    lds_signal(cGO + 4 * wave);
    STAMP(5);
    if (j == 7) { STAMP_OUT(); return; }
    {
      const CAS Params* kp = KARGS();
      const __amdgpu_buffer_rsrc_t rs_out = __builtin_amdgcn_make_buffer_rsrc(kp->out, 0, 2048 * 896 * 4, 0x00020000);
      lds_wait(cWO, 4);
      u32x4 of[16];
      const unsigned ob = WS_O + (((2 * c + th) * 16 + 8 * chh) * 2) * 1024;
#define LDO(k) of[k] = BLD(rs_ws, lo16 + ((k) & 3) * 1024, ob + ((k) >> 2) * 4096)
      const int rowb = 224 * chh + 4 * (g4 >> 1) + tq;
      const unsigned wb = (L_WO + rowb * 128 + ((32 * (g4 & 1) + 8 * tp) ^ ((tq & 2) << 5))) ^ (chh << 6);
      bf16x8 w0[16], w1[16];
#pragma unroll
      for (int k = 0; k < 16; ++k) w1[k] = ldstr2(lds + (wb ^ 64) + (28 * (k >> 1) + 16 * (k & 1)) * 128, 1024);
      SBAR();
      {
        unsigned have = 0;
#pragma unroll 1
        for (unsigned s = 0; s < SPIN_CAP; ++s) {
          const unsigned w = __builtin_amdgcn_raw_buffer_load_b32(rs_ws, (lane & 7) * 2048 + 2044, ob, SC1);
          const unsigned rdy = (s + 1 < SPIN_CAP ? (unsigned)__builtin_amdgcn_ballot_w64(w != ~0u) : ~0u) & 0xffu & ~have;
#pragma unroll
          for (int h = 0; h < 8; ++h) if (rdy >> h & 1) { LDO(2 * h); LDO(2 * h + 1); }
          have |= rdy;
          if (have == 0xffu) break;
          __builtin_amdgcn_s_sleep(2);
        }
      }
      f32x16 acc0 = zero16, acc1 = zero16;
#define OBAD(h) (!wall(umax(umax(umax(umax(of[2 * (h)][0], of[2 * (h)][1]), umax(of[2 * (h)][2], of[2 * (h)][3])), umax(of[2 * (h) + 1][0], of[2 * (h) + 1][1])), umax(of[2 * (h) + 1][2], of[2 * (h) + 1][3])) != ~0u))
#define OHEAD(h) do { \
        if (OBAD(h)) { \
          _Pragma("unroll 1") for (unsigned s = 0; s < SPIN_CAP; ++s) { LDO(2 * h); LDO(2 * h + 1); if (!OBAD(h)) break; __builtin_amdgcn_s_sleep(2); } \
        } \
        acc1 = MFMA(BF(of[2 * h]), w1[2 * h], acc1); acc1 = MFMA(BF(of[2 * h + 1]), w1[2 * h + 1], acc1); PIN(acc1); \
        w0[2 * h] = ldstr2(lds + wb + 28 * h * 128, 1024); w0[2 * h + 1] = ldstr2(lds + wb + (28 * h + 16) * 128, 1024); } while (0)
      OHEAD(0); STAMP(6); OHEAD(1); OHEAD(2); OHEAD(3); OHEAD(4); OHEAD(5); OHEAD(6); OHEAD(7);
      STAMP(11);
      lds_wait(cOC, 4);
#pragma unroll
      for (int k = 0; k < 4; ++k) acc0 = MFMA(BF(of[k]), w0[k], acc0);
      SBAR();
      const lptr tw = lds + L_OC + hi * 512 + r32 * 4;
#pragma unroll
      for (int r = 0; r < 16; ++r) *(LAS float*)(tw + (wave ^ 1) * 4096 + ((r & 3) + 8 * (r >> 2)) * 128) = acc1[r];
      SBAR();
#pragma unroll
      for (int k = 4; k < 8; ++k) acc0 = MFMA(BF(of[k]), w0[k], acc0);
      PIN(acc0);
      lds_signal(cPR + 4 * th);
#pragma unroll
      for (int k = 8; k < 16; ++k) acc0 = MFMA(BF(of[k]), w0[k], acc0);
      PIN(acc0);
      lds_wait(cPR + 4 * th, 2);
      f32x4 pa[4];
#pragma unroll
      for (int k = 0; k < 4; ++k) pa[k] = *(LAS f32x4*)(lds + L_OC + wave * 4096 + k * 1024 + lo16);
#pragma unroll
      for (int r = 0; r < 16; ++r) *(LAS float*)(tw + 16384 + wave * 4096 + ((r & 3) + 8 * (r >> 2)) * 128) = acc0[r];
      MEMBAR();
      STAMP(12);
      const unsigned oo = ((64 * c + 32 * th + (lane >> 3)) * 896 + 448 + 64 * j + 32 * chh + (lane & 7) * 4) * 4;
#pragma unroll
      for (int k = 0; k < 4; ++k)
        __builtin_amdgcn_raw_buffer_store_b128(U4(pa[k] + *(LAS f32x4*)(lds + L_OC + 16384 + wave * 4096 + k * 1024 + lo16)), rs_out, oo + k * 28672, 0, 17);
    }
    STAMP(7);
    STAMP_OUT();
  } else {
    if (j == 7) {
      VMW(6); BAR(); VMW(0); BAR();
      lds_wait_<4>(cPQ, 4);
      { const u32x2 t2 = *(LAS u32x2*)(lds + L_TAG);
        tag = ((u64)(unsigned)__builtin_amdgcn_readfirstlane(t2[1]) << 32) | (unsigned)__builtin_amdgcn_readfirstlane(t2[0]); }
      __builtin_amdgcn_s_sleep(64);
      MARK();
      publish4(ws, WS_FH + (c * 8 + 7) * 128, tag, lane, cPB);
      if (c == 0 && wa == 0) {
        __builtin_amdgcn_s_sleep(127);
#pragma unroll 1
        for (int k = 0; k < 4; ++k) poll(ws, WS_FP + (k * 64 + lane) * 128, tag);
        if (lane == 0) __hip_atomic_store((GAS u64*)(ws + WS_EPOCH), tag, RLX_AGENT);
      }
      STAMP_OUT();
      return;
    }
    VMW(6); BAR(); LDCH(L_SA); VMW(6); BAR();
    cf lam, coef, l16, l64; f32x16 ur = zero16, ui = zero16;
    f32x16 acc[4] = {zero16, zero16, zero16, zero16};
    const unsigned cs0 = wa * 16384u, cs1 = L_R1 + wa * 12288u;
    unsigned a_htw = L_HT + wa * 4096 + r32 * 64 + hi * 32, a_htr = L_HT + wa * 4096 + (8 * (g4 >> 1) + tq) * 64 + (g4 & 1) * 32 + tp * 8;
    unsigned a_hst = WS_H + ((2 * c + th) * 56 + 4 * j + 2 * chh) * 1024 + lo16;
    unsigned a_c[4], a_cn[2];
#pragma unroll
    for (int f = 0; f < 4; ++f) a_c[f] = cs0 + (32 * chh + r32) * 128 + (((2 * f + hi) ^ xsw) << 4);
#pragma unroll
    for (int f = 0; f < 2; ++f) a_cn[f] = cs1 + 8192 + (32 * chh + r32) * 64 + (((2 * f + hi) ^ ((r32 >> 2) & 3)) << 4);
    unsigned a_vh = (lane >> 2) * 896 + (((lane & 3) ^ ((lane >> 4) & 3)) << 4) + 384 + chh * 64, a_vo = ve ^ 64;
    unsigned a_vw = lrow * 896 + (((lane & 7) ^ ((lrow & 2) << 1)) << 4);
    unsigned a_fl = WS_FH + (c * 8 + (lane & 7)) * 128;
    unsigned a_el = (31 - umin(r32, c - 1)) * 512 + 4, a_eo = (32 - c + hi) * 512 + r32 * 16;
    {
      LDCH(0); LDCH(CH);
      { const f32x4 c0 = *(LAS f32x4*)(lds + L_EX + (32 * chh + r32) * 16), c1 = *(LAS f32x4*)(lds + L_EX + 1024 + (32 * chh + r32) * 16);
        lam = {c0[0], c0[1]}; coef = {c0[2], c0[3]}; l16 = {c1[0], c1[1]}; l64 = {c1[2], c1[3]}; ud = *(LAS unsigned*)(lds + L_EX + 2048 + (32 * chh + r32) * 4); }
      PIN(a_htw); PIN(a_htr); PIN(a_hst); PIN(a_c[0]); PIN(a_c[1]); PIN(a_c[2]); PIN(a_c[3]); PIN(a_cn[0]); PIN(a_cn[1]); PIN(a_vh); PIN(a_vw); PIN(a_fl); PIN(a_el); PIN(a_eo); PIN(a_vo);
      const int trow = 32 * th + 16 * ((r32 >> 2) & 1) + (r32 & 3) + 4 * (r32 >> 3);
      const unsigned xa_off = trow * 128, xa_sw = (trow >> 1) & 7, b_off = 8192 + (32 * chh + r32) * 128;
      bf16x8 gax[4], gar[4], gai[4], gbx[4], gbr[4], gbi[4];
#define GRD(B, s_) _Pragma("unroll") for (int ks = 0; ks < 4; ++ks) { B##x[ks] = ldsf(lds + (s_) + xa_off + (((2 * ks + hi) ^ xa_sw) << 4)); \
        B##r[ks] = ldsf(lds + (s_) + b_off + (((2 * ks + hi) ^ xsw) << 4)); B##i[ks] = ldsf(lds + (s_) + b_off + 8192 + (((2 * ks + hi) ^ xsw) << 4)); } SBAR()
#define GMM(B) _Pragma("unroll") for (int ks = 0; ks < 4; ++ks) { ur = MFMA(B##x[ks], B##r[ks], ur); ui = MFMA(B##x[ks], B##i[ks], ui); } PIN(ur); PIN(ui); SBAR()
#define GAP() do { SBAR(); asm volatile("s_nop %0" :: "i"(7)); SBAR(); } while (0)
#define GMS(B) _Pragma("unroll") for (int ks = 0; ks < 4; ++ks) { ur = MFMA(B##x[ks], B##r[ks], ur); GAP(); ui = MFMA(B##x[ks], B##i[ks], ui); GAP(); } PIN(ur); PIN(ui); SBAR()
      __builtin_amdgcn_s_setprio(1);
      STAMP(12);
      lds_wait_<1>(cPQ, 4);
      LDCH(2 * CH); LDCH(3 * CH); LDCH(4 * CH);
      VMW(18); lds_barrierA(cAB, 4); GRD(ga, L_SA); GRD(gb, 0); GMM(ga); GRD(ga, CH); GMM(gb);
      VMW(6); lds_barrierA(cAB, 8);
      STAMP(13);
      GRD(gb, 2 * CH); LDCH(L_SA); GMM(ga); GRD(ga, 3 * CH); GMS(gb);
      VMW(6); lds_barrierA(cAB, 12);
      STAMP(14);
      GRD(gb, 4 * CH); GMS(ga);
      VMW(0); lds_barrierA(cAB, 16);
      STAMP(15);
      GRD(ga, L_SA); GMS(gb); GMS(ga);
      { const u32x2 t2 = *(LAS u32x2*)(lds + L_TAG);
        tag = ((u64)(unsigned)__builtin_amdgcn_readfirstlane(t2[1]) << 32) | (unsigned)__builtin_amdgcn_readfirstlane(t2[0]); }
    }
    {
      const int e = r32 - 8 * hi; const unsigned dv = ud << ((e & 1) * 16);
      const lptr xr = lds + L_SA + (32 * th + r32) * 128;
#pragma unroll
      for (int s2 = 0; s2 < 2; ++s2) {
        const u32x4 dg = {(e >> 1) == 8 * s2 ? dv : 0u, (e >> 1) == 8 * s2 + 1 ? dv : 0u, (e >> 1) == 8 * s2 + 2 ? dv : 0u, (e >> 1) == 8 * s2 + 3 ? dv : 0u};
        acc[0] = MFMA(ldsf(xr + (((4 * chh + 2 * s2 + hi) ^ xsw) << 4)), BF(dg), acc[0]);
      }
      PIN(acc[0]);
    }
    STAMP(1);
    cf e = {0.f, 0.f};
#pragma unroll
    for (int r = 0; r < 16; ++r) {
      const float a = coef.r * ur[r] - coef.i * ui[r], b2 = -(coef.r * ui[r] + coef.i * ur[r]);
      ur[r] = a; ui[r] = b2; e = r ? cfma(lam, e, (cf){a, b2}) : (cf){a, b2};
    }
    const int seg = 2 * th + hi;
    *(LAS f32x2*)(lds + L_EX + (seg * 64 + 32 * chh + r32) * 8) = (f32x2){e.r, e.i};
    lds_barrierA(cAB, 20);
    cf g0 = {0.f, 0.f}, lseg = {1.f, 0.f};
    {
      cf E = {0.f, 0.f};
#pragma unroll
      for (int s = 0; s < 4; ++s) {
        const f32x2 v = *(LAS f32x2*)(lds + L_EX + (s * 64 + 32 * chh + r32) * 8);
        if (s == seg) g0 = E;
        if (s < seg) lseg = cmul(lseg, l16);
        E = cfma(l16, E, (cf){v[0], v[1]});
      }
      if (th == 0 && hi == 0 && c < 31)
        BST(((u32x4){f2u(E.r), (unsigned)tag, f2u(E.i), (unsigned)tag}), rs_ws, r32 * 16, WS_E + ((2 * j + chh) * 32 + 31 - c) * 512);
    }
    __builtin_amdgcn_s_setprio(0);
    const CAS Params* kp = KARGS();
    {
      const u32x4 rc = mkrs((const char*)(th ? kp->cim : kp->cre) + (size_t)j * 57344, 57344);
      int n2 = (6 * cl + 2 * ((b & 7) % 3)) % 24;
#pragma unroll
      for (int n = 0; n < 24; ++n) {
        DMA(rc, (n & 1) ? a_vo : ve, (n2 & 7) * 7168 + (n2 >> 3) * 128 + chh * 512, (n2 < 16 ? cs0 : cs1 - 16384) + n2 * 1024, 0);
        n2 = n2 == 23 ? 0 : n2 + 1; }
#pragma unroll
      for (int n = 0; n < 4; ++n) DMA(rc, a_vh, n * 14336, cs1, 8192 + n * 1024);
    }
    STAMP(2);
    cf G = g0;
    if (c > 0) {
      const cf l128 = cmul(l64, l64);
      const __amdgpu_buffer_rsrc_t re = __builtin_amdgcn_make_buffer_rsrc(ws + WS_E + (2 * j + chh) * 16384, 0, 16384, 0x00020000);
      const unsigned eo = a_eo, want = (c - hi + 1) >> 1;
      u32x4 ev[16];
#pragma unroll 1
      for (unsigned s = 0; s < SPIN_CAP; ++s) {
        const unsigned t1 = __builtin_amdgcn_raw_buffer_load_b32(re, a_el, 0, SC1);
        if (wall(t1 == (unsigned)tag)) break;
        __builtin_amdgcn_s_sleep(2);
      }
      MEMBAR();
#pragma unroll 1
      for (unsigned s = 0; s < SPIN_CAP; ++s) {
        unsigned got = 0;
#pragma unroll
        for (int m = 0; m < 8; ++m) ev[m] = BLD(re, eo, m * 1024);
        if (c > 16) {
#pragma unroll
          for (int m = 8; m < 16; ++m) ev[m] = BLD(re, eo, m * 1024);
        }
        VMW(0);
        __builtin_amdgcn_s_setprio(1);
        if (c > 16) {
#pragma unroll
          for (int m = 8; m < 16; ++m) got += ((ev[m][1] ^ (unsigned)tag) | (ev[m][3] ^ (unsigned)tag)) == 0;
        }
#pragma unroll
        for (int m = 0; m < 8; ++m) got += ((ev[m][1] ^ (unsigned)tag) | (ev[m][3] ^ (unsigned)tag)) == 0;
        if (wall(got == want)) break;
        __builtin_amdgcn_s_sleep(4);
      }
      STAMP(3);
      cf H = {0.f, 0.f};
      if (c > 16) {
#pragma unroll
        for (int m = 15; m >= 8; --m) H = cfma(l128, H, (cf){u2f(ev[m][0]), u2f(ev[m][2])});
      }
#pragma unroll
      for (int m = 7; m >= 0; --m) H = cfma(l128, H, (cf){u2f(ev[m][0]), u2f(ev[m][2])});
      H = cfma(l64, (cf){swap_hi(H.r), swap_hi(H.i)}, (cf){swap_lo(H.r), swap_lo(H.i)});
      G = cfma(lseg, H, g0);
    }
    STAMP(4);
    __builtin_amdgcn_s_setprio(1);
    {
#pragma unroll
      for (int r = 0; r < 16; ++r) { G = cfma(lam, G, (cf){ur[r], ui[r]}); ur[r] = G.r; ui[r] = G.i; }
      *(LAS bf16x8*)(lds + a_htw) = pack8(ur, 0); *(LAS bf16x8*)(lds + a_htw + 16) = pack8(ur, 1);
      *(LAS bf16x8*)(lds + a_htw + 2048) = pack8(ui, 0); *(LAS bf16x8*)(lds + a_htw + 2064) = pack8(ui, 1);
      unsigned a_hs2 = a_hst + 28 * 1024; PIN(a_hs2);
      __builtin_amdgcn_s_setprio(0);
      asm volatile("s_waitcnt lgkmcnt(0)" ::: "memory");
#pragma unroll
      for (int k = 0; k < 4; ++k) BST(U4(ldstr2(lds + a_htr + (k >> 1) * 2048 + (k & 1) * 1024, 256)), rs_ws, (k >> 1) ? a_hs2 : a_hst, (k & 1) * 1024);
      MARK();
      publish4(ws, WS_FH + (c * 8 + j) * 128, tag, lane, cPB);
    }
    STAMP(5);
    bf16x8 cfr[14][2];
    const int cflip = chh ? -1 : 1;
#define LDC(f) do { if ((f) < 12) { const unsigned a_ = a_c[(f) & 3] + ((f) < 8 ? ((f) >> 2) * 8192 : cs1 - cs0); cfr[f][0] = ldsf(lds + a_); cfr[f][1] = ldsf(lds + a_ + cflip * 4096); } \
                    else { cfr[f][0] = ldsf(lds + a_cn[(f) & 1]); cfr[f][1] = ldsf(lds + a_cn[(f) & 1] + cflip * 2048); } } while (0)
#pragma unroll
    for (int f = 0; f < 8; ++f) LDC(f);
    lds_signal(cCR);
    STAMP(10);
    lds_wait_<1>(cCR, 4);
    {
      const u32x4 rw = mkrs((const char*)kp->wo + j * 128 + wa * 7168, 401408 - j * 128 - wa * 7168);
      int m2 = (4 * cl + (b & 7)) % 14;
#pragma unroll
      for (int m = 0; m < 14; ++m) { DMA(rw, a_vw, m2 * 28672, wa * 1024 + m2 * 4096, L_WO); m2 = m2 == 13 ? 0 : m2 + 1; }
      if (wa == 0 && lane < 32) *(LAS u32x4*)(lds + L_WO + 57344 + lo16) = (u32x4){0, 0, 0, 0};
    }
    poll(ws, a_fl, tag);
    VMW(0);
    lds_signal(cWO);
    STAMP(6);
    {
      const unsigned hb = WS_H + (2 * c * 56 + 14 * wa) * 1024, h0 = hb + th * 57344, h1 = hb + (th ^ 1) * 57344;
      const unsigned fa = chh * 2048, fb = chh ? 0u - 12288u : 0u;
      u32x4 hf[14][2];
#define LDH(f) do { hf[f][0] = BLD(rs_ws, lo16, h0 + (f) * 1024 + ((f) < 12 ? fa : fb)); hf[f][1] = BLD(rs_ws, lo16, h1 + (f) * 1024 + ((f) < 12 ? fa : fb)); } while (0)
#define MMH(f) do { acc[0] = MFMA(BF(hf[f][0]), cfr[f][0], acc[0]); acc[1] = MFMA(BF(hf[f][0]), cfr[f][1], acc[1]); \
                    acc[2] = MFMA(BF(hf[f][1]), cfr[f][0], acc[2]); acc[3] = MFMA(BF(hf[f][1]), cfr[f][1], acc[3]); \
                    PIN(acc[0]); PIN(acc[1]); PIN(acc[2]); PIN(acc[3]); } while (0)
#pragma unroll
      for (int f = 0; f < 14; ++f) LDH(f);
      SBAR();
      lds_wait_<4>(cGO + 4 * wa, 1);
      STAMP(9);
      __builtin_amdgcn_s_setprio(1);
#pragma unroll
      for (int f = 0; f < 8; ++f) { MMH(f); SBAR(); if (f >= 1 && f < 7) LDC(f + 7); SBAR(); }
      lds_signal(cP4);
#pragma unroll
      for (int f = 8; f < 14; ++f) MMH(f);
    }
    lds_wait(cP4, 4);
    STAMP(11);
    rs_write(lds + L_G2, wa, lane, acc[1], acc[2], acc[3]);
    lds_barrierA(cAB, 24);
    f32x16 y = rs_read(lds + L_G2, wa, lane, acc[0]);
    const __amdgpu_buffer_rsrc_t rs_out = __builtin_amdgcn_make_buffer_rsrc(kp->out, 0, 2048 * 896 * 4, 0x00020000);
    MEMBAR();
#pragma unroll
    for (int r = 0; r < 16; ++r) *(LAS float*)(lds + L_G2 + wa * 12288 + hi * 512 + r32 * 4 + ((r & 3) + 8 * (r >> 2)) * 128) = y[r];
    MEMBAR();
    const unsigned oo = ((64 * c + 32 * th + (lane >> 3)) * 896 + 64 * j + 32 * chh + (lane & 7) * 4) * 4;
#pragma unroll
    for (int k = 0; k < 4; ++k) __builtin_amdgcn_raw_buffer_store_b128(*(LAS u32x4*)(lds + L_G2 + wa * 12288 + k * 1024 + lo16), rs_out, oo + k * 28672, 0, 17);
    STAMP(7);
    STAMP_OUT();
  }
}

extern "C" void kernel_launch(void* const* d_in, const int* in_sizes, int n_in, void* d_out, int out_size, void* d_ws, size_t ws_size,
                              hipStream_t stream) {
  Params p;
  p.xa = (const u16*)d_in[0]; p.lre = (const u16*)d_in[1]; p.lim = (const u16*)d_in[2]; p.bre = (const u16*)d_in[3]; p.bim = (const u16*)d_in[4];
  p.cre = (const u16*)d_in[5]; p.cim = (const u16*)d_in[6]; p.ldt = (const u16*)d_in[7]; p.dsk = (const u16*)d_in[8];
  p.xb = (const u16*)d_in[9]; p.wq = (const u16*)d_in[10]; p.wk = (const u16*)d_in[11]; p.wv = (const u16*)d_in[12]; p.wo = (const u16*)d_in[13];
  p.out = (float*)d_out; p.ws = (char*)d_ws;
  fused_kernel<<<256, 512, 0, stream>>>(p);
}
```

```cpp
#include <hip/hip_runtime.h>
#include <stdint.h>

#define DI __device__ __forceinline__
#define GAS __attribute__((address_space(1)))
#define LAS __attribute__((address_space(3)))
#define CAS __attribute__((address_space(4)))
typedef short bf16x8 __attribute__((ext_vector_type(8)));
typedef short s16x4 __attribute__((ext_vector_type(4)));
typedef float f32x16 __attribute__((ext_vector_type(16)));
typedef float f32x4 __attribute__((ext_vector_type(4)));
typedef float f32x2 __attribute__((ext_vector_type(2)));
typedef unsigned u32x4 __attribute__((ext_vector_type(4)));
typedef unsigned u32x2 __attribute__((ext_vector_type(2)));
typedef __bf16 bf16x2_t __attribute__((ext_vector_type(2)));
typedef unsigned long long u64;
typedef unsigned short u16;
typedef LAS char* lptr;
#define RLX_AGENT __ATOMIC_RELAXED, __HIP_MEMORY_SCOPE_AGENT
#define MFMA(a, b, c) __builtin_amdgcn_mfma_f32_32x32x16_bf16((a), (b), (c), 0, 0, 0)
#define SBAR() __builtin_amdgcn_sched_barrier(0)
#define PIN(x) asm volatile("" : "+v"(x))
#define MEMBAR() asm volatile("" ::: "memory")
#define SC1 16

constexpr unsigned WS_EPOCH = 0, WS_FH = 8192, WS_E = 0x10000, WS_FP = 0x48000,
                   WS_KV = 0x50000, WS_O = 0x100000, WS_H = 0x300000, WS_Q = 0x680000, WS_END = 0x880000;
constexpr unsigned SK = 16384, K_W = 8192;
constexpr unsigned CH = 24576, L_SA = 135168;
constexpr unsigned L_R1 = 107520, L_HT = 65536, L_WO = 0, L_OC = 57856, L_G2 = 107520, L_MISC = 159744, L_EX = L_MISC + 128, L_TOTAL = 163840;
constexpr unsigned cBB = L_MISC, cAB = L_MISC + 4, cCR = L_MISC + 8, cWO = L_MISC + 12, cOC = L_MISC + 16, cPB = L_MISC + 20, cPR = L_MISC + 24, cP4 = L_MISC + 32, cPQ = L_MISC + 52, cGO = L_MISC + 36, cPE = L_MISC + 56, L_TAG = L_MISC + 64;
constexpr unsigned SPIN_CAP = 1u << 18;

struct Params {
  const u16 *xb, *wq, *wk, *wv; char* ws; const u16 *lre, *lim, *ldt, *dsk, *xa, *bre, *bim, *cre, *cim, *wo;
  float* out;
};

DI float u2f(unsigned v) { return __builtin_bit_cast(float, v); }
DI unsigned f2u(float v) { return __builtin_bit_cast(unsigned, v); }
DI unsigned umin(unsigned a, unsigned b) { return a < b ? a : b; }
DI unsigned umax(unsigned a, unsigned b) { return a > b ? a : b; }
DI bool wall(bool x) { return __builtin_amdgcn_ballot_w64(!x) == 0; }
DI float bf2f(unsigned v) { return u2f(v << 16); }
DI unsigned pk(float a, float b) { f32x2 v = {a, b}; return __builtin_bit_cast(unsigned, __builtin_convertvector(v, bf16x2_t)); }
DI float rbf(float a) { return u2f(pk(0.f, a)); }
DI bf16x8 pack8(const f32x16& x, int s) {
  u32x4 p = {pk(x[8 * s], x[8 * s + 1]), pk(x[8 * s + 2], x[8 * s + 3]), pk(x[8 * s + 4], x[8 * s + 5]), pk(x[8 * s + 6], x[8 * s + 7])};
  return __builtin_bit_cast(bf16x8, p);
}
DI u32x4 mkrs(const void* base, unsigned bytes) { const u64 a = (u64)base; return (u32x4){(unsigned)a, (unsigned)(a >> 32) & 0xffffu, bytes, 0x20000u}; }
#define DMA(rs, voff, soff, ldsb, IMM) asm volatile("s_add_u32 m0, %3, %4\n\ts_nop 0\n\tbuffer_load_dwordx4 %0, %1, %2 offen lds" \
                                                    :: "v"(voff), "s"(rs), "s"(soff), "s"(ldsb), "i"(IMM) : "memory", "scc")
#define DMAX(rs, voff, soff, ldsb, IMM) asm volatile("s_add_u32 m0, %3, %4\n\ts_nop 0\n\tbuffer_load_dwordx4 %0, %1, %2 offen nt lds" \
                                                     :: "v"(voff), "s"(rs), "s"(soff), "s"(ldsb), "i"(IMM) : "memory", "scc")
DI bf16x8 ldsf(lptr p) { return *(LAS bf16x8*)p; }
DI s16x4 ldstr(lptr p) { return __builtin_amdgcn_ds_read_tr16_b64_v4i16((LAS s16x4*)p); }
DI bf16x8 ldstr2(lptr p, int second) { s16x4 lo = ldstr(p), hi = ldstr(p + second); return __builtin_shufflevector(lo, hi, 0, 1, 2, 3, 4, 5, 6, 7); }
DI void lds_signal(unsigned c) {
  asm volatile("s_waitcnt lgkmcnt(0)\n\ts_mov_b64 exec, 1\n\tds_add_u32 %0, %1\n\ts_mov_b64 exec, -1" :: "v"(c), "v"(1u) : "memory");
}
template <int SLEEP> DI void lds_wait_(unsigned c, unsigned target) {
  unsigned v, s, n;
  asm volatile("s_mov_b32 %2, 0x400000\n1:\n\tds_read_b32 %0, %3\n\ts_waitcnt lgkmcnt(0)\n\tv_readfirstlane_b32 %1, %0\n\ts_cmp_ge_u32 %1, %4\n\ts_cbranch_scc1 2f\n\t"
               "s_sleep %5\n\ts_sub_u32 %2, %2, 1\n\ts_cbranch_scc0 1b\n2:" : "=&v"(v), "=&s"(s), "=&s"(n) : "v"(c), "i"(target), "i"(SLEEP) : "memory", "scc");
}
#define lds_wait(c, t) lds_wait_<0>(c, t)
#define lds_barrier(c, t) do { lds_signal(c); lds_wait(c, t); } while (0)
#define lds_barrierA(c, t) do { lds_signal(c); lds_wait_<1>(c, t); } while (0)
DI void poll(const char* ws, unsigned off, u64 tag) {
#pragma unroll 1
  for (unsigned s = 0; s < SPIN_CAP; ++s) {
    u64 v = __hip_atomic_load((GAS u64*)(ws + off), RLX_AGENT);
    if (wall(v == tag)) break;
    __builtin_amdgcn_s_sleep(2);
  }
  MEMBAR();
}
DI void publish4(char* ws, unsigned off, u64 tag, int lane, unsigned cnt) {
  unsigned old;
  asm volatile("s_waitcnt vmcnt(0)\n\ts_mov_b64 exec, 1\n\tds_add_rtn_u32 %0, %1, %2\n\ts_waitcnt lgkmcnt(0)\n\ts_mov_b64 exec, -1" : "=&v"(old) : "v"(cnt), "v"(1u) : "memory");
  if (__builtin_amdgcn_readfirstlane(old) == 3 && lane == 0) __hip_atomic_store((GAS u64*)(ws + off), tag, RLX_AGENT);
}
DI float swap_lo(float x) {
  auto r = __builtin_amdgcn_permlane32_swap(f2u(x), f2u(x), false, false); return u2f(r[0]); }
DI float swap_hi(float x) {
  auto r = __builtin_amdgcn_permlane32_swap(f2u(x), f2u(x), false, false); return u2f(r[1]); }
struct cf { float r, i; };
DI cf cmul(cf a, cf b) { return {a.r * b.r - a.i * b.i, a.r * b.i + a.i * b.r}; }
DI cf cfma(cf a, cf b, cf c) { return {__builtin_fmaf(a.r, b.r, __builtin_fmaf(-a.i, b.i, c.r)), __builtin_fmaf(a.r, b.i, __builtin_fmaf(a.i, b.r, c.i))}; }

struct KV { bf16x8 k0, k1, v0, v1; };

DI void rs_write(lptr base, int w, int lane, const f32x16& t1, const f32x16& t2, const f32x16& t3) {
#define RSW(T, I) _Pragma("unroll") for (int q = 0; q < 4; ++q) *(LAS f32x4*)(base + ((w ^ I) * 3 + I - 1) * 4096 + q * 1024 + lane * 16) = (f32x4){T[4 * q], T[4 * q + 1], T[4 * q + 2], T[4 * q + 3]}
  RSW(t1, 1); RSW(t2, 2); RSW(t3, 3);
}
DI f32x16 rs_read(lptr base, int w, int lane, f32x16 a) {
  f32x4 v[12];
#pragma unroll
  for (int s = 0; s < 12; ++s) v[s] = *(LAS f32x4*)(base + w * 12288 + s * 1024 + lane * 16);
  SBAR();
#pragma unroll
  for (int s = 0; s < 12; ++s) { const int q = s & 3; a[4 * q] += v[s][0]; a[4 * q + 1] += v[s][1]; a[4 * q + 2] += v[s][2]; a[4 * q + 3] += v[s][3]; }
  return a;
}

#define VMW_(n) asm volatile("s_waitcnt vmcnt(" #n ")" ::: "memory")
#define VMW(n) VMW_(n)
#define BAR() asm volatile("s_waitcnt lgkmcnt(0)\n\ts_barrier" ::: "memory")
#define BLD(R, V, S) __builtin_amdgcn_raw_buffer_load_b128(R, V, S, SC1)
#define BST(D, R, V, S) __builtin_amdgcn_raw_buffer_store_b128(D, R, (V) + (S), 0, SC1)
#define BF(x) __builtin_bit_cast(bf16x8, x)
#define U4(x) __builtin_bit_cast(u32x4, x)
#define EX(x) __builtin_amdgcn_exp2f(x)
#define KARGS() ({ const CAS Params* kp_ = (const CAS Params*)__builtin_amdgcn_kernarg_segment_ptr(); asm volatile("" : "+s"(kp_)); kp_; })
#define NEXTK() do { k += step; k = k >= 7 ? k - 7 : k; } while (0)

#if defined(__HIP_DEVICE_COMPILE__)
#define NO_PK __attribute__((target("no-packed-fp32-ops")))
#else
#define NO_PK
#endif
__global__ void __launch_bounds__(512, 2) NO_PK fused_kernel(Params p) {
  __shared__ __attribute__((aligned(128))) char smem[L_TOTAL];
  const lptr lds = (lptr)smem;
  const int tid = threadIdx.x, lane = tid & 63, wave = __builtin_amdgcn_readfirstlane(tid >> 6);
  const int b = blockIdx.x, loc = b >> 3, cl = loc >> 3, c = (b & 7) * 4 + cl, j = loc & 7;
  const int lrow = lane >> 3;
  const unsigned ve = lrow * 896 + (((lane & 7) ^ (lrow >> 1)) << 4);
  const int step = 1 + (cl + (b & 7) * 4) % 6, k0 = j == 7 ? 0 : j;
  const bool isB = wave < 4;
  const int wa = wave & 3;
  char* const ws = p.ws;
  unsigned ua = 0, ub = 0, uc = 0, ud = 0;
  u32x4 rxa, rbr, rbi; int ka = k0;
  const unsigned vo = (ve ^ 64) + 7168, lba = wa * 2048;
#define NEXTKA() do { ka += step; ka = ka >= 7 ? ka - 7 : ka; } while (0)
#define LDCH(slot) do { NEXTKA(); const unsigned k_ = ka * 128; DMAX(rxa, ve, k_, lba, (slot)); DMAX(rxa, vo, k_, lba, (slot) + 1024); \
      DMA(rbr, ve, k_, lba, (slot) + 8192); DMA(rbr, vo, k_, lba, (slot) + 9216); DMA(rbi, ve, k_, lba, (slot) + 16384); DMA(rbi, vo, k_, lba, (slot) + 17408); } while (0)
  {
    const u32x4 rx = mkrs((const char*)p.xb + (size_t)c * 57344 + wave * 7168, 7168);
    const unsigned vx = ve ^ ((wave & 1) << 6), lb = wave * 1024;
    int k = k0;
    if (j == 7) {
      const u32x4 rw = mkrs(isB ? p.wk : p.wv, 25088);
      const unsigned vw = wa * 1024 + lane * 16, lw = K_W + (isB ? 0 : 3584) + wa * 1024; const bool half = wa < 3 || lane < 32;
#pragma unroll
      for (int i = 0; i < 7; ++i) { NEXTK(); DMAX(rx, vx, k * 128, lb, i * SK); if (half) DMA(rw, vw, k * 3584, lw, i * SK); }
    } else {
      const u32x4 rw = mkrs((const char*)p.wq + j * 128 + wave * 7168, 401408 - j * 128 - wave * 7168);
      const unsigned vw = lrow * 896 + (((lane & 7) ^ ((lrow & 2) << 1)) << 4);
      if (wave == 0) { const unsigned co = (64 * j + lane) * 2;
        asm volatile("global_load_ushort %0, %4, %5\n\tglobal_load_ushort %1, %4, %6\n\tglobal_load_ushort %2, %4, %7\n\tglobal_load_ushort %3, %4, %8"
                     : "=&v"(ua), "=&v"(ub), "=&v"(uc), "=&v"(ud) : "v"(co), "s"(p.lre), "s"(p.lim), "s"(p.ldt), "s"(p.dsk) : "memory"); }
#pragma unroll
      for (int i = 0; i < 7; ++i) { NEXTK(); DMAX(rx, vx, k * 128, lb, i * SK); DMA(rw, vw, k * 57344, lb, i * SK + K_W); }
      rxa = mkrs((const char*)p.xa + (size_t)c * 57344 + wa * 14336, 14336);
      rbr = mkrs((const char*)p.bre + (size_t)j * 57344 + wa * 14336, 14336);
      rbi = mkrs((const char*)p.bim + (size_t)j * 57344 + wa * 14336, 14336);
    }
  }
  if (tid < 16) ((LAS unsigned*)(lds + L_MISC))[tid] = 0;
  const int r32 = lane & 31, hi = lane >> 5;
  const int g4 = lane >> 4, tq = (lane & 15) >> 2, tp = lane & 3;
  const unsigned xsw = (r32 >> 1) & 7, lo16 = lane * 16;
  const int th = wa >> 1, chh = wa & 1;
  u64 tag;
  const __amdgpu_buffer_rsrc_t rs_ws = __builtin_amdgcn_make_buffer_rsrc(ws, 0, WS_END, 0x00020000);
  const f32x16 zero16 = {};
  const unsigned o_own = WS_O + (((2 * c + th) * 16 + 2 * j + chh) * 2) * 1024;
#define MARK() do { const u32x4 ones = {~0u, ~0u, ~0u, ~0u};       \
    BST(ones, rs_ws, lo16, o_own); BST(ones, rs_ws, lo16 + 1024, o_own); } while (0)
#define LDEPOCH() asm volatile("s_load_dwordx2 %0, %1, 0x0\n\ts_waitcnt lgkmcnt(0)" : "=s"(tag) : "s"(ws) : "memory"); tag = (tag | 1) + 2

  if (isB) {
    if (wave == 0 && j < 7) {
      asm volatile("s_waitcnt vmcnt(14)" : "+v"(ua), "+v"(ub), "+v"(uc), "+v"(ud) :: "memory");
      const float pa = bf2f(ua), pb = bf2f(ub), dt = rbf(expf(bf2f(uc)));
      const float er = expf(dt * pa); float sn, cs; sincosf(dt * pb, &sn, &cs);
      const float nr = er * cs - 1.f, ni = er * sn, den = pa * pa + pb * pb;
      const cf lam = {er * cs, -ni};
      cf l16 = cmul(lam, lam); l16 = cmul(l16, l16); l16 = cmul(l16, l16); l16 = cmul(l16, l16);
      cf l64 = cmul(l16, l16); l64 = cmul(l64, l64);
      *(LAS f32x4*)(lds + L_EX + lo16) = (f32x4){lam.r, lam.i, 2.f * (nr * pa + ni * pb) / den, 2.f * (ni * pa - nr * pb) / den};
      *(LAS f32x4*)(lds + L_EX + 1024 + lo16) = (f32x4){l16.r, l16.i, l64.r, l64.i};
      *(LAS unsigned*)(lds + L_EX + 2048 + lane * 4) = ud;
    }
    LDEPOCH();
    if (tid == 0) *(LAS u64*)(lds + L_TAG) = tag;
    {
      const unsigned x_off = (32 * th + r32) * 128;
      const int wrow = 8 * (g4 >> 1) + tq;
      bf16x8 fax[4], faw[4], fbx[4], fbw[4];
      f32x16 q = zero16;
#define PRD(B, i, P) _Pragma("unroll") for (int ks = 0; ks < 4; ++ks) { B##x[ks] = ldsf(lds + (i) * SK + x_off + (((2 * ks + hi) ^ xsw) << 4)); B##w[ks] = ldstr2(lds + (i) * SK + w_off + ks * 16 * (P), 4 * (P)); } SBAR()
#define PMM(B, SW) _Pragma("unroll") for (int ks = 0; ks < 4; ++ks) q = (SW) ? MFMA(B##x[ks], B##w[ks], q) : MFMA(B##w[ks], B##x[ks], q); PIN(q); SBAR()
#define PROJ(P, SW) do { VMW(6); BAR(); PRD(fa, 0, P); PRD(fb, 1, P); PMM(fa, SW); PRD(fa, 2, P); PMM(fb, SW); PRD(fb, 3, P); PMM(fa, SW); \
        VMW(0); BAR(); PRD(fa, 4, P); PMM(fb, SW); PRD(fb, 5, P); PMM(fa, SW); PRD(fa, 6, P); PMM(fb, SW); PMM(fa, SW); } while (0)
      if (j < 7) {
        const unsigned w_off = K_W + wrow * 128 + ((64 * chh + 32 * (g4 & 1) + 8 * tp) ^ ((tq & 2) << 5));
        PROJ(128, false);
        const float qs = 0.18898223650461361f * 1.4426950408889634f;
#pragma unroll
        for (int g = 0; g < 4; ++g)
          __builtin_amdgcn_raw_buffer_store_b64((u32x2){pk(q[4 * g] * qs, q[4 * g + 1] * qs), pk(q[4 * g + 2] * qs, q[4 * g + 3] * qs)}, rs_ws, lane * 8 + g * 512,
                                                WS_Q + ((2 * c + th) * 112 + 16 * j + 8 * chh) * 256, SC1);
      } else {
        const unsigned w_off = K_W + chh * 3584 + wrow * 56 + (16 * (g4 & 1) + 4 * tp) * 2;
        if (chh) { PROJ(56, true);
#pragma unroll
          for (int r = 0; r < 16; ++r) q[r] = r32 < 28 ? q[r] : (r32 == 28 ? 1.f : 0.f);
        } else { PROJ(56, false); if (hi) { q[12] = 0.f; q[13] = 0.f; q[14] = 0.f; q[15] = 0.f; } }
        const unsigned ko = WS_KV + (2 * c + th) * 4096 + chh * 2048;
        BST(U4(pack8(q, 0)), rs_ws, lo16, ko); BST(U4(pack8(q, 1)), rs_ws, lo16 + 1024, ko);
      }
      publish4(ws, WS_FP + (c * 8 + j) * 128, tag, lane, cPQ);
    }
    KV A0, A1, A2, A3;
    A3.v1 = (bf16x8){};
    f32x16 O[4] = {zero16, zero16, zero16, zero16};
    f32x16 Sa, Sb = zero16; u32x4 Pa, Pb = {};
    PIN(O[0]); PIN(O[1]); PIN(O[2]); PIN(O[3]); PIN(Sb); PIN(Pb); PIN(A3.v1);
    poll(ws, WS_FP + (lane < 8 ? (wave * 8 + lane) * 8 + 7 : c * 8 + ((56 * j + (lane & 1) * 55) >> 6)) * 128, tag);
    const __amdgpu_buffer_rsrc_t rs_kv = __builtin_amdgcn_make_buffer_rsrc(ws + WS_KV + wave * 65536, 0, 65536, 0x00020000);
#define LDKV(B, T) do { const unsigned so_ = (((T) + loc) & 15) * 4096; \
                        B.k0 = BF(BLD(rs_kv, lo16, so_)); B.k1 = BF(BLD(rs_kv, lo16, so_ + 1024)); \
                        B.v0 = BF(BLD(rs_kv, lo16, so_ + 2048)); B.v1 = BF(BLD(rs_kv, lo16, so_ + 3072)); } while (0)
    bf16x8 Q0[4], Q1[4];
    u32x2 qp[4][4];
#define LDQ(i) _Pragma("unroll") for (int u = 0; u < 4; ++u) qp[i][u] = __builtin_amdgcn_raw_buffer_load_b64(rs_ws, lane * 8 + u * 512, \
                 WS_Q + ((2 * c + ((wave ^ (3 - (i))) >> 1)) * 112 + 7 * (2 * j + ((wave ^ (3 - (i))) & 1))) * 256, SC1)
    LDQ(0); LDKV(A0, 0); LDQ(1); LDQ(2); LDQ(3); LDKV(A1, 1); LDKV(A2, 2);
    SBAR();
#pragma unroll
    for (int i = 0; i < 4; ++i) { Q0[i] = BF(((u32x4){qp[i][0][0], qp[i][0][1], qp[i][1][0], qp[i][1][1]}));
      Q1[i] = BF(((u32x4){qp[i][2][0], qp[i][2][1], hi ? 0u : qp[i][3][0], hi ? 0u : qp[i][3][1]})); }
    Sa = MFMA(A0.k0, Q0[0], zero16); Sa = MFMA(A0.k1, Q1[0], Sa);
#define UNIT(S, Sn, KN, QN, VP, OP, VC, OC, X) do { \
      Pb[2] = pk(Sn[12], Sn[13]); Pb[3] = pk(Sn[14], Sn[15]); \
      if ((QN) >= 0) Sn = MFMA(KN.k0, Q0[(QN) & 3], zero16); S[0] = EX(S[0]); S[1] = EX(S[1]); S[2] = EX(S[2]); S[3] = EX(S[3]); SBAR(); \
      if ((QN) >= 0) Sn = MFMA(KN.k1, Q1[(QN) & 3], Sn); Pa[0] = pk(S[0], S[1]); Pa[1] = pk(S[2], S[3]); S[4] = EX(S[4]); S[5] = EX(S[5]); S[6] = EX(S[6]); S[7] = EX(S[7]); SBAR(); \
      X; SBAR(); \
      OP = MFMA(VP.v1, BF(Pb), OP); Pa[2] = pk(S[4], S[5]); Pa[3] = pk(S[6], S[7]); S[8] = EX(S[8]); S[9] = EX(S[9]); S[10] = EX(S[10]); S[11] = EX(S[11]); SBAR(); \
      OC = MFMA(VC.v0, BF(Pa), OC); Pb[0] = pk(S[8], S[9]); Pb[1] = pk(S[10], S[11]); S[12] = EX(S[12]); S[13] = EX(S[13]); S[14] = EX(S[14]); S[15] = EX(S[15]); SBAR(); \
    } while (0)
#define TILE(CUR, NXT, PRV, T, LD) do { \
      UNIT(Sa, Sb, CUR, 1, PRV, O[3], CUR, O[0], ); \
      if (LD) LDKV(PRV, (T) + 3); SBAR(); \
      UNIT(Sb, Sa, CUR, 2, CUR, O[0], CUR, O[1], ); \
      UNIT(Sa, Sb, CUR, 3, CUR, O[1], CUR, O[2], ); \
      UNIT(Sb, Sa, NXT, 0, CUR, O[2], CUR, O[3], ); } while (0)
#pragma unroll 1
    for (int t = 0; t < 12; t += 4) {
      TILE(A0, A1, A3, t, 1); TILE(A1, A2, A0, t + 1, 1); TILE(A2, A3, A1, t + 2, 1); TILE(A3, A0, A2, t + 3, 1);
    }
    TILE(A0, A1, A3, 12, 1);
    const lptr oc = lds + L_OC + lo16;
#define OUTP(T, I) _Pragma("unroll") for (int q4 = 0; q4 < 4; ++q4) *(LAS f32x4*)(oc + ((wave ^ I) * 3 + I - 1) * 4096 + q4 * 1024) = (f32x4){T[4 * q4], T[4 * q4 + 1], T[4 * q4 + 2], T[4 * q4 + 3]}
    f32x4 pv[12];
    UNIT(Sa, Sb, A1, 1, A0, O[3], A1, O[0], ); UNIT(Sb, Sa, A1, 2, A1, O[0], A1, O[1], ); UNIT(Sa, Sb, A2, 0, A1, O[1], A1, O[2], );
    UNIT(Sb, Sa, A2, 1, A1, O[2], A2, O[0], ); UNIT(Sa, Sb, A2, 2, A2, O[0], A2, O[1], ); UNIT(Sb, Sa, A3, 0, A2, O[1], A2, O[2], );
    UNIT(Sa, Sb, A3, 1, A2, O[2], A3, O[0], ); UNIT(Sb, Sa, A3, 2, A3, O[0], A3, O[1], lds_wait(j < 7 ? cCR : cPB, 4));
    UNIT(Sa, Sb, A1, 3, A3, O[1], A3, O[2], OUTP(O[0], 3));
    UNIT(Sb, Sa, A2, 3, A3, O[2], A1, O[3], OUTP(O[1], 2));
    UNIT(Sa, Sb, A3, 3, A1, O[3], A2, O[3], OUTP(O[2], 1); lds_signal(cBB));
    UNIT(Sb, Sa, A3, -1, A2, O[3], A3, O[3], lds_wait(cBB, 4); _Pragma("unroll") for (int s2 = 0; s2 < 12; ++s2) pv[s2] = *(LAS f32x4*)(oc + wave * 12288 + s2 * 1024));
    Pb[2] = pk(Sb[12], Sb[13]); Pb[3] = pk(Sb[14], Sb[15]);
    O[3] = MFMA(A3.v1, BF(Pb), O[3]);
    __builtin_amdgcn_s_setprio(2);
#pragma unroll
    for (int s2 = 0; s2 < 4; ++s2) pv[s2] = pv[s2] + pv[s2 + 4] + pv[s2 + 8];
    unsigned lo16b = lo16; PIN(lo16b);
    const int lane = lo16b >> 4, r32 = lane & 31, hi = lane >> 5, g4 = lane >> 4, tq = (lane & 15) >> 2, tp = lane & 3;
    f32x16 o = O[3];
#pragma unroll
    for (int r = 0; r < 16; ++r) o[r] += pv[r >> 2][r & 3];
    {
      float l = swap_hi(o[12]);
      float am = 0.f;
#pragma unroll
      for (int r = 0; r < 16; r += 2) am = __builtin_fmaxf(__builtin_fmaxf(am, __builtin_fabsf(o[r])), __builtin_fabsf(o[r + 1]));
      const bool ok = (l >= 0x1p-90f) && (l <= 0x1p110f) && (am <= 0x1p126f);
      const bool slow = !wall(ok);
      if (slow) {
        const __amdgpu_buffer_rsrc_t rk = __builtin_amdgcn_make_buffer_rsrc(ws + WS_KV, 0, 262144, 0x00020000);
        poll(ws, WS_FP + (r32 * 8 + 7) * 128, tag);
        float m = -__builtin_inff();
#pragma unroll 1
        for (int t = 0; t < 64; ++t) {
          f32x16 s = MFMA(BF(BLD(rk, lo16, t * 4096)), Q0[3], zero16); s = MFMA(BF(BLD(rk, lo16 + 1024, t * 4096)), Q1[3], s);
#pragma unroll
          for (int r = 0; r < 16; ++r) m = __builtin_fmaxf(m, s[r]);
        }
        m = __builtin_fmaxf(swap_lo(m), swap_hi(m));
        o = zero16;
#pragma unroll 1
        for (int t = 0; t < 64; ++t) {
          f32x16 s = MFMA(BF(BLD(rk, lo16, t * 4096)), Q0[3], zero16); s = MFMA(BF(BLD(rk, lo16 + 1024, t * 4096)), Q1[3], s);
#pragma unroll
          for (int r = 0; r < 16; ++r) s[r] = EX(s[r] - m);
          o = MFMA(BF(BLD(rk, lo16 + 2048, t * 4096)), pack8(s, 0), o); o = MFMA(BF(BLD(rk, lo16 + 3072, t * 4096)), pack8(s, 1), o);
        }
        l = swap_hi(o[12]);
      }
      const float inv = __builtin_amdgcn_rcpf(l);
#pragma unroll
      for (int r = 0; r < 16; ++r) o[r] *= inv;
      if (hi) { o[12] = 0.f; o[13] = 0.f; o[14] = 0.f; o[15] = 0.f; }
      u32x4 f0 = U4(pack8(o, 0)), f1 = U4(pack8(o, 1));
      if (slow) {
#pragma unroll
        for (int k = 0; k < 4; ++k) { f0[k] = umin(f0[k], 0xfffffffeu); f1[k] = umin(f1[k], 0xfffffffeu); }
      }
      BST(f0, rs_ws, lo16, o_own); BST(f1, rs_ws, lo16 + 1024, o_own);
    }
    lds_signal(cOC);
    lds_signal(cGO + 4 * wave);
    if (j == 7) return;
    {
      const CAS Params* kp = KARGS();
      const __amdgpu_buffer_rsrc_t rs_out = __builtin_amdgcn_make_buffer_rsrc(kp->out, 0, 2048 * 896 * 4, 0x00020000);
      lds_wait(cWO, 4);
      u32x4 of[16];
      const unsigned ob = WS_O + (((2 * c + th) * 16 + 8 * chh) * 2) * 1024;
#define LDO(k) of[k] = BLD(rs_ws, lo16 + ((k) & 3) * 1024, ob + ((k) >> 2) * 4096)
      const int rowb = 224 * chh + 4 * (g4 >> 1) + tq;
      const unsigned wb = (L_WO + rowb * 128 + ((32 * (g4 & 1) + 8 * tp) ^ ((tq & 2) << 5))) ^ (chh << 6);
      bf16x8 w0[16], w1[16];
#pragma unroll
      for (int k = 0; k < 16; ++k) w1[k] = ldstr2(lds + (wb ^ 64) + (28 * (k >> 1) + 16 * (k & 1)) * 128, 1024);
      SBAR();
      {
        unsigned have = 0;
#pragma unroll 1
        for (unsigned s = 0; s < SPIN_CAP; ++s) {
          const unsigned w = __builtin_amdgcn_raw_buffer_load_b32(rs_ws, (lane & 7) * 2048 + 2044, ob, SC1);
          const unsigned rdy = (s + 1 < SPIN_CAP ? (unsigned)__builtin_amdgcn_ballot_w64(w != ~0u) : ~0u) & 0xffu & ~have;
#pragma unroll
          for (int h = 0; h < 8; ++h) if (rdy >> h & 1) { LDO(2 * h); LDO(2 * h + 1); }
          have |= rdy;
          if (have == 0xffu) break;
          __builtin_amdgcn_s_sleep(2);
        }
      }
      f32x16 acc0 = zero16, acc1 = zero16;
#define OBAD(h) (!wall(umax(umax(umax(umax(of[2 * (h)][0], of[2 * (h)][1]), umax(of[2 * (h)][2], of[2 * (h)][3])), umax(of[2 * (h) + 1][0], of[2 * (h) + 1][1])), umax(of[2 * (h) + 1][2], of[2 * (h) + 1][3])) != ~0u))
#define OHEAD(h) do { \
        if (OBAD(h)) { \
          _Pragma("unroll 1") for (unsigned s = 0; s < SPIN_CAP; ++s) { LDO(2 * h); LDO(2 * h + 1); if (!OBAD(h)) break; __builtin_amdgcn_s_sleep(2); } \
        } \
        acc1 = MFMA(BF(of[2 * h]), w1[2 * h], acc1); acc1 = MFMA(BF(of[2 * h + 1]), w1[2 * h + 1], acc1); PIN(acc1); \
        w0[2 * h] = ldstr2(lds + wb + 28 * h * 128, 1024); w0[2 * h + 1] = ldstr2(lds + wb + (28 * h + 16) * 128, 1024); } while (0)
      OHEAD(0); OHEAD(1); OHEAD(2); OHEAD(3); OHEAD(4); OHEAD(5); OHEAD(6); OHEAD(7);
      lds_wait(cOC, 4);
#pragma unroll
      for (int k = 0; k < 4; ++k) acc0 = MFMA(BF(of[k]), w0[k], acc0);
      SBAR();
      const lptr tw = lds + L_OC + hi * 512 + r32 * 4;
#pragma unroll
      for (int r = 0; r < 16; ++r) *(LAS float*)(tw + (wave ^ 1) * 4096 + ((r & 3) + 8 * (r >> 2)) * 128) = acc1[r];
      SBAR();
#pragma unroll
      for (int k = 4; k < 8; ++k) acc0 = MFMA(BF(of[k]), w0[k], acc0);
      PIN(acc0);
      lds_signal(cPR + 4 * th);
#pragma unroll
      for (int k = 8; k < 16; ++k) acc0 = MFMA(BF(of[k]), w0[k], acc0);
      PIN(acc0);
      lds_wait(cPR + 4 * th, 2);
      f32x4 pa[4];
#pragma unroll
      for (int k = 0; k < 4; ++k) pa[k] = *(LAS f32x4*)(lds + L_OC + wave * 4096 + k * 1024 + lo16);
#pragma unroll
      for (int r = 0; r < 16; ++r) *(LAS float*)(tw + 16384 + wave * 4096 + ((r & 3) + 8 * (r >> 2)) * 128) = acc0[r];
      MEMBAR();
      const unsigned oo = ((64 * c + 32 * th + (lane >> 3)) * 896 + 448 + 64 * j + 32 * chh + (lane & 7) * 4) * 4;
#pragma unroll
      for (int k = 0; k < 4; ++k)
        __builtin_amdgcn_raw_buffer_store_b128(U4(pa[k] + *(LAS f32x4*)(lds + L_OC + 16384 + wave * 4096 + k * 1024 + lo16)), rs_out, oo + k * 28672, 0, 17);
    }
  } else {
    if (j == 7) {
      VMW(6); BAR(); VMW(0); BAR();
      lds_wait_<4>(cPQ, 4);
      { const u32x2 t2 = *(LAS u32x2*)(lds + L_TAG);
        tag = ((u64)(unsigned)__builtin_amdgcn_readfirstlane(t2[1]) << 32) | (unsigned)__builtin_amdgcn_readfirstlane(t2[0]); }
      __builtin_amdgcn_s_sleep(64);
      MARK();
      publish4(ws, WS_FH + (c * 8 + 7) * 128, tag, lane, cPB);
      if (c == 0 && wa == 0) {
        __builtin_amdgcn_s_sleep(127);
#pragma unroll 1
        for (int k = 0; k < 4; ++k) poll(ws, WS_FP + (k * 64 + lane) * 128, tag);
        if (lane == 0) __hip_atomic_store((GAS u64*)(ws + WS_EPOCH), tag, RLX_AGENT);
      }
      return;
    }
    VMW(6); BAR(); LDCH(L_SA); VMW(6); BAR();
    cf lam, coef, l16, l64; f32x16 ur = zero16, ui = zero16;
    f32x16 acc[4] = {zero16, zero16, zero16, zero16};
    const unsigned cs0 = wa * 16384u, cs1 = L_R1 + wa * 12288u;
    unsigned a_htw = L_HT + wa * 4096 + r32 * 64 + hi * 32, a_htr = L_HT + wa * 4096 + (8 * (g4 >> 1) + tq) * 64 + (g4 & 1) * 32 + tp * 8;
    unsigned a_hst = WS_H + ((2 * c + th) * 56 + 4 * j + 2 * chh) * 1024 + lo16;
    unsigned a_c[4], a_cn[2], a_x[2], a_ex, e_want; u32x4 dg[2]; cf l256, lw, lo_;
    const int seg = 2 * th + hi;
#pragma unroll
    for (int f = 0; f < 4; ++f) a_c[f] = cs0 + (32 * chh + r32) * 128 + (((2 * f + hi) ^ xsw) << 4);
#pragma unroll
    for (int f = 0; f < 2; ++f) a_cn[f] = cs1 + 8192 + (32 * chh + r32) * 64 + (((2 * f + hi) ^ ((r32 >> 2) & 3)) << 4);
    unsigned a_vh = (lane >> 2) * 896 + (((lane & 3) ^ ((lane >> 4) & 3)) << 4) + 384 + chh * 64, a_vo = ve ^ 64;
    unsigned a_vw = lrow * 896 + (((lane & 7) ^ ((lrow & 2) << 1)) << 4);
    unsigned a_fl = WS_FH + (c * 8 + (lane & 7)) * 128;
    unsigned a_el = (31 - umin(r32, c - 1)) * 512 + 4, a_eo = (32 - c + 2 * th + hi) * 512 + r32 * 16, a_hx = L_EX + 2048 + (32 * chh + r32) * 8;
    {
      LDCH(0); LDCH(CH);
      { const f32x4 c0 = *(LAS f32x4*)(lds + L_EX + (32 * chh + r32) * 16), c1 = *(LAS f32x4*)(lds + L_EX + 1024 + (32 * chh + r32) * 16);
        lam = {c0[0], c0[1]}; coef = {c0[2], c0[3]}; l16 = {c1[0], c1[1]}; l64 = {c1[2], c1[3]}; ud = *(LAS unsigned*)(lds + L_EX + 2048 + (32 * chh + r32) * 4); }
      { const int e = r32 - 8 * hi; const unsigned dv = ud << ((e & 1) * 16);
#pragma unroll
        for (int s2 = 0; s2 < 2; ++s2) {
          dg[s2] = (u32x4){(e >> 1) == 8 * s2 ? dv : 0u, (e >> 1) == 8 * s2 + 1 ? dv : 0u, (e >> 1) == 8 * s2 + 2 ? dv : 0u, (e >> 1) == 8 * s2 + 3 ? dv : 0u};
          a_x[s2] = L_SA + (32 * th + r32) * 128 + (((4 * chh + 2 * s2 + hi) ^ xsw) << 4); PIN(dg[s2]); PIN(a_x[s2]); }
        cf lseg = hi ? l16 : (cf){1.f, 0.f};
        if (th) lseg = cmul(lseg, cmul(l16, l16));
        const cf l128 = cmul(l64, l64), ls128 = cmul(lseg, l128);
        l256 = cmul(l128, l128); lw = th ? ls128 : lseg; lo_ = th ? lseg : ls128; e_want = (c - seg + 3) >> 2; a_ex = L_EX + (32 * chh + r32) * 8;
        PIN(l256.r); PIN(l256.i); PIN(lw.r); PIN(lw.i); PIN(lo_.r); PIN(lo_.i); PIN(e_want); PIN(a_ex); }
      PIN(a_htw); PIN(a_htr); PIN(a_hst); PIN(a_c[0]); PIN(a_c[1]); PIN(a_c[2]); PIN(a_c[3]); PIN(a_cn[0]); PIN(a_cn[1]); PIN(a_vh); PIN(a_vw); PIN(a_fl); PIN(a_el); PIN(a_eo); PIN(a_hx); PIN(a_vo);
      const int trow = 32 * th + 16 * ((r32 >> 2) & 1) + (r32 & 3) + 4 * (r32 >> 3);
      const unsigned xa_off = trow * 128, xa_sw = (trow >> 1) & 7, b_off = 8192 + (32 * chh + r32) * 128;
      bf16x8 gax[4], gar[4], gai[4], gbx[4], gbr[4], gbi[4];
      unsigned gx[3][4], gw[3][4];
#define GB(s_) ((s_) < 49152 ? 0u : (s_) < 110592 ? 49152u : 110592u)
#define GT(s_) ((s_) < 49152 ? 0 : (s_) < 110592 ? 1 : 2)
#pragma unroll
      for (int t = 0; t < 3; ++t)
#pragma unroll
        for (int ks = 0; ks < 4; ++ks) { gx[t][ks] = GB(t * 60000) + xa_off + (((2 * ks + hi) ^ xa_sw) << 4); gw[t][ks] = GB(t * 60000) + b_off + (((2 * ks + hi) ^ xsw) << 4); PIN(gx[t][ks]); PIN(gw[t][ks]); }
#define GRD(B, s_) _Pragma("unroll") for (int ks = 0; ks < 4; ++ks) { B##x[ks] = ldsf(lds + gx[GT(s_)][ks] + ((s_) - GB(s_))); \
        B##r[ks] = ldsf(lds + gw[GT(s_)][ks] + ((s_) - GB(s_))); B##i[ks] = ldsf(lds + gw[GT(s_)][ks] + ((s_) - GB(s_) + 8192)); } SBAR()
#define GMM(B) _Pragma("unroll") for (int ks = 0; ks < 4; ++ks) { ur = MFMA(B##x[ks], B##r[ks], ur); ui = MFMA(B##x[ks], B##i[ks], ui); } PIN(ur); PIN(ui); SBAR()
#define GAP() do { SBAR(); asm volatile("s_nop %0" :: "i"(7)); SBAR(); } while (0)
#define GMS(B) _Pragma("unroll") for (int ks = 0; ks < 4; ++ks) { ur = MFMA(B##x[ks], B##r[ks], ur); GAP(); ui = MFMA(B##x[ks], B##i[ks], ui); GAP(); } PIN(ur); PIN(ui); SBAR()
      __builtin_amdgcn_s_setprio(1);
      lds_wait_<1>(cPQ, 4);
      LDCH(2 * CH); LDCH(3 * CH); LDCH(4 * CH);
      VMW(18); lds_barrierA(cAB, 4); GRD(ga, L_SA); GRD(gb, 0); GMM(ga); GRD(ga, CH); GMM(gb);
      VMW(6); lds_barrierA(cAB, 8);
      GRD(gb, 2 * CH); LDCH(L_SA); GMM(ga); GRD(ga, 3 * CH); GMS(gb);
      VMW(6); lds_barrierA(cAB, 12);
      GRD(gb, 4 * CH); GMS(ga);
      VMW(0); lds_barrierA(cAB, 16);
      GRD(ga, L_SA); GMS(gb); GMS(ga);
      { const u32x2 t2 = *(LAS u32x2*)(lds + L_TAG);
        tag = ((u64)(unsigned)__builtin_amdgcn_readfirstlane(t2[1]) << 32) | (unsigned)__builtin_amdgcn_readfirstlane(t2[0]); }
    }
    acc[0] = MFMA(ldsf(lds + a_x[0]), BF(dg[0]), acc[0]); acc[0] = MFMA(ldsf(lds + a_x[1]), BF(dg[1]), acc[0]);
    PIN(acc[0]);
    cf e = {0.f, 0.f};
#pragma unroll
    for (int r = 0; r < 16; ++r) {
      const float a = coef.r * ur[r] - coef.i * ui[r], b2 = -(coef.r * ui[r] + coef.i * ur[r]);
      ur[r] = a; ui[r] = b2; e = r ? cfma(lam, e, (cf){a, b2}) : (cf){a, b2};
    }
    *(LAS f32x2*)(lds + a_ex + th * 1024 + hi * 512) = (f32x2){e.r, e.i};
    __builtin_amdgcn_s_setprio(0);
    lds_barrierA(cAB, 20);
    cf g0;
    {
      const f32x2 e0 = *(LAS f32x2*)(lds + a_ex), e1 = *(LAS f32x2*)(lds + a_ex + 512), e2 = *(LAS f32x2*)(lds + a_ex + 1024), e3 = *(LAS f32x2*)(lds + a_ex + 1536);
      __builtin_amdgcn_s_setprio(1);
      const cf E2 = cfma(l16, (cf){e0[0], e0[1]}, (cf){e1[0], e1[1]}), E3 = cfma(l16, E2, (cf){e2[0], e2[1]}), E = cfma(l16, E3, (cf){e3[0], e3[1]});
      if (th) g0 = hi ? E3 : E2; else g0 = hi ? (cf){e0[0], e0[1]} : (cf){0.f, 0.f};
      PIN(g0.r); PIN(g0.i);
      if (th == 0 && hi == 0 && c < 31)
        BST(((u32x4){f2u(E.r), (unsigned)tag, f2u(E.i), (unsigned)tag}), rs_ws, r32 * 16, WS_E + ((2 * j + chh) * 32 + 31 - c) * 512);
    }
    __builtin_amdgcn_s_setprio(0);
    const CAS Params* kp = KARGS();
    {
      const u32x4 rc = mkrs((const char*)(th ? kp->cim : kp->cre) + (size_t)j * 57344, 57344);
      int n2 = (6 * cl + 2 * ((b & 7) % 3)) % 24;
#pragma unroll
      for (int n = 0; n < 24; ++n) {
        DMA(rc, (n & 1) ? a_vo : ve, (n2 & 7) * 7168 + (n2 >> 3) * 128 + chh * 512, (n2 < 16 ? cs0 : cs1 - 16384) + n2 * 1024, 0);
        n2 = n2 == 23 ? 0 : n2 + 1; }
#pragma unroll
      for (int n = 0; n < 4; ++n) DMA(rc, a_vh, n * 14336, cs1, 8192 + n * 1024);
    }
    cf G = g0;
    if (c > 0) {
      const __amdgpu_buffer_rsrc_t re = __builtin_amdgcn_make_buffer_rsrc(ws + WS_E + (2 * j + chh) * 16384, 0, 16384, 0x00020000);
      u32x4 ev[8];
#pragma unroll 1
      for (unsigned s = 0; s < SPIN_CAP; ++s) {
        const unsigned t1 = __builtin_amdgcn_raw_buffer_load_b32(re, a_el, 0, SC1);
        if (wall(t1 == (unsigned)tag)) break;
        __builtin_amdgcn_s_sleep(2);
      }
      MEMBAR();
#pragma unroll 1
      for (unsigned s = 0; s < SPIN_CAP; ++s) {
        unsigned got = 0;
#pragma unroll
        for (int m = 0; m < 8; ++m) ev[m] = BLD(re, a_eo, m * 2048);
        VMW(0);
        __builtin_amdgcn_s_setprio(1);
#pragma unroll
        for (int m = 0; m < 8; ++m) got += ((ev[m][1] ^ (unsigned)tag) | (ev[m][3] ^ (unsigned)tag)) == 0;
        if (wall(got == e_want)) break;
        __builtin_amdgcn_s_setprio(0);
        __builtin_amdgcn_s_sleep(4);
      }
      cf H = {u2f(ev[7][0]), u2f(ev[7][2])};
#pragma unroll
      for (int m = 6; m >= 0; --m) H = cfma(l256, H, (cf){u2f(ev[m][0]), u2f(ev[m][2])});
      H = cfma(l64, (cf){swap_hi(H.r), swap_hi(H.i)}, (cf){swap_lo(H.r), swap_lo(H.i)});
      *(LAS f32x2*)(lds + a_hx + th * 512) = (f32x2){H.r, H.i};
      G = cfma(lw, H, g0);
      __builtin_amdgcn_s_setprio(0);
      lds_barrierA(cPE + 4 * chh, 2);
      const f32x2 ho = *(LAS f32x2*)(lds + a_hx + (th ^ 1) * 512);
      __builtin_amdgcn_s_setprio(1);
      G = cfma(lo_, (cf){ho[0], ho[1]}, G);
    }
    __builtin_amdgcn_s_setprio(1);
    {
#pragma unroll
      for (int r = 0; r < 16; ++r) { G = cfma(lam, G, (cf){ur[r], ui[r]}); ur[r] = G.r; ui[r] = G.i; }
      *(LAS bf16x8*)(lds + a_htw) = pack8(ur, 0); *(LAS bf16x8*)(lds + a_htw + 16) = pack8(ur, 1);
      *(LAS bf16x8*)(lds + a_htw + 2048) = pack8(ui, 0); *(LAS bf16x8*)(lds + a_htw + 2064) = pack8(ui, 1);
      unsigned a_hs2 = a_hst + 28 * 1024; PIN(a_hs2);
      __builtin_amdgcn_s_setprio(0);
      asm volatile("s_waitcnt lgkmcnt(0)" ::: "memory");
#pragma unroll
      for (int k = 0; k < 4; ++k) BST(U4(ldstr2(lds + a_htr + (k >> 1) * 2048 + (k & 1) * 1024, 256)), rs_ws, (k >> 1) ? a_hs2 : a_hst, (k & 1) * 1024);
      MARK();
      publish4(ws, WS_FH + (c * 8 + j) * 128, tag, lane, cPB);
    }
    bf16x8 cfr[14][2];
    const int cflip = chh ? -1 : 1;
#define LDC(f) do { if ((f) < 12) { const unsigned a_ = a_c[(f) & 3] + ((f) < 8 ? ((f) >> 2) * 8192 : cs1 - cs0); cfr[f][0] = ldsf(lds + a_); cfr[f][1] = ldsf(lds + a_ + cflip * 4096); } \
                    else { cfr[f][0] = ldsf(lds + a_cn[(f) & 1]); cfr[f][1] = ldsf(lds + a_cn[(f) & 1] + cflip * 2048); } } while (0)
#pragma unroll
    for (int f = 0; f < 8; ++f) LDC(f);
    lds_signal(cCR);
    lds_wait_<1>(cCR, 4);
    {
      const u32x4 rw = mkrs((const char*)kp->wo + j * 128 + wa * 7168, 401408 - j * 128 - wa * 7168);
      int m2 = (4 * cl + (b & 7)) % 14;
#pragma unroll
      for (int m = 0; m < 14; ++m) { DMA(rw, a_vw, m2 * 28672, wa * 1024 + m2 * 4096, L_WO); m2 = m2 == 13 ? 0 : m2 + 1; }
      if (wa == 0 && lane < 32) *(LAS u32x4*)(lds + L_WO + 57344 + lo16) = (u32x4){0, 0, 0, 0};
    }
    poll(ws, a_fl, tag);
    VMW(0);
    lds_signal(cWO);
    {
      const unsigned hb = WS_H + (2 * c * 56 + 14 * wa) * 1024, h0 = hb + th * 57344, h1 = hb + (th ^ 1) * 57344;
      const unsigned fa = chh * 2048, fb = chh ? 0u - 12288u : 0u;
      u32x4 hf[14][2];
#define LDH(f) do { hf[f][0] = BLD(rs_ws, lo16, h0 + (f) * 1024 + ((f) < 12 ? fa : fb)); hf[f][1] = BLD(rs_ws, lo16, h1 + (f) * 1024 + ((f) < 12 ? fa : fb)); } while (0)
#define MMH(f) do { acc[0] = MFMA(BF(hf[f][0]), cfr[f][0], acc[0]); acc[1] = MFMA(BF(hf[f][0]), cfr[f][1], acc[1]); \
                    acc[2] = MFMA(BF(hf[f][1]), cfr[f][0], acc[2]); acc[3] = MFMA(BF(hf[f][1]), cfr[f][1], acc[3]); \
                    PIN(acc[0]); PIN(acc[1]); PIN(acc[2]); PIN(acc[3]); } while (0)
#pragma unroll
      for (int f = 0; f < 14; ++f) LDH(f);
      SBAR();
      lds_wait_<4>(cGO + 4 * wa, 1);
      __builtin_amdgcn_s_setprio(1);
#pragma unroll
      for (int f = 0; f < 8; ++f) { MMH(f); SBAR(); if (f >= 1 && f < 7) LDC(f + 7); SBAR(); }
      lds_signal(cP4);
#pragma unroll
      for (int f = 8; f < 14; ++f) MMH(f);
    }
    lds_wait(cP4, 4);
    rs_write(lds + L_G2, wa, lane, acc[1], acc[2], acc[3]);
    lds_barrierA(cAB, 24);
    f32x16 y = rs_read(lds + L_G2, wa, lane, acc[0]);
    const __amdgpu_buffer_rsrc_t rs_out = __builtin_amdgcn_make_buffer_rsrc(kp->out, 0, 2048 * 896 * 4, 0x00020000);
    MEMBAR();
#pragma unroll
    for (int r = 0; r < 16; ++r) *(LAS float*)(lds + L_G2 + wa * 12288 + hi * 512 + r32 * 4 + ((r & 3) + 8 * (r >> 2)) * 128) = y[r];
    MEMBAR();
    const unsigned oo = ((64 * c + 32 * th + (lane >> 3)) * 896 + 64 * j + 32 * chh + (lane & 7) * 4) * 4;
#pragma unroll
    for (int k = 0; k < 4; ++k) __builtin_amdgcn_raw_buffer_store_b128(*(LAS u32x4*)(lds + L_G2 + wa * 12288 + k * 1024 + lo16), rs_out, oo + k * 28672, 0, 17);
  }
}

extern "C" void kernel_launch(void* const* d_in, const int* in_sizes, int n_in, void* d_out, int out_size, void* d_ws, size_t ws_size,
                              hipStream_t stream) {
  Params p;
  p.xa = (const u16*)d_in[0]; p.lre = (const u16*)d_in[1]; p.lim = (const u16*)d_in[2]; p.bre = (const u16*)d_in[3]; p.bim = (const u16*)d_in[4];
  p.cre = (const u16*)d_in[5]; p.cim = (const u16*)d_in[6]; p.ldt = (const u16*)d_in[7]; p.dsk = (const u16*)d_in[8];
  p.xb = (const u16*)d_in[9]; p.wq = (const u16*)d_in[10]; p.wk = (const u16*)d_in[11]; p.wv = (const u16*)d_in[12]; p.wo = (const u16*)d_in[13];
  p.out = (float*)d_out; p.ws = (char*)d_ws;
  fused_kernel<<<256, 512, 0, stream>>>(p);
}
```

```cpp
#define PB 1
#define PK 99
#include <hip/hip_runtime.h>
#include <stdint.h>

#define DI __device__ __forceinline__
#define GAS __attribute__((address_space(1)))
#define LAS __attribute__((address_space(3)))
#define CAS __attribute__((address_space(4)))
typedef short bf16x8 __attribute__((ext_vector_type(8)));
typedef short s16x4 __attribute__((ext_vector_type(4)));
typedef float f32x16 __attribute__((ext_vector_type(16)));
typedef float f32x4 __attribute__((ext_vector_type(4)));
typedef float f32x2 __attribute__((ext_vector_type(2)));
typedef unsigned u32x4 __attribute__((ext_vector_type(4)));
typedef unsigned u32x2 __attribute__((ext_vector_type(2)));
typedef __bf16 bf16x2_t __attribute__((ext_vector_type(2)));
typedef unsigned long long u64;
typedef unsigned short u16;
typedef LAS char* lptr;
#define RLX_AGENT __ATOMIC_RELAXED, __HIP_MEMORY_SCOPE_AGENT
#define MFMA(a, b, c) __builtin_amdgcn_mfma_f32_32x32x16_bf16((a), (b), (c), 0, 0, 0)
#define SBAR() __builtin_amdgcn_sched_barrier(0)
#define PIN(x) asm volatile("" : "+v"(x))
#define MEMBAR() asm volatile("" ::: "memory")
#define SC1 16

constexpr unsigned WS_EPOCH = 0, WS_FH = 8192, WS_E = 0x10000, WS_FP = 0x48000,
                   WS_KV = 0x50000, WS_O = 0x100000, WS_H = 0x300000, WS_Q = 0x680000, WS_END = 0x880000, WS_DBG = 0x900000;
constexpr unsigned SK = 16384, K_W = 8192;
constexpr unsigned CH = 24576, L_SA = 135168;
constexpr unsigned L_R1 = 107520, L_HT = 65536, L_WO = 0, L_OC = 57856, L_G2 = 107520, L_MISC = 159744, L_EX = L_MISC + 128, L_TOTAL = 163840;
constexpr unsigned cBB = L_MISC, cAB = L_MISC + 4, cCR = L_MISC + 8, cWO = L_MISC + 12, cOC = L_MISC + 16, cPB = L_MISC + 20, cPR = L_MISC + 24, cP4 = L_MISC + 32, cPQ = L_MISC + 52, cGO = L_MISC + 36, cPE = L_MISC + 56, L_TAG = L_MISC + 64;
constexpr unsigned SPIN_CAP = 1u << 18;

struct Params {
  const u16 *xb, *wq, *wk, *wv; char* ws; const u16 *lre, *lim, *ldt, *dsk, *xa, *bre, *bim, *cre, *cim, *wo;
  float* out;
};

DI float u2f(unsigned v) { return __builtin_bit_cast(float, v); }
DI unsigned f2u(float v) { return __builtin_bit_cast(unsigned, v); }
DI unsigned umin(unsigned a, unsigned b) { return a < b ? a : b; }
DI unsigned umax(unsigned a, unsigned b) { return a > b ? a : b; }
DI bool wall(bool x) { return __builtin_amdgcn_ballot_w64(!x) == 0; }
DI float bf2f(unsigned v) { return u2f(v << 16); }
DI unsigned pk(float a, float b) { f32x2 v = {a, b}; return __builtin_bit_cast(unsigned, __builtin_convertvector(v, bf16x2_t)); }
DI float rbf(float a) { return u2f(pk(0.f, a)); }
DI bf16x8 pack8(const f32x16& x, int s) {
  u32x4 p = {pk(x[8 * s], x[8 * s + 1]), pk(x[8 * s + 2], x[8 * s + 3]), pk(x[8 * s + 4], x[8 * s + 5]), pk(x[8 * s + 6], x[8 * s + 7])};
  return __builtin_bit_cast(bf16x8, p);
}
DI u32x4 mkrs(const void* base, unsigned bytes) { const u64 a = (u64)base; return (u32x4){(unsigned)a, (unsigned)(a >> 32) & 0xffffu, bytes, 0x20000u}; }
#define DMA(rs, voff, soff, ldsb, IMM) asm volatile("s_add_u32 m0, %3, %4\n\ts_nop 0\n\tbuffer_load_dwordx4 %0, %1, %2 offen lds" \
                                                    :: "v"(voff), "s"(rs), "s"(soff), "s"(ldsb), "i"(IMM) : "memory", "scc")
#define DMAX(rs, voff, soff, ldsb, IMM) asm volatile("s_add_u32 m0, %3, %4\n\ts_nop 0\n\tbuffer_load_dwordx4 %0, %1, %2 offen nt lds" \
                                                     :: "v"(voff), "s"(rs), "s"(soff), "s"(ldsb), "i"(IMM) : "memory", "scc")
DI bf16x8 ldsf(lptr p) { return *(LAS bf16x8*)p; }
DI s16x4 ldstr(lptr p) { return __builtin_amdgcn_ds_read_tr16_b64_v4i16((LAS s16x4*)p); }
DI bf16x8 ldstr2(lptr p, int second) { s16x4 lo = ldstr(p), hi = ldstr(p + second); return __builtin_shufflevector(lo, hi, 0, 1, 2, 3, 4, 5, 6, 7); }
DI void lds_signal(unsigned c) {
  asm volatile("s_waitcnt lgkmcnt(0)\n\ts_mov_b64 exec, 1\n\tds_add_u32 %0, %1\n\ts_mov_b64 exec, -1" :: "v"(c), "v"(1u) : "memory");
}
template <int SLEEP> DI void lds_wait_(unsigned c, unsigned target) {
  unsigned v, s, n;
  asm volatile("s_mov_b32 %2, 0x400000\n1:\n\tds_read_b32 %0, %3\n\ts_waitcnt lgkmcnt(0)\n\tv_readfirstlane_b32 %1, %0\n\ts_cmp_ge_u32 %1, %4\n\ts_cbranch_scc1 2f\n\t"
               "s_sleep %5\n\ts_sub_u32 %2, %2, 1\n\ts_cbranch_scc0 1b\n2:" : "=&v"(v), "=&s"(s), "=&s"(n) : "v"(c), "i"(target), "i"(SLEEP) : "memory", "scc");
}
#define lds_wait(c, t) lds_wait_<0>(c, t)
#define lds_barrier(c, t) do { lds_signal(c); lds_wait(c, t); } while (0)
#define lds_barrierA(c, t) do { lds_signal(c); lds_wait_<1>(c, t); } while (0)
DI void poll(const char* ws, unsigned off, u64 tag) {
#pragma unroll 1
  for (unsigned s = 0; s < SPIN_CAP; ++s) {
    u64 v = __hip_atomic_load((GAS u64*)(ws + off), RLX_AGENT);
    if (wall(v == tag)) break;
    __builtin_amdgcn_s_sleep(2);
  }
  MEMBAR();
}
DI void publish4(char* ws, unsigned off, u64 tag, int lane, unsigned cnt) {
  unsigned old;
  asm volatile("s_waitcnt vmcnt(0)\n\ts_mov_b64 exec, 1\n\tds_add_rtn_u32 %0, %1, %2\n\ts_waitcnt lgkmcnt(0)\n\ts_mov_b64 exec, -1" : "=&v"(old) : "v"(cnt), "v"(1u) : "memory");
  if (__builtin_amdgcn_readfirstlane(old) == 3 && lane == 0) __hip_atomic_store((GAS u64*)(ws + off), tag, RLX_AGENT);
}
DI float swap_lo(float x) {
  auto r = __builtin_amdgcn_permlane32_swap(f2u(x), f2u(x), false, false); return u2f(r[0]); }
DI float swap_hi(float x) {
  auto r = __builtin_amdgcn_permlane32_swap(f2u(x), f2u(x), false, false); return u2f(r[1]); }
struct cf { float r, i; };
DI cf cmul(cf a, cf b) { return {a.r * b.r - a.i * b.i, a.r * b.i + a.i * b.r}; }
DI cf cfma(cf a, cf b, cf c) { return {__builtin_fmaf(a.r, b.r, __builtin_fmaf(-a.i, b.i, c.r)), __builtin_fmaf(a.r, b.i, __builtin_fmaf(a.i, b.r, c.i))}; }

#define STAMP(k) do { if ((k) == PK && isB == PB) evt_ = __builtin_amdgcn_s_memrealtime() - t_start; } while (0)
#define STAMP_OUT() do { if (isB == PB) while (__builtin_amdgcn_s_memrealtime() - t_start < 3000 + evt_) __builtin_amdgcn_s_sleep(1); } while (0)
struct KV { bf16x8 k0, k1, v0, v1; };

DI void rs_write(lptr base, int w, int lane, const f32x16& t1, const f32x16& t2, const f32x16& t3) {
#define RSW(T, I) _Pragma("unroll") for (int q = 0; q < 4; ++q) *(LAS f32x4*)(base + ((w ^ I) * 3 + I - 1) * 4096 + q * 1024 + lane * 16) = (f32x4){T[4 * q], T[4 * q + 1], T[4 * q + 2], T[4 * q + 3]}
  RSW(t1, 1); RSW(t2, 2); RSW(t3, 3);
}
DI f32x16 rs_read(lptr base, int w, int lane, f32x16 a) {
  f32x4 v[12];
#pragma unroll
  for (int s = 0; s < 12; ++s) v[s] = *(LAS f32x4*)(base + w * 12288 + s * 1024 + lane * 16);
  SBAR();
#pragma unroll
  for (int s = 0; s < 12; ++s) { const int q = s & 3; a[4 * q] += v[s][0]; a[4 * q + 1] += v[s][1]; a[4 * q + 2] += v[s][2]; a[4 * q + 3] += v[s][3]; }
  return a;
}

#define VMW_(n) asm volatile("s_waitcnt vmcnt(" #n ")" ::: "memory")
#define VMW(n) VMW_(n)
#define BAR() asm volatile("s_waitcnt lgkmcnt(0)\n\ts_barrier" ::: "memory")
#define BLD(R, V, S) __builtin_amdgcn_raw_buffer_load_b128(R, V, S, SC1)
#define BST(D, R, V, S) __builtin_amdgcn_raw_buffer_store_b128(D, R, (V) + (S), 0, SC1)
#define BF(x) __builtin_bit_cast(bf16x8, x)
#define U4(x) __builtin_bit_cast(u32x4, x)
#define EX(x) __builtin_amdgcn_exp2f(x)
#define KARGS() ({ const CAS Params* kp_ = (const CAS Params*)__builtin_amdgcn_kernarg_segment_ptr(); asm volatile("" : "+s"(kp_)); kp_; })
#define NEXTK() do { k += step; k = k >= 7 ? k - 7 : k; } while (0)

#if defined(__HIP_DEVICE_COMPILE__)
#define NO_PK __attribute__((target("no-packed-fp32-ops")))
#else
#define NO_PK
#endif
__global__ void __launch_bounds__(512, 2) NO_PK fused_kernel(Params p) {
  __shared__ __attribute__((aligned(128))) char smem[L_TOTAL];
  const lptr lds = (lptr)smem;
  const int tid = threadIdx.x, lane = tid & 63, wave = __builtin_amdgcn_readfirstlane(tid >> 6);
  const int b = blockIdx.x, loc = b >> 3, cl = loc >> 3, c = (b & 7) * 4 + cl, j = loc & 7;
  const int lrow = lane >> 3;
  const unsigned ve = lrow * 896 + (((lane & 7) ^ (lrow >> 1)) << 4);
  const int step = 1 + (cl + (b & 7) * 4) % 6, k0 = j == 7 ? 0 : j;
  const bool isB = wave < 4;
  const int wa = wave & 3;
  char* const ws = p.ws;
  const u64 t_start = __builtin_amdgcn_s_memrealtime(); u64 evt_ = 0;
  unsigned ua = 0, ub = 0, uc = 0, ud = 0;
  u32x4 rxa, rbr, rbi; int ka = k0;
  const unsigned vo = (ve ^ 64) + 7168, lba = wa * 2048;
#define NEXTKA() do { ka += step; ka = ka >= 7 ? ka - 7 : ka; } while (0)
#define LDCH(slot) do { NEXTKA(); const unsigned k_ = ka * 128; DMAX(rxa, ve, k_, lba, (slot)); DMAX(rxa, vo, k_, lba, (slot) + 1024); \
      DMA(rbr, ve, k_, lba, (slot) + 8192); DMA(rbr, vo, k_, lba, (slot) + 9216); DMA(rbi, ve, k_, lba, (slot) + 16384); DMA(rbi, vo, k_, lba, (slot) + 17408); } while (0)
  {
    const u32x4 rx = mkrs((const char*)p.xb + (size_t)c * 57344 + wave * 7168, 7168);
    const unsigned vx = ve ^ ((wave & 1) << 6), lb = wave * 1024;
    int k = k0;
    if (j == 7) {
      const u32x4 rw = mkrs(isB ? p.wk : p.wv, 25088);
      const unsigned vw = wa * 1024 + lane * 16, lw = K_W + (isB ? 0 : 3584) + wa * 1024; const bool half = wa < 3 || lane < 32;
#pragma unroll
      for (int i = 0; i < 7; ++i) { NEXTK(); DMAX(rx, vx, k * 128, lb, i * SK); if (half) DMA(rw, vw, k * 3584, lw, i * SK); }
    } else {
      const u32x4 rw = mkrs((const char*)p.wq + j * 128 + wave * 7168, 401408 - j * 128 - wave * 7168);
      const unsigned vw = lrow * 896 + (((lane & 7) ^ ((lrow & 2) << 1)) << 4);
      if (wave == 0) { const unsigned co = (64 * j + lane) * 2;
        asm volatile("global_load_ushort %0, %4, %5\n\tglobal_load_ushort %1, %4, %6\n\tglobal_load_ushort %2, %4, %7\n\tglobal_load_ushort %3, %4, %8"
                     : "=&v"(ua), "=&v"(ub), "=&v"(uc), "=&v"(ud) : "v"(co), "s"(p.lre), "s"(p.lim), "s"(p.ldt), "s"(p.dsk) : "memory"); }
#pragma unroll
      for (int i = 0; i < 7; ++i) { NEXTK(); DMAX(rx, vx, k * 128, lb, i * SK); DMA(rw, vw, k * 57344, lb, i * SK + K_W); }
      rxa = mkrs((const char*)p.xa + (size_t)c * 57344 + wa * 14336, 14336);
      rbr = mkrs((const char*)p.bre + (size_t)j * 57344 + wa * 14336, 14336);
      rbi = mkrs((const char*)p.bim + (size_t)j * 57344 + wa * 14336, 14336);
    }
  }
  STAMP(8);
  if (tid < 16) ((LAS unsigned*)(lds + L_MISC))[tid] = 0;
  const int r32 = lane & 31, hi = lane >> 5;
  const int g4 = lane >> 4, tq = (lane & 15) >> 2, tp = lane & 3;
  const unsigned xsw = (r32 >> 1) & 7, lo16 = lane * 16;
  const int th = wa >> 1, chh = wa & 1;
  u64 tag;
  const __amdgpu_buffer_rsrc_t rs_ws = __builtin_amdgcn_make_buffer_rsrc(ws, 0, WS_END, 0x00020000);
  const f32x16 zero16 = {};
  const unsigned o_own = WS_O + (((2 * c + th) * 16 + 2 * j + chh) * 2) * 1024;
#define MARK() do { const u32x4 ones = {~0u, ~0u, ~0u, ~0u};       \
    BST(ones, rs_ws, lo16, o_own); BST(ones, rs_ws, lo16 + 1024, o_own); } while (0)
#define LDEPOCH() asm volatile("s_load_dwordx2 %0, %1, 0x0\n\ts_waitcnt lgkmcnt(0)" : "=s"(tag) : "s"(ws) : "memory"); tag = (tag | 1) + 2

  if (isB) {
    if (wave == 0 && j < 7) {
      asm volatile("s_waitcnt vmcnt(14)" : "+v"(ua), "+v"(ub), "+v"(uc), "+v"(ud) :: "memory");
      const float pa = bf2f(ua), pb = bf2f(ub), dt = rbf(expf(bf2f(uc)));
      const float er = expf(dt * pa); float sn, cs; sincosf(dt * pb, &sn, &cs);
      const float nr = er * cs - 1.f, ni = er * sn, den = pa * pa + pb * pb;
      const cf lam = {er * cs, -ni};
      cf l16 = cmul(lam, lam); l16 = cmul(l16, l16); l16 = cmul(l16, l16); l16 = cmul(l16, l16);
      cf l64 = cmul(l16, l16); l64 = cmul(l64, l64);
      *(LAS f32x4*)(lds + L_EX + lo16) = (f32x4){lam.r, lam.i, 2.f * (nr * pa + ni * pb) / den, 2.f * (ni * pa - nr * pb) / den};
      *(LAS f32x4*)(lds + L_EX + 1024 + lo16) = (f32x4){l16.r, l16.i, l64.r, l64.i};
      *(LAS unsigned*)(lds + L_EX + 2048 + lane * 4) = ud;
    }
    LDEPOCH();
    if (tid == 0) *(LAS u64*)(lds + L_TAG) = tag;
    {
      const unsigned x_off = (32 * th + r32) * 128;
      const int wrow = 8 * (g4 >> 1) + tq;
      bf16x8 fax[4], faw[4], fbx[4], fbw[4];
      f32x16 q = zero16;
#define PRD(B, i, P) _Pragma("unroll") for (int ks = 0; ks < 4; ++ks) { B##x[ks] = ldsf(lds + (i) * SK + x_off + (((2 * ks + hi) ^ xsw) << 4)); B##w[ks] = ldstr2(lds + (i) * SK + w_off + ks * 16 * (P), 4 * (P)); } SBAR()
#define PMM(B, SW) _Pragma("unroll") for (int ks = 0; ks < 4; ++ks) q = (SW) ? MFMA(B##x[ks], B##w[ks], q) : MFMA(B##w[ks], B##x[ks], q); PIN(q); SBAR()
#define PROJ(P, SW) do { VMW(6); BAR(); PRD(fa, 0, P); PRD(fb, 1, P); PMM(fa, SW); PRD(fa, 2, P); PMM(fb, SW); PRD(fb, 3, P); PMM(fa, SW); \
        VMW(0); BAR(); PRD(fa, 4, P); PMM(fb, SW); PRD(fb, 5, P); PMM(fa, SW); PRD(fa, 6, P); PMM(fb, SW); PMM(fa, SW); } while (0)
      if (j < 7) {
        const unsigned w_off = K_W + wrow * 128 + ((64 * chh + 32 * (g4 & 1) + 8 * tp) ^ ((tq & 2) << 5));
        PROJ(128, false);
        STAMP(1);
        const float qs = 0.18898223650461361f * 1.4426950408889634f;
#pragma unroll
        for (int g = 0; g < 4; ++g)
          __builtin_amdgcn_raw_buffer_store_b64((u32x2){pk(q[4 * g] * qs, q[4 * g + 1] * qs), pk(q[4 * g + 2] * qs, q[4 * g + 3] * qs)}, rs_ws, lane * 8 + g * 512,
                                                WS_Q + ((2 * c + th) * 112 + 16 * j + 8 * chh) * 256, SC1);
      } else {
        const unsigned w_off = K_W + chh * 3584 + wrow * 56 + (16 * (g4 & 1) + 4 * tp) * 2;
        if (chh) { PROJ(56, true);
#pragma unroll
          for (int r = 0; r < 16; ++r) q[r] = r32 < 28 ? q[r] : (r32 == 28 ? 1.f : 0.f);
        } else { PROJ(56, false); if (hi) { q[12] = 0.f; q[13] = 0.f; q[14] = 0.f; q[15] = 0.f; } }
        STAMP(1);
        const unsigned ko = WS_KV + (2 * c + th) * 4096 + chh * 2048;
        BST(U4(pack8(q, 0)), rs_ws, lo16, ko); BST(U4(pack8(q, 1)), rs_ws, lo16 + 1024, ko);
      }
      publish4(ws, WS_FP + (c * 8 + j) * 128, tag, lane, cPQ);
    }
    KV A0, A1, A2, A3;
    A3.v1 = (bf16x8){};
    f32x16 O[4] = {zero16, zero16, zero16, zero16};
    f32x16 Sa, Sb = zero16; u32x4 Pa, Pb = {};
    PIN(O[0]); PIN(O[1]); PIN(O[2]); PIN(O[3]); PIN(Sb); PIN(Pb); PIN(A3.v1);
    STAMP(2);
    poll(ws, WS_FP + (lane < 8 ? (wave * 8 + lane) * 8 + 7 : c * 8 + ((56 * j + (lane & 1) * 55) >> 6)) * 128, tag);
    STAMP(3);
    const __amdgpu_buffer_rsrc_t rs_kv = __builtin_amdgcn_make_buffer_rsrc(ws + WS_KV + wave * 65536, 0, 65536, 0x00020000);
#define LDKV(B, T) do { const unsigned so_ = (((T) + loc) & 15) * 4096; \
                        B.k0 = BF(BLD(rs_kv, lo16, so_)); B.k1 = BF(BLD(rs_kv, lo16, so_ + 1024)); \
                        B.v0 = BF(BLD(rs_kv, lo16, so_ + 2048)); B.v1 = BF(BLD(rs_kv, lo16, so_ + 3072)); } while (0)
    bf16x8 Q0[4], Q1[4];
    u32x2 qp[4][4];
#define LDQ(i) _Pragma("unroll") for (int u = 0; u < 4; ++u) qp[i][u] = __builtin_amdgcn_raw_buffer_load_b64(rs_ws, lane * 8 + u * 512, \
                 WS_Q + ((2 * c + ((wave ^ (3 - (i))) >> 1)) * 112 + 7 * (2 * j + ((wave ^ (3 - (i))) & 1))) * 256, SC1)
    LDQ(0); LDKV(A0, 0); LDQ(1); LDQ(2); LDQ(3); LDKV(A1, 1); LDKV(A2, 2);
    SBAR();
#pragma unroll
    for (int i = 0; i < 4; ++i) { Q0[i] = BF(((u32x4){qp[i][0][0], qp[i][0][1], qp[i][1][0], qp[i][1][1]}));
      Q1[i] = BF(((u32x4){qp[i][2][0], qp[i][2][1], hi ? 0u : qp[i][3][0], hi ? 0u : qp[i][3][1]})); }
    Sa = MFMA(A0.k0, Q0[0], zero16); Sa = MFMA(A0.k1, Q1[0], Sa);
    STAMP(9);
#define UNIT(S, Sn, KN, QN, VP, OP, VC, OC, X) do { \
      Pb[2] = pk(Sn[12], Sn[13]); Pb[3] = pk(Sn[14], Sn[15]); \
      if ((QN) >= 0) Sn = MFMA(KN.k0, Q0[(QN) & 3], zero16); S[0] = EX(S[0]); S[1] = EX(S[1]); S[2] = EX(S[2]); S[3] = EX(S[3]); SBAR(); \
      if ((QN) >= 0) Sn = MFMA(KN.k1, Q1[(QN) & 3], Sn); Pa[0] = pk(S[0], S[1]); Pa[1] = pk(S[2], S[3]); S[4] = EX(S[4]); S[5] = EX(S[5]); S[6] = EX(S[6]); S[7] = EX(S[7]); SBAR(); \
      X; SBAR(); \
      OP = MFMA(VP.v1, BF(Pb), OP); Pa[2] = pk(S[4], S[5]); Pa[3] = pk(S[6], S[7]); S[8] = EX(S[8]); S[9] = EX(S[9]); S[10] = EX(S[10]); S[11] = EX(S[11]); SBAR(); \
      OC = MFMA(VC.v0, BF(Pa), OC); Pb[0] = pk(S[8], S[9]); Pb[1] = pk(S[10], S[11]); S[12] = EX(S[12]); S[13] = EX(S[13]); S[14] = EX(S[14]); S[15] = EX(S[15]); SBAR(); \
    } while (0)
#define TILE(CUR, NXT, PRV, T, LD) do { \
      UNIT(Sa, Sb, CUR, 1, PRV, O[3], CUR, O[0], ); \
      if (LD) LDKV(PRV, (T) + 3); SBAR(); \
      UNIT(Sb, Sa, CUR, 2, CUR, O[0], CUR, O[1], ); \
      UNIT(Sa, Sb, CUR, 3, CUR, O[1], CUR, O[2], ); \
      UNIT(Sb, Sa, NXT, 0, CUR, O[2], CUR, O[3], ); } while (0)
#pragma unroll 1
    for (int t = 0; t < 12; t += 4) {
      TILE(A0, A1, A3, t, 1); TILE(A1, A2, A0, t + 1, 1); TILE(A2, A3, A1, t + 2, 1); TILE(A3, A0, A2, t + 3, 1);
    }
    TILE(A0, A1, A3, 12, 1);
    const lptr oc = lds + L_OC + lo16;
#define OUTP(T, I) _Pragma("unroll") for (int q4 = 0; q4 < 4; ++q4) *(LAS f32x4*)(oc + ((wave ^ I) * 3 + I - 1) * 4096 + q4 * 1024) = (f32x4){T[4 * q4], T[4 * q4 + 1], T[4 * q4 + 2], T[4 * q4 + 3]}
    f32x4 pv[12];
    UNIT(Sa, Sb, A1, 1, A0, O[3], A1, O[0], ); UNIT(Sb, Sa, A1, 2, A1, O[0], A1, O[1], ); UNIT(Sa, Sb, A2, 0, A1, O[1], A1, O[2], );
    UNIT(Sb, Sa, A2, 1, A1, O[2], A2, O[0], ); UNIT(Sa, Sb, A2, 2, A2, O[0], A2, O[1], ); UNIT(Sb, Sa, A3, 0, A2, O[1], A2, O[2], );
    UNIT(Sa, Sb, A3, 1, A2, O[2], A3, O[0], ); UNIT(Sb, Sa, A3, 2, A3, O[0], A3, O[1], lds_wait(j < 7 ? cCR : cPB, 4));
    UNIT(Sa, Sb, A1, 3, A3, O[1], A3, O[2], OUTP(O[0], 3));
    UNIT(Sb, Sa, A2, 3, A3, O[2], A1, O[3], OUTP(O[1], 2));
    UNIT(Sa, Sb, A3, 3, A1, O[3], A2, O[3], OUTP(O[2], 1); lds_signal(cBB));
    UNIT(Sb, Sa, A3, -1, A2, O[3], A3, O[3], lds_wait(cBB, 4); _Pragma("unroll") for (int s2 = 0; s2 < 12; ++s2) pv[s2] = *(LAS f32x4*)(oc + wave * 12288 + s2 * 1024));
    Pb[2] = pk(Sb[12], Sb[13]); Pb[3] = pk(Sb[14], Sb[15]);
    O[3] = MFMA(A3.v1, BF(Pb), O[3]);
    STAMP(4);
    __builtin_amdgcn_s_setprio(2);
#pragma unroll
    for (int s2 = 0; s2 < 4; ++s2) pv[s2] = pv[s2] + pv[s2 + 4] + pv[s2 + 8];
    STAMP(10);
    unsigned lo16b = lo16; PIN(lo16b);
    const int lane = lo16b >> 4, r32 = lane & 31, hi = lane >> 5, g4 = lane >> 4, tq = (lane & 15) >> 2, tp = lane & 3;
    f32x16 o = O[3];
#pragma unroll
    for (int r = 0; r < 16; ++r) o[r] += pv[r >> 2][r & 3];
    {
      float l = swap_hi(o[12]);
      float am = 0.f;
#pragma unroll
      for (int r = 0; r < 16; r += 2) am = __builtin_fmaxf(__builtin_fmaxf(am, __builtin_fabsf(o[r])), __builtin_fabsf(o[r + 1]));
      const bool ok = (l >= 0x1p-90f) && (l <= 0x1p110f) && (am <= 0x1p126f);
      const bool slow = !wall(ok);
      if (slow) {
        const __amdgpu_buffer_rsrc_t rk = __builtin_amdgcn_make_buffer_rsrc(ws + WS_KV, 0, 262144, 0x00020000);
        poll(ws, WS_FP + (r32 * 8 + 7) * 128, tag);
        float m = -__builtin_inff();
#pragma unroll 1
        for (int t = 0; t < 64; ++t) {
          f32x16 s = MFMA(BF(BLD(rk, lo16, t * 4096)), Q0[3], zero16); s = MFMA(BF(BLD(rk, lo16 + 1024, t * 4096)), Q1[3], s);
#pragma unroll
          for (int r = 0; r < 16; ++r) m = __builtin_fmaxf(m, s[r]);
        }
        m = __builtin_fmaxf(swap_lo(m), swap_hi(m));
        o = zero16;
#pragma unroll 1
        for (int t = 0; t < 64; ++t) {
          f32x16 s = MFMA(BF(BLD(rk, lo16, t * 4096)), Q0[3], zero16); s = MFMA(BF(BLD(rk, lo16 + 1024, t * 4096)), Q1[3], s);
#pragma unroll
          for (int r = 0; r < 16; ++r) s[r] = EX(s[r] - m);
          o = MFMA(BF(BLD(rk, lo16 + 2048, t * 4096)), pack8(s, 0), o); o = MFMA(BF(BLD(rk, lo16 + 3072, t * 4096)), pack8(s, 1), o);
        }
        l = swap_hi(o[12]);
      }
      const float inv = __builtin_amdgcn_rcpf(l);
#pragma unroll
      for (int r = 0; r < 16; ++r) o[r] *= inv;
      if (hi) { o[12] = 0.f; o[13] = 0.f; o[14] = 0.f; o[15] = 0.f; }
      u32x4 f0 = U4(pack8(o, 0)), f1 = U4(pack8(o, 1));
      if (slow) {
#pragma unroll
        for (int k = 0; k < 4; ++k) { f0[k] = umin(f0[k], 0xfffffffeu); f1[k] = umin(f1[k], 0xfffffffeu); }
      }
      BST(f0, rs_ws, lo16, o_own); BST(f1, rs_ws, lo16 + 1024, o_own);
    }
    lds_signal(cOC);
    lds_signal(cGO + 4 * wave);
    STAMP(5);
    if (j == 7) { STAMP_OUT(); return; }
    {
      const CAS Params* kp = KARGS();
      const __amdgpu_buffer_rsrc_t rs_out = __builtin_amdgcn_make_buffer_rsrc(kp->out, 0, 2048 * 896 * 4, 0x00020000);
      lds_wait(cWO, 4);
      u32x4 of[16];
      const unsigned ob = WS_O + (((2 * c + th) * 16 + 8 * chh) * 2) * 1024;
#define LDO(k) of[k] = BLD(rs_ws, lo16 + ((k) & 3) * 1024, ob + ((k) >> 2) * 4096)
      const int rowb = 224 * chh + 4 * (g4 >> 1) + tq;
      const unsigned wb = (L_WO + rowb * 128 + ((32 * (g4 & 1) + 8 * tp) ^ ((tq & 2) << 5))) ^ (chh << 6);
      bf16x8 w0[16], w1[16];
#pragma unroll
      for (int k = 0; k < 16; ++k) w1[k] = ldstr2(lds + (wb ^ 64) + (28 * (k >> 1) + 16 * (k & 1)) * 128, 1024);
      SBAR();
      {
        unsigned have = 0;
#pragma unroll 1
        for (unsigned s = 0; s < SPIN_CAP; ++s) {
          const unsigned w = __builtin_amdgcn_raw_buffer_load_b32(rs_ws, (lane & 7) * 2048 + 2044, ob, SC1);
          const unsigned rdy = (s + 1 < SPIN_CAP ? (unsigned)__builtin_amdgcn_ballot_w64(w != ~0u) : ~0u) & 0xffu & ~have;
#pragma unroll
          for (int h = 0; h < 8; ++h) if (rdy >> h & 1) { LDO(2 * h); LDO(2 * h + 1); }
          have |= rdy;
          if (have == 0xffu) break;
          __builtin_amdgcn_s_sleep(2);
        }
      }
      f32x16 acc0 = zero16, acc1 = zero16;
#define OBAD(h) (!wall(umax(umax(umax(umax(of[2 * (h)][0], of[2 * (h)][1]), umax(of[2 * (h)][2], of[2 * (h)][3])), umax(of[2 * (h) + 1][0], of[2 * (h) + 1][1])), umax(of[2 * (h) + 1][2], of[2 * (h) + 1][3])) != ~0u))
#define OHEAD(h) do { \
        if (OBAD(h)) { \
          _Pragma("unroll 1") for (unsigned s = 0; s < SPIN_CAP; ++s) { LDO(2 * h); LDO(2 * h + 1); if (!OBAD(h)) break; __builtin_amdgcn_s_sleep(2); } \
        } \
        acc1 = MFMA(BF(of[2 * h]), w1[2 * h], acc1); acc1 = MFMA(BF(of[2 * h + 1]), w1[2 * h + 1], acc1); PIN(acc1); \
        w0[2 * h] = ldstr2(lds + wb + 28 * h * 128, 1024); w0[2 * h + 1] = ldstr2(lds + wb + (28 * h + 16) * 128, 1024); } while (0)
      OHEAD(0); STAMP(6); OHEAD(1); OHEAD(2); OHEAD(3); OHEAD(4); OHEAD(5); OHEAD(6); OHEAD(7);
      STAMP(11);
      lds_wait(cOC, 4);
#pragma unroll
      for (int k = 0; k < 4; ++k) acc0 = MFMA(BF(of[k]), w0[k], acc0);
      SBAR();
      const lptr tw = lds + L_OC + hi * 512 + r32 * 4;
#pragma unroll
      for (int r = 0; r < 16; ++r) *(LAS float*)(tw + (wave ^ 1) * 4096 + ((r & 3) + 8 * (r >> 2)) * 128) = acc1[r];
      SBAR();
#pragma unroll
      for (int k = 4; k < 8; ++k) acc0 = MFMA(BF(of[k]), w0[k], acc0);
      PIN(acc0);
      lds_signal(cPR + 4 * th);
#pragma unroll
      for (int k = 8; k < 16; ++k) acc0 = MFMA(BF(of[k]), w0[k], acc0);
      PIN(acc0);
      lds_wait(cPR + 4 * th, 2);
      f32x4 pa[4];
#pragma unroll
      for (int k = 0; k < 4; ++k) pa[k] = *(LAS f32x4*)(lds + L_OC + wave * 4096 + k * 1024 + lo16);
#pragma unroll
      for (int r = 0; r < 16; ++r) *(LAS float*)(tw + 16384 + wave * 4096 + ((r & 3) + 8 * (r >> 2)) * 128) = acc0[r];
      MEMBAR();
      STAMP(12);
      const unsigned oo = ((64 * c + 32 * th + (lane >> 3)) * 896 + 448 + 64 * j + 32 * chh + (lane & 7) * 4) * 4;
#pragma unroll
      for (int k = 0; k < 4; ++k)
        __builtin_amdgcn_raw_buffer_store_b128(U4(pa[k] + *(LAS f32x4*)(lds + L_OC + 16384 + wave * 4096 + k * 1024 + lo16)), rs_out, oo + k * 28672, 0, 17);
    }
    STAMP(7);
    STAMP_OUT();
  } else {
    if (j == 7) {
      VMW(6); BAR(); VMW(0); BAR();
      lds_wait_<4>(cPQ, 4);
      { const u32x2 t2 = *(LAS u32x2*)(lds + L_TAG);
        tag = ((u64)(unsigned)__builtin_amdgcn_readfirstlane(t2[1]) << 32) | (unsigned)__builtin_amdgcn_readfirstlane(t2[0]); }
      __builtin_amdgcn_s_sleep(64);
      MARK();
      publish4(ws, WS_FH + (c * 8 + 7) * 128, tag, lane, cPB);
      if (c == 0 && wa == 0) {
        __builtin_amdgcn_s_sleep(127);
#pragma unroll 1
        for (int k = 0; k < 4; ++k) poll(ws, WS_FP + (k * 64 + lane) * 128, tag);
        if (lane == 0) __hip_atomic_store((GAS u64*)(ws + WS_EPOCH), tag, RLX_AGENT);
      }
      STAMP_OUT();
      return;
    }
    VMW(6); BAR(); LDCH(L_SA); VMW(6); BAR();
    cf lam, coef, l16, l64; f32x16 ur = zero16, ui = zero16;
    f32x16 acc[4] = {zero16, zero16, zero16, zero16};
    const unsigned cs0 = wa * 16384u, cs1 = L_R1 + wa * 12288u;
    unsigned a_htw = L_HT + wa * 4096 + r32 * 64 + hi * 32, a_htr = L_HT + wa * 4096 + (8 * (g4 >> 1) + tq) * 64 + (g4 & 1) * 32 + tp * 8;
    unsigned a_hst = WS_H + ((2 * c + th) * 56 + 4 * j + 2 * chh) * 1024 + lo16;
    unsigned a_c[4], a_cn[2], a_x[2], a_ex, e_want; u32x4 dg[2]; cf l256, lw, lo_;
    const int seg = 2 * th + hi;
#pragma unroll
    for (int f = 0; f < 4; ++f) a_c[f] = cs0 + (32 * chh + r32) * 128 + (((2 * f + hi) ^ xsw) << 4);
#pragma unroll
    for (int f = 0; f < 2; ++f) a_cn[f] = cs1 + 8192 + (32 * chh + r32) * 64 + (((2 * f + hi) ^ ((r32 >> 2) & 3)) << 4);
    unsigned a_vh = (lane >> 2) * 896 + (((lane & 3) ^ ((lane >> 4) & 3)) << 4) + 384 + chh * 64, a_vo = ve ^ 64;
    unsigned a_vw = lrow * 896 + (((lane & 7) ^ ((lrow & 2) << 1)) << 4);
    unsigned a_fl = WS_FH + (c * 8 + (lane & 7)) * 128;
    unsigned a_el = (31 - umin(r32, c - 1)) * 512 + 4, a_eo = (32 - c + 2 * th + hi) * 512 + r32 * 16, a_hx = L_EX + 2048 + (32 * chh + r32) * 8;
    {
      LDCH(0); LDCH(CH);
      { const f32x4 c0 = *(LAS f32x4*)(lds + L_EX + (32 * chh + r32) * 16), c1 = *(LAS f32x4*)(lds + L_EX + 1024 + (32 * chh + r32) * 16);
        lam = {c0[0], c0[1]}; coef = {c0[2], c0[3]}; l16 = {c1[0], c1[1]}; l64 = {c1[2], c1[3]}; ud = *(LAS unsigned*)(lds + L_EX + 2048 + (32 * chh + r32) * 4); }
      { const int e = r32 - 8 * hi; const unsigned dv = ud << ((e & 1) * 16);
#pragma unroll
        for (int s2 = 0; s2 < 2; ++s2) {
          dg[s2] = (u32x4){(e >> 1) == 8 * s2 ? dv : 0u, (e >> 1) == 8 * s2 + 1 ? dv : 0u, (e >> 1) == 8 * s2 + 2 ? dv : 0u, (e >> 1) == 8 * s2 + 3 ? dv : 0u};
          a_x[s2] = L_SA + (32 * th + r32) * 128 + (((4 * chh + 2 * s2 + hi) ^ xsw) << 4); PIN(dg[s2]); PIN(a_x[s2]); }
        cf lseg = hi ? l16 : (cf){1.f, 0.f};
        if (th) lseg = cmul(lseg, cmul(l16, l16));
        const cf l128 = cmul(l64, l64), ls128 = cmul(lseg, l128);
        l256 = cmul(l128, l128); lw = th ? ls128 : lseg; lo_ = th ? lseg : ls128; e_want = (c - seg + 3) >> 2; a_ex = L_EX + (32 * chh + r32) * 8;
        PIN(l256.r); PIN(l256.i); PIN(lw.r); PIN(lw.i); PIN(lo_.r); PIN(lo_.i); PIN(e_want); PIN(a_ex); }
      PIN(a_htw); PIN(a_htr); PIN(a_hst); PIN(a_c[0]); PIN(a_c[1]); PIN(a_c[2]); PIN(a_c[3]); PIN(a_cn[0]); PIN(a_cn[1]); PIN(a_vh); PIN(a_vw); PIN(a_fl); PIN(a_el); PIN(a_eo); PIN(a_hx); PIN(a_vo);
      const int trow = 32 * th + 16 * ((r32 >> 2) & 1) + (r32 & 3) + 4 * (r32 >> 3);
      const unsigned xa_off = trow * 128, xa_sw = (trow >> 1) & 7, b_off = 8192 + (32 * chh + r32) * 128;
      bf16x8 gax[4], gar[4], gai[4], gbx[4], gbr[4], gbi[4];
      unsigned gx[3][4], gw[3][4];
#define GB(s_) ((s_) < 49152 ? 0u : (s_) < 110592 ? 49152u : 110592u)
#define GT(s_) ((s_) < 49152 ? 0 : (s_) < 110592 ? 1 : 2)
#pragma unroll
      for (int t = 0; t < 3; ++t)
#pragma unroll
        for (int ks = 0; ks < 4; ++ks) { gx[t][ks] = GB(t * 60000) + xa_off + (((2 * ks + hi) ^ xa_sw) << 4); gw[t][ks] = GB(t * 60000) + b_off + (((2 * ks + hi) ^ xsw) << 4); PIN(gx[t][ks]); PIN(gw[t][ks]); }
#define GRD(B, s_) _Pragma("unroll") for (int ks = 0; ks < 4; ++ks) { B##x[ks] = ldsf(lds + gx[GT(s_)][ks] + ((s_) - GB(s_))); \
        B##r[ks] = ldsf(lds + gw[GT(s_)][ks] + ((s_) - GB(s_))); B##i[ks] = ldsf(lds + gw[GT(s_)][ks] + ((s_) - GB(s_) + 8192)); } SBAR()
#define GMM(B) _Pragma("unroll") for (int ks = 0; ks < 4; ++ks) { ur = MFMA(B##x[ks], B##r[ks], ur); ui = MFMA(B##x[ks], B##i[ks], ui); } PIN(ur); PIN(ui); SBAR()
#define GAP() do { SBAR(); asm volatile("s_nop %0" :: "i"(7)); SBAR(); } while (0)
#define GMS(B) _Pragma("unroll") for (int ks = 0; ks < 4; ++ks) { ur = MFMA(B##x[ks], B##r[ks], ur); GAP(); ui = MFMA(B##x[ks], B##i[ks], ui); GAP(); } PIN(ur); PIN(ui); SBAR()
      __builtin_amdgcn_s_setprio(1);
      STAMP(12);
      lds_wait_<1>(cPQ, 4);
      LDCH(2 * CH); LDCH(3 * CH); LDCH(4 * CH);
      VMW(18); lds_barrierA(cAB, 4); GRD(ga, L_SA); GRD(gb, 0); GMM(ga); GRD(ga, CH); GMM(gb);
      VMW(6); lds_barrierA(cAB, 8);
      STAMP(13);
      GRD(gb, 2 * CH); LDCH(L_SA); GMM(ga); GRD(ga, 3 * CH); GMS(gb);
      VMW(6); lds_barrierA(cAB, 12);
      STAMP(14);
      GRD(gb, 4 * CH); GMS(ga);
      VMW(0); lds_barrierA(cAB, 16);
      STAMP(15);
      GRD(ga, L_SA); GMS(gb); GMS(ga);
      { const u32x2 t2 = *(LAS u32x2*)(lds + L_TAG);
        tag = ((u64)(unsigned)__builtin_amdgcn_readfirstlane(t2[1]) << 32) | (unsigned)__builtin_amdgcn_readfirstlane(t2[0]); }
    }
    acc[0] = MFMA(ldsf(lds + a_x[0]), BF(dg[0]), acc[0]); acc[0] = MFMA(ldsf(lds + a_x[1]), BF(dg[1]), acc[0]);
    PIN(acc[0]);
    STAMP(1);
    cf e = {0.f, 0.f};
#pragma unroll
    for (int r = 0; r < 16; ++r) {
      const float a = coef.r * ur[r] - coef.i * ui[r], b2 = -(coef.r * ui[r] + coef.i * ur[r]);
      ur[r] = a; ui[r] = b2; e = r ? cfma(lam, e, (cf){a, b2}) : (cf){a, b2};
    }
    *(LAS f32x2*)(lds + a_ex + th * 1024 + hi * 512) = (f32x2){e.r, e.i};
    __builtin_amdgcn_s_setprio(0);
    lds_barrierA(cAB, 20);
    cf g0;
    {
      const f32x2 e0 = *(LAS f32x2*)(lds + a_ex), e1 = *(LAS f32x2*)(lds + a_ex + 512), e2 = *(LAS f32x2*)(lds + a_ex + 1024), e3 = *(LAS f32x2*)(lds + a_ex + 1536);
      __builtin_amdgcn_s_setprio(1);
      const cf E2 = cfma(l16, (cf){e0[0], e0[1]}, (cf){e1[0], e1[1]}), E3 = cfma(l16, E2, (cf){e2[0], e2[1]}), E = cfma(l16, E3, (cf){e3[0], e3[1]});
      if (th) g0 = hi ? E3 : E2; else g0 = hi ? (cf){e0[0], e0[1]} : (cf){0.f, 0.f};
      PIN(g0.r); PIN(g0.i);
      if (th == 0 && hi == 0 && c < 31)
        BST(((u32x4){f2u(E.r), (unsigned)tag, f2u(E.i), (unsigned)tag}), rs_ws, r32 * 16, WS_E + ((2 * j + chh) * 32 + 31 - c) * 512);
    }
    __builtin_amdgcn_s_setprio(0);
    const CAS Params* kp = KARGS();
    {
      const u32x4 rc = mkrs((const char*)(th ? kp->cim : kp->cre) + (size_t)j * 57344, 57344);
      int n2 = (6 * cl + 2 * ((b & 7) % 3)) % 24;
#pragma unroll
      for (int n = 0; n < 24; ++n) {
        DMA(rc, (n & 1) ? a_vo : ve, (n2 & 7) * 7168 + (n2 >> 3) * 128 + chh * 512, (n2 < 16 ? cs0 : cs1 - 16384) + n2 * 1024, 0);
        n2 = n2 == 23 ? 0 : n2 + 1; }
#pragma unroll
      for (int n = 0; n < 4; ++n) DMA(rc, a_vh, n * 14336, cs1, 8192 + n * 1024);
    }
    STAMP(2);
    cf G = g0;
    if (c > 0) {
      const __amdgpu_buffer_rsrc_t re = __builtin_amdgcn_make_buffer_rsrc(ws + WS_E + (2 * j + chh) * 16384, 0, 16384, 0x00020000);
      u32x4 ev[8];
#pragma unroll 1
      for (unsigned s = 0; s < SPIN_CAP; ++s) {
        const unsigned t1 = __builtin_amdgcn_raw_buffer_load_b32(re, a_el, 0, SC1);
        if (wall(t1 == (unsigned)tag)) break;
        __builtin_amdgcn_s_sleep(2);
      }
      MEMBAR();
#pragma unroll 1
      for (unsigned s = 0; s < SPIN_CAP; ++s) {
        unsigned got = 0;
#pragma unroll
        for (int m = 0; m < 8; ++m) ev[m] = BLD(re, a_eo, m * 2048);
        VMW(0);
        __builtin_amdgcn_s_setprio(1);
#pragma unroll
        for (int m = 0; m < 8; ++m) got += ((ev[m][1] ^ (unsigned)tag) | (ev[m][3] ^ (unsigned)tag)) == 0;
        if (wall(got == e_want)) break;
        __builtin_amdgcn_s_setprio(0);
        __builtin_amdgcn_s_sleep(4);
      }
      STAMP(3);
      cf H = {u2f(ev[7][0]), u2f(ev[7][2])};
#pragma unroll
      for (int m = 6; m >= 0; --m) H = cfma(l256, H, (cf){u2f(ev[m][0]), u2f(ev[m][2])});
      H = cfma(l64, (cf){swap_hi(H.r), swap_hi(H.i)}, (cf){swap_lo(H.r), swap_lo(H.i)});
      *(LAS f32x2*)(lds + a_hx + th * 512) = (f32x2){H.r, H.i};
      G = cfma(lw, H, g0);
      __builtin_amdgcn_s_setprio(0);
      lds_barrierA(cPE + 4 * chh, 2);
      const f32x2 ho = *(LAS f32x2*)(lds + a_hx + (th ^ 1) * 512);
      __builtin_amdgcn_s_setprio(1);
      G = cfma(lo_, (cf){ho[0], ho[1]}, G);
    }
    STAMP(4);
    __builtin_amdgcn_s_setprio(1);
    {
#pragma unroll
      for (int r = 0; r < 16; ++r) { G = cfma(lam, G, (cf){ur[r], ui[r]}); ur[r] = G.r; ui[r] = G.i; }
      *(LAS bf16x8*)(lds + a_htw) = pack8(ur, 0); *(LAS bf16x8*)(lds + a_htw + 16) = pack8(ur, 1);
      *(LAS bf16x8*)(lds + a_htw + 2048) = pack8(ui, 0); *(LAS bf16x8*)(lds + a_htw + 2064) = pack8(ui, 1);
      unsigned a_hs2 = a_hst + 28 * 1024; PIN(a_hs2);
      __builtin_amdgcn_s_setprio(0);
      asm volatile("s_waitcnt lgkmcnt(0)" ::: "memory");
#pragma unroll
      for (int k = 0; k < 4; ++k) BST(U4(ldstr2(lds + a_htr + (k >> 1) * 2048 + (k & 1) * 1024, 256)), rs_ws, (k >> 1) ? a_hs2 : a_hst, (k & 1) * 1024);
      MARK();
      publish4(ws, WS_FH + (c * 8 + j) * 128, tag, lane, cPB);
    }
    STAMP(5);
    bf16x8 cfr[14][2];
    const int cflip = chh ? -1 : 1;
#define LDC(f) do { if ((f) < 12) { const unsigned a_ = a_c[(f) & 3] + ((f) < 8 ? ((f) >> 2) * 8192 : cs1 - cs0); cfr[f][0] = ldsf(lds + a_); cfr[f][1] = ldsf(lds + a_ + cflip * 4096); } \
                    else { cfr[f][0] = ldsf(lds + a_cn[(f) & 1]); cfr[f][1] = ldsf(lds + a_cn[(f) & 1] + cflip * 2048); } } while (0)
#pragma unroll
    for (int f = 0; f < 8; ++f) LDC(f);
    lds_signal(cCR);
    STAMP(10);
    lds_wait_<1>(cCR, 4);
    {
      const u32x4 rw = mkrs((const char*)kp->wo + j * 128 + wa * 7168, 401408 - j * 128 - wa * 7168);
      int m2 = (4 * cl + (b & 7)) % 14;
#pragma unroll
      for (int m = 0; m < 14; ++m) { DMA(rw, a_vw, m2 * 28672, wa * 1024 + m2 * 4096, L_WO); m2 = m2 == 13 ? 0 : m2 + 1; }
      if (wa == 0 && lane < 32) *(LAS u32x4*)(lds + L_WO + 57344 + lo16) = (u32x4){0, 0, 0, 0};
    }
    poll(ws, a_fl, tag);
    VMW(0);
    lds_signal(cWO);
    STAMP(6);
    {
      const unsigned hb = WS_H + (2 * c * 56 + 14 * wa) * 1024, h0 = hb + th * 57344, h1 = hb + (th ^ 1) * 57344;
      const unsigned fa = chh * 2048, fb = chh ? 0u - 12288u : 0u;
      u32x4 hf[14][2];
#define LDH(f) do { hf[f][0] = BLD(rs_ws, lo16, h0 + (f) * 1024 + ((f) < 12 ? fa : fb)); hf[f][1] = BLD(rs_ws, lo16, h1 + (f) * 1024 + ((f) < 12 ? fa : fb)); } while (0)
#define MMH(f) do { acc[0] = MFMA(BF(hf[f][0]), cfr[f][0], acc[0]); acc[1] = MFMA(BF(hf[f][0]), cfr[f][1], acc[1]); \
                    acc[2] = MFMA(BF(hf[f][1]), cfr[f][0], acc[2]); acc[3] = MFMA(BF(hf[f][1]), cfr[f][1], acc[3]); \
                    PIN(acc[0]); PIN(acc[1]); PIN(acc[2]); PIN(acc[3]); } while (0)
#pragma unroll
      for (int f = 0; f < 14; ++f) LDH(f);
      SBAR();
      lds_wait_<4>(cGO + 4 * wa, 1);
      STAMP(9);
      __builtin_amdgcn_s_setprio(1);
#pragma unroll
      for (int f = 0; f < 8; ++f) { MMH(f); SBAR(); if (f >= 1 && f < 7) LDC(f + 7); SBAR(); }
      lds_signal(cP4);
#pragma unroll
      for (int f = 8; f < 14; ++f) MMH(f);
    }
    lds_wait(cP4, 4);
    STAMP(11);
    rs_write(lds + L_G2, wa, lane, acc[1], acc[2], acc[3]);
    lds_barrierA(cAB, 24);
    f32x16 y = rs_read(lds + L_G2, wa, lane, acc[0]);
    const __amdgpu_buffer_rsrc_t rs_out = __builtin_amdgcn_make_buffer_rsrc(kp->out, 0, 2048 * 896 * 4, 0x00020000);
    MEMBAR();
#pragma unroll
    for (int r = 0; r < 16; ++r) *(LAS float*)(lds + L_G2 + wa * 12288 + hi * 512 + r32 * 4 + ((r & 3) + 8 * (r >> 2)) * 128) = y[r];
    MEMBAR();
    const unsigned oo = ((64 * c + 32 * th + (lane >> 3)) * 896 + 64 * j + 32 * chh + (lane & 7) * 4) * 4;
#pragma unroll
    for (int k = 0; k < 4; ++k) __builtin_amdgcn_raw_buffer_store_b128(*(LAS u32x4*)(lds + L_G2 + wa * 12288 + k * 1024 + lo16), rs_out, oo + k * 28672, 0, 17);
    STAMP(7);
    STAMP_OUT();
  }
}

extern "C" void kernel_launch(void* const* d_in, const int* in_sizes, int n_in, void* d_out, int out_size, void* d_ws, size_t ws_size,
                              hipStream_t stream) {
  Params p;
  p.xa = (const u16*)d_in[0]; p.lre = (const u16*)d_in[1]; p.lim = (const u16*)d_in[2]; p.bre = (const u16*)d_in[3]; p.bim = (const u16*)d_in[4];
  p.cre = (const u16*)d_in[5]; p.cim = (const u16*)d_in[6]; p.ldt = (const u16*)d_in[7]; p.dsk = (const u16*)d_in[8];
  p.xb = (const u16*)d_in[9]; p.wq = (const u16*)d_in[10]; p.wk = (const u16*)d_in[11]; p.wv = (const u16*)d_in[12]; p.wo = (const u16*)d_in[13];
  p.out = (float*)d_out; p.ws = (char*)d_ws;
  fused_kernel<<<256, 512, 0, stream>>>(p);
}
```

```cpp
#include <hip/hip_runtime.h>
#include <stdint.h>

#define DI __device__ __forceinline__
#define GAS __attribute__((address_space(1)))
#define LAS __attribute__((address_space(3)))
#define CAS __attribute__((address_space(4)))
typedef short bf16x8 __attribute__((ext_vector_type(8)));
typedef short s16x4 __attribute__((ext_vector_type(4)));
typedef float f32x16 __attribute__((ext_vector_type(16)));
typedef float f32x4 __attribute__((ext_vector_type(4)));
typedef float f32x2 __attribute__((ext_vector_type(2)));
typedef unsigned u32x4 __attribute__((ext_vector_type(4)));
typedef unsigned u32x2 __attribute__((ext_vector_type(2)));
typedef __bf16 bf16x2_t __attribute__((ext_vector_type(2)));
typedef unsigned long long u64;
typedef unsigned short u16;
typedef LAS char* lptr;
#define RLX_AGENT __ATOMIC_RELAXED, __HIP_MEMORY_SCOPE_AGENT
#define MFMA(a, b, c) __builtin_amdgcn_mfma_f32_32x32x16_bf16((a), (b), (c), 0, 0, 0)
#define SBAR() __builtin_amdgcn_sched_barrier(0)
#define PIN(x) asm volatile("" : "+v"(x))
#define MEMBAR() asm volatile("" ::: "memory")
#define SC1 16

constexpr unsigned WS_EPOCH = 0, WS_FH = 8192, WS_E = 0x10000, WS_FP = 0x48000,
                   WS_KV = 0x50000, WS_O = 0x100000, WS_H = 0x300000, WS_Q = 0x680000, WS_END = 0x880000, WS_DBG = 0x900000;
constexpr unsigned SK = 16384, K_W = 8192;
constexpr unsigned CH = 24576, L_SA = 135168;
constexpr unsigned L_R1 = 107520, L_HT = 65536, L_WO = 0, L_OC = 57856, L_G2 = 107520, L_MISC = 159744, L_EX = L_MISC + 128, L_TOTAL = 163840;
constexpr unsigned cBB = L_MISC, cAB = L_MISC + 4, cCR = L_MISC + 8, cWO = L_MISC + 12, cOC = L_MISC + 16, cPB = L_MISC + 20, cPR = L_MISC + 24, cP4 = L_MISC + 32, cPQ = L_MISC + 52, cGO = L_MISC + 36, cPE = L_MISC + 56, L_TAG = L_MISC + 64;
constexpr unsigned SPIN_CAP = 1u << 18;

struct Params {
  const u16 *xb, *wq, *wk, *wv; char* ws; const u16 *lre, *lim, *ldt, *dsk, *xa, *bre, *bim, *cre, *cim, *wo;
  float* out;
};

DI float u2f(unsigned v) { return __builtin_bit_cast(float, v); }
DI unsigned f2u(float v) { return __builtin_bit_cast(unsigned, v); }
DI unsigned umin(unsigned a, unsigned b) { return a < b ? a : b; }
DI unsigned umax(unsigned a, unsigned b) { return a > b ? a : b; }
DI bool wall(bool x) { return __builtin_amdgcn_ballot_w64(!x) == 0; }
DI float bf2f(unsigned v) { return u2f(v << 16); }
DI unsigned pk(float a, float b) { f32x2 v = {a, b}; return __builtin_bit_cast(unsigned, __builtin_convertvector(v, bf16x2_t)); }
DI float rbf(float a) { return u2f(pk(0.f, a)); }
DI bf16x8 pack8(const f32x16& x, int s) {
  u32x4 p = {pk(x[8 * s], x[8 * s + 1]), pk(x[8 * s + 2], x[8 * s + 3]), pk(x[8 * s + 4], x[8 * s + 5]), pk(x[8 * s + 6], x[8 * s + 7])};
  return __builtin_bit_cast(bf16x8, p);
}
DI u32x4 mkrs(const void* base, unsigned bytes) { const u64 a = (u64)base; return (u32x4){(unsigned)a, (unsigned)(a >> 32) & 0xffffu, bytes, 0x20000u}; }
#define DMA(rs, voff, soff, ldsb, IMM) asm volatile("s_add_u32 m0, %3, %4\n\ts_nop 0\n\tbuffer_load_dwordx4 %0, %1, %2 offen lds" \
                                                    :: "v"(voff), "s"(rs), "s"(soff), "s"(ldsb), "i"(IMM) : "memory", "scc")
#define DMAX(rs, voff, soff, ldsb, IMM) asm volatile("s_add_u32 m0, %3, %4\n\ts_nop 0\n\tbuffer_load_dwordx4 %0, %1, %2 offen nt lds" \
                                                     :: "v"(voff), "s"(rs), "s"(soff), "s"(ldsb), "i"(IMM) : "memory", "scc")
DI bf16x8 ldsf(lptr p) { return *(LAS bf16x8*)p; }
DI s16x4 ldstr(lptr p) { return __builtin_amdgcn_ds_read_tr16_b64_v4i16((LAS s16x4*)p); }
DI bf16x8 ldstr2(lptr p, int second) { s16x4 lo = ldstr(p), hi = ldstr(p + second); return __builtin_shufflevector(lo, hi, 0, 1, 2, 3, 4, 5, 6, 7); }
DI void lds_signal(unsigned c) {
  asm volatile("s_waitcnt lgkmcnt(0)\n\ts_mov_b64 exec, 1\n\tds_add_u32 %0, %1\n\ts_mov_b64 exec, -1" :: "v"(c), "v"(1u) : "memory");
}
template <int SLEEP> DI void lds_wait_(unsigned c, unsigned target) {
  unsigned v, s, n;
  asm volatile("s_mov_b32 %2, 0x400000\n1:\n\tds_read_b32 %0, %3\n\ts_waitcnt lgkmcnt(0)\n\tv_readfirstlane_b32 %1, %0\n\ts_cmp_ge_u32 %1, %4\n\ts_cbranch_scc1 2f\n\t"
               "s_sleep %5\n\ts_sub_u32 %2, %2, 1\n\ts_cbranch_scc0 1b\n2:" : "=&v"(v), "=&s"(s), "=&s"(n) : "v"(c), "i"(target), "i"(SLEEP) : "memory", "scc");
}
#define lds_wait(c, t) lds_wait_<0>(c, t)
#define lds_barrier(c, t) do { lds_signal(c); lds_wait(c, t); } while (0)
#define lds_barrierA(c, t) do { lds_signal(c); lds_wait_<1>(c, t); } while (0)
DI void poll(const char* ws, unsigned off, u64 tag) {
#pragma unroll 1
  for (unsigned s = 0; s < SPIN_CAP; ++s) {
    u64 v = __hip_atomic_load((GAS u64*)(ws + off), RLX_AGENT);
    if (wall(v == tag)) break;
    __builtin_amdgcn_s_sleep(2);
  }
  MEMBAR();
}
DI void publish4(char* ws, unsigned off, u64 tag, int lane, unsigned cnt) {
  unsigned old;
  asm volatile("s_waitcnt vmcnt(0)\n\ts_mov_b64 exec, 1\n\tds_add_rtn_u32 %0, %1, %2\n\ts_waitcnt lgkmcnt(0)\n\ts_mov_b64 exec, -1" : "=&v"(old) : "v"(cnt), "v"(1u) : "memory");
  if (__builtin_amdgcn_readfirstlane(old) == 3 && lane == 0) __hip_atomic_store((GAS u64*)(ws + off), tag, RLX_AGENT);
}
DI float swap_lo(float x) {
  auto r = __builtin_amdgcn_permlane32_swap(f2u(x), f2u(x), false, false); return u2f(r[0]); }
DI float swap_hi(float x) {
  auto r = __builtin_amdgcn_permlane32_swap(f2u(x), f2u(x), false, false); return u2f(r[1]); }
struct cf { float r, i; };
DI cf cmul(cf a, cf b) { return {a.r * b.r - a.i * b.i, a.r * b.i + a.i * b.r}; }
DI cf cfma(cf a, cf b, cf c) { return {__builtin_fmaf(a.r, b.r, __builtin_fmaf(-a.i, b.i, c.r)), __builtin_fmaf(a.r, b.i, __builtin_fmaf(a.i, b.r, c.i))}; }

#define STAMP(k) do {} while (0)
#define STAMP_OUT() do {} while (0)

struct KV { bf16x8 k0, k1, v0, v1; };

DI void rs_write(lptr base, int w, int lane, const f32x16& t1, const f32x16& t2, const f32x16& t3) {
#define RSW(T, I) _Pragma("unroll") for (int q = 0; q < 4; ++q) *(LAS f32x4*)(base + ((w ^ I) * 3 + I - 1) * 4096 + q * 1024 + lane * 16) = (f32x4){T[4 * q], T[4 * q + 1], T[4 * q + 2], T[4 * q + 3]}
  RSW(t1, 1); RSW(t2, 2); RSW(t3, 3);
}
DI f32x16 rs_read(lptr base, int w, int lane, f32x16 a) {
  f32x4 v[12];
#pragma unroll
  for (int s = 0; s < 12; ++s) v[s] = *(LAS f32x4*)(base + w * 12288 + s * 1024 + lane * 16);
  SBAR();
#pragma unroll
  for (int s = 0; s < 12; ++s) { const int q = s & 3; a[4 * q] += v[s][0]; a[4 * q + 1] += v[s][1]; a[4 * q + 2] += v[s][2]; a[4 * q + 3] += v[s][3]; }
  return a;
}

#define VMW_(n) asm volatile("s_waitcnt vmcnt(" #n ")" ::: "memory")
#define VMW(n) VMW_(n)
#define BAR() asm volatile("s_waitcnt lgkmcnt(0)\n\ts_barrier" ::: "memory")
#define BLD(R, V, S) __builtin_amdgcn_raw_buffer_load_b128(R, V, S, SC1)
#define BST(D, R, V, S) __builtin_amdgcn_raw_buffer_store_b128(D, R, (V) + (S), 0, SC1)
#define BF(x) __builtin_bit_cast(bf16x8, x)
#define U4(x) __builtin_bit_cast(u32x4, x)
#define EX(x) __builtin_amdgcn_exp2f(x)
#define KARGS() ({ const CAS Params* kp_ = (const CAS Params*)__builtin_amdgcn_kernarg_segment_ptr(); asm volatile("" : "+s"(kp_)); kp_; })
#define NEXTK() do { k += step; k = k >= 7 ? k - 7 : k; } while (0)

#if defined(__HIP_DEVICE_COMPILE__)
#define NO_PK __attribute__((target("no-packed-fp32-ops")))
#else
#define NO_PK
#endif
__global__ void __launch_bounds__(512, 2) NO_PK fused_kernel(Params p) {
  __shared__ __attribute__((aligned(128))) char smem[L_TOTAL];
  const lptr lds = (lptr)smem;
  const int tid = threadIdx.x, lane = tid & 63, wave = __builtin_amdgcn_readfirstlane(tid >> 6);
  const int b = blockIdx.x, loc = b >> 3, cl = loc >> 3, c = (b & 7) * 4 + cl, j = loc & 7;
  const int lrow = lane >> 3;
  const unsigned ve = lrow * 896 + (((lane & 7) ^ (lrow >> 1)) << 4);
  const int step = 1 + (cl + (b & 7) * 4) % 6, k0 = j == 7 ? 0 : j;
  const bool isB = wave < 4;
  const int wa = wave & 3;
  char* const ws = p.ws;
  STAMP(0);
  unsigned ua = 0, ub = 0, uc = 0, ud = 0;
  u32x4 rxa, rbr, rbi; int ka = k0;
  const unsigned vo = (ve ^ 64) + 7168, lba = wa * 2048;
#define NEXTKA() do { ka += step; ka = ka >= 7 ? ka - 7 : ka; } while (0)
#define LDCH(slot) do { NEXTKA(); const unsigned k_ = ka * 128; DMAX(rxa, ve, k_, lba, (slot)); DMAX(rxa, vo, k_, lba, (slot) + 1024); \
      DMA(rbr, ve, k_, lba, (slot) + 8192); DMA(rbr, vo, k_, lba, (slot) + 9216); DMA(rbi, ve, k_, lba, (slot) + 16384); DMA(rbi, vo, k_, lba, (slot) + 17408); } while (0)
  {
    const u32x4 rx = mkrs((const char*)p.xb + (size_t)c * 57344 + wave * 7168, 7168);
    const unsigned vx = ve ^ ((wave & 1) << 6), lb = wave * 1024;
    int k = k0;
    if (j == 7) {
      const u32x4 rw = mkrs(isB ? p.wk : p.wv, 25088);
      const unsigned vw = wa * 1024 + lane * 16, lw = K_W + (isB ? 0 : 3584) + wa * 1024; const bool half = wa < 3 || lane < 32;
#pragma unroll
      for (int i = 0; i < 7; ++i) { NEXTK(); DMAX(rx, vx, k * 128, lb, i * SK); if (half) DMA(rw, vw, k * 3584, lw, i * SK); }
    } else {
      const u32x4 rw = mkrs((const char*)p.wq + j * 128 + wave * 7168, 401408 - j * 128 - wave * 7168);
      const unsigned vw = lrow * 896 + (((lane & 7) ^ ((lrow & 2) << 1)) << 4);
      if (wave == 0) { const unsigned co = (64 * j + lane) * 2;
        asm volatile("global_load_ushort %0, %4, %5\n\tglobal_load_ushort %1, %4, %6\n\tglobal_load_ushort %2, %4, %7\n\tglobal_load_ushort %3, %4, %8"
                     : "=&v"(ua), "=&v"(ub), "=&v"(uc), "=&v"(ud) : "v"(co), "s"(p.lre), "s"(p.lim), "s"(p.ldt), "s"(p.dsk) : "memory"); }
#pragma unroll
      for (int i = 0; i < 7; ++i) { NEXTK(); DMAX(rx, vx, k * 128, lb, i * SK); DMA(rw, vw, k * 57344, lb, i * SK + K_W); }
      rxa = mkrs((const char*)p.xa + (size_t)c * 57344 + wa * 14336, 14336);
      rbr = mkrs((const char*)p.bre + (size_t)j * 57344 + wa * 14336, 14336);
      rbi = mkrs((const char*)p.bim + (size_t)j * 57344 + wa * 14336, 14336);
    }
  }
  STAMP(8);
  if (tid < 16) ((LAS unsigned*)(lds + L_MISC))[tid] = 0;
  const int r32 = lane & 31, hi = lane >> 5;
  const int g4 = lane >> 4, tq = (lane & 15) >> 2, tp = lane & 3;
  const unsigned xsw = (r32 >> 1) & 7, lo16 = lane * 16;
  const int th = wa >> 1, chh = wa & 1;
  u64 tag;
  const __amdgpu_buffer_rsrc_t rs_ws = __builtin_amdgcn_make_buffer_rsrc(ws, 0, WS_END, 0x00020000);
  const f32x16 zero16 = {};
  const unsigned o_own = WS_O + (((2 * c + th) * 16 + 2 * j + chh) * 2) * 1024;
#define MARK() do { const u32x4 ones = {~0u, ~0u, ~0u, ~0u};       \
    BST(ones, rs_ws, lo16, o_own); BST(ones, rs_ws, lo16 + 1024, o_own); } while (0)
#define LDEPOCH() asm volatile("s_load_dwordx2 %0, %1, 0x0\n\ts_waitcnt lgkmcnt(0)" : "=s"(tag) : "s"(ws) : "memory"); tag = (tag | 1) + 2

  if (isB) {
    if (wave == 0 && j < 7) {
      asm volatile("s_waitcnt vmcnt(14)" : "+v"(ua), "+v"(ub), "+v"(uc), "+v"(ud) :: "memory");
      const float pa = bf2f(ua), pb = bf2f(ub), dt = rbf(expf(bf2f(uc)));
      const float er = expf(dt * pa); float sn, cs; sincosf(dt * pb, &sn, &cs);
      const float nr = er * cs - 1.f, ni = er * sn, den = pa * pa + pb * pb;
      const cf lam = {er * cs, -ni};
      cf l16 = cmul(lam, lam); l16 = cmul(l16, l16); l16 = cmul(l16, l16); l16 = cmul(l16, l16);
      cf l64 = cmul(l16, l16); l64 = cmul(l64, l64);
      *(LAS f32x4*)(lds + L_EX + lo16) = (f32x4){lam.r, lam.i, 2.f * (nr * pa + ni * pb) / den, 2.f * (ni * pa - nr * pb) / den};
      *(LAS f32x4*)(lds + L_EX + 1024 + lo16) = (f32x4){l16.r, l16.i, l64.r, l64.i};
      *(LAS unsigned*)(lds + L_EX + 2048 + lane * 4) = ud;
    }
    LDEPOCH();
    if (tid == 0) *(LAS u64*)(lds + L_TAG) = tag;
    {
      const unsigned x_off = (32 * th + r32) * 128;
      const int wrow = 8 * (g4 >> 1) + tq;
      bf16x8 fax[4], faw[4], fbx[4], fbw[4];
      f32x16 q = zero16;
#define PRD(B, i, P) _Pragma("unroll") for (int ks = 0; ks < 4; ++ks) { B##x[ks] = ldsf(lds + (i) * SK + x_off + (((2 * ks + hi) ^ xsw) << 4)); B##w[ks] = ldstr2(lds + (i) * SK + w_off + ks * 16 * (P), 4 * (P)); } SBAR()
#define PMM(B, SW) _Pragma("unroll") for (int ks = 0; ks < 4; ++ks) q = (SW) ? MFMA(B##x[ks], B##w[ks], q) : MFMA(B##w[ks], B##x[ks], q); PIN(q); SBAR()
#define PROJ(P, SW) do { VMW(6); BAR(); PRD(fa, 0, P); PRD(fb, 1, P); PMM(fa, SW); PRD(fa, 2, P); PMM(fb, SW); PRD(fb, 3, P); PMM(fa, SW); \
        VMW(0); BAR(); PRD(fa, 4, P); PMM(fb, SW); PRD(fb, 5, P); PMM(fa, SW); PRD(fa, 6, P); PMM(fb, SW); PMM(fa, SW); } while (0)
      if (j < 7) {
        const unsigned w_off = K_W + wrow * 128 + ((64 * chh + 32 * (g4 & 1) + 8 * tp) ^ ((tq & 2) << 5));
        PROJ(128, false);
        STAMP(1);
        const float qs = 0.18898223650461361f * 1.4426950408889634f;
#pragma unroll
        for (int g = 0; g < 4; ++g)
          __builtin_amdgcn_raw_buffer_store_b64((u32x2){pk(q[4 * g] * qs, q[4 * g + 1] * qs), pk(q[4 * g + 2] * qs, q[4 * g + 3] * qs)}, rs_ws, lane * 8 + g * 512,
                                                WS_Q + ((2 * c + th) * 112 + 16 * j + 8 * chh) * 256, SC1);
      } else {
        const unsigned w_off = K_W + chh * 3584 + wrow * 56 + (16 * (g4 & 1) + 4 * tp) * 2;
        if (chh) { PROJ(56, true);
#pragma unroll
          for (int r = 0; r < 16; ++r) q[r] = r32 < 28 ? q[r] : (r32 == 28 ? 1.f : 0.f);
        } else { PROJ(56, false); if (hi) { q[12] = 0.f; q[13] = 0.f; q[14] = 0.f; q[15] = 0.f; } }
        STAMP(1);
        const unsigned ko = WS_KV + (2 * c + th) * 4096 + chh * 2048;
        BST(U4(pack8(q, 0)), rs_ws, lo16, ko); BST(U4(pack8(q, 1)), rs_ws, lo16 + 1024, ko);
      }
      publish4(ws, WS_FP + (c * 8 + j) * 128, tag, lane, cPQ);
    }
    KV A0, A1, A2, A3;
    A3.v1 = (bf16x8){};
    f32x16 O[4] = {zero16, zero16, zero16, zero16};
    f32x16 Sa, Sb = zero16; u32x4 Pa, Pb = {};
    PIN(O[0]); PIN(O[1]); PIN(O[2]); PIN(O[3]); PIN(Sb); PIN(Pb); PIN(A3.v1);
    STAMP(2);
    poll(ws, WS_FP + (lane < 8 ? (wave * 8 + lane) * 8 + 7 : c * 8 + ((56 * j + (lane & 1) * 55) >> 6)) * 128, tag);
    STAMP(3);
    const __amdgpu_buffer_rsrc_t rs_kv = __builtin_amdgcn_make_buffer_rsrc(ws + WS_KV + wave * 65536, 0, 65536, 0x00020000);
#define LDKV(B, T) do { const unsigned so_ = (((T) + loc) & 15) * 4096; \
                        B.k0 = BF(BLD(rs_kv, lo16, so_)); B.k1 = BF(BLD(rs_kv, lo16, so_ + 1024)); \
                        B.v0 = BF(BLD(rs_kv, lo16, so_ + 2048)); B.v1 = BF(BLD(rs_kv, lo16, so_ + 3072)); } while (0)
    bf16x8 Q0[4], Q1[4];
    u32x2 qp[4][4];
#define LDQ(i) _Pragma("unroll") for (int u = 0; u < 4; ++u) qp[i][u] = __builtin_amdgcn_raw_buffer_load_b64(rs_ws, lane * 8 + u * 512, \
                 WS_Q + ((2 * c + ((wave ^ (3 - (i))) >> 1)) * 112 + 7 * (2 * j + ((wave ^ (3 - (i))) & 1))) * 256, SC1)
    LDQ(0); LDKV(A0, 0); LDQ(1); LDQ(2); LDQ(3); LDKV(A1, 1); LDKV(A2, 2);
    SBAR();
#pragma unroll
    for (int i = 0; i < 4; ++i) { Q0[i] = BF(((u32x4){qp[i][0][0], qp[i][0][1], qp[i][1][0], qp[i][1][1]}));
      Q1[i] = BF(((u32x4){qp[i][2][0], qp[i][2][1], hi ? 0u : qp[i][3][0], hi ? 0u : qp[i][3][1]})); }
    Sa = MFMA(A0.k0, Q0[0], zero16); Sa = MFMA(A0.k1, Q1[0], Sa);
    STAMP(9);
#define UNIT(S, Sn, KN, QN, VP, OP, VC, OC, X) do { \
      Pb[2] = pk(Sn[12], Sn[13]); Pb[3] = pk(Sn[14], Sn[15]); \
      if ((QN) >= 0) Sn = MFMA(KN.k0, Q0[(QN) & 3], zero16); S[0] = EX(S[0]); S[1] = EX(S[1]); S[2] = EX(S[2]); S[3] = EX(S[3]); SBAR(); \
      if ((QN) >= 0) Sn = MFMA(KN.k1, Q1[(QN) & 3], Sn); Pa[0] = pk(S[0], S[1]); Pa[1] = pk(S[2], S[3]); S[4] = EX(S[4]); S[5] = EX(S[5]); S[6] = EX(S[6]); S[7] = EX(S[7]); SBAR(); \
      X; SBAR(); \
      OP = MFMA(VP.v1, BF(Pb), OP); Pa[2] = pk(S[4], S[5]); Pa[3] = pk(S[6], S[7]); S[8] = EX(S[8]); S[9] = EX(S[9]); S[10] = EX(S[10]); S[11] = EX(S[11]); SBAR(); \
      OC = MFMA(VC.v0, BF(Pa), OC); Pb[0] = pk(S[8], S[9]); Pb[1] = pk(S[10], S[11]); S[12] = EX(S[12]); S[13] = EX(S[13]); S[14] = EX(S[14]); S[15] = EX(S[15]); SBAR(); \
    } while (0)
#define TILE(CUR, NXT, PRV, T, LD) do { \
      UNIT(Sa, Sb, CUR, 1, PRV, O[3], CUR, O[0], ); \
      if (LD) LDKV(PRV, (T) + 3); SBAR(); \
      UNIT(Sb, Sa, CUR, 2, CUR, O[0], CUR, O[1], ); \
      UNIT(Sa, Sb, CUR, 3, CUR, O[1], CUR, O[2], ); \
      UNIT(Sb, Sa, NXT, 0, CUR, O[2], CUR, O[3], ); } while (0)
#pragma unroll 1
    for (int t = 0; t < 12; t += 4) {
      TILE(A0, A1, A3, t, 1); TILE(A1, A2, A0, t + 1, 1); TILE(A2, A3, A1, t + 2, 1); TILE(A3, A0, A2, t + 3, 1);
    }
    TILE(A0, A1, A3, 12, 1);
    const lptr oc = lds + L_OC + lo16;
#define OUTP(T, I) _Pragma("unroll") for (int q4 = 0; q4 < 4; ++q4) *(LAS f32x4*)(oc + ((wave ^ I) * 3 + I - 1) * 4096 + q4 * 1024) = (f32x4){T[4 * q4], T[4 * q4 + 1], T[4 * q4 + 2], T[4 * q4 + 3]}
    f32x4 pv[12];
    UNIT(Sa, Sb, A1, 1, A0, O[3], A1, O[0], ); UNIT(Sb, Sa, A1, 2, A1, O[0], A1, O[1], ); UNIT(Sa, Sb, A2, 0, A1, O[1], A1, O[2], );
    UNIT(Sb, Sa, A2, 1, A1, O[2], A2, O[0], ); UNIT(Sa, Sb, A2, 2, A2, O[0], A2, O[1], ); UNIT(Sb, Sa, A3, 0, A2, O[1], A2, O[2], );
    UNIT(Sa, Sb, A3, 1, A2, O[2], A3, O[0], ); UNIT(Sb, Sa, A3, 2, A3, O[0], A3, O[1], lds_wait(j < 7 ? cCR : cPB, 4));
    UNIT(Sa, Sb, A1, 3, A3, O[1], A3, O[2], OUTP(O[0], 3));
    UNIT(Sb, Sa, A2, 3, A3, O[2], A1, O[3], OUTP(O[1], 2));
    UNIT(Sa, Sb, A3, 3, A1, O[3], A2, O[3], OUTP(O[2], 1); lds_signal(cBB));
    UNIT(Sb, Sa, A3, -1, A2, O[3], A3, O[3], lds_wait(cBB, 4); _Pragma("unroll") for (int s2 = 0; s2 < 12; ++s2) pv[s2] = *(LAS f32x4*)(oc + wave * 12288 + s2 * 1024));
    Pb[2] = pk(Sb[12], Sb[13]); Pb[3] = pk(Sb[14], Sb[15]);
    O[3] = MFMA(A3.v1, BF(Pb), O[3]);
    STAMP(4);
    __builtin_amdgcn_s_setprio(2);
#pragma unroll
    for (int s2 = 0; s2 < 4; ++s2) pv[s2] = pv[s2] + pv[s2 + 4] + pv[s2 + 8];
    STAMP(10);
    unsigned lo16b = lo16; PIN(lo16b);
    const int lane = lo16b >> 4, r32 = lane & 31, hi = lane >> 5, g4 = lane >> 4, tq = (lane & 15) >> 2, tp = lane & 3;
    f32x16 o = O[3];
#pragma unroll
    for (int r = 0; r < 16; ++r) o[r] += pv[r >> 2][r & 3];
    {
      float l = swap_hi(o[12]);
      float am = 0.f;
#pragma unroll
      for (int r = 0; r < 16; r += 2) am = __builtin_fmaxf(__builtin_fmaxf(am, __builtin_fabsf(o[r])), __builtin_fabsf(o[r + 1]));
      const bool ok = (l >= 0x1p-90f) && (l <= 0x1p110f) && (am <= 0x1p126f);
      const bool slow = !wall(ok);
      if (slow) {
        const __amdgpu_buffer_rsrc_t rk = __builtin_amdgcn_make_buffer_rsrc(ws + WS_KV, 0, 262144, 0x00020000);
        poll(ws, WS_FP + (r32 * 8 + 7) * 128, tag);
        float m = -__builtin_inff();
#pragma unroll 1
        for (int t = 0; t < 64; ++t) {
          f32x16 s = MFMA(BF(BLD(rk, lo16, t * 4096)), Q0[3], zero16); s = MFMA(BF(BLD(rk, lo16 + 1024, t * 4096)), Q1[3], s);
#pragma unroll
          for (int r = 0; r < 16; ++r) m = __builtin_fmaxf(m, s[r]);
        }
        m = __builtin_fmaxf(swap_lo(m), swap_hi(m));
        o = zero16;
#pragma unroll 1
        for (int t = 0; t < 64; ++t) {
          f32x16 s = MFMA(BF(BLD(rk, lo16, t * 4096)), Q0[3], zero16); s = MFMA(BF(BLD(rk, lo16 + 1024, t * 4096)), Q1[3], s);
#pragma unroll
          for (int r = 0; r < 16; ++r) s[r] = EX(s[r] - m);
          o = MFMA(BF(BLD(rk, lo16 + 2048, t * 4096)), pack8(s, 0), o); o = MFMA(BF(BLD(rk, lo16 + 3072, t * 4096)), pack8(s, 1), o);
        }
        l = swap_hi(o[12]);
      }
      const float inv = __builtin_amdgcn_rcpf(l);
#pragma unroll
      for (int r = 0; r < 16; ++r) o[r] *= inv;
      if (hi) { o[12] = 0.f; o[13] = 0.f; o[14] = 0.f; o[15] = 0.f; }
      u32x4 f0 = U4(pack8(o, 0)), f1 = U4(pack8(o, 1));
      if (slow) {
#pragma unroll
        for (int k = 0; k < 4; ++k) { f0[k] = umin(f0[k], 0xfffffffeu); f1[k] = umin(f1[k], 0xfffffffeu); }
      }
      BST(f0, rs_ws, lo16, o_own); BST(f1, rs_ws, lo16 + 1024, o_own);
    }
    lds_signal(cOC);
    lds_signal(cGO + 4 * wave);
    STAMP(5);
    if (j == 7) { STAMP_OUT(); return; }
    {
      const CAS Params* kp = KARGS();
      const __amdgpu_buffer_rsrc_t rs_out = __builtin_amdgcn_make_buffer_rsrc(kp->out, 0, 2048 * 896 * 4, 0x00020000);
      lds_wait(cWO, 4);
      u32x4 of[16];
      const unsigned ob = WS_O + (((2 * c + th) * 16 + 8 * chh) * 2) * 1024;
#define LDO(k) of[k] = BLD(rs_ws, lo16 + ((k) & 3) * 1024, ob + ((k) >> 2) * 4096)
      const int rowb = 224 * chh + 4 * (g4 >> 1) + tq;
      const unsigned wb = (L_WO + rowb * 128 + ((32 * (g4 & 1) + 8 * tp) ^ ((tq & 2) << 5))) ^ (chh << 6);
      bf16x8 w0[16], w1[16];
#pragma unroll
      for (int k = 0; k < 16; ++k) w1[k] = ldstr2(lds + (wb ^ 64) + (28 * (k >> 1) + 16 * (k & 1)) * 128, 1024);
      SBAR();
      {
        unsigned have = 0;
#pragma unroll 1
        for (unsigned s = 0; s < SPIN_CAP; ++s) {
          const unsigned w = __builtin_amdgcn_raw_buffer_load_b32(rs_ws, (lane & 7) * 2048 + 2044, ob, SC1);
          const unsigned rdy = (s + 1 < SPIN_CAP ? (unsigned)__builtin_amdgcn_ballot_w64(w != ~0u) : ~0u) & 0xffu & ~have;
#pragma unroll
          for (int h = 0; h < 8; ++h) if (rdy >> h & 1) { LDO(2 * h); LDO(2 * h + 1); }
          have |= rdy;
          if (have == 0xffu) break;
          __builtin_amdgcn_s_sleep(2);
        }
      }
      f32x16 acc0 = zero16, acc1 = zero16;
#define OBAD(h) (!wall(umax(umax(umax(umax(of[2 * (h)][0], of[2 * (h)][1]), umax(of[2 * (h)][2], of[2 * (h)][3])), umax(of[2 * (h) + 1][0], of[2 * (h) + 1][1])), umax(of[2 * (h) + 1][2], of[2 * (h) + 1][3])) != ~0u))
#define OHEAD(h) do { \
        if (OBAD(h)) { \
          _Pragma("unroll 1") for (unsigned s = 0; s < SPIN_CAP; ++s) { LDO(2 * h); LDO(2 * h + 1); if (!OBAD(h)) break; __builtin_amdgcn_s_sleep(2); } \
        } \
        acc1 = MFMA(BF(of[2 * h]), w1[2 * h], acc1); acc1 = MFMA(BF(of[2 * h + 1]), w1[2 * h + 1], acc1); PIN(acc1); \
        w0[2 * h] = ldstr2(lds + wb + 28 * h * 128, 1024); w0[2 * h + 1] = ldstr2(lds + wb + (28 * h + 16) * 128, 1024); } while (0)
      OHEAD(0); STAMP(6); OHEAD(1); OHEAD(2); OHEAD(3); OHEAD(4); OHEAD(5); OHEAD(6); OHEAD(7);
      STAMP(11);
      lds_wait(cOC, 4);
#pragma unroll
      for (int k = 0; k < 4; ++k) acc0 = MFMA(BF(of[k]), w0[k], acc0);
      SBAR();
      const lptr tw = lds + L_OC + hi * 512 + r32 * 4;
#pragma unroll
      for (int r = 0; r < 16; ++r) *(LAS float*)(tw + (wave ^ 1) * 4096 + ((r & 3) + 8 * (r >> 2)) * 128) = acc1[r];
      SBAR();
#pragma unroll
      for (int k = 4; k < 8; ++k) acc0 = MFMA(BF(of[k]), w0[k], acc0);
      PIN(acc0);
      lds_signal(cPR + 4 * th);
#pragma unroll
      for (int k = 8; k < 16; ++k) acc0 = MFMA(BF(of[k]), w0[k], acc0);
      PIN(acc0);
      lds_wait(cPR + 4 * th, 2);
      f32x4 pa[4];
#pragma unroll
      for (int k = 0; k < 4; ++k) pa[k] = *(LAS f32x4*)(lds + L_OC + wave * 4096 + k * 1024 + lo16);
#pragma unroll
      for (int r = 0; r < 16; ++r) *(LAS float*)(tw + 16384 + wave * 4096 + ((r & 3) + 8 * (r >> 2)) * 128) = acc0[r];
      MEMBAR();
      STAMP(12);
      const unsigned oo = ((64 * c + 32 * th + (lane >> 3)) * 896 + 448 + 64 * j + 32 * chh + (lane & 7) * 4) * 4;
#pragma unroll
      for (int k = 0; k < 4; ++k)
        __builtin_amdgcn_raw_buffer_store_b128(U4(pa[k] + *(LAS f32x4*)(lds + L_OC + 16384 + wave * 4096 + k * 1024 + lo16)), rs_out, oo + k * 28672, 0, 17);
    }
    STAMP(7);
    STAMP_OUT();
  } else {
    if (j == 7) {
      VMW(6); BAR(); VMW(0); BAR();
      lds_wait_<4>(cPQ, 4);
      { const u32x2 t2 = *(LAS u32x2*)(lds + L_TAG);
        tag = ((u64)(unsigned)__builtin_amdgcn_readfirstlane(t2[1]) << 32) | (unsigned)__builtin_amdgcn_readfirstlane(t2[0]); }
      __builtin_amdgcn_s_sleep(64);
      MARK();
      publish4(ws, WS_FH + (c * 8 + 7) * 128, tag, lane, cPB);
      if (c == 0 && wa == 0) {
        __builtin_amdgcn_s_sleep(127);
#pragma unroll 1
        for (int k = 0; k < 4; ++k) poll(ws, WS_FP + (k * 64 + lane) * 128, tag);
        if (lane == 0) __hip_atomic_store((GAS u64*)(ws + WS_EPOCH), tag, RLX_AGENT);
      }
      STAMP_OUT();
      return;
    }
    VMW(6); BAR(); LDCH(L_SA); VMW(6); BAR();
    cf lam, coef, l16, l64; f32x16 ur = zero16, ui = zero16;
    f32x16 acc[4] = {zero16, zero16, zero16, zero16};
    const unsigned cs0 = wa * 16384u, cs1 = L_R1 + wa * 12288u;
    unsigned a_htw = L_HT + wa * 4096 + r32 * 64 + hi * 32, a_htr = L_HT + wa * 4096 + (8 * (g4 >> 1) + tq) * 64 + (g4 & 1) * 32 + tp * 8;
    unsigned a_hst = WS_H + ((2 * c + th) * 56 + 4 * j + 2 * chh) * 1024 + lo16;
    unsigned a_c[4], a_cn[2], a_x[2], a_ex, e_want; u32x4 dg[2]; cf l256, lw, lo_;
    const int seg = 2 * th + hi;
#pragma unroll
    for (int f = 0; f < 4; ++f) a_c[f] = cs0 + (32 * chh + r32) * 128 + (((2 * f + hi) ^ xsw) << 4);
#pragma unroll
    for (int f = 0; f < 2; ++f) a_cn[f] = cs1 + 8192 + (32 * chh + r32) * 64 + (((2 * f + hi) ^ ((r32 >> 2) & 3)) << 4);
    unsigned a_vh = (lane >> 2) * 896 + (((lane & 3) ^ ((lane >> 4) & 3)) << 4) + 384 + chh * 64, a_vo = ve ^ 64;
    unsigned a_vw = lrow * 896 + (((lane & 7) ^ ((lrow & 2) << 1)) << 4);
    unsigned a_fl = WS_FH + (c * 8 + (lane & 7)) * 128;
    unsigned a_el = (31 - umin(r32, c - 1)) * 512 + 4, a_eo = (32 - c + 2 * th + hi) * 512 + r32 * 16, a_hx = L_EX + 2048 + (32 * chh + r32) * 8;
    {
      LDCH(0); LDCH(CH);
      { const f32x4 c0 = *(LAS f32x4*)(lds + L_EX + (32 * chh + r32) * 16), c1 = *(LAS f32x4*)(lds + L_EX + 1024 + (32 * chh + r32) * 16);
        lam = {c0[0], c0[1]}; coef = {c0[2], c0[3]}; l16 = {c1[0], c1[1]}; l64 = {c1[2], c1[3]}; ud = *(LAS unsigned*)(lds + L_EX + 2048 + (32 * chh + r32) * 4); }
      { const int e = r32 - 8 * hi; const unsigned dv = ud << ((e & 1) * 16);
#pragma unroll
        for (int s2 = 0; s2 < 2; ++s2) {
          dg[s2] = (u32x4){(e >> 1) == 8 * s2 ? dv : 0u, (e >> 1) == 8 * s2 + 1 ? dv : 0u, (e >> 1) == 8 * s2 + 2 ? dv : 0u, (e >> 1) == 8 * s2 + 3 ? dv : 0u};
          a_x[s2] = L_SA + (32 * th + r32) * 128 + (((4 * chh + 2 * s2 + hi) ^ xsw) << 4); PIN(dg[s2]); PIN(a_x[s2]); }
        cf lseg = hi ? l16 : (cf){1.f, 0.f};
        if (th) lseg = cmul(lseg, cmul(l16, l16));
        const cf l128 = cmul(l64, l64), ls128 = cmul(lseg, l128);
        l256 = cmul(l128, l128); lw = th ? ls128 : lseg; lo_ = th ? lseg : ls128; e_want = (c - seg + 3) >> 2; a_ex = L_EX + (32 * chh + r32) * 8;
        PIN(l256.r); PIN(l256.i); PIN(lw.r); PIN(lw.i); PIN(lo_.r); PIN(lo_.i); PIN(e_want); PIN(a_ex); }
      PIN(a_htw); PIN(a_htr); PIN(a_hst); PIN(a_c[0]); PIN(a_c[1]); PIN(a_c[2]); PIN(a_c[3]); PIN(a_cn[0]); PIN(a_cn[1]); PIN(a_vh); PIN(a_vw); PIN(a_fl); PIN(a_el); PIN(a_eo); PIN(a_hx); PIN(a_vo);
      const int trow = 32 * th + 16 * ((r32 >> 2) & 1) + (r32 & 3) + 4 * (r32 >> 3);
      const unsigned xa_off = trow * 128, xa_sw = (trow >> 1) & 7, b_off = 8192 + (32 * chh + r32) * 128;
      bf16x8 gax[4], gar[4], gai[4], gbx[4], gbr[4], gbi[4];
      unsigned gx[3][4], gw[3][4];
#define GB(s_) ((s_) < 49152 ? 0u : (s_) < 110592 ? 49152u : 110592u)
#define GT(s_) ((s_) < 49152 ? 0 : (s_) < 110592 ? 1 : 2)
#pragma unroll
      for (int t = 0; t < 3; ++t)
#pragma unroll
        for (int ks = 0; ks < 4; ++ks) { gx[t][ks] = GB(t * 60000) + xa_off + (((2 * ks + hi) ^ xa_sw) << 4); gw[t][ks] = GB(t * 60000) + b_off + (((2 * ks + hi) ^ xsw) << 4); PIN(gx[t][ks]); PIN(gw[t][ks]); }
#define GRD(B, s_) _Pragma("unroll") for (int ks = 0; ks < 4; ++ks) { B##x[ks] = ldsf(lds + gx[GT(s_)][ks] + ((s_) - GB(s_))); \
        B##r[ks] = ldsf(lds + gw[GT(s_)][ks] + ((s_) - GB(s_))); B##i[ks] = ldsf(lds + gw[GT(s_)][ks] + ((s_) - GB(s_) + 8192)); } SBAR()
#define GMM(B) _Pragma("unroll") for (int ks = 0; ks < 4; ++ks) { ur = MFMA(B##x[ks], B##r[ks], ur); ui = MFMA(B##x[ks], B##i[ks], ui); } PIN(ur); PIN(ui); SBAR()
#define GAP() do { SBAR(); asm volatile("s_nop %0" :: "i"(7)); SBAR(); } while (0)
#define GMS(B) _Pragma("unroll") for (int ks = 0; ks < 4; ++ks) { ur = MFMA(B##x[ks], B##r[ks], ur); GAP(); ui = MFMA(B##x[ks], B##i[ks], ui); GAP(); } PIN(ur); PIN(ui); SBAR()
      __builtin_amdgcn_s_setprio(1);
      STAMP(12);
      lds_wait_<1>(cPQ, 4);
      LDCH(2 * CH); LDCH(3 * CH); LDCH(4 * CH);
      VMW(18); lds_barrierA(cAB, 4); GRD(ga, L_SA); GRD(gb, 0); GMM(ga); GRD(ga, CH); GMM(gb);
      VMW(6); lds_barrierA(cAB, 8);
      STAMP(13);
      GRD(gb, 2 * CH); LDCH(L_SA); GMM(ga); GRD(ga, 3 * CH); GMS(gb);
      VMW(6); lds_barrierA(cAB, 12);
      STAMP(14);
      GRD(gb, 4 * CH); GMS(ga);
      VMW(0); lds_barrierA(cAB, 16);
      STAMP(15);
      GRD(ga, L_SA); GMS(gb); GMS(ga);
      { const u32x2 t2 = *(LAS u32x2*)(lds + L_TAG);
        tag = ((u64)(unsigned)__builtin_amdgcn_readfirstlane(t2[1]) << 32) | (unsigned)__builtin_amdgcn_readfirstlane(t2[0]); }
    }
    acc[0] = MFMA(ldsf(lds + a_x[0]), BF(dg[0]), acc[0]); acc[0] = MFMA(ldsf(lds + a_x[1]), BF(dg[1]), acc[0]);
    PIN(acc[0]);
    STAMP(1);
    cf e = {0.f, 0.f};
#pragma unroll
    for (int r = 0; r < 16; ++r) {
      const float a = coef.r * ur[r] - coef.i * ui[r], b2 = -(coef.r * ui[r] + coef.i * ur[r]);
      ur[r] = a; ui[r] = b2; e = r ? cfma(lam, e, (cf){a, b2}) : (cf){a, b2};
    }
    *(LAS f32x2*)(lds + a_ex + th * 1024 + hi * 512) = (f32x2){e.r, e.i};
    __builtin_amdgcn_s_setprio(0);
    lds_barrierA(cAB, 20);
    cf g0;
    {
      const f32x2 e0 = *(LAS f32x2*)(lds + a_ex), e1 = *(LAS f32x2*)(lds + a_ex + 512), e2 = *(LAS f32x2*)(lds + a_ex + 1024), e3 = *(LAS f32x2*)(lds + a_ex + 1536);
      __builtin_amdgcn_s_setprio(1);
      const cf E2 = cfma(l16, (cf){e0[0], e0[1]}, (cf){e1[0], e1[1]}), E3 = cfma(l16, E2, (cf){e2[0], e2[1]}), E = cfma(l16, E3, (cf){e3[0], e3[1]});
      if (th) g0 = hi ? E3 : E2; else g0 = hi ? (cf){e0[0], e0[1]} : (cf){0.f, 0.f};
      PIN(g0.r); PIN(g0.i);
      if (th == 0 && hi == 0 && c < 31)
        BST(((u32x4){f2u(E.r), (unsigned)tag, f2u(E.i), (unsigned)tag}), rs_ws, r32 * 16, WS_E + ((2 * j + chh) * 32 + 31 - c) * 512);
    }
    __builtin_amdgcn_s_setprio(0);
    const CAS Params* kp = KARGS();
    {
      const u32x4 rc = mkrs((const char*)(th ? kp->cim : kp->cre) + (size_t)j * 57344, 57344);
      int n2 = (6 * cl + 2 * ((b & 7) % 3)) % 24;
#pragma unroll
      for (int n = 0; n < 24; ++n) {
        DMA(rc, (n & 1) ? a_vo : ve, (n2 & 7) * 7168 + (n2 >> 3) * 128 + chh * 512, (n2 < 16 ? cs0 : cs1 - 16384) + n2 * 1024, 0);
        n2 = n2 == 23 ? 0 : n2 + 1; }
#pragma unroll
      for (int n = 0; n < 4; ++n) DMA(rc, a_vh, n * 14336, cs1, 8192 + n * 1024);
    }
    STAMP(2);
    cf G = g0;
    if (c > 0) {
      const __amdgpu_buffer_rsrc_t re = __builtin_amdgcn_make_buffer_rsrc(ws + WS_E + (2 * j + chh) * 16384, 0, 16384, 0x00020000);
      u32x4 ev[8];
#pragma unroll 1
      for (unsigned s = 0; s < SPIN_CAP; ++s) {
        const unsigned t1 = __builtin_amdgcn_raw_buffer_load_b32(re, a_el, 0, SC1);
        if (wall(t1 == (unsigned)tag)) break;
        __builtin_amdgcn_s_sleep(2);
      }
      MEMBAR();
#pragma unroll 1
      for (unsigned s = 0; s < SPIN_CAP; ++s) {
        unsigned got = 0;
#pragma unroll
        for (int m = 0; m < 8; ++m) ev[m] = BLD(re, a_eo, m * 2048);
        VMW(0);
        __builtin_amdgcn_s_setprio(1);
#pragma unroll
        for (int m = 0; m < 8; ++m) got += ((ev[m][1] ^ (unsigned)tag) | (ev[m][3] ^ (unsigned)tag)) == 0;
        if (wall(got == e_want)) break;
        __builtin_amdgcn_s_setprio(0);
        __builtin_amdgcn_s_sleep(4);
      }
      STAMP(3);
      cf H = {u2f(ev[7][0]), u2f(ev[7][2])};
#pragma unroll
      for (int m = 6; m >= 0; --m) H = cfma(l256, H, (cf){u2f(ev[m][0]), u2f(ev[m][2])});
      H = cfma(l64, (cf){swap_hi(H.r), swap_hi(H.i)}, (cf){swap_lo(H.r), swap_lo(H.i)});
      *(LAS f32x2*)(lds + a_hx + th * 512) = (f32x2){H.r, H.i};
      G = cfma(lw, H, g0);
      __builtin_amdgcn_s_setprio(0);
      lds_barrierA(cPE + 4 * chh, 2);
      const f32x2 ho = *(LAS f32x2*)(lds + a_hx + (th ^ 1) * 512);
      __builtin_amdgcn_s_setprio(1);
      G = cfma(lo_, (cf){ho[0], ho[1]}, G);
    }
    STAMP(4);
    __builtin_amdgcn_s_setprio(1);
    {
#pragma unroll
      for (int r = 0; r < 16; ++r) { G = cfma(lam, G, (cf){ur[r], ui[r]}); ur[r] = G.r; ui[r] = G.i; }
      *(LAS bf16x8*)(lds + a_htw) = pack8(ur, 0); *(LAS bf16x8*)(lds + a_htw + 16) = pack8(ur, 1);
      *(LAS bf16x8*)(lds + a_htw + 2048) = pack8(ui, 0); *(LAS bf16x8*)(lds + a_htw + 2064) = pack8(ui, 1);
      unsigned a_hs2 = a_hst + 28 * 1024; PIN(a_hs2);
      __builtin_amdgcn_s_setprio(0);
      asm volatile("s_waitcnt lgkmcnt(0)" ::: "memory");
#pragma unroll
      for (int k = 0; k < 4; ++k) BST(U4(ldstr2(lds + a_htr + (k >> 1) * 2048 + (k & 1) * 1024, 256)), rs_ws, (k >> 1) ? a_hs2 : a_hst, (k & 1) * 1024);
      MARK();
      publish4(ws, WS_FH + (c * 8 + j) * 128, tag, lane, cPB);
    }
    STAMP(5);
    bf16x8 cfr[14][2];
    const int cflip = chh ? -1 : 1;
#define LDC(f) do { if ((f) < 12) { const unsigned a_ = a_c[(f) & 3] + ((f) < 8 ? ((f) >> 2) * 8192 : cs1 - cs0); cfr[f][0] = ldsf(lds + a_); cfr[f][1] = ldsf(lds + a_ + cflip * 4096); } \
                    else { cfr[f][0] = ldsf(lds + a_cn[(f) & 1]); cfr[f][1] = ldsf(lds + a_cn[(f) & 1] + cflip * 2048); } } while (0)
#pragma unroll
    for (int f = 0; f < 8; ++f) LDC(f);
    lds_signal(cCR);
    STAMP(10);
    lds_wait_<1>(cCR, 4);
    {
      const u32x4 rw = mkrs((const char*)kp->wo + j * 128 + wa * 7168, 401408 - j * 128 - wa * 7168);
      int m2 = (4 * cl + (b & 7)) % 14;
#pragma unroll
      for (int m = 0; m < 14; ++m) { DMA(rw, a_vw, m2 * 28672, wa * 1024 + m2 * 4096, L_WO); m2 = m2 == 13 ? 0 : m2 + 1; }
      if (wa == 0 && lane < 32) *(LAS u32x4*)(lds + L_WO + 57344 + lo16) = (u32x4){0, 0, 0, 0};
    }
    poll(ws, a_fl, tag);
    VMW(0);
    lds_signal(cWO);
    STAMP(6);
    {
      const unsigned hb = WS_H + (2 * c * 56 + 14 * wa) * 1024, h0 = hb + th * 57344, h1 = hb + (th ^ 1) * 57344;
      const unsigned fa = chh * 2048, fb = chh ? 0u - 12288u : 0u;
      u32x4 hf[14][2];
#define LDH(f) do { hf[f][0] = BLD(rs_ws, lo16, h0 + (f) * 1024 + ((f) < 12 ? fa : fb)); hf[f][1] = BLD(rs_ws, lo16, h1 + (f) * 1024 + ((f) < 12 ? fa : fb)); } while (0)
#define MMH(f) do { acc[0] = MFMA(BF(hf[f][0]), cfr[f][0], acc[0]); acc[1] = MFMA(BF(hf[f][0]), cfr[f][1], acc[1]); \
                    acc[2] = MFMA(BF(hf[f][1]), cfr[f][0], acc[2]); acc[3] = MFMA(BF(hf[f][1]), cfr[f][1], acc[3]); \
                    PIN(acc[0]); PIN(acc[1]); PIN(acc[2]); PIN(acc[3]); } while (0)
#pragma unroll
      for (int f = 0; f < 14; ++f) LDH(f);
      SBAR();
      lds_wait_<4>(cGO + 4 * wa, 1);
      STAMP(9);
      __builtin_amdgcn_s_setprio(1);
#pragma unroll
      for (int f = 0; f < 8; ++f) { MMH(f); SBAR(); if (f >= 1 && f < 7) LDC(f + 7); SBAR(); }
      lds_signal(cP4);
#pragma unroll
      for (int f = 8; f < 14; ++f) MMH(f);
    }
    lds_wait(cP4, 4);
    STAMP(11);
    rs_write(lds + L_G2, wa, lane, acc[1], acc[2], acc[3]);
    lds_barrierA(cAB, 24);
    f32x16 y = rs_read(lds + L_G2, wa, lane, acc[0]);
    const __amdgpu_buffer_rsrc_t rs_out = __builtin_amdgcn_make_buffer_rsrc(kp->out, 0, 2048 * 896 * 4, 0x00020000);
    MEMBAR();
#pragma unroll
    for (int r = 0; r < 16; ++r) *(LAS float*)(lds + L_G2 + wa * 12288 + hi * 512 + r32 * 4 + ((r & 3) + 8 * (r >> 2)) * 128) = y[r];
    MEMBAR();
    const unsigned oo = ((64 * c + 32 * th + (lane >> 3)) * 896 + 64 * j + 32 * chh + (lane & 7) * 4) * 4;
#pragma unroll
    for (int k = 0; k < 4; ++k) __builtin_amdgcn_raw_buffer_store_b128(*(LAS u32x4*)(lds + L_G2 + wa * 12288 + k * 1024 + lo16), rs_out, oo + k * 28672, 0, 17);
    STAMP(7);
    STAMP_OUT();
  }
}

extern "C" void kernel_launch(void* const* d_in, const int* in_sizes, int n_in, void* d_out, int out_size, void* d_ws, size_t ws_size,
                              hipStream_t stream) {
  Params p;
  p.xa = (const u16*)d_in[0]; p.lre = (const u16*)d_in[1]; p.lim = (const u16*)d_in[2]; p.bre = (const u16*)d_in[3]; p.bim = (const u16*)d_in[4];
  p.cre = (const u16*)d_in[5]; p.cim = (const u16*)d_in[6]; p.ldt = (const u16*)d_in[7]; p.dsk = (const u16*)d_in[8];
  p.xb = (const u16*)d_in[9]; p.wq = (const u16*)d_in[10]; p.wk = (const u16*)d_in[11]; p.wv = (const u16*)d_in[12]; p.wo = (const u16*)d_in[13];
  p.out = (float*)d_out; p.ws = (char*)d_ws;
  fused_kernel<<<256, 512, 0, stream>>>(p);
}
```

```cpp
#include <hip/hip_runtime.h>
#include <stdint.h>

#define DI __device__ __forceinline__
#define GAS __attribute__((address_space(1)))
#define LAS __attribute__((address_space(3)))
#define CAS __attribute__((address_space(4)))
typedef short bf16x8 __attribute__((ext_vector_type(8)));
typedef short s16x4 __attribute__((ext_vector_type(4)));
typedef float f32x16 __attribute__((ext_vector_type(16)));
typedef float f32x4 __attribute__((ext_vector_type(4)));
typedef float f32x2 __attribute__((ext_vector_type(2)));
typedef unsigned u32x4 __attribute__((ext_vector_type(4)));
typedef unsigned u32x2 __attribute__((ext_vector_type(2)));
typedef __bf16 bf16x2_t __attribute__((ext_vector_type(2)));
typedef unsigned long long u64;
typedef unsigned short u16;
typedef LAS char* lptr;
#define RLX_AGENT __ATOMIC_RELAXED, __HIP_MEMORY_SCOPE_AGENT
#define MFMA(a, b, c) __builtin_amdgcn_mfma_f32_32x32x16_bf16((a), (b), (c), 0, 0, 0)
#define SBAR() __builtin_amdgcn_sched_barrier(0)
#define PIN(x) asm volatile("" : "+v"(x))
#define MEMBAR() asm volatile("" ::: "memory")
#define SC1 16

constexpr unsigned WS_EPOCH = 0, WS_FH = 8192, WS_E = 0x10000, WS_FP = 0x48000,
                   WS_KV = 0x50000, WS_O = 0x100000, WS_H = 0x300000, WS_Q = 0x680000, WS_END = 0x880000, WS_DBG = 0x900000;
constexpr unsigned SK = 16384, K_W = 8192;
constexpr unsigned CH = 24576, L_SA = 135168;
constexpr unsigned L_R1 = 107520, L_HT = 65536, L_WO = 0, L_OC = 57856, L_G2 = 107520, L_MISC = 159744, L_EX = L_MISC + 128, L_TOTAL = 163840;
constexpr unsigned cBB = L_MISC, cAB = L_MISC + 4, cCR = L_MISC + 8, cWO = L_MISC + 12, cOC = L_MISC + 16, cPB = L_MISC + 20, cPR = L_MISC + 24, cP4 = L_MISC + 32, cPQ = L_MISC + 52, cGO = L_MISC + 36, cPE = L_MISC + 56, L_TAG = L_MISC + 64;
constexpr unsigned SPIN_CAP = 1u << 18;

struct Params {
  const u16 *xb, *wq, *wk, *wv; char* ws; const u16 *lre, *lim, *ldt, *dsk, *xa, *bre, *bim, *cre, *cim, *wo;
  float* out;
};

DI float u2f(unsigned v) { return __builtin_bit_cast(float, v); }
DI unsigned f2u(float v) { return __builtin_bit_cast(unsigned, v); }
DI unsigned umin(unsigned a, unsigned b) { return a < b ? a : b; }
DI unsigned umax(unsigned a, unsigned b) { return a > b ? a : b; }
DI bool wall(bool x) { return __builtin_amdgcn_ballot_w64(!x) == 0; }
DI float bf2f(unsigned v) { return u2f(v << 16); }
DI unsigned pk(float a, float b) { f32x2 v = {a, b}; return __builtin_bit_cast(unsigned, __builtin_convertvector(v, bf16x2_t)); }
DI float rbf(float a) { return u2f(pk(0.f, a)); }
DI bf16x8 pack8(const f32x16& x, int s) {
  u32x4 p = {pk(x[8 * s], x[8 * s + 1]), pk(x[8 * s + 2], x[8 * s + 3]), pk(x[8 * s + 4], x[8 * s + 5]), pk(x[8 * s + 6], x[8 * s + 7])};
  return __builtin_bit_cast(bf16x8, p);
}
DI u32x4 mkrs(const void* base, unsigned bytes) { const u64 a = (u64)base; return (u32x4){(unsigned)a, (unsigned)(a >> 32) & 0xffffu, bytes, 0x20000u}; }
#define DMA(rs, voff, soff, ldsb, IMM) asm volatile("s_add_u32 m0, %3, %4\n\ts_nop 0\n\tbuffer_load_dwordx4 %0, %1, %2 offen lds" \
                                                    :: "v"(voff), "s"(rs), "s"(soff), "s"(ldsb), "i"(IMM) : "memory", "scc")
#define DMAX(rs, voff, soff, ldsb, IMM) asm volatile("s_add_u32 m0, %3, %4\n\ts_nop 0\n\tbuffer_load_dwordx4 %0, %1, %2 offen nt lds" \
                                                     :: "v"(voff), "s"(rs), "s"(soff), "s"(ldsb), "i"(IMM) : "memory", "scc")
DI bf16x8 ldsf(lptr p) { return *(LAS bf16x8*)p; }
DI s16x4 ldstr(lptr p) { return __builtin_amdgcn_ds_read_tr16_b64_v4i16((LAS s16x4*)p); }
DI bf16x8 ldstr2(lptr p, int second) { s16x4 lo = ldstr(p), hi = ldstr(p + second); return __builtin_shufflevector(lo, hi, 0, 1, 2, 3, 4, 5, 6, 7); }
DI void lds_signal(unsigned c) {
  asm volatile("s_waitcnt lgkmcnt(0)\n\ts_mov_b64 exec, 1\n\tds_add_u32 %0, %1\n\ts_mov_b64 exec, -1" :: "v"(c), "v"(1u) : "memory");
}
template <int SLEEP> DI void lds_wait_(unsigned c, unsigned target) {
  unsigned v, s, n;
  asm volatile("s_mov_b32 %2, 0x400000\n1:\n\tds_read_b32 %0, %3\n\ts_waitcnt lgkmcnt(0)\n\tv_readfirstlane_b32 %1, %0\n\ts_cmp_ge_u32 %1, %4\n\ts_cbranch_scc1 2f\n\t"
               "s_sleep %5\n\ts_sub_u32 %2, %2, 1\n\ts_cbranch_scc0 1b\n2:" : "=&v"(v), "=&s"(s), "=&s"(n) : "v"(c), "i"(target), "i"(SLEEP) : "memory", "scc");
}
#define lds_wait(c, t) lds_wait_<0>(c, t)
#define lds_barrier(c, t) do { lds_signal(c); lds_wait(c, t); } while (0)
#define lds_barrierA(c, t) do { lds_signal(c); lds_wait_<1>(c, t); } while (0)
DI void poll(const char* ws, unsigned off, u64 tag) {
#pragma unroll 1
  for (unsigned s = 0; s < SPIN_CAP; ++s) {
    u64 v = __hip_atomic_load((GAS u64*)(ws + off), RLX_AGENT);
    if (wall(v == tag)) break;
    __builtin_amdgcn_s_sleep(2);
  }
  MEMBAR();
}
DI void publish4(char* ws, unsigned off, u64 tag, int lane, unsigned cnt) {
  unsigned old;
  asm volatile("s_waitcnt vmcnt(0)\n\ts_mov_b64 exec, 1\n\tds_add_rtn_u32 %0, %1, %2\n\ts_waitcnt lgkmcnt(0)\n\ts_mov_b64 exec, -1" : "=&v"(old) : "v"(cnt), "v"(1u) : "memory");
  if (__builtin_amdgcn_readfirstlane(old) == 3 && lane == 0) __hip_atomic_store((GAS u64*)(ws + off), tag, RLX_AGENT);
}
DI float swap_lo(float x) {
  auto r = __builtin_amdgcn_permlane32_swap(f2u(x), f2u(x), false, false); return u2f(r[0]); }
DI float swap_hi(float x) {
  auto r = __builtin_amdgcn_permlane32_swap(f2u(x), f2u(x), false, false); return u2f(r[1]); }
struct cf { float r, i; };
DI cf cmul(cf a, cf b) { return {a.r * b.r - a.i * b.i, a.r * b.i + a.i * b.r}; }
DI cf cfma(cf a, cf b, cf c) { return {__builtin_fmaf(a.r, b.r, __builtin_fmaf(-a.i, b.i, c.r)), __builtin_fmaf(a.r, b.i, __builtin_fmaf(a.i, b.r, c.i))}; }

#define STAMP(k) do {} while (0)
#define STAMP_OUT() do {} while (0)

struct KV { bf16x8 k0, k1, v0, v1; };

DI void rs_write(lptr base, int w, int lane, const f32x16& t1, const f32x16& t2, const f32x16& t3) {
#define RSW(T, I) _Pragma("unroll") for (int q = 0; q < 4; ++q) *(LAS f32x4*)(base + ((w ^ I) * 3 + I - 1) * 4096 + q * 1024 + lane * 16) = (f32x4){T[4 * q], T[4 * q + 1], T[4 * q + 2], T[4 * q + 3]}
  RSW(t1, 1); RSW(t2, 2); RSW(t3, 3);
}
DI f32x16 rs_read(lptr base, int w, int lane, f32x16 a) {
  f32x4 v[12];
#pragma unroll
  for (int s = 0; s < 12; ++s) v[s] = *(LAS f32x4*)(base + w * 12288 + s * 1024 + lane * 16);
  SBAR();
#pragma unroll
  for (int s = 0; s < 12; ++s) { const int q = s & 3; a[4 * q] += v[s][0]; a[4 * q + 1] += v[s][1]; a[4 * q + 2] += v[s][2]; a[4 * q + 3] += v[s][3]; }
  return a;
}

#define VMW_(n) asm volatile("s_waitcnt vmcnt(" #n ")" ::: "memory")
#define VMW(n) VMW_(n)
#define BAR() asm volatile("s_waitcnt lgkmcnt(0)\n\ts_barrier" ::: "memory")
#define BLD(R, V, S) __builtin_amdgcn_raw_buffer_load_b128(R, V, S, SC1)
#define BST(D, R, V, S) __builtin_amdgcn_raw_buffer_store_b128(D, R, (V) + (S), 0, SC1)
#define BF(x) __builtin_bit_cast(bf16x8, x)
#define U4(x) __builtin_bit_cast(u32x4, x)
#define EX(x) __builtin_amdgcn_exp2f(x)
#define KARGS() ({ const CAS Params* kp_ = (const CAS Params*)__builtin_amdgcn_kernarg_segment_ptr(); asm volatile("" : "+s"(kp_)); kp_; })
#define NEXTK() do { k += step; k = k >= 7 ? k - 7 : k; } while (0)

#if defined(__HIP_DEVICE_COMPILE__)
#define NO_PK __attribute__((target("no-packed-fp32-ops")))
#else
#define NO_PK
#endif
__global__ void __launch_bounds__(512, 2) NO_PK fused_kernel(Params p) {
  __shared__ __attribute__((aligned(128))) char smem[L_TOTAL];
  const lptr lds = (lptr)smem;
  const int tid = threadIdx.x, lane = tid & 63, wave = __builtin_amdgcn_readfirstlane(tid >> 6);
  const int b = blockIdx.x, loc = b >> 3, cl = loc >> 3, c = (b & 7) * 4 + cl, j = loc & 7;
  const int lrow = lane >> 3;
  const unsigned ve = lrow * 896 + (((lane & 7) ^ (lrow >> 1)) << 4);
  const int step = 1 + (cl + (b & 7) * 4) % 6, k0 = j == 7 ? 0 : j;
  const bool isB = wave < 4;
  const int wa = wave & 3;
  char* const ws = p.ws;
  STAMP(0);
  unsigned ua = 0, ub = 0, uc = 0, ud = 0;
  u32x4 rxa, rbr, rbi; int ka = k0;
  const unsigned vo = (ve ^ 64) + 7168, lba = wa * 2048;
#define NEXTKA() do { ka += step; ka = ka >= 7 ? ka - 7 : ka; } while (0)
#define LDCH(slot) do { NEXTKA(); const unsigned k_ = ka * 128; DMAX(rxa, ve, k_, lba, (slot)); DMAX(rxa, vo, k_, lba, (slot) + 1024); \
      DMA(rbr, ve, k_, lba, (slot) + 8192); DMA(rbr, vo, k_, lba, (slot) + 9216); DMA(rbi, ve, k_, lba, (slot) + 16384); DMA(rbi, vo, k_, lba, (slot) + 17408); } while (0)
  {
    const u32x4 rx = mkrs((const char*)p.xb + (size_t)c * 57344 + wave * 7168, 7168);
    const unsigned vx = ve ^ ((wave & 1) << 6), lb = wave * 1024;
    int k = k0;
    if (j == 7) {
      const u32x4 rw = mkrs(isB ? p.wk : p.wv, 25088);
      const unsigned vw = wa * 1024 + lane * 16, lw = K_W + (isB ? 0 : 3584) + wa * 1024; const bool half = wa < 3 || lane < 32;
#pragma unroll
      for (int i = 0; i < 7; ++i) { NEXTK(); DMAX(rx, vx, k * 128, lb, i * SK); if (half) DMA(rw, vw, k * 3584, lw, i * SK); }
    } else {
      const u32x4 rw = mkrs((const char*)p.wq + j * 128 + wave * 7168, 401408 - j * 128 - wave * 7168);
      const unsigned vw = lrow * 896 + (((lane & 7) ^ ((lrow & 2) << 1)) << 4);
      if (wave == 0) { const unsigned co = (64 * j + lane) * 2;
        asm volatile("global_load_ushort %0, %4, %5\n\tglobal_load_ushort %1, %4, %6\n\tglobal_load_ushort %2, %4, %7\n\tglobal_load_ushort %3, %4, %8"
                     : "=&v"(ua), "=&v"(ub), "=&v"(uc), "=&v"(ud) : "v"(co), "s"(p.lre), "s"(p.lim), "s"(p.ldt), "s"(p.dsk) : "memory"); }
#pragma unroll
      for (int i = 0; i < 7; ++i) { NEXTK(); DMAX(rx, vx, k * 128, lb, i * SK); DMA(rw, vw, k * 57344, lb, i * SK + K_W); }
      rxa = mkrs((const char*)p.xa + (size_t)c * 57344 + wa * 14336, 14336);
      rbr = mkrs((const char*)p.bre + (size_t)j * 57344 + wa * 14336, 14336);
      rbi = mkrs((const char*)p.bim + (size_t)j * 57344 + wa * 14336, 14336);
    }
  }
  STAMP(8);
  if (tid < 16) ((LAS unsigned*)(lds + L_MISC))[tid] = 0;
  const int r32 = lane & 31, hi = lane >> 5;
  const int g4 = lane >> 4, tq = (lane & 15) >> 2, tp = lane & 3;
  const unsigned xsw = (r32 >> 1) & 7, lo16 = lane * 16;
  const int th = wa >> 1, chh = wa & 1;
  u64 tag;
  const __amdgpu_buffer_rsrc_t rs_ws = __builtin_amdgcn_make_buffer_rsrc(ws, 0, WS_END, 0x00020000);
  const f32x16 zero16 = {};
  const unsigned o_own = WS_O + (((2 * c + th) * 16 + 2 * j + chh) * 2) * 1024;
#define MARK() do { const u32x4 ones = {~0u, ~0u, ~0u, ~0u};       \
    BST(ones, rs_ws, lo16, o_own); BST(ones, rs_ws, lo16 + 1024, o_own); } while (0)
#define LDEPOCH() asm volatile("s_load_dwordx2 %0, %1, 0x0\n\ts_waitcnt lgkmcnt(0)" : "=s"(tag) : "s"(ws) : "memory"); tag = (tag | 1) + 2

  if (isB) {
    if (wave == 0 && j < 7) {
      asm volatile("s_waitcnt vmcnt(14)" : "+v"(ua), "+v"(ub), "+v"(uc), "+v"(ud) :: "memory");
      const float pa = bf2f(ua), pb = bf2f(ub), dt = rbf(expf(bf2f(uc)));
      const float er = expf(dt * pa); float sn, cs; sincosf(dt * pb, &sn, &cs);
      const float nr = er * cs - 1.f, ni = er * sn, den = pa * pa + pb * pb;
      const cf lam = {er * cs, -ni};
      cf l16 = cmul(lam, lam); l16 = cmul(l16, l16); l16 = cmul(l16, l16); l16 = cmul(l16, l16);
      cf l64 = cmul(l16, l16); l64 = cmul(l64, l64);
      *(LAS f32x4*)(lds + L_EX + lo16) = (f32x4){lam.r, lam.i, 2.f * (nr * pa + ni * pb) / den, 2.f * (ni * pa - nr * pb) / den};
      *(LAS f32x4*)(lds + L_EX + 1024 + lo16) = (f32x4){l16.r, l16.i, l64.r, l64.i};
      *(LAS unsigned*)(lds + L_EX + 2048 + lane * 4) = ud;
    }
    LDEPOCH();
    if (tid == 0) *(LAS u64*)(lds + L_TAG) = tag;
    {
      const unsigned x_off = (32 * th + r32) * 128;
      const int wrow = 8 * (g4 >> 1) + tq;
      bf16x8 fax[4], faw[4], fbx[4], fbw[4];
      f32x16 q = zero16;
#define PRD(B, i, P) _Pragma("unroll") for (int ks = 0; ks < 4; ++ks) { B##x[ks] = ldsf(lds + (i) * SK + x_off + (((2 * ks + hi) ^ xsw) << 4)); B##w[ks] = ldstr2(lds + (i) * SK + w_off + ks * 16 * (P), 4 * (P)); } SBAR()
#define PMM(B, SW) _Pragma("unroll") for (int ks = 0; ks < 4; ++ks) q = (SW) ? MFMA(B##x[ks], B##w[ks], q) : MFMA(B##w[ks], B##x[ks], q); PIN(q); SBAR()
#define PROJ(P, SW) do { VMW(6); BAR(); PRD(fa, 0, P); PRD(fb, 1, P); PMM(fa, SW); PRD(fa, 2, P); PMM(fb, SW); PRD(fb, 3, P); PMM(fa, SW); \
        VMW(0); BAR(); PRD(fa, 4, P); PMM(fb, SW); PRD(fb, 5, P); PMM(fa, SW); PRD(fa, 6, P); PMM(fb, SW); PMM(fa, SW); } while (0)
      if (j < 7) {
        const unsigned w_off = K_W + wrow * 128 + ((64 * chh + 32 * (g4 & 1) + 8 * tp) ^ ((tq & 2) << 5));
        PROJ(128, false);
        STAMP(1);
        const float qs = 0.18898223650461361f * 1.4426950408889634f;
#pragma unroll
        for (int g = 0; g < 4; g += 2) {
          const auto s0 = __builtin_amdgcn_permlane32_swap(pk(q[4 * g] * qs, q[4 * g + 1] * qs), pk(q[4 * g + 4] * qs, q[4 * g + 5] * qs), false, false);
          const auto s1 = __builtin_amdgcn_permlane32_swap(pk(q[4 * g + 2] * qs, q[4 * g + 3] * qs), pk(q[4 * g + 6] * qs, q[4 * g + 7] * qs), false, false);
          BST(((u32x4){s0[0], s1[0], s0[1], s1[1]}), rs_ws, lo16 + g * 512, WS_Q + ((2 * c + th) * 56 + 8 * j + 4 * chh) * 512);
        }
      } else {
        const unsigned w_off = K_W + chh * 3584 + wrow * 56 + (16 * (g4 & 1) + 4 * tp) * 2;
        if (chh) { PROJ(56, true);
#pragma unroll
          for (int r = 0; r < 16; ++r) q[r] = r32 < 28 ? q[r] : (r32 == 28 ? 1.f : 0.f);
        } else { PROJ(56, false); if (hi) { q[12] = 0.f; q[13] = 0.f; q[14] = 0.f; q[15] = 0.f; } }
        STAMP(1);
        const unsigned ko = WS_KV + (2 * c + th) * 4096 + chh * 2048;
        BST(U4(pack8(q, 0)), rs_ws, lo16, ko); BST(U4(pack8(q, 1)), rs_ws, lo16 + 1024, ko);
      }
      publish4(ws, WS_FP + (c * 8 + j) * 128, tag, lane, cPQ);
    }
    KV A0, A1, A2, A3;
    A3.v1 = (bf16x8){};
    f32x16 O[4] = {zero16, zero16, zero16, zero16};
    f32x16 Sa, Sb = zero16; u32x4 Pa, Pb = {};
    PIN(O[0]); PIN(O[1]); PIN(O[2]); PIN(O[3]); PIN(Sb); PIN(Pb); PIN(A3.v1);
    STAMP(2);
    poll(ws, WS_FP + (lane < 8 ? (wave * 8 + lane) * 8 + 7 : c * 8 + ((56 * j + (lane & 1) * 55) >> 6)) * 128, tag);
    STAMP(3);
    const __amdgpu_buffer_rsrc_t rs_kv = __builtin_amdgcn_make_buffer_rsrc(ws + WS_KV + wave * 65536, 0, 65536, 0x00020000);
#define LDKV(B, T) do { const unsigned so_ = (((T) + loc) & 15) * 4096; \
                        B.k0 = BF(BLD(rs_kv, lo16, so_)); B.k1 = BF(BLD(rs_kv, lo16, so_ + 1024)); \
                        B.v0 = BF(BLD(rs_kv, lo16, so_ + 2048)); B.v1 = BF(BLD(rs_kv, lo16, so_ + 3072)); } while (0)
#define LDK(B, T) do { const unsigned so_ = (((T) + loc) & 15) * 4096; B.k0 = BF(BLD(rs_kv, lo16, so_)); B.k1 = BF(BLD(rs_kv, lo16, so_ + 1024)); } while (0)
#define LDV(B, T) do { const unsigned so_ = (((T) + loc) & 15) * 4096; B.v0 = BF(BLD(rs_kv, lo16, so_ + 2048)); B.v1 = BF(BLD(rs_kv, lo16, so_ + 3072)); } while (0)
    bf16x8 Q0[4], Q1[4];
    u32x2 qp[4][4];
    const unsigned q_e = r32 * 16 + hi * 8, q_o = r32 * 16 + (hi ? 512 : 8);
#define LDQ(i) _Pragma("unroll") for (int u = 0; u < 4; ++u) qp[i][u] = __builtin_amdgcn_raw_buffer_load_b64(rs_ws, (((wave ^ (3 - (i))) & 1) ? q_o : q_e) + u * 512, \
                 WS_Q + ((2 * c + ((wave ^ (3 - (i))) >> 1)) * 56 + 7 * j + 3 * ((wave ^ (3 - (i))) & 1)) * 512, SC1)
    LDQ(0); LDKV(A0, 0); LDQ(1); LDQ(2); LDQ(3); LDKV(A1, 1); LDKV(A2, 2); LDK(A3, 3);
    SBAR();
#pragma unroll
    for (int i = 0; i < 4; ++i) { Q0[i] = BF(((u32x4){qp[i][0][0], qp[i][0][1], qp[i][1][0], qp[i][1][1]}));
      Q1[i] = BF(((u32x4){qp[i][2][0], qp[i][2][1], hi ? 0u : qp[i][3][0], hi ? 0u : qp[i][3][1]})); }
    Sa = MFMA(A0.k0, Q0[0], zero16); Sa = MFMA(A0.k1, Q1[0], Sa);
    STAMP(9);
#define UNIT(S, Sn, KN, QN, VP, OP, VC, OC, X) do { \
      Pb[2] = pk(Sn[12], Sn[13]); Pb[3] = pk(Sn[14], Sn[15]); \
      if ((QN) >= 0) Sn = MFMA(KN.k0, Q0[(QN) & 3], zero16); S[0] = EX(S[0]); S[1] = EX(S[1]); S[2] = EX(S[2]); S[3] = EX(S[3]); SBAR(); \
      if ((QN) >= 0) Sn = MFMA(KN.k1, Q1[(QN) & 3], Sn); Pa[0] = pk(S[0], S[1]); Pa[1] = pk(S[2], S[3]); S[4] = EX(S[4]); S[5] = EX(S[5]); S[6] = EX(S[6]); S[7] = EX(S[7]); SBAR(); \
      X; SBAR(); \
      OP = MFMA(VP.v1, BF(Pb), OP); Pa[2] = pk(S[4], S[5]); Pa[3] = pk(S[6], S[7]); S[8] = EX(S[8]); S[9] = EX(S[9]); S[10] = EX(S[10]); S[11] = EX(S[11]); SBAR(); \
      OC = MFMA(VC.v0, BF(Pa), OC); Pb[0] = pk(S[8], S[9]); Pb[1] = pk(S[10], S[11]); S[12] = EX(S[12]); S[13] = EX(S[13]); S[14] = EX(S[14]); S[15] = EX(S[15]); SBAR(); \
    } while (0)
#define TILE(CUR, NXT, PRV, T, LD) do { \
      UNIT(Sa, Sb, CUR, 1, PRV, O[3], CUR, O[0], ); \
      LDV(PRV, (T) + 3); SBAR(); \
      UNIT(Sb, Sa, CUR, 2, CUR, O[0], CUR, O[1], ); \
      UNIT(Sa, Sb, CUR, 3, CUR, O[1], CUR, O[2], if (LD) LDK(CUR, (T) + 4)); \
      UNIT(Sb, Sa, NXT, 0, CUR, O[2], CUR, O[3], ); } while (0)
#pragma unroll 1
    for (int t = 0; t < 12; t += 4) {
      TILE(A0, A1, A3, t, 1); TILE(A1, A2, A0, t + 1, 1); TILE(A2, A3, A1, t + 2, 1); TILE(A3, A0, A2, t + 3, 1);
    }
    TILE(A0, A1, A3, 12, 0);
    const lptr oc = lds + L_OC + lo16;
#define OUTP(T, I) _Pragma("unroll") for (int q4 = 0; q4 < 4; ++q4) *(LAS f32x4*)(oc + ((wave ^ I) * 3 + I - 1) * 4096 + q4 * 1024) = (f32x4){T[4 * q4], T[4 * q4 + 1], T[4 * q4 + 2], T[4 * q4 + 3]}
    f32x4 pv[12];
    UNIT(Sa, Sb, A1, 1, A0, O[3], A1, O[0], ); UNIT(Sb, Sa, A1, 2, A1, O[0], A1, O[1], ); UNIT(Sa, Sb, A2, 0, A1, O[1], A1, O[2], );
    UNIT(Sb, Sa, A2, 1, A1, O[2], A2, O[0], ); UNIT(Sa, Sb, A2, 2, A2, O[0], A2, O[1], ); UNIT(Sb, Sa, A3, 0, A2, O[1], A2, O[2], );
    UNIT(Sa, Sb, A3, 1, A2, O[2], A3, O[0], ); UNIT(Sb, Sa, A3, 2, A3, O[0], A3, O[1], lds_wait(j < 7 ? cCR : cPB, 4));
    UNIT(Sa, Sb, A1, 3, A3, O[1], A3, O[2], OUTP(O[0], 3));
    UNIT(Sb, Sa, A2, 3, A3, O[2], A1, O[3], OUTP(O[1], 2));
    UNIT(Sa, Sb, A3, 3, A1, O[3], A2, O[3], OUTP(O[2], 1); lds_signal(cBB));
    UNIT(Sb, Sa, A3, -1, A2, O[3], A3, O[3], lds_wait(cBB, 4); _Pragma("unroll") for (int s2 = 0; s2 < 12; ++s2) pv[s2] = *(LAS f32x4*)(oc + wave * 12288 + s2 * 1024));
    Pb[2] = pk(Sb[12], Sb[13]); Pb[3] = pk(Sb[14], Sb[15]);
    O[3] = MFMA(A3.v1, BF(Pb), O[3]);
    STAMP(4);
    __builtin_amdgcn_s_setprio(2);
#pragma unroll
    for (int s2 = 0; s2 < 4; ++s2) pv[s2] = pv[s2] + pv[s2 + 4] + pv[s2 + 8];
    STAMP(10);
    unsigned lo16b = lo16; PIN(lo16b);
    const int lane = lo16b >> 4, r32 = lane & 31, hi = lane >> 5, g4 = lane >> 4, tq = (lane & 15) >> 2, tp = lane & 3;
    f32x16 o = O[3];
#pragma unroll
    for (int r = 0; r < 16; ++r) o[r] += pv[r >> 2][r & 3];
    {
      float l = swap_hi(o[12]);
      float am = 0.f;
#pragma unroll
      for (int r = 0; r < 16; r += 2) am = __builtin_fmaxf(__builtin_fmaxf(am, __builtin_fabsf(o[r])), __builtin_fabsf(o[r + 1]));
      const bool ok = (l >= 0x1p-90f) && (l <= 0x1p110f) && (am <= 0x1p126f);
      const bool slow = !wall(ok);
      if (slow) {
        const __amdgpu_buffer_rsrc_t rk = __builtin_amdgcn_make_buffer_rsrc(ws + WS_KV, 0, 262144, 0x00020000);
        poll(ws, WS_FP + (r32 * 8 + 7) * 128, tag);
        float m = -__builtin_inff();
#pragma unroll 1
        for (int t = 0; t < 64; ++t) {
          f32x16 s = MFMA(BF(BLD(rk, lo16, t * 4096)), Q0[3], zero16); s = MFMA(BF(BLD(rk, lo16 + 1024, t * 4096)), Q1[3], s);
#pragma unroll
          for (int r = 0; r < 16; ++r) m = __builtin_fmaxf(m, s[r]);
        }
        m = __builtin_fmaxf(swap_lo(m), swap_hi(m));
        o = zero16;
#pragma unroll 1
        for (int t = 0; t < 64; ++t) {
          f32x16 s = MFMA(BF(BLD(rk, lo16, t * 4096)), Q0[3], zero16); s = MFMA(BF(BLD(rk, lo16 + 1024, t * 4096)), Q1[3], s);
#pragma unroll
          for (int r = 0; r < 16; ++r) s[r] = EX(s[r] - m);
          o = MFMA(BF(BLD(rk, lo16 + 2048, t * 4096)), pack8(s, 0), o); o = MFMA(BF(BLD(rk, lo16 + 3072, t * 4096)), pack8(s, 1), o);
        }
        l = swap_hi(o[12]);
      }
      const float inv = __builtin_amdgcn_rcpf(l);
#pragma unroll
      for (int r = 0; r < 16; ++r) o[r] *= inv;
      if (hi) { o[12] = 0.f; o[13] = 0.f; o[14] = 0.f; o[15] = 0.f; }
      u32x4 f0 = U4(pack8(o, 0)), f1 = U4(pack8(o, 1));
      if (slow) {
#pragma unroll
        for (int k = 0; k < 4; ++k) { f0[k] = umin(f0[k], 0xfffffffeu); f1[k] = umin(f1[k], 0xfffffffeu); }
      }
      BST(f0, rs_ws, lo16, o_own); BST(f1, rs_ws, lo16 + 1024, o_own);
    }
    lds_signal(cOC);
    lds_signal(cGO + 4 * wave);
    STAMP(5);
    if (j == 7) { STAMP_OUT(); return; }
    {
      const CAS Params* kp = KARGS();
      const __amdgpu_buffer_rsrc_t rs_out = __builtin_amdgcn_make_buffer_rsrc(kp->out, 0, 2048 * 896 * 4, 0x00020000);
      lds_wait(cWO, 4);
      u32x4 of[16];
      const unsigned ob = WS_O + (((2 * c + th) * 16 + 8 * chh) * 2) * 1024;
#define LDO(k) of[k] = BLD(rs_ws, lo16 + ((k) & 3) * 1024, ob + ((k) >> 2) * 4096)
      const int rowb = 224 * chh + 4 * (g4 >> 1) + tq;
      const unsigned wb = (L_WO + rowb * 128 + ((32 * (g4 & 1) + 8 * tp) ^ ((tq & 2) << 5))) ^ (chh << 6);
      bf16x8 w0[16], w1[16];
#pragma unroll
      for (int k = 0; k < 16; ++k) w1[k] = ldstr2(lds + (wb ^ 64) + (28 * (k >> 1) + 16 * (k & 1)) * 128, 1024);
      SBAR();
      {
        unsigned have = 0;
#pragma unroll 1
        for (unsigned s = 0; s < SPIN_CAP; ++s) {
          const unsigned w = __builtin_amdgcn_raw_buffer_load_b32(rs_ws, (lane & 7) * 2048 + 2044, ob, SC1);
          const unsigned rdy = (s + 1 < SPIN_CAP ? (unsigned)__builtin_amdgcn_ballot_w64(w != ~0u) : ~0u) & 0xffu & ~have;
#pragma unroll
          for (int h = 0; h < 8; ++h) if (rdy >> h & 1) { LDO(2 * h); LDO(2 * h + 1); }
          have |= rdy;
          if (have == 0xffu) break;
          __builtin_amdgcn_s_sleep(2);
        }
      }
      f32x16 acc0 = zero16, acc1 = zero16;
#define OBAD(h) (!wall(umax(umax(umax(umax(of[2 * (h)][0], of[2 * (h)][1]), umax(of[2 * (h)][2], of[2 * (h)][3])), umax(of[2 * (h) + 1][0], of[2 * (h) + 1][1])), umax(of[2 * (h) + 1][2], of[2 * (h) + 1][3])) != ~0u))
#define OHEAD(h) do { \
        if (OBAD(h)) { \
          _Pragma("unroll 1") for (unsigned s = 0; s < SPIN_CAP; ++s) { LDO(2 * h); LDO(2 * h + 1); if (!OBAD(h)) break; __builtin_amdgcn_s_sleep(2); } \
        } \
        acc1 = MFMA(BF(of[2 * h]), w1[2 * h], acc1); acc1 = MFMA(BF(of[2 * h + 1]), w1[2 * h + 1], acc1); PIN(acc1); \
        w0[2 * h] = ldstr2(lds + wb + 28 * h * 128, 1024); w0[2 * h + 1] = ldstr2(lds + wb + (28 * h + 16) * 128, 1024); } while (0)
      OHEAD(0); STAMP(6); OHEAD(1); OHEAD(2); OHEAD(3); OHEAD(4); OHEAD(5); OHEAD(6); OHEAD(7);
      STAMP(11);
      lds_wait(cOC, 4);
#pragma unroll
      for (int k = 0; k < 4; ++k) acc0 = MFMA(BF(of[k]), w0[k], acc0);
      SBAR();
      const lptr tw = lds + L_OC + hi * 512 + r32 * 4;
#pragma unroll
      for (int r = 0; r < 16; ++r) *(LAS float*)(tw + (wave ^ 1) * 4096 + ((r & 3) + 8 * (r >> 2)) * 128) = acc1[r];
      SBAR();
#pragma unroll
      for (int k = 4; k < 8; ++k) acc0 = MFMA(BF(of[k]), w0[k], acc0);
      PIN(acc0);
      lds_signal(cPR + 4 * th);
#pragma unroll
      for (int k = 8; k < 16; ++k) acc0 = MFMA(BF(of[k]), w0[k], acc0);
      PIN(acc0);
      lds_wait(cPR + 4 * th, 2);
      f32x4 pa[4];
#pragma unroll
      for (int k = 0; k < 4; ++k) pa[k] = *(LAS f32x4*)(lds + L_OC + wave * 4096 + k * 1024 + lo16);
#pragma unroll
      for (int r = 0; r < 16; ++r) *(LAS float*)(tw + 16384 + wave * 4096 + ((r & 3) + 8 * (r >> 2)) * 128) = acc0[r];
      MEMBAR();
      STAMP(12);
      const unsigned oo = ((64 * c + 32 * th + (lane >> 3)) * 896 + 448 + 64 * j + 32 * chh + (lane & 7) * 4) * 4;
#pragma unroll
      for (int k = 0; k < 4; ++k)
        __builtin_amdgcn_raw_buffer_store_b128(U4(pa[k] + *(LAS f32x4*)(lds + L_OC + 16384 + wave * 4096 + k * 1024 + lo16)), rs_out, oo + k * 28672, 0, 17);
    }
    STAMP(7);
    STAMP_OUT();
  } else {
    if (j == 7) {
      VMW(6); BAR(); VMW(0); BAR();
      lds_wait_<4>(cPQ, 4);
      { const u32x2 t2 = *(LAS u32x2*)(lds + L_TAG);
        tag = ((u64)(unsigned)__builtin_amdgcn_readfirstlane(t2[1]) << 32) | (unsigned)__builtin_amdgcn_readfirstlane(t2[0]); }
      __builtin_amdgcn_s_sleep(64);
      MARK();
      publish4(ws, WS_FH + (c * 8 + 7) * 128, tag, lane, cPB);
      if (c == 0 && wa == 0) {
        __builtin_amdgcn_s_sleep(127);
#pragma unroll 1
        for (int k = 0; k < 4; ++k) poll(ws, WS_FP + (k * 64 + lane) * 128, tag);
        if (lane == 0) __hip_atomic_store((GAS u64*)(ws + WS_EPOCH), tag, RLX_AGENT);
      }
      STAMP_OUT();
      return;
    }
    VMW(6); BAR(); LDCH(L_SA); VMW(6); BAR();
    cf lam, coef, l16, l64; f32x16 ur = zero16, ui = zero16;
    f32x16 acc[4] = {zero16, zero16, zero16, zero16};
    const unsigned cs0 = wa * 16384u, cs1 = L_R1 + wa * 12288u;
    unsigned a_htw = L_HT + wa * 4096 + r32 * 64 + hi * 32, a_htr = L_HT + wa * 4096 + (8 * (g4 >> 1) + tq) * 64 + (g4 & 1) * 32 + tp * 8;
    unsigned a_hst = WS_H + ((2 * c + th) * 56 + 4 * j + 2 * chh) * 1024 + lo16;
    unsigned a_c[4], a_cn[2], a_x[2], a_ex, e_want; u32x4 dg[2]; cf l256, lw, lo_;
    const int seg = 2 * th + hi;
#pragma unroll
    for (int f = 0; f < 4; ++f) a_c[f] = cs0 + (32 * chh + r32) * 128 + (((2 * f + hi) ^ xsw) << 4);
#pragma unroll
    for (int f = 0; f < 2; ++f) a_cn[f] = cs1 + 8192 + (32 * chh + r32) * 64 + (((2 * f + hi) ^ ((r32 >> 2) & 3)) << 4);
    unsigned a_vh = (lane >> 2) * 896 + (((lane & 3) ^ ((lane >> 4) & 3)) << 4) + 384 + chh * 64, a_vo = ve ^ 64;
    unsigned a_vw = lrow * 896 + (((lane & 7) ^ ((lrow & 2) << 1)) << 4);
    unsigned a_fl = WS_FH + (c * 8 + (lane & 7)) * 128;
    unsigned a_el = (31 - umin(r32, c - 1)) * 512 + 4, a_eo = (32 - c + 2 * th + hi) * 512 + r32 * 16, a_hx = L_EX + 2048 + (32 * chh + r32) * 8;
    {
      LDCH(0); LDCH(CH);
      { const f32x4 c0 = *(LAS f32x4*)(lds + L_EX + (32 * chh + r32) * 16), c1 = *(LAS f32x4*)(lds + L_EX + 1024 + (32 * chh + r32) * 16);
        lam = {c0[0], c0[1]}; coef = {c0[2], c0[3]}; l16 = {c1[0], c1[1]}; l64 = {c1[2], c1[3]}; ud = *(LAS unsigned*)(lds + L_EX + 2048 + (32 * chh + r32) * 4); }
      { const int e = r32 - 8 * hi; const unsigned dv = ud << ((e & 1) * 16);
#pragma unroll
        for (int s2 = 0; s2 < 2; ++s2) {
          dg[s2] = (u32x4){(e >> 1) == 8 * s2 ? dv : 0u, (e >> 1) == 8 * s2 + 1 ? dv : 0u, (e >> 1) == 8 * s2 + 2 ? dv : 0u, (e >> 1) == 8 * s2 + 3 ? dv : 0u};
          a_x[s2] = L_SA + (32 * th + r32) * 128 + (((4 * chh + 2 * s2 + hi) ^ xsw) << 4); PIN(dg[s2]); PIN(a_x[s2]); }
        cf lseg = hi ? l16 : (cf){1.f, 0.f};
        if (th) lseg = cmul(lseg, cmul(l16, l16));
        const cf l128 = cmul(l64, l64), ls128 = cmul(lseg, l128);
        l256 = cmul(l128, l128); lw = th ? ls128 : lseg; lo_ = th ? lseg : ls128; e_want = (c - seg + 3) >> 2; a_ex = L_EX + (32 * chh + r32) * 8;
        PIN(l256.r); PIN(l256.i); PIN(lw.r); PIN(lw.i); PIN(lo_.r); PIN(lo_.i); PIN(e_want); PIN(a_ex); }
      PIN(a_htw); PIN(a_htr); PIN(a_hst); PIN(a_c[0]); PIN(a_c[1]); PIN(a_c[2]); PIN(a_c[3]); PIN(a_cn[0]); PIN(a_cn[1]); PIN(a_vh); PIN(a_vw); PIN(a_fl); PIN(a_el); PIN(a_eo); PIN(a_hx); PIN(a_vo);
      const int trow = 32 * th + 16 * ((r32 >> 2) & 1) + (r32 & 3) + 4 * (r32 >> 3);
      const unsigned xa_off = trow * 128, xa_sw = (trow >> 1) & 7, b_off = 8192 + (32 * chh + r32) * 128;
      bf16x8 gax[4], gar[4], gai[4], gbx[4], gbr[4], gbi[4];
      unsigned gx[3][4], gw[3][4];
#define GB(s_) ((s_) < 49152 ? 0u : (s_) < 110592 ? 49152u : 110592u)
#define GT(s_) ((s_) < 49152 ? 0 : (s_) < 110592 ? 1 : 2)
#pragma unroll
      for (int t = 0; t < 3; ++t)
#pragma unroll
        for (int ks = 0; ks < 4; ++ks) { gx[t][ks] = GB(t * 60000) + xa_off + (((2 * ks + hi) ^ xa_sw) << 4); gw[t][ks] = GB(t * 60000) + b_off + (((2 * ks + hi) ^ xsw) << 4); PIN(gx[t][ks]); PIN(gw[t][ks]); }
#define GRD(B, s_) _Pragma("unroll") for (int ks = 0; ks < 4; ++ks) { B##x[ks] = ldsf(lds + gx[GT(s_)][ks] + ((s_) - GB(s_))); \
        B##r[ks] = ldsf(lds + gw[GT(s_)][ks] + ((s_) - GB(s_))); B##i[ks] = ldsf(lds + gw[GT(s_)][ks] + ((s_) - GB(s_) + 8192)); } SBAR()
#define GMM(B) _Pragma("unroll") for (int ks = 0; ks < 4; ++ks) { ur = MFMA(B##x[ks], B##r[ks], ur); ui = MFMA(B##x[ks], B##i[ks], ui); } PIN(ur); PIN(ui); SBAR()
#define GAP() do { SBAR(); asm volatile("s_nop %0" :: "i"(7)); SBAR(); } while (0)
#define GMS(B) _Pragma("unroll") for (int ks = 0; ks < 4; ++ks) { ur = MFMA(B##x[ks], B##r[ks], ur); GAP(); ui = MFMA(B##x[ks], B##i[ks], ui); GAP(); } PIN(ur); PIN(ui); SBAR()
      __builtin_amdgcn_s_setprio(1);
      STAMP(12);
      lds_wait_<1>(cPQ, 4);
      LDCH(2 * CH); LDCH(3 * CH); LDCH(4 * CH);
      VMW(18); lds_barrierA(cAB, 4); GRD(ga, L_SA); GRD(gb, 0); GMM(ga); GRD(ga, CH); GMM(gb);
      VMW(6); lds_barrierA(cAB, 8);
      STAMP(13);
      GRD(gb, 2 * CH); LDCH(L_SA); GMM(ga); GRD(ga, 3 * CH); GMS(gb);
      VMW(6); lds_barrierA(cAB, 12);
      STAMP(14);
      GRD(gb, 4 * CH); GMS(ga);
      VMW(0); lds_barrierA(cAB, 16);
      STAMP(15);
      GRD(ga, L_SA); GMS(gb); GMS(ga);
      { const u32x2 t2 = *(LAS u32x2*)(lds + L_TAG);
        tag = ((u64)(unsigned)__builtin_amdgcn_readfirstlane(t2[1]) << 32) | (unsigned)__builtin_amdgcn_readfirstlane(t2[0]); }
    }
    acc[0] = MFMA(ldsf(lds + a_x[0]), BF(dg[0]), acc[0]); acc[0] = MFMA(ldsf(lds + a_x[1]), BF(dg[1]), acc[0]);
    PIN(acc[0]);
    STAMP(1);
    cf e = {0.f, 0.f};
#pragma unroll
    for (int r = 0; r < 16; ++r) {
      const float a = coef.r * ur[r] - coef.i * ui[r], b2 = -(coef.r * ui[r] + coef.i * ur[r]);
      ur[r] = a; ui[r] = b2; e = r ? cfma(lam, e, (cf){a, b2}) : (cf){a, b2};
    }
    *(LAS f32x2*)(lds + a_ex + th * 1024 + hi * 512) = (f32x2){e.r, e.i};
    __builtin_amdgcn_s_setprio(0);
    lds_barrierA(cAB, 20);
    cf g0;
    {
      const f32x2 e0 = *(LAS f32x2*)(lds + a_ex), e1 = *(LAS f32x2*)(lds + a_ex + 512), e2 = *(LAS f32x2*)(lds + a_ex + 1024), e3 = *(LAS f32x2*)(lds + a_ex + 1536);
      __builtin_amdgcn_s_setprio(1);
      const cf E2 = cfma(l16, (cf){e0[0], e0[1]}, (cf){e1[0], e1[1]}), E3 = cfma(l16, E2, (cf){e2[0], e2[1]}), E = cfma(l16, E3, (cf){e3[0], e3[1]});
      if (th) g0 = hi ? E3 : E2; else g0 = hi ? (cf){e0[0], e0[1]} : (cf){0.f, 0.f};
      PIN(g0.r); PIN(g0.i);
      if (th == 0 && hi == 0 && c < 31)
        BST(((u32x4){f2u(E.r), (unsigned)tag, f2u(E.i), (unsigned)tag}), rs_ws, r32 * 16, WS_E + ((2 * j + chh) * 32 + 31 - c) * 512);
    }
    __builtin_amdgcn_s_setprio(0);
    const CAS Params* kp = KARGS();
    {
      const u32x4 rc = mkrs((const char*)(th ? kp->cim : kp->cre) + (size_t)j * 57344, 57344);
      int n2 = (6 * cl + 2 * ((b & 7) % 3)) % 24;
#pragma unroll
      for (int n = 0; n < 24; ++n) {
        DMA(rc, (n & 1) ? a_vo : ve, (n2 & 7) * 7168 + (n2 >> 3) * 128 + chh * 512, (n2 < 16 ? cs0 : cs1 - 16384) + n2 * 1024, 0);
        n2 = n2 == 23 ? 0 : n2 + 1; }
#pragma unroll
      for (int n = 0; n < 4; ++n) DMA(rc, a_vh, n * 14336, cs1, 8192 + n * 1024);
    }
    STAMP(2);
    cf G = g0;
    if (c > 0) {
      const __amdgpu_buffer_rsrc_t re = __builtin_amdgcn_make_buffer_rsrc(ws + WS_E + (2 * j + chh) * 16384, 0, 16384, 0x00020000);
      u32x4 ev[8];
#pragma unroll 1
      for (unsigned s = 0; s < SPIN_CAP; ++s) {
        const unsigned t1 = __builtin_amdgcn_raw_buffer_load_b32(re, a_el, 0, SC1);
        if (wall(t1 == (unsigned)tag)) break;
        __builtin_amdgcn_s_sleep(2);
      }
      MEMBAR();
#pragma unroll 1
      for (unsigned s = 0; s < SPIN_CAP; ++s) {
        unsigned got = 0;
#pragma unroll
        for (int m = 0; m < 8; ++m) ev[m] = BLD(re, a_eo, m * 2048);
        VMW(0);
        __builtin_amdgcn_s_setprio(1);
#pragma unroll
        for (int m = 0; m < 8; ++m) got += ((ev[m][1] ^ (unsigned)tag) | (ev[m][3] ^ (unsigned)tag)) == 0;
        if (wall(got == e_want)) break;
        __builtin_amdgcn_s_setprio(0);
        __builtin_amdgcn_s_sleep(4);
      }
      STAMP(3);
      cf H = {u2f(ev[7][0]), u2f(ev[7][2])};
#pragma unroll
      for (int m = 6; m >= 0; --m) H = cfma(l256, H, (cf){u2f(ev[m][0]), u2f(ev[m][2])});
      H = cfma(l64, (cf){swap_hi(H.r), swap_hi(H.i)}, (cf){swap_lo(H.r), swap_lo(H.i)});
      *(LAS f32x2*)(lds + a_hx + th * 512) = (f32x2){H.r, H.i};
      G = cfma(lw, H, g0);
      __builtin_amdgcn_s_setprio(0);
      lds_barrierA(cPE + 4 * chh, 2);
      const f32x2 ho = *(LAS f32x2*)(lds + a_hx + (th ^ 1) * 512);
      __builtin_amdgcn_s_setprio(1);
      G = cfma(lo_, (cf){ho[0], ho[1]}, G);
    }
    STAMP(4);
    __builtin_amdgcn_s_setprio(1);
    {
#pragma unroll
      for (int r = 0; r < 16; ++r) { G = cfma(lam, G, (cf){ur[r], ui[r]}); ur[r] = G.r; ui[r] = G.i; }
      *(LAS bf16x8*)(lds + a_htw) = pack8(ur, 0); *(LAS bf16x8*)(lds + a_htw + 16) = pack8(ur, 1);
      *(LAS bf16x8*)(lds + a_htw + 2048) = pack8(ui, 0); *(LAS bf16x8*)(lds + a_htw + 2064) = pack8(ui, 1);
      unsigned a_hs2 = a_hst + 28 * 1024; PIN(a_hs2);
      __builtin_amdgcn_s_setprio(0);
      asm volatile("s_waitcnt lgkmcnt(0)" ::: "memory");
#pragma unroll
      for (int k = 0; k < 4; ++k) BST(U4(ldstr2(lds + a_htr + (k >> 1) * 2048 + (k & 1) * 1024, 256)), rs_ws, (k >> 1) ? a_hs2 : a_hst, (k & 1) * 1024);
      MARK();
      publish4(ws, WS_FH + (c * 8 + j) * 128, tag, lane, cPB);
    }
    STAMP(5);
    bf16x8 cfr[14][2];
    const int cflip = chh ? -1 : 1;
#define LDC(f) do { if ((f) < 12) { const unsigned a_ = a_c[(f) & 3] + ((f) < 8 ? ((f) >> 2) * 8192 : cs1 - cs0); cfr[f][0] = ldsf(lds + a_); cfr[f][1] = ldsf(lds + a_ + cflip * 4096); } \
                    else { cfr[f][0] = ldsf(lds + a_cn[(f) & 1]); cfr[f][1] = ldsf(lds + a_cn[(f) & 1] + cflip * 2048); } } while (0)
#pragma unroll
    for (int f = 0; f < 8; ++f) LDC(f);
    lds_signal(cCR);
    STAMP(10);
    lds_wait_<1>(cCR, 4);
    {
      const u32x4 rw = mkrs((const char*)kp->wo + j * 128 + wa * 7168, 401408 - j * 128 - wa * 7168);
      int m2 = (4 * cl + (b & 7)) % 14;
#pragma unroll
      for (int m = 0; m < 14; ++m) { DMA(rw, a_vw, m2 * 28672, wa * 1024 + m2 * 4096, L_WO); m2 = m2 == 13 ? 0 : m2 + 1; }
      if (wa == 0 && lane < 32) *(LAS u32x4*)(lds + L_WO + 57344 + lo16) = (u32x4){0, 0, 0, 0};
    }
    poll(ws, a_fl, tag);
    VMW(0);
    lds_signal(cWO);
    STAMP(6);
    {
      const unsigned hb = WS_H + (2 * c * 56 + 14 * wa) * 1024, h0 = hb + th * 57344, h1 = hb + (th ^ 1) * 57344;
      const unsigned fa = chh * 2048, fb = chh ? 0u - 12288u : 0u;
      u32x4 hf[14][2];
#define LDH(f) do { hf[f][0] = BLD(rs_ws, lo16, h0 + (f) * 1024 + ((f) < 12 ? fa : fb)); hf[f][1] = BLD(rs_ws, lo16, h1 + (f) * 1024 + ((f) < 12 ? fa : fb)); } while (0)
#define MMH(f) do { acc[0] = MFMA(BF(hf[f][0]), cfr[f][0], acc[0]); acc[1] = MFMA(BF(hf[f][0]), cfr[f][1], acc[1]); \
                    acc[2] = MFMA(BF(hf[f][1]), cfr[f][0], acc[2]); acc[3] = MFMA(BF(hf[f][1]), cfr[f][1], acc[3]); \
                    PIN(acc[0]); PIN(acc[1]); PIN(acc[2]); PIN(acc[3]); } while (0)
#pragma unroll
      for (int f = 0; f < 14; ++f) LDH(f);
      SBAR();
      lds_wait_<4>(cGO + 4 * wa, 1);
      STAMP(9);
      __builtin_amdgcn_s_setprio(1);
#pragma unroll
      for (int f = 0; f < 8; ++f) { MMH(f); SBAR(); if (f >= 1 && f < 7) LDC(f + 7); SBAR(); }
      lds_signal(cP4);
#pragma unroll
      for (int f = 8; f < 14; ++f) MMH(f);
    }
    lds_wait(cP4, 4);
    STAMP(11);
    rs_write(lds + L_G2, wa, lane, acc[1], acc[2], acc[3]);
    lds_barrierA(cAB, 24);
    f32x16 y = rs_read(lds + L_G2, wa, lane, acc[0]);
    const __amdgpu_buffer_rsrc_t rs_out = __builtin_amdgcn_make_buffer_rsrc(kp->out, 0, 2048 * 896 * 4, 0x00020000);
    MEMBAR();
#pragma unroll
    for (int r = 0; r < 16; ++r) *(LAS float*)(lds + L_G2 + wa * 12288 + hi * 512 + r32 * 4 + ((r & 3) + 8 * (r >> 2)) * 128) = y[r];
    MEMBAR();
    const unsigned oo = ((64 * c + 32 * th + (lane >> 3)) * 896 + 64 * j + 32 * chh + (lane & 7) * 4) * 4;
#pragma unroll
    for (int k = 0; k < 4; ++k) __builtin_amdgcn_raw_buffer_store_b128(*(LAS u32x4*)(lds + L_G2 + wa * 12288 + k * 1024 + lo16), rs_out, oo + k * 28672, 0, 17);
    STAMP(7);
    STAMP_OUT();
  }
}

extern "C" void kernel_launch(void* const* d_in, const int* in_sizes, int n_in, void* d_out, int out_size, void* d_ws, size_t ws_size,
                              hipStream_t stream) {
  Params p;
  p.xa = (const u16*)d_in[0]; p.lre = (const u16*)d_in[1]; p.lim = (const u16*)d_in[2]; p.bre = (const u16*)d_in[3]; p.bim = (const u16*)d_in[4];
  p.cre = (const u16*)d_in[5]; p.cim = (const u16*)d_in[6]; p.ldt = (const u16*)d_in[7]; p.dsk = (const u16*)d_in[8];
  p.xb = (const u16*)d_in[9]; p.wq = (const u16*)d_in[10]; p.wk = (const u16*)d_in[11]; p.wv = (const u16*)d_in[12]; p.wo = (const u16*)d_in[13];
  p.out = (float*)d_out; p.ws = (char*)d_ws;
  fused_kernel<<<256, 512, 0, stream>>>(p);
}
```

```cpp
#define PB 1
#define PK 99
#include <hip/hip_runtime.h>
#include <stdint.h>

#define DI __device__ __forceinline__
#define GAS __attribute__((address_space(1)))
#define LAS __attribute__((address_space(3)))
#define CAS __attribute__((address_space(4)))
typedef short bf16x8 __attribute__((ext_vector_type(8)));
typedef short s16x4 __attribute__((ext_vector_type(4)));
typedef float f32x16 __attribute__((ext_vector_type(16)));
typedef float f32x4 __attribute__((ext_vector_type(4)));
typedef float f32x2 __attribute__((ext_vector_type(2)));
typedef unsigned u32x4 __attribute__((ext_vector_type(4)));
typedef unsigned u32x2 __attribute__((ext_vector_type(2)));
typedef __bf16 bf16x2_t __attribute__((ext_vector_type(2)));
typedef unsigned long long u64;
typedef unsigned short u16;
typedef LAS char* lptr;
#define RLX_AGENT __ATOMIC_RELAXED, __HIP_MEMORY_SCOPE_AGENT
#define MFMA(a, b, c) __builtin_amdgcn_mfma_f32_32x32x16_bf16((a), (b), (c), 0, 0, 0)
#define SBAR() __builtin_amdgcn_sched_barrier(0)
#define PIN(x) asm volatile("" : "+v"(x))
#define MEMBAR() asm volatile("" ::: "memory")
#define SC1 16

constexpr unsigned WS_EPOCH = 0, WS_FH = 8192, WS_E = 0x10000, WS_FP = 0x48000,
                   WS_KV = 0x50000, WS_O = 0x100000, WS_H = 0x300000, WS_Q = 0x680000, WS_END = 0x880000, WS_DBG = 0x900000;
constexpr unsigned SK = 16384, K_W = 8192;
constexpr unsigned CH = 24576, L_SA = 135168;
constexpr unsigned L_R1 = 107520, L_HT = 65536, L_WO = 0, L_OC = 57856, L_G2 = 107520, L_MISC = 159744, L_EX = L_MISC + 128, L_TOTAL = 163840;
constexpr unsigned cBB = L_MISC, cAB = L_MISC + 4, cCR = L_MISC + 8, cWO = L_MISC + 12, cOC = L_MISC + 16, cPB = L_MISC + 20, cPR = L_MISC + 24, cP4 = L_MISC + 32, cPQ = L_MISC + 52, cGO = L_MISC + 36, cPE = L_MISC + 56, L_TAG = L_MISC + 64;
constexpr unsigned SPIN_CAP = 1u << 18;

struct Params {
  const u16 *xb, *wq, *wk, *wv; char* ws; const u16 *lre, *lim, *ldt, *dsk, *xa, *bre, *bim, *cre, *cim, *wo;
  float* out;
};

DI float u2f(unsigned v) { return __builtin_bit_cast(float, v); }
DI unsigned f2u(float v) { return __builtin_bit_cast(unsigned, v); }
DI unsigned umin(unsigned a, unsigned b) { return a < b ? a : b; }
DI unsigned umax(unsigned a, unsigned b) { return a > b ? a : b; }
DI bool wall(bool x) { return __builtin_amdgcn_ballot_w64(!x) == 0; }
DI float bf2f(unsigned v) { return u2f(v << 16); }
DI unsigned pk(float a, float b) { f32x2 v = {a, b}; return __builtin_bit_cast(unsigned, __builtin_convertvector(v, bf16x2_t)); }
DI float rbf(float a) { return u2f(pk(0.f, a)); }
DI bf16x8 pack8(const f32x16& x, int s) {
  u32x4 p = {pk(x[8 * s], x[8 * s + 1]), pk(x[8 * s + 2], x[8 * s + 3]), pk(x[8 * s + 4], x[8 * s + 5]), pk(x[8 * s + 6], x[8 * s + 7])};
  return __builtin_bit_cast(bf16x8, p);
}
DI u32x4 mkrs(const void* base, unsigned bytes) { const u64 a = (u64)base; return (u32x4){(unsigned)a, (unsigned)(a >> 32) & 0xffffu, bytes, 0x20000u}; }
#define DMA(rs, voff, soff, ldsb, IMM) asm volatile("s_add_u32 m0, %3, %4\n\ts_nop 0\n\tbuffer_load_dwordx4 %0, %1, %2 offen lds" \
                                                    :: "v"(voff), "s"(rs), "s"(soff), "s"(ldsb), "i"(IMM) : "memory", "scc")
#define DMAX(rs, voff, soff, ldsb, IMM) asm volatile("s_add_u32 m0, %3, %4\n\ts_nop 0\n\tbuffer_load_dwordx4 %0, %1, %2 offen nt lds" \
                                                     :: "v"(voff), "s"(rs), "s"(soff), "s"(ldsb), "i"(IMM) : "memory", "scc")
DI bf16x8 ldsf(lptr p) { return *(LAS bf16x8*)p; }
DI s16x4 ldstr(lptr p) { return __builtin_amdgcn_ds_read_tr16_b64_v4i16((LAS s16x4*)p); }
DI bf16x8 ldstr2(lptr p, int second) { s16x4 lo = ldstr(p), hi = ldstr(p + second); return __builtin_shufflevector(lo, hi, 0, 1, 2, 3, 4, 5, 6, 7); }
DI void lds_signal(unsigned c) {
  asm volatile("s_waitcnt lgkmcnt(0)\n\ts_mov_b64 exec, 1\n\tds_add_u32 %0, %1\n\ts_mov_b64 exec, -1" :: "v"(c), "v"(1u) : "memory");
}
template <int SLEEP> DI void lds_wait_(unsigned c, unsigned target) {
  unsigned v, s, n;
  asm volatile("s_mov_b32 %2, 0x400000\n1:\n\tds_read_b32 %0, %3\n\ts_waitcnt lgkmcnt(0)\n\tv_readfirstlane_b32 %1, %0\n\ts_cmp_ge_u32 %1, %4\n\ts_cbranch_scc1 2f\n\t"
               "s_sleep %5\n\ts_sub_u32 %2, %2, 1\n\ts_cbranch_scc0 1b\n2:" : "=&v"(v), "=&s"(s), "=&s"(n) : "v"(c), "i"(target), "i"(SLEEP) : "memory", "scc");
}
#define lds_wait(c, t) lds_wait_<0>(c, t)
#define lds_barrier(c, t) do { lds_signal(c); lds_wait(c, t); } while (0)
#define lds_barrierA(c, t) do { lds_signal(c); lds_wait_<1>(c, t); } while (0)
DI void poll(const char* ws, unsigned off, u64 tag) {
#pragma unroll 1
  for (unsigned s = 0; s < SPIN_CAP; ++s) {
    u64 v = __hip_atomic_load((GAS u64*)(ws + off), RLX_AGENT);
    if (wall(v == tag)) break;
    __builtin_amdgcn_s_sleep(2);
  }
  MEMBAR();
}
DI void publish4(char* ws, unsigned off, u64 tag, int lane, unsigned cnt) {
  unsigned old;
  asm volatile("s_waitcnt vmcnt(0)\n\ts_mov_b64 exec, 1\n\tds_add_rtn_u32 %0, %1, %2\n\ts_waitcnt lgkmcnt(0)\n\ts_mov_b64 exec, -1" : "=&v"(old) : "v"(cnt), "v"(1u) : "memory");
  if (__builtin_amdgcn_readfirstlane(old) == 3 && lane == 0) __hip_atomic_store((GAS u64*)(ws + off), tag, RLX_AGENT);
}
DI float swap_lo(float x) {
  auto r = __builtin_amdgcn_permlane32_swap(f2u(x), f2u(x), false, false); return u2f(r[0]); }
DI float swap_hi(float x) {
  auto r = __builtin_amdgcn_permlane32_swap(f2u(x), f2u(x), false, false); return u2f(r[1]); }
struct cf { float r, i; };
DI cf cmul(cf a, cf b) { return {a.r * b.r - a.i * b.i, a.r * b.i + a.i * b.r}; }
DI cf cfma(cf a, cf b, cf c) { return {__builtin_fmaf(a.r, b.r, __builtin_fmaf(-a.i, b.i, c.r)), __builtin_fmaf(a.r, b.i, __builtin_fmaf(a.i, b.r, c.i))}; }

#define STAMP(k) do { if ((k) == PK && isB == PB) evt_ = __builtin_amdgcn_s_memrealtime() - t_start; } while (0)
#define STAMP_OUT() do { if (isB == PB) while (__builtin_amdgcn_s_memrealtime() - t_start < 3000 + evt_) __builtin_amdgcn_s_sleep(1); } while (0)
struct KV { bf16x8 k0, k1, v0, v1; };

DI void rs_write(lptr base, int w, int lane, const f32x16& t1, const f32x16& t2, const f32x16& t3) {
#define RSW(T, I) _Pragma("unroll") for (int q = 0; q < 4; ++q) *(LAS f32x4*)(base + ((w ^ I) * 3 + I - 1) * 4096 + q * 1024 + lane * 16) = (f32x4){T[4 * q], T[4 * q + 1], T[4 * q + 2], T[4 * q + 3]}
  RSW(t1, 1); RSW(t2, 2); RSW(t3, 3);
}
DI f32x16 rs_read(lptr base, int w, int lane, f32x16 a) {
  f32x4 v[12];
#pragma unroll
  for (int s = 0; s < 12; ++s) v[s] = *(LAS f32x4*)(base + w * 12288 + s * 1024 + lane * 16);
  SBAR();
#pragma unroll
  for (int s = 0; s < 12; ++s) { const int q = s & 3; a[4 * q] += v[s][0]; a[4 * q + 1] += v[s][1]; a[4 * q + 2] += v[s][2]; a[4 * q + 3] += v[s][3]; }
  return a;
}

#define VMW_(n) asm volatile("s_waitcnt vmcnt(" #n ")" ::: "memory")
#define VMW(n) VMW_(n)
#define BAR() asm volatile("s_waitcnt lgkmcnt(0)\n\ts_barrier" ::: "memory")
#define BLD(R, V, S) __builtin_amdgcn_raw_buffer_load_b128(R, V, S, SC1)
#define BST(D, R, V, S) __builtin_amdgcn_raw_buffer_store_b128(D, R, (V) + (S), 0, SC1)
#define BF(x) __builtin_bit_cast(bf16x8, x)
#define U4(x) __builtin_bit_cast(u32x4, x)
#define EX(x) __builtin_amdgcn_exp2f(x)
#define KARGS() ({ const CAS Params* kp_ = (const CAS Params*)__builtin_amdgcn_kernarg_segment_ptr(); asm volatile("" : "+s"(kp_)); kp_; })
#define NEXTK() do { k += step; k = k >= 7 ? k - 7 : k; } while (0)

#if defined(__HIP_DEVICE_COMPILE__)
#define NO_PK __attribute__((target("no-packed-fp32-ops")))
#else
#define NO_PK
#endif
__global__ void __launch_bounds__(512, 2) NO_PK fused_kernel(Params p) {
  __shared__ __attribute__((aligned(128))) char smem[L_TOTAL];
  const lptr lds = (lptr)smem;
  const int tid = threadIdx.x, lane = tid & 63, wave = __builtin_amdgcn_readfirstlane(tid >> 6);
  const int b = blockIdx.x, loc = b >> 3, cl = loc >> 3, c = (b & 7) * 4 + cl, j = loc & 7;
  const int lrow = lane >> 3;
  const unsigned ve = lrow * 896 + (((lane & 7) ^ (lrow >> 1)) << 4);
  const int step = 1 + (cl + (b & 7) * 4) % 6, k0 = j == 7 ? 0 : j;
  const bool isB = wave < 4;
  const int wa = wave & 3;
  char* const ws = p.ws;
  const u64 t_start = __builtin_amdgcn_s_memrealtime(); u64 evt_ = 0;
  unsigned ua = 0, ub = 0, uc = 0, ud = 0;
  u32x4 rxa, rbr, rbi; int ka = k0;
  const unsigned vo = (ve ^ 64) + 7168, lba = wa * 2048;
#define NEXTKA() do { ka += step; ka = ka >= 7 ? ka - 7 : ka; } while (0)
#define LDCH(slot) do { NEXTKA(); const unsigned k_ = ka * 128; DMAX(rxa, ve, k_, lba, (slot)); DMAX(rxa, vo, k_, lba, (slot) + 1024); \
      DMA(rbr, ve, k_, lba, (slot) + 8192); DMA(rbr, vo, k_, lba, (slot) + 9216); DMA(rbi, ve, k_, lba, (slot) + 16384); DMA(rbi, vo, k_, lba, (slot) + 17408); } while (0)
  {
    const u32x4 rx = mkrs((const char*)p.xb + (size_t)c * 57344 + wave * 7168, 7168);
    const unsigned vx = ve ^ ((wave & 1) << 6), lb = wave * 1024;
    int k = k0;
    if (j == 7) {
      const u32x4 rw = mkrs(isB ? p.wk : p.wv, 25088);
      const unsigned vw = wa * 1024 + lane * 16, lw = K_W + (isB ? 0 : 3584) + wa * 1024; const bool half = wa < 3 || lane < 32;
#pragma unroll
      for (int i = 0; i < 7; ++i) { NEXTK(); DMAX(rx, vx, k * 128, lb, i * SK); if (half) DMA(rw, vw, k * 3584, lw, i * SK); }
    } else {
      const u32x4 rw = mkrs((const char*)p.wq + j * 128 + wave * 7168, 401408 - j * 128 - wave * 7168);
      const unsigned vw = lrow * 896 + (((lane & 7) ^ ((lrow & 2) << 1)) << 4);
      if (wave == 0) { const unsigned co = (64 * j + lane) * 2;
        asm volatile("global_load_ushort %0, %4, %5\n\tglobal_load_ushort %1, %4, %6\n\tglobal_load_ushort %2, %4, %7\n\tglobal_load_ushort %3, %4, %8"
                     : "=&v"(ua), "=&v"(ub), "=&v"(uc), "=&v"(ud) : "v"(co), "s"(p.lre), "s"(p.lim), "s"(p.ldt), "s"(p.dsk) : "memory"); }
#pragma unroll
      for (int i = 0; i < 7; ++i) { NEXTK(); DMAX(rx, vx, k * 128, lb, i * SK); DMA(rw, vw, k * 57344, lb, i * SK + K_W); }
      rxa = mkrs((const char*)p.xa + (size_t)c * 57344 + wa * 14336, 14336);
      rbr = mkrs((const char*)p.bre + (size_t)j * 57344 + wa * 14336, 14336);
      rbi = mkrs((const char*)p.bim + (size_t)j * 57344 + wa * 14336, 14336);
    }
  }
  STAMP(8);
  if (tid < 16) ((LAS unsigned*)(lds + L_MISC))[tid] = 0;
  const int r32 = lane & 31, hi = lane >> 5;
  const int g4 = lane >> 4, tq = (lane & 15) >> 2, tp = lane & 3;
  const unsigned xsw = (r32 >> 1) & 7, lo16 = lane * 16;
  const int th = wa >> 1, chh = wa & 1;
  u64 tag;
  const __amdgpu_buffer_rsrc_t rs_ws = __builtin_amdgcn_make_buffer_rsrc(ws, 0, WS_END, 0x00020000);
  const f32x16 zero16 = {};
  const unsigned o_own = WS_O + (((2 * c + th) * 16 + 2 * j + chh) * 2) * 1024;
#define MARK() do { const u32x4 ones = {~0u, ~0u, ~0u, ~0u};       \
    BST(ones, rs_ws, lo16, o_own); BST(ones, rs_ws, lo16 + 1024, o_own); } while (0)
#define LDEPOCH() asm volatile("s_load_dwordx2 %0, %1, 0x0\n\ts_waitcnt lgkmcnt(0)" : "=s"(tag) : "s"(ws) : "memory"); tag = (tag | 1) + 2

  if (isB) {
    if (wave == 0 && j < 7) {
      asm volatile("s_waitcnt vmcnt(14)" : "+v"(ua), "+v"(ub), "+v"(uc), "+v"(ud) :: "memory");
      const float pa = bf2f(ua), pb = bf2f(ub), dt = rbf(expf(bf2f(uc)));
      const float er = expf(dt * pa); float sn, cs; sincosf(dt * pb, &sn, &cs);
      const float nr = er * cs - 1.f, ni = er * sn, den = pa * pa + pb * pb;
      const cf lam = {er * cs, -ni};
      cf l16 = cmul(lam, lam); l16 = cmul(l16, l16); l16 = cmul(l16, l16); l16 = cmul(l16, l16);
      cf l64 = cmul(l16, l16); l64 = cmul(l64, l64);
      *(LAS f32x4*)(lds + L_EX + lo16) = (f32x4){lam.r, lam.i, 2.f * (nr * pa + ni * pb) / den, 2.f * (ni * pa - nr * pb) / den};
      *(LAS f32x4*)(lds + L_EX + 1024 + lo16) = (f32x4){l16.r, l16.i, l64.r, l64.i};
      *(LAS unsigned*)(lds + L_EX + 2048 + lane * 4) = ud;
    }
    LDEPOCH();
    if (tid == 0) *(LAS u64*)(lds + L_TAG) = tag;
    {
      const unsigned x_off = (32 * th + r32) * 128;
      const int wrow = 8 * (g4 >> 1) + tq;
      bf16x8 fax[4], faw[4], fbx[4], fbw[4];
      f32x16 q = zero16;
#define PRD(B, i, P) _Pragma("unroll") for (int ks = 0; ks < 4; ++ks) { B##x[ks] = ldsf(lds + (i) * SK + x_off + (((2 * ks + hi) ^ xsw) << 4)); B##w[ks] = ldstr2(lds + (i) * SK + w_off + ks * 16 * (P), 4 * (P)); } SBAR()
#define PMM(B, SW) _Pragma("unroll") for (int ks = 0; ks < 4; ++ks) q = (SW) ? MFMA(B##x[ks], B##w[ks], q) : MFMA(B##w[ks], B##x[ks], q); PIN(q); SBAR()
#define PROJ(P, SW) do { VMW(6); BAR(); PRD(fa, 0, P); PRD(fb, 1, P); PMM(fa, SW); PRD(fa, 2, P); PMM(fb, SW); PRD(fb, 3, P); PMM(fa, SW); \
        VMW(0); BAR(); PRD(fa, 4, P); PMM(fb, SW); PRD(fb, 5, P); PMM(fa, SW); PRD(fa, 6, P); PMM(fb, SW); PMM(fa, SW); } while (0)
      if (j < 7) {
        const unsigned w_off = K_W + wrow * 128 + ((64 * chh + 32 * (g4 & 1) + 8 * tp) ^ ((tq & 2) << 5));
        PROJ(128, false);
        STAMP(1);
        const float qs = 0.18898223650461361f * 1.4426950408889634f;
#pragma unroll
        for (int g = 0; g < 4; g += 2) {
          const auto s0 = __builtin_amdgcn_permlane32_swap(pk(q[4 * g] * qs, q[4 * g + 1] * qs), pk(q[4 * g + 4] * qs, q[4 * g + 5] * qs), false, false);
          const auto s1 = __builtin_amdgcn_permlane32_swap(pk(q[4 * g + 2] * qs, q[4 * g + 3] * qs), pk(q[4 * g + 6] * qs, q[4 * g + 7] * qs), false, false);
          BST(((u32x4){s0[0], s1[0], s0[1], s1[1]}), rs_ws, lo16 + g * 512, WS_Q + ((2 * c + th) * 56 + 8 * j + 4 * chh) * 512);
        }
      } else {
        const unsigned w_off = K_W + chh * 3584 + wrow * 56 + (16 * (g4 & 1) + 4 * tp) * 2;
        if (chh) { PROJ(56, true);
#pragma unroll
          for (int r = 0; r < 16; ++r) q[r] = r32 < 28 ? q[r] : (r32 == 28 ? 1.f : 0.f);
        } else { PROJ(56, false); if (hi) { q[12] = 0.f; q[13] = 0.f; q[14] = 0.f; q[15] = 0.f; } }
        STAMP(1);
        const unsigned ko = WS_KV + (2 * c + th) * 4096 + chh * 2048;
        BST(U4(pack8(q, 0)), rs_ws, lo16, ko); BST(U4(pack8(q, 1)), rs_ws, lo16 + 1024, ko);
      }
      publish4(ws, WS_FP + (c * 8 + j) * 128, tag, lane, cPQ);
    }
    KV A0, A1, A2, A3;
    A3.v1 = (bf16x8){};
    f32x16 O[4] = {zero16, zero16, zero16, zero16};
    f32x16 Sa, Sb = zero16; u32x4 Pa, Pb = {};
    PIN(O[0]); PIN(O[1]); PIN(O[2]); PIN(O[3]); PIN(Sb); PIN(Pb); PIN(A3.v1);
    STAMP(2);
    poll(ws, WS_FP + (lane < 8 ? (wave * 8 + lane) * 8 + 7 : c * 8 + ((56 * j + (lane & 1) * 55) >> 6)) * 128, tag);
    STAMP(3);
    const __amdgpu_buffer_rsrc_t rs_kv = __builtin_amdgcn_make_buffer_rsrc(ws + WS_KV + wave * 65536, 0, 65536, 0x00020000);
#define LDKV(B, T) do { const unsigned so_ = (((T) + loc) & 15) * 4096; \
                        B.k0 = BF(BLD(rs_kv, lo16, so_)); B.k1 = BF(BLD(rs_kv, lo16, so_ + 1024)); \
                        B.v0 = BF(BLD(rs_kv, lo16, so_ + 2048)); B.v1 = BF(BLD(rs_kv, lo16, so_ + 3072)); } while (0)
#define LDK(B, T) do { const unsigned so_ = (((T) + loc) & 15) * 4096; B.k0 = BF(BLD(rs_kv, lo16, so_)); B.k1 = BF(BLD(rs_kv, lo16, so_ + 1024)); } while (0)
#define LDV(B, T) do { const unsigned so_ = (((T) + loc) & 15) * 4096; B.v0 = BF(BLD(rs_kv, lo16, so_ + 2048)); B.v1 = BF(BLD(rs_kv, lo16, so_ + 3072)); } while (0)
    bf16x8 Q0[4], Q1[4];
    u32x2 qp[4][4];
    const unsigned q_e = r32 * 16 + hi * 8, q_o = r32 * 16 + (hi ? 512 : 8);
#define LDQ(i) _Pragma("unroll") for (int u = 0; u < 4; ++u) qp[i][u] = __builtin_amdgcn_raw_buffer_load_b64(rs_ws, (((wave ^ (3 - (i))) & 1) ? q_o : q_e) + u * 512, \
                 WS_Q + ((2 * c + ((wave ^ (3 - (i))) >> 1)) * 56 + 7 * j + 3 * ((wave ^ (3 - (i))) & 1)) * 512, SC1)
    LDQ(0); LDKV(A0, 0); LDQ(1); LDQ(2); LDQ(3); LDKV(A1, 1); LDKV(A2, 2); LDK(A3, 3);
    SBAR();
#pragma unroll
    for (int i = 0; i < 4; ++i) { Q0[i] = BF(((u32x4){qp[i][0][0], qp[i][0][1], qp[i][1][0], qp[i][1][1]}));
      Q1[i] = BF(((u32x4){qp[i][2][0], qp[i][2][1], hi ? 0u : qp[i][3][0], hi ? 0u : qp[i][3][1]})); }
    Sa = MFMA(A0.k0, Q0[0], zero16); Sa = MFMA(A0.k1, Q1[0], Sa);
    STAMP(9);
#define UNIT(S, Sn, KN, QN, VP, OP, VC, OC, X) do { \
      Pb[2] = pk(Sn[12], Sn[13]); Pb[3] = pk(Sn[14], Sn[15]); \
      if ((QN) >= 0) Sn = MFMA(KN.k0, Q0[(QN) & 3], zero16); S[0] = EX(S[0]); S[1] = EX(S[1]); S[2] = EX(S[2]); S[3] = EX(S[3]); SBAR(); \
      if ((QN) >= 0) Sn = MFMA(KN.k1, Q1[(QN) & 3], Sn); Pa[0] = pk(S[0], S[1]); Pa[1] = pk(S[2], S[3]); S[4] = EX(S[4]); S[5] = EX(S[5]); S[6] = EX(S[6]); S[7] = EX(S[7]); SBAR(); \
      X; SBAR(); \
      OP = MFMA(VP.v1, BF(Pb), OP); Pa[2] = pk(S[4], S[5]); Pa[3] = pk(S[6], S[7]); S[8] = EX(S[8]); S[9] = EX(S[9]); S[10] = EX(S[10]); S[11] = EX(S[11]); SBAR(); \
      OC = MFMA(VC.v0, BF(Pa), OC); Pb[0] = pk(S[8], S[9]); Pb[1] = pk(S[10], S[11]); S[12] = EX(S[12]); S[13] = EX(S[13]); S[14] = EX(S[14]); S[15] = EX(S[15]); SBAR(); \
    } while (0)
#define TILE(CUR, NXT, PRV, T, LD) do { \
      UNIT(Sa, Sb, CUR, 1, PRV, O[3], CUR, O[0], ); \
      LDV(PRV, (T) + 3); SBAR(); \
      UNIT(Sb, Sa, CUR, 2, CUR, O[0], CUR, O[1], ); \
      UNIT(Sa, Sb, CUR, 3, CUR, O[1], CUR, O[2], if (LD) LDK(CUR, (T) + 4)); \
      UNIT(Sb, Sa, NXT, 0, CUR, O[2], CUR, O[3], ); } while (0)
#pragma unroll 1
    for (int t = 0; t < 12; t += 4) {
      TILE(A0, A1, A3, t, 1); TILE(A1, A2, A0, t + 1, 1); TILE(A2, A3, A1, t + 2, 1); TILE(A3, A0, A2, t + 3, 1);
    }
    TILE(A0, A1, A3, 12, 0);
    const lptr oc = lds + L_OC + lo16;
#define OUTP(T, I) _Pragma("unroll") for (int q4 = 0; q4 < 4; ++q4) *(LAS f32x4*)(oc + ((wave ^ I) * 3 + I - 1) * 4096 + q4 * 1024) = (f32x4){T[4 * q4], T[4 * q4 + 1], T[4 * q4 + 2], T[4 * q4 + 3]}
    f32x4 pv[12];
    UNIT(Sa, Sb, A1, 1, A0, O[3], A1, O[0], ); UNIT(Sb, Sa, A1, 2, A1, O[0], A1, O[1], ); UNIT(Sa, Sb, A2, 0, A1, O[1], A1, O[2], );
    UNIT(Sb, Sa, A2, 1, A1, O[2], A2, O[0], ); UNIT(Sa, Sb, A2, 2, A2, O[0], A2, O[1], ); UNIT(Sb, Sa, A3, 0, A2, O[1], A2, O[2], );
    UNIT(Sa, Sb, A3, 1, A2, O[2], A3, O[0], ); UNIT(Sb, Sa, A3, 2, A3, O[0], A3, O[1], lds_wait(j < 7 ? cCR : cPB, 4));
    UNIT(Sa, Sb, A1, 3, A3, O[1], A3, O[2], OUTP(O[0], 3));
    UNIT(Sb, Sa, A2, 3, A3, O[2], A1, O[3], OUTP(O[1], 2));
    UNIT(Sa, Sb, A3, 3, A1, O[3], A2, O[3], OUTP(O[2], 1); lds_signal(cBB));
    UNIT(Sb, Sa, A3, -1, A2, O[3], A3, O[3], lds_wait(cBB, 4); _Pragma("unroll") for (int s2 = 0; s2 < 12; ++s2) pv[s2] = *(LAS f32x4*)(oc + wave * 12288 + s2 * 1024));
    Pb[2] = pk(Sb[12], Sb[13]); Pb[3] = pk(Sb[14], Sb[15]);
    O[3] = MFMA(A3.v1, BF(Pb), O[3]);
    STAMP(4);
    __builtin_amdgcn_s_setprio(2);
#pragma unroll
    for (int s2 = 0; s2 < 4; ++s2) pv[s2] = pv[s2] + pv[s2 + 4] + pv[s2 + 8];
    STAMP(10);
    unsigned lo16b = lo16; PIN(lo16b);
    const int lane = lo16b >> 4, r32 = lane & 31, hi = lane >> 5, g4 = lane >> 4, tq = (lane & 15) >> 2, tp = lane & 3;
    f32x16 o = O[3];
#pragma unroll
    for (int r = 0; r < 16; ++r) o[r] += pv[r >> 2][r & 3];
    {
      float l = swap_hi(o[12]);
      float am = 0.f;
#pragma unroll
      for (int r = 0; r < 16; r += 2) am = __builtin_fmaxf(__builtin_fmaxf(am, __builtin_fabsf(o[r])), __builtin_fabsf(o[r + 1]));
      const bool ok = (l >= 0x1p-90f) && (l <= 0x1p110f) && (am <= 0x1p126f);
      const bool slow = !wall(ok);
      if (slow) {
        const __amdgpu_buffer_rsrc_t rk = __builtin_amdgcn_make_buffer_rsrc(ws + WS_KV, 0, 262144, 0x00020000);
        poll(ws, WS_FP + (r32 * 8 + 7) * 128, tag);
        float m = -__builtin_inff();
#pragma unroll 1
        for (int t = 0; t < 64; ++t) {
          f32x16 s = MFMA(BF(BLD(rk, lo16, t * 4096)), Q0[3], zero16); s = MFMA(BF(BLD(rk, lo16 + 1024, t * 4096)), Q1[3], s);
#pragma unroll
          for (int r = 0; r < 16; ++r) m = __builtin_fmaxf(m, s[r]);
        }
        m = __builtin_fmaxf(swap_lo(m), swap_hi(m));
        o = zero16;
#pragma unroll 1
        for (int t = 0; t < 64; ++t) {
          f32x16 s = MFMA(BF(BLD(rk, lo16, t * 4096)), Q0[3], zero16); s = MFMA(BF(BLD(rk, lo16 + 1024, t * 4096)), Q1[3], s);
#pragma unroll
          for (int r = 0; r < 16; ++r) s[r] = EX(s[r] - m);
          o = MFMA(BF(BLD(rk, lo16 + 2048, t * 4096)), pack8(s, 0), o); o = MFMA(BF(BLD(rk, lo16 + 3072, t * 4096)), pack8(s, 1), o);
        }
        l = swap_hi(o[12]);
      }
      const float inv = __builtin_amdgcn_rcpf(l);
#pragma unroll
      for (int r = 0; r < 16; ++r) o[r] *= inv;
      if (hi) { o[12] = 0.f; o[13] = 0.f; o[14] = 0.f; o[15] = 0.f; }
      u32x4 f0 = U4(pack8(o, 0)), f1 = U4(pack8(o, 1));
      if (slow) {
#pragma unroll
        for (int k = 0; k < 4; ++k) { f0[k] = umin(f0[k], 0xfffffffeu); f1[k] = umin(f1[k], 0xfffffffeu); }
      }
      BST(f0, rs_ws, lo16, o_own); BST(f1, rs_ws, lo16 + 1024, o_own);
    }
    lds_signal(cOC);
    lds_signal(cGO + 4 * wave);
    STAMP(5);
    if (j == 7) { STAMP_OUT(); return; }
    {
      const CAS Params* kp = KARGS();
      const __amdgpu_buffer_rsrc_t rs_out = __builtin_amdgcn_make_buffer_rsrc(kp->out, 0, 2048 * 896 * 4, 0x00020000);
      lds_wait(cWO, 4);
      u32x4 of[16];
      const unsigned ob = WS_O + (((2 * c + th) * 16 + 8 * chh) * 2) * 1024;
#define LDO(k) of[k] = BLD(rs_ws, lo16 + ((k) & 3) * 1024, ob + ((k) >> 2) * 4096)
      const int rowb = 224 * chh + 4 * (g4 >> 1) + tq;
      const unsigned wb = (L_WO + rowb * 128 + ((32 * (g4 & 1) + 8 * tp) ^ ((tq & 2) << 5))) ^ (chh << 6);
      bf16x8 w0[16], w1[16];
#pragma unroll
      for (int k = 0; k < 16; ++k) w1[k] = ldstr2(lds + (wb ^ 64) + (28 * (k >> 1) + 16 * (k & 1)) * 128, 1024);
      SBAR();
      {
        unsigned have = 0;
#pragma unroll 1
        for (unsigned s = 0; s < SPIN_CAP; ++s) {
          const unsigned w = __builtin_amdgcn_raw_buffer_load_b32(rs_ws, (lane & 7) * 2048 + 2044, ob, SC1);
          const unsigned rdy = (s + 1 < SPIN_CAP ? (unsigned)__builtin_amdgcn_ballot_w64(w != ~0u) : ~0u) & 0xffu & ~have;
#pragma unroll
          for (int h = 0; h < 8; ++h) if (rdy >> h & 1) { LDO(2 * h); LDO(2 * h + 1); }
          have |= rdy;
          if (have == 0xffu) break;
          __builtin_amdgcn_s_sleep(2);
        }
      }
      f32x16 acc0 = zero16, acc1 = zero16;
#define OBAD(h) (!wall(umax(umax(umax(umax(of[2 * (h)][0], of[2 * (h)][1]), umax(of[2 * (h)][2], of[2 * (h)][3])), umax(of[2 * (h) + 1][0], of[2 * (h) + 1][1])), umax(of[2 * (h) + 1][2], of[2 * (h) + 1][3])) != ~0u))
#define OHEAD(h) do { \
        if (OBAD(h)) { \
          _Pragma("unroll 1") for (unsigned s = 0; s < SPIN_CAP; ++s) { LDO(2 * h); LDO(2 * h + 1); if (!OBAD(h)) break; __builtin_amdgcn_s_sleep(2); } \
        } \
        acc1 = MFMA(BF(of[2 * h]), w1[2 * h], acc1); acc1 = MFMA(BF(of[2 * h + 1]), w1[2 * h + 1], acc1); PIN(acc1); \
        w0[2 * h] = ldstr2(lds + wb + 28 * h * 128, 1024); w0[2 * h + 1] = ldstr2(lds + wb + (28 * h + 16) * 128, 1024); } while (0)
      OHEAD(0); STAMP(6); OHEAD(1); OHEAD(2); OHEAD(3); OHEAD(4); OHEAD(5); OHEAD(6); OHEAD(7);
      STAMP(11);
      lds_wait(cOC, 4);
#pragma unroll
      for (int k = 0; k < 4; ++k) acc0 = MFMA(BF(of[k]), w0[k], acc0);
      SBAR();
      const lptr tw = lds + L_OC + hi * 512 + r32 * 4;
#pragma unroll
      for (int r = 0; r < 16; ++r) *(LAS float*)(tw + (wave ^ 1) * 4096 + ((r & 3) + 8 * (r >> 2)) * 128) = acc1[r];
      SBAR();
#pragma unroll
      for (int k = 4; k < 8; ++k) acc0 = MFMA(BF(of[k]), w0[k], acc0);
      PIN(acc0);
      lds_signal(cPR + 4 * th);
#pragma unroll
      for (int k = 8; k < 16; ++k) acc0 = MFMA(BF(of[k]), w0[k], acc0);
      PIN(acc0);
      lds_wait(cPR + 4 * th, 2);
      f32x4 pa[4];
#pragma unroll
      for (int k = 0; k < 4; ++k) pa[k] = *(LAS f32x4*)(lds + L_OC + wave * 4096 + k * 1024 + lo16);
#pragma unroll
      for (int r = 0; r < 16; ++r) *(LAS float*)(tw + 16384 + wave * 4096 + ((r & 3) + 8 * (r >> 2)) * 128) = acc0[r];
      MEMBAR();
      STAMP(12);
      const unsigned oo = ((64 * c + 32 * th + (lane >> 3)) * 896 + 448 + 64 * j + 32 * chh + (lane & 7) * 4) * 4;
#pragma unroll
      for (int k = 0; k < 4; ++k)
        __builtin_amdgcn_raw_buffer_store_b128(U4(pa[k] + *(LAS f32x4*)(lds + L_OC + 16384 + wave * 4096 + k * 1024 + lo16)), rs_out, oo + k * 28672, 0, 17);
    }
    STAMP(7);
    STAMP_OUT();
  } else {
    if (j == 7) {
      VMW(6); BAR(); VMW(0); BAR();
      lds_wait_<4>(cPQ, 4);
      { const u32x2 t2 = *(LAS u32x2*)(lds + L_TAG);
        tag = ((u64)(unsigned)__builtin_amdgcn_readfirstlane(t2[1]) << 32) | (unsigned)__builtin_amdgcn_readfirstlane(t2[0]); }
      __builtin_amdgcn_s_sleep(64);
      MARK();
      publish4(ws, WS_FH + (c * 8 + 7) * 128, tag, lane, cPB);
      if (c == 0 && wa == 0) {
        __builtin_amdgcn_s_sleep(127);
#pragma unroll 1
        for (int k = 0; k < 4; ++k) poll(ws, WS_FP + (k * 64 + lane) * 128, tag);
        if (lane == 0) __hip_atomic_store((GAS u64*)(ws + WS_EPOCH), tag, RLX_AGENT);
      }
      STAMP_OUT();
      return;
    }
    VMW(6); BAR(); LDCH(L_SA); VMW(6); BAR();
    cf lam, coef, l16, l64; f32x16 ur = zero16, ui = zero16;
    f32x16 acc[4] = {zero16, zero16, zero16, zero16};
    const unsigned cs0 = wa * 16384u, cs1 = L_R1 + wa * 12288u;
    unsigned a_htw = L_HT + wa * 4096 + r32 * 64 + hi * 32, a_htr = L_HT + wa * 4096 + (8 * (g4 >> 1) + tq) * 64 + (g4 & 1) * 32 + tp * 8;
    unsigned a_hst = WS_H + ((2 * c + th) * 56 + 4 * j + 2 * chh) * 1024 + lo16;
    unsigned a_c[4], a_cn[2], a_x[2], a_ex, e_want; u32x4 dg[2]; cf l256, lw, lo_;
    const int seg = 2 * th + hi;
#pragma unroll
    for (int f = 0; f < 4; ++f) a_c[f] = cs0 + (32 * chh + r32) * 128 + (((2 * f + hi) ^ xsw) << 4);
#pragma unroll
    for (int f = 0; f < 2; ++f) a_cn[f] = cs1 + 8192 + (32 * chh + r32) * 64 + (((2 * f + hi) ^ ((r32 >> 2) & 3)) << 4);
    unsigned a_vh = (lane >> 2) * 896 + (((lane & 3) ^ ((lane >> 4) & 3)) << 4) + 384 + chh * 64, a_vo = ve ^ 64;
    unsigned a_vw = lrow * 896 + (((lane & 7) ^ ((lrow & 2) << 1)) << 4);
    unsigned a_fl = WS_FH + (c * 8 + (lane & 7)) * 128;
    unsigned a_el = (31 - umin(r32, c - 1)) * 512 + 4, a_eo = (32 - c + 2 * th + hi) * 512 + r32 * 16, a_hx = L_EX + 2048 + (32 * chh + r32) * 8;
    {
      LDCH(0); LDCH(CH);
      { const f32x4 c0 = *(LAS f32x4*)(lds + L_EX + (32 * chh + r32) * 16), c1 = *(LAS f32x4*)(lds + L_EX + 1024 + (32 * chh + r32) * 16);
        lam = {c0[0], c0[1]}; coef = {c0[2], c0[3]}; l16 = {c1[0], c1[1]}; l64 = {c1[2], c1[3]}; ud = *(LAS unsigned*)(lds + L_EX + 2048 + (32 * chh + r32) * 4); }
      { const int e = r32 - 8 * hi; const unsigned dv = ud << ((e & 1) * 16);
#pragma unroll
        for (int s2 = 0; s2 < 2; ++s2) {
          dg[s2] = (u32x4){(e >> 1) == 8 * s2 ? dv : 0u, (e >> 1) == 8 * s2 + 1 ? dv : 0u, (e >> 1) == 8 * s2 + 2 ? dv : 0u, (e >> 1) == 8 * s2 + 3 ? dv : 0u};
          a_x[s2] = L_SA + (32 * th + r32) * 128 + (((4 * chh + 2 * s2 + hi) ^ xsw) << 4); PIN(dg[s2]); PIN(a_x[s2]); }
        cf lseg = hi ? l16 : (cf){1.f, 0.f};
        if (th) lseg = cmul(lseg, cmul(l16, l16));
        const cf l128 = cmul(l64, l64), ls128 = cmul(lseg, l128);
        l256 = cmul(l128, l128); lw = th ? ls128 : lseg; lo_ = th ? lseg : ls128; e_want = (c - seg + 3) >> 2; a_ex = L_EX + (32 * chh + r32) * 8;
        PIN(l256.r); PIN(l256.i); PIN(lw.r); PIN(lw.i); PIN(lo_.r); PIN(lo_.i); PIN(e_want); PIN(a_ex); }
      PIN(a_htw); PIN(a_htr); PIN(a_hst); PIN(a_c[0]); PIN(a_c[1]); PIN(a_c[2]); PIN(a_c[3]); PIN(a_cn[0]); PIN(a_cn[1]); PIN(a_vh); PIN(a_vw); PIN(a_fl); PIN(a_el); PIN(a_eo); PIN(a_hx); PIN(a_vo);
      const int trow = 32 * th + 16 * ((r32 >> 2) & 1) + (r32 & 3) + 4 * (r32 >> 3);
      const unsigned xa_off = trow * 128, xa_sw = (trow >> 1) & 7, b_off = 8192 + (32 * chh + r32) * 128;
      bf16x8 gax[4], gar[4], gai[4], gbx[4], gbr[4], gbi[4];
      unsigned gx[3][4], gw[3][4];
#define GB(s_) ((s_) < 49152 ? 0u : (s_) < 110592 ? 49152u : 110592u)
#define GT(s_) ((s_) < 49152 ? 0 : (s_) < 110592 ? 1 : 2)
#pragma unroll
      for (int t = 0; t < 3; ++t)
#pragma unroll
        for (int ks = 0; ks < 4; ++ks) { gx[t][ks] = GB(t * 60000) + xa_off + (((2 * ks + hi) ^ xa_sw) << 4); gw[t][ks] = GB(t * 60000) + b_off + (((2 * ks + hi) ^ xsw) << 4); PIN(gx[t][ks]); PIN(gw[t][ks]); }
#define GRD(B, s_) _Pragma("unroll") for (int ks = 0; ks < 4; ++ks) { B##x[ks] = ldsf(lds + gx[GT(s_)][ks] + ((s_) - GB(s_))); \
        B##r[ks] = ldsf(lds + gw[GT(s_)][ks] + ((s_) - GB(s_))); B##i[ks] = ldsf(lds + gw[GT(s_)][ks] + ((s_) - GB(s_) + 8192)); } SBAR()
#define GMM(B) _Pragma("unroll") for (int ks = 0; ks < 4; ++ks) { ur = MFMA(B##x[ks], B##r[ks], ur); ui = MFMA(B##x[ks], B##i[ks], ui); } PIN(ur); PIN(ui); SBAR()
#define GAP() do { SBAR(); asm volatile("s_nop %0" :: "i"(7)); SBAR(); } while (0)
#define GMS(B) _Pragma("unroll") for (int ks = 0; ks < 4; ++ks) { ur = MFMA(B##x[ks], B##r[ks], ur); GAP(); ui = MFMA(B##x[ks], B##i[ks], ui); GAP(); } PIN(ur); PIN(ui); SBAR()
      __builtin_amdgcn_s_setprio(1);
      STAMP(12);
      lds_wait_<1>(cPQ, 4);
      LDCH(2 * CH); LDCH(3 * CH); LDCH(4 * CH);
      VMW(18); lds_barrierA(cAB, 4); GRD(ga, L_SA); GRD(gb, 0); GMM(ga); GRD(ga, CH); GMM(gb);
      VMW(6); lds_barrierA(cAB, 8);
      STAMP(13);
      GRD(gb, 2 * CH); LDCH(L_SA); GMM(ga); GRD(ga, 3 * CH); GMS(gb);
      VMW(6); lds_barrierA(cAB, 12);
      STAMP(14);
      GRD(gb, 4 * CH); GMS(ga);
      VMW(0); lds_barrierA(cAB, 16);
      STAMP(15);
      GRD(ga, L_SA); GMS(gb); GMS(ga);
      { const u32x2 t2 = *(LAS u32x2*)(lds + L_TAG);
        tag = ((u64)(unsigned)__builtin_amdgcn_readfirstlane(t2[1]) << 32) | (unsigned)__builtin_amdgcn_readfirstlane(t2[0]); }
    }
    acc[0] = MFMA(ldsf(lds + a_x[0]), BF(dg[0]), acc[0]); acc[0] = MFMA(ldsf(lds + a_x[1]), BF(dg[1]), acc[0]);
    PIN(acc[0]);
    STAMP(1);
    cf e = {0.f, 0.f};
#pragma unroll
    for (int r = 0; r < 16; ++r) {
      const float a = coef.r * ur[r] - coef.i * ui[r], b2 = -(coef.r * ui[r] + coef.i * ur[r]);
      ur[r] = a; ui[r] = b2; e = r ? cfma(lam, e, (cf){a, b2}) : (cf){a, b2};
    }
    *(LAS f32x2*)(lds + a_ex + th * 1024 + hi * 512) = (f32x2){e.r, e.i};
    __builtin_amdgcn_s_setprio(0);
    lds_barrierA(cAB, 20);
    cf g0;
    {
      const f32x2 e0 = *(LAS f32x2*)(lds + a_ex), e1 = *(LAS f32x2*)(lds + a_ex + 512), e2 = *(LAS f32x2*)(lds + a_ex + 1024), e3 = *(LAS f32x2*)(lds + a_ex + 1536);
      __builtin_amdgcn_s_setprio(1);
      const cf E2 = cfma(l16, (cf){e0[0], e0[1]}, (cf){e1[0], e1[1]}), E3 = cfma(l16, E2, (cf){e2[0], e2[1]}), E = cfma(l16, E3, (cf){e3[0], e3[1]});
      if (th) g0 = hi ? E3 : E2; else g0 = hi ? (cf){e0[0], e0[1]} : (cf){0.f, 0.f};
      PIN(g0.r); PIN(g0.i);
      if (th == 0 && hi == 0 && c < 31)
        BST(((u32x4){f2u(E.r), (unsigned)tag, f2u(E.i), (unsigned)tag}), rs_ws, r32 * 16, WS_E + ((2 * j + chh) * 32 + 31 - c) * 512);
    }
    __builtin_amdgcn_s_setprio(0);
    const CAS Params* kp = KARGS();
    {
      const u32x4 rc = mkrs((const char*)(th ? kp->cim : kp->cre) + (size_t)j * 57344, 57344);
      int n2 = (6 * cl + 2 * ((b & 7) % 3)) % 24;
#pragma unroll
      for (int n = 0; n < 24; ++n) {
        DMA(rc, (n & 1) ? a_vo : ve, (n2 & 7) * 7168 + (n2 >> 3) * 128 + chh * 512, (n2 < 16 ? cs0 : cs1 - 16384) + n2 * 1024, 0);
        n2 = n2 == 23 ? 0 : n2 + 1; }
#pragma unroll
      for (int n = 0; n < 4; ++n) DMA(rc, a_vh, n * 14336, cs1, 8192 + n * 1024);
    }
    STAMP(2);
    cf G = g0;
    if (c > 0) {
      const __amdgpu_buffer_rsrc_t re = __builtin_amdgcn_make_buffer_rsrc(ws + WS_E + (2 * j + chh) * 16384, 0, 16384, 0x00020000);
      u32x4 ev[8];
#pragma unroll 1
      for (unsigned s = 0; s < SPIN_CAP; ++s) {
        const unsigned t1 = __builtin_amdgcn_raw_buffer_load_b32(re, a_el, 0, SC1);
        if (wall(t1 == (unsigned)tag)) break;
        __builtin_amdgcn_s_sleep(2);
      }
      MEMBAR();
#pragma unroll 1
      for (unsigned s = 0; s < SPIN_CAP; ++s) {
        unsigned got = 0;
#pragma unroll
        for (int m = 0; m < 8; ++m) ev[m] = BLD(re, a_eo, m * 2048);
        VMW(0);
        __builtin_amdgcn_s_setprio(1);
#pragma unroll
        for (int m = 0; m < 8; ++m) got += ((ev[m][1] ^ (unsigned)tag) | (ev[m][3] ^ (unsigned)tag)) == 0;
        if (wall(got == e_want)) break;
        __builtin_amdgcn_s_setprio(0);
        __builtin_amdgcn_s_sleep(4);
      }
      STAMP(3);
      cf H = {u2f(ev[7][0]), u2f(ev[7][2])};
#pragma unroll
      for (int m = 6; m >= 0; --m) H = cfma(l256, H, (cf){u2f(ev[m][0]), u2f(ev[m][2])});
      H = cfma(l64, (cf){swap_hi(H.r), swap_hi(H.i)}, (cf){swap_lo(H.r), swap_lo(H.i)});
      *(LAS f32x2*)(lds + a_hx + th * 512) = (f32x2){H.r, H.i};
      G = cfma(lw, H, g0);
      __builtin_amdgcn_s_setprio(0);
      lds_barrierA(cPE + 4 * chh, 2);
      const f32x2 ho = *(LAS f32x2*)(lds + a_hx + (th ^ 1) * 512);
      __builtin_amdgcn_s_setprio(1);
      G = cfma(lo_, (cf){ho[0], ho[1]}, G);
    }
    STAMP(4);
    __builtin_amdgcn_s_setprio(1);
    {
#pragma unroll
      for (int r = 0; r < 16; ++r) { G = cfma(lam, G, (cf){ur[r], ui[r]}); ur[r] = G.r; ui[r] = G.i; }
      *(LAS bf16x8*)(lds + a_htw) = pack8(ur, 0); *(LAS bf16x8*)(lds + a_htw + 16) = pack8(ur, 1);
      *(LAS bf16x8*)(lds + a_htw + 2048) = pack8(ui, 0); *(LAS bf16x8*)(lds + a_htw + 2064) = pack8(ui, 1);
      unsigned a_hs2 = a_hst + 28 * 1024; PIN(a_hs2);
      __builtin_amdgcn_s_setprio(0);
      asm volatile("s_waitcnt lgkmcnt(0)" ::: "memory");
#pragma unroll
      for (int k = 0; k < 4; ++k) BST(U4(ldstr2(lds + a_htr + (k >> 1) * 2048 + (k & 1) * 1024, 256)), rs_ws, (k >> 1) ? a_hs2 : a_hst, (k & 1) * 1024);
      MARK();
      publish4(ws, WS_FH + (c * 8 + j) * 128, tag, lane, cPB);
    }
    STAMP(5);
    bf16x8 cfr[14][2];
    const int cflip = chh ? -1 : 1;
#define LDC(f) do { if ((f) < 12) { const unsigned a_ = a_c[(f) & 3] + ((f) < 8 ? ((f) >> 2) * 8192 : cs1 - cs0); cfr[f][0] = ldsf(lds + a_); cfr[f][1] = ldsf(lds + a_ + cflip * 4096); } \
                    else { cfr[f][0] = ldsf(lds + a_cn[(f) & 1]); cfr[f][1] = ldsf(lds + a_cn[(f) & 1] + cflip * 2048); } } while (0)
#pragma unroll
    for (int f = 0; f < 8; ++f) LDC(f);
    lds_signal(cCR);
    STAMP(10);
    lds_wait_<1>(cCR, 4);
    {
      const u32x4 rw = mkrs((const char*)kp->wo + j * 128 + wa * 7168, 401408 - j * 128 - wa * 7168);
      int m2 = (4 * cl + (b & 7)) % 14;
#pragma unroll
      for (int m = 0; m < 14; ++m) { DMA(rw, a_vw, m2 * 28672, wa * 1024 + m2 * 4096, L_WO); m2 = m2 == 13 ? 0 : m2 + 1; }
      if (wa == 0 && lane < 32) *(LAS u32x4*)(lds + L_WO + 57344 + lo16) = (u32x4){0, 0, 0, 0};
    }
    poll(ws, a_fl, tag);
    VMW(0);
    lds_signal(cWO);
    STAMP(6);
    {
      const unsigned hb = WS_H + (2 * c * 56 + 14 * wa) * 1024, h0 = hb + th * 57344, h1 = hb + (th ^ 1) * 57344;
      const unsigned fa = chh * 2048, fb = chh ? 0u - 12288u : 0u;
      u32x4 hf[14][2];
#define LDH(f) do { hf[f][0] = BLD(rs_ws, lo16, h0 + (f) * 1024 + ((f) < 12 ? fa : fb)); hf[f][1] = BLD(rs_ws, lo16, h1 + (f) * 1024 + ((f) < 12 ? fa : fb)); } while (0)
#define MMH(f) do { acc[0] = MFMA(BF(hf[f][0]), cfr[f][0], acc[0]); acc[1] = MFMA(BF(hf[f][0]), cfr[f][1], acc[1]); \
                    acc[2] = MFMA(BF(hf[f][1]), cfr[f][0], acc[2]); acc[3] = MFMA(BF(hf[f][1]), cfr[f][1], acc[3]); \
                    PIN(acc[0]); PIN(acc[1]); PIN(acc[2]); PIN(acc[3]); } while (0)
#pragma unroll
      for (int f = 0; f < 14; ++f) LDH(f);
      SBAR();
      lds_wait_<4>(cGO + 4 * wa, 1);
      STAMP(9);
      __builtin_amdgcn_s_setprio(1);
#pragma unroll
      for (int f = 0; f < 8; ++f) { MMH(f); SBAR(); if (f >= 1 && f < 7) LDC(f + 7); SBAR(); }
      lds_signal(cP4);
#pragma unroll
      for (int f = 8; f < 14; ++f) MMH(f);
    }
    lds_wait(cP4, 4);
    STAMP(11);
    rs_write(lds + L_G2, wa, lane, acc[1], acc[2], acc[3]);
    lds_barrierA(cAB, 24);
    f32x16 y = rs_read(lds + L_G2, wa, lane, acc[0]);
    const __amdgpu_buffer_rsrc_t rs_out = __builtin_amdgcn_make_buffer_rsrc(kp->out, 0, 2048 * 896 * 4, 0x00020000);
    MEMBAR();
#pragma unroll
    for (int r = 0; r < 16; ++r) *(LAS float*)(lds + L_G2 + wa * 12288 + hi * 512 + r32 * 4 + ((r & 3) + 8 * (r >> 2)) * 128) = y[r];
    MEMBAR();
    const unsigned oo = ((64 * c + 32 * th + (lane >> 3)) * 896 + 64 * j + 32 * chh + (lane & 7) * 4) * 4;
#pragma unroll
    for (int k = 0; k < 4; ++k) __builtin_amdgcn_raw_buffer_store_b128(*(LAS u32x4*)(lds + L_G2 + wa * 12288 + k * 1024 + lo16), rs_out, oo + k * 28672, 0, 17);
    STAMP(7);
    STAMP_OUT();
  }
}

extern "C" void kernel_launch(void* const* d_in, const int* in_sizes, int n_in, void* d_out, int out_size, void* d_ws, size_t ws_size,
                              hipStream_t stream) {
  Params p;
  p.xa = (const u16*)d_in[0]; p.lre = (const u16*)d_in[1]; p.lim = (const u16*)d_in[2]; p.bre = (const u16*)d_in[3]; p.bim = (const u16*)d_in[4];
  p.cre = (const u16*)d_in[5]; p.cim = (const u16*)d_in[6]; p.ldt = (const u16*)d_in[7]; p.dsk = (const u16*)d_in[8];
  p.xb = (const u16*)d_in[9]; p.wq = (const u16*)d_in[10]; p.wk = (const u16*)d_in[11]; p.wv = (const u16*)d_in[12]; p.wo = (const u16*)d_in[13];
  p.out = (float*)d_out; p.ws = (char*)d_ws;
  fused_kernel<<<256, 512, 0, stream>>>(p);
}
```

```cpp
#include <hip/hip_runtime.h>
#include <stdint.h>

#define DI __device__ __forceinline__
#define GAS __attribute__((address_space(1)))
#define LAS __attribute__((address_space(3)))
#define CAS __attribute__((address_space(4)))
typedef short bf16x8 __attribute__((ext_vector_type(8)));
typedef short s16x4 __attribute__((ext_vector_type(4)));
typedef float f32x16 __attribute__((ext_vector_type(16)));
typedef float f32x4 __attribute__((ext_vector_type(4)));
typedef float f32x2 __attribute__((ext_vector_type(2)));
typedef unsigned u32x4 __attribute__((ext_vector_type(4)));
typedef unsigned u32x2 __attribute__((ext_vector_type(2)));
typedef __bf16 bf16x2_t __attribute__((ext_vector_type(2)));
typedef unsigned long long u64;
typedef unsigned short u16;
typedef LAS char* lptr;
#define RLX_AGENT __ATOMIC_RELAXED, __HIP_MEMORY_SCOPE_AGENT
#define MFMA(a, b, c) __builtin_amdgcn_mfma_f32_32x32x16_bf16((a), (b), (c), 0, 0, 0)
#define SBAR() __builtin_amdgcn_sched_barrier(0)
#define PIN(x) asm volatile("" : "+v"(x))
#define MEMBAR() asm volatile("" ::: "memory")
#define SC1 16

constexpr unsigned WS_EPOCH = 0, WS_FH = 8192, WS_E = 0x10000, WS_FP = 0x48000,
                   WS_KV = 0x50000, WS_O = 0x100000, WS_H = 0x300000, WS_Q = 0x680000, WS_END = 0x880000;
constexpr unsigned SK = 16384, K_W = 8192;
constexpr unsigned CH = 24576, L_SA = 135168;
constexpr unsigned L_R1 = 107520, L_HT = 65536, L_WO = 0, L_OC = 57856, L_G2 = 107520, L_MISC = 159744, L_EX = L_MISC + 128, L_TOTAL = 163840;
constexpr unsigned cBB = L_MISC, cAB = L_MISC + 4, cCR = L_MISC + 8, cWO = L_MISC + 12, cOC = L_MISC + 16, cPB = L_MISC + 20, cPR = L_MISC + 24, cP4 = L_MISC + 32, cPQ = L_MISC + 52, cGO = L_MISC + 36, cPE = L_MISC + 56, L_TAG = L_MISC + 64;
constexpr unsigned SPIN_CAP = 1u << 18;

struct Params {
  const u16 *xb, *wq, *wk, *wv; char* ws; const u16 *lre, *lim, *ldt, *dsk, *xa, *bre, *bim, *cre, *cim, *wo;
  float* out;
};

DI float u2f(unsigned v) { return __builtin_bit_cast(float, v); }
DI unsigned f2u(float v) { return __builtin_bit_cast(unsigned, v); }
DI unsigned umin(unsigned a, unsigned b) { return a < b ? a : b; }
DI unsigned umax(unsigned a, unsigned b) { return a > b ? a : b; }
DI bool wall(bool x) { return __builtin_amdgcn_ballot_w64(!x) == 0; }
DI float bf2f(unsigned v) { return u2f(v << 16); }
DI unsigned pk(float a, float b) { f32x2 v = {a, b}; return __builtin_bit_cast(unsigned, __builtin_convertvector(v, bf16x2_t)); }
DI float rbf(float a) { return u2f(pk(0.f, a)); }
DI bf16x8 pack8(const f32x16& x, int s) {
  u32x4 p = {pk(x[8 * s], x[8 * s + 1]), pk(x[8 * s + 2], x[8 * s + 3]), pk(x[8 * s + 4], x[8 * s + 5]), pk(x[8 * s + 6], x[8 * s + 7])};
  return __builtin_bit_cast(bf16x8, p);
}
DI u32x4 mkrs(const void* base, unsigned bytes) { const u64 a = (u64)base; return (u32x4){(unsigned)a, (unsigned)(a >> 32) & 0xffffu, bytes, 0x20000u}; }
#define DMA(rs, voff, soff, ldsb, IMM) asm volatile("s_add_u32 m0, %3, %4\n\ts_nop 0\n\tbuffer_load_dwordx4 %0, %1, %2 offen lds" \
                                                    :: "v"(voff), "s"(rs), "s"(soff), "s"(ldsb), "i"(IMM) : "memory", "scc")
#define DMAX(rs, voff, soff, ldsb, IMM) asm volatile("s_add_u32 m0, %3, %4\n\ts_nop 0\n\tbuffer_load_dwordx4 %0, %1, %2 offen nt lds" \
                                                     :: "v"(voff), "s"(rs), "s"(soff), "s"(ldsb), "i"(IMM) : "memory", "scc")
DI bf16x8 ldsf(lptr p) { return *(LAS bf16x8*)p; }
DI s16x4 ldstr(lptr p) { return __builtin_amdgcn_ds_read_tr16_b64_v4i16((LAS s16x4*)p); }
DI bf16x8 ldstr2(lptr p, int second) { s16x4 lo = ldstr(p), hi = ldstr(p + second); return __builtin_shufflevector(lo, hi, 0, 1, 2, 3, 4, 5, 6, 7); }
DI void lds_signal(unsigned c) {
  asm volatile("s_waitcnt lgkmcnt(0)\n\ts_mov_b64 exec, 1\n\tds_add_u32 %0, %1\n\ts_mov_b64 exec, -1" :: "v"(c), "v"(1u) : "memory");
}
template <int SLEEP> DI void lds_wait_(unsigned c, unsigned target) {
  unsigned v, s, n;
  asm volatile("s_mov_b32 %2, 0x400000\n1:\n\tds_read_b32 %0, %3\n\ts_waitcnt lgkmcnt(0)\n\tv_readfirstlane_b32 %1, %0\n\ts_cmp_ge_u32 %1, %4\n\ts_cbranch_scc1 2f\n\t"
               "s_sleep %5\n\ts_sub_u32 %2, %2, 1\n\ts_cbranch_scc0 1b\n2:" : "=&v"(v), "=&s"(s), "=&s"(n) : "v"(c), "i"(target), "i"(SLEEP) : "memory", "scc");
}
#define lds_wait(c, t) lds_wait_<0>(c, t)
#define lds_barrier(c, t) do { lds_signal(c); lds_wait(c, t); } while (0)
#define lds_barrierA(c, t) do { lds_signal(c); lds_wait_<1>(c, t); } while (0)
DI void poll(const char* ws, unsigned off, u64 tag) {
#pragma unroll 1
  for (unsigned s = 0; s < SPIN_CAP; ++s) {
    u64 v = __hip_atomic_load((GAS u64*)(ws + off), RLX_AGENT);
    if (wall(v == tag)) break;
    __builtin_amdgcn_s_sleep(2);
  }
  MEMBAR();
}
DI void publish4(char* ws, unsigned off, u64 tag, int lane, unsigned cnt) {
  unsigned old;
  asm volatile("s_waitcnt vmcnt(0)\n\ts_mov_b64 exec, 1\n\tds_add_rtn_u32 %0, %1, %2\n\ts_waitcnt lgkmcnt(0)\n\ts_mov_b64 exec, -1" : "=&v"(old) : "v"(cnt), "v"(1u) : "memory");
  if (__builtin_amdgcn_readfirstlane(old) == 3 && lane == 0) __hip_atomic_store((GAS u64*)(ws + off), tag, RLX_AGENT);
}
DI float swap_lo(float x) {
  auto r = __builtin_amdgcn_permlane32_swap(f2u(x), f2u(x), false, false); return u2f(r[0]); }
DI float swap_hi(float x) {
  auto r = __builtin_amdgcn_permlane32_swap(f2u(x), f2u(x), false, false); return u2f(r[1]); }
struct cf { float r, i; };
DI cf cmul(cf a, cf b) { return {a.r * b.r - a.i * b.i, a.r * b.i + a.i * b.r}; }
DI cf cfma(cf a, cf b, cf c) { return {__builtin_fmaf(a.r, b.r, __builtin_fmaf(-a.i, b.i, c.r)), __builtin_fmaf(a.r, b.i, __builtin_fmaf(a.i, b.r, c.i))}; }

struct KV { bf16x8 k0, k1, v0, v1; };

DI void rs_write(lptr base, int w, int lane, const f32x16& t1, const f32x16& t2, const f32x16& t3) {
#define RSW(T, I) _Pragma("unroll") for (int q = 0; q < 4; ++q) *(LAS f32x4*)(base + ((w ^ I) * 3 + I - 1) * 4096 + q * 1024 + lane * 16) = (f32x4){T[4 * q], T[4 * q + 1], T[4 * q + 2], T[4 * q + 3]}
  RSW(t1, 1); RSW(t2, 2); RSW(t3, 3);
}
DI f32x16 rs_read(lptr base, int w, int lane, f32x16 a) {
  f32x4 v[12];
#pragma unroll
  for (int s = 0; s < 12; ++s) v[s] = *(LAS f32x4*)(base + w * 12288 + s * 1024 + lane * 16);
  SBAR();
#pragma unroll
  for (int s = 0; s < 12; ++s) { const int q = s & 3; a[4 * q] += v[s][0]; a[4 * q + 1] += v[s][1]; a[4 * q + 2] += v[s][2]; a[4 * q + 3] += v[s][3]; }
  return a;
}

#define VMW_(n) asm volatile("s_waitcnt vmcnt(" #n ")" ::: "memory")
#define VMW(n) VMW_(n)
#define BAR() asm volatile("s_waitcnt lgkmcnt(0)\n\ts_barrier" ::: "memory")
#define BLD(R, V, S) __builtin_amdgcn_raw_buffer_load_b128(R, V, S, SC1)
#define BST(D, R, V, S) __builtin_amdgcn_raw_buffer_store_b128(D, R, (V) + (S), 0, SC1)
#define BF(x) __builtin_bit_cast(bf16x8, x)
#define U4(x) __builtin_bit_cast(u32x4, x)
#define EX(x) __builtin_amdgcn_exp2f(x)
#define KARGS() ({ const CAS Params* kp_ = (const CAS Params*)__builtin_amdgcn_kernarg_segment_ptr(); asm volatile("" : "+s"(kp_)); kp_; })
#define NEXTK() do { k += step; k = k >= 7 ? k - 7 : k; } while (0)

#if defined(__HIP_DEVICE_COMPILE__)
#define NO_PK __attribute__((target("no-packed-fp32-ops")))
#else
#define NO_PK
#endif
__global__ void __launch_bounds__(512, 2) NO_PK fused_kernel(Params p) {
  __shared__ __attribute__((aligned(128))) char smem[L_TOTAL];
  const lptr lds = (lptr)smem;
  const int tid = threadIdx.x, lane = tid & 63, wave = __builtin_amdgcn_readfirstlane(tid >> 6);
  const int b = blockIdx.x, loc = b >> 3, cl = loc >> 3, c = (b & 7) * 4 + cl, j = loc & 7;
  const int lrow = lane >> 3;
  const unsigned ve = lrow * 896 + (((lane & 7) ^ (lrow >> 1)) << 4);
  const int step = 1 + (cl + (b & 7) * 4) % 6, k0 = j == 7 ? 0 : j;
  const bool isB = wave < 4;
  const int wa = wave & 3;
  char* const ws = p.ws;
  unsigned ua = 0, ub = 0, uc = 0, ud = 0;
  u32x4 rxa, rbr, rbi; int ka = k0;
  const unsigned vo = (ve ^ 64) + 7168, lba = wa * 2048;
#define NEXTKA() do { ka += step; ka = ka >= 7 ? ka - 7 : ka; } while (0)
#define LDCH(slot) do { NEXTKA(); const unsigned k_ = ka * 128; DMAX(rxa, ve, k_, lba, (slot)); DMAX(rxa, vo, k_, lba, (slot) + 1024); \
      DMA(rbr, ve, k_, lba, (slot) + 8192); DMA(rbr, vo, k_, lba, (slot) + 9216); DMA(rbi, ve, k_, lba, (slot) + 16384); DMA(rbi, vo, k_, lba, (slot) + 17408); } while (0)
  {
    const u32x4 rx = mkrs((const char*)p.xb + (size_t)c * 57344 + wave * 7168, 7168);
    const unsigned vx = ve ^ ((wave & 1) << 6), lb = wave * 1024;
    int k = k0;
    if (j == 7) {
      const u32x4 rw = mkrs(isB ? p.wk : p.wv, 25088);
      const unsigned vw = wa * 1024 + lane * 16, lw = K_W + (isB ? 0 : 3584) + wa * 1024; const bool half = wa < 3 || lane < 32;
#pragma unroll
      for (int i = 0; i < 7; ++i) { NEXTK(); DMAX(rx, vx, k * 128, lb, i * SK); if (half) DMA(rw, vw, k * 3584, lw, i * SK); }
    } else {
      const u32x4 rw = mkrs((const char*)p.wq + j * 128 + wave * 7168, 401408 - j * 128 - wave * 7168);
      const unsigned vw = lrow * 896 + (((lane & 7) ^ ((lrow & 2) << 1)) << 4);
      if (wave == 0) { const unsigned co = (64 * j + lane) * 2;
        asm volatile("global_load_ushort %0, %4, %5\n\tglobal_load_ushort %1, %4, %6\n\tglobal_load_ushort %2, %4, %7\n\tglobal_load_ushort %3, %4, %8"
                     : "=&v"(ua), "=&v"(ub), "=&v"(uc), "=&v"(ud) : "v"(co), "s"(p.lre), "s"(p.lim), "s"(p.ldt), "s"(p.dsk) : "memory"); }
#pragma unroll
      for (int i = 0; i < 7; ++i) { NEXTK(); DMAX(rx, vx, k * 128, lb, i * SK); DMA(rw, vw, k * 57344, lb, i * SK + K_W); }
      rxa = mkrs((const char*)p.xa + (size_t)c * 57344 + wa * 14336, 14336);
      rbr = mkrs((const char*)p.bre + (size_t)j * 57344 + wa * 14336, 14336);
      rbi = mkrs((const char*)p.bim + (size_t)j * 57344 + wa * 14336, 14336);
    }
  }
  if (tid < 16) ((LAS unsigned*)(lds + L_MISC))[tid] = 0;
  const int r32 = lane & 31, hi = lane >> 5;
  const int g4 = lane >> 4, tq = (lane & 15) >> 2, tp = lane & 3;
  const unsigned xsw = (r32 >> 1) & 7, lo16 = lane * 16;
  const int th = wa >> 1, chh = wa & 1;
  u64 tag;
  const __amdgpu_buffer_rsrc_t rs_ws = __builtin_amdgcn_make_buffer_rsrc(ws, 0, WS_END, 0x00020000);
  const f32x16 zero16 = {};
  const unsigned o_own = WS_O + (((2 * c + th) * 16 + 2 * j + chh) * 2) * 1024;
#define MARK() do { const u32x4 ones = {~0u, ~0u, ~0u, ~0u};       \
    BST(ones, rs_ws, lo16, o_own); BST(ones, rs_ws, lo16 + 1024, o_own); } while (0)
#define LDEPOCH() asm volatile("s_load_dwordx2 %0, %1, 0x0\n\ts_waitcnt lgkmcnt(0)" : "=s"(tag) : "s"(ws) : "memory"); tag = (tag | 1) + 2

  if (isB) {
    if (wave == 0 && j < 7) {
      asm volatile("s_waitcnt vmcnt(14)" : "+v"(ua), "+v"(ub), "+v"(uc), "+v"(ud) :: "memory");
      const float pa = bf2f(ua), pb = bf2f(ub), dt = rbf(expf(bf2f(uc)));
      const float er = expf(dt * pa); float sn, cs; sincosf(dt * pb, &sn, &cs);
      const float nr = er * cs - 1.f, ni = er * sn, den = pa * pa + pb * pb;
      const cf lam = {er * cs, -ni};
      cf l16 = cmul(lam, lam); l16 = cmul(l16, l16); l16 = cmul(l16, l16); l16 = cmul(l16, l16);
      cf l64 = cmul(l16, l16); l64 = cmul(l64, l64);
      *(LAS f32x4*)(lds + L_EX + lo16) = (f32x4){lam.r, lam.i, 2.f * (nr * pa + ni * pb) / den, 2.f * (ni * pa - nr * pb) / den};
      *(LAS f32x4*)(lds + L_EX + 1024 + lo16) = (f32x4){l16.r, l16.i, l64.r, l64.i};
      *(LAS unsigned*)(lds + L_EX + 2048 + lane * 4) = ud;
    }
    LDEPOCH();
    if (tid == 0) *(LAS u64*)(lds + L_TAG) = tag;
    {
      const unsigned x_off = (32 * th + r32) * 128;
      const int wrow = 8 * (g4 >> 1) + tq;
      bf16x8 fax[4], faw[4], fbx[4], fbw[4];
      f32x16 q = zero16;
#define PRD(B, i, P) _Pragma("unroll") for (int ks = 0; ks < 4; ++ks) { B##x[ks] = ldsf(lds + (i) * SK + x_off + (((2 * ks + hi) ^ xsw) << 4)); B##w[ks] = ldstr2(lds + (i) * SK + w_off + ks * 16 * (P), 4 * (P)); } SBAR()
#define PMM(B, SW) _Pragma("unroll") for (int ks = 0; ks < 4; ++ks) q = (SW) ? MFMA(B##x[ks], B##w[ks], q) : MFMA(B##w[ks], B##x[ks], q); PIN(q); SBAR()
#define PROJ(P, SW) do { VMW(6); BAR(); PRD(fa, 0, P); PRD(fb, 1, P); PMM(fa, SW); PRD(fa, 2, P); PMM(fb, SW); PRD(fb, 3, P); PMM(fa, SW); \
        VMW(0); BAR(); PRD(fa, 4, P); PMM(fb, SW); PRD(fb, 5, P); PMM(fa, SW); PRD(fa, 6, P); PMM(fb, SW); PMM(fa, SW); } while (0)
      if (j < 7) {
        const unsigned w_off = K_W + wrow * 128 + ((64 * chh + 32 * (g4 & 1) + 8 * tp) ^ ((tq & 2) << 5));
        PROJ(128, false);
        const float qs = 0.18898223650461361f * 1.4426950408889634f;
#pragma unroll
        for (int g = 0; g < 4; g += 2) {
          const auto s0 = __builtin_amdgcn_permlane32_swap(pk(q[4 * g] * qs, q[4 * g + 1] * qs), pk(q[4 * g + 4] * qs, q[4 * g + 5] * qs), false, false);
          const auto s1 = __builtin_amdgcn_permlane32_swap(pk(q[4 * g + 2] * qs, q[4 * g + 3] * qs), pk(q[4 * g + 6] * qs, q[4 * g + 7] * qs), false, false);
          BST(((u32x4){s0[0], s1[0], s0[1], s1[1]}), rs_ws, lo16 + g * 512, WS_Q + ((2 * c + th) * 56 + 8 * j + 4 * chh) * 512);
        }
      } else {
        const unsigned w_off = K_W + chh * 3584 + wrow * 56 + (16 * (g4 & 1) + 4 * tp) * 2;
        if (chh) { PROJ(56, true);
#pragma unroll
          for (int r = 0; r < 16; ++r) q[r] = r32 < 28 ? q[r] : (r32 == 28 ? 1.f : 0.f);
        } else { PROJ(56, false); if (hi) { q[12] = 0.f; q[13] = 0.f; q[14] = 0.f; q[15] = 0.f; } }
        const unsigned ko = WS_KV + (2 * c + th) * 4096 + chh * 2048;
        BST(U4(pack8(q, 0)), rs_ws, lo16, ko); BST(U4(pack8(q, 1)), rs_ws, lo16 + 1024, ko);
      }
      publish4(ws, WS_FP + (c * 8 + j) * 128, tag, lane, cPQ);
    }
    KV A0, A1, A2, A3;
    A3.v1 = (bf16x8){};
    f32x16 O[4] = {zero16, zero16, zero16, zero16};
    f32x16 Sa, Sb = zero16; u32x4 Pa, Pb = {};
    PIN(O[0]); PIN(O[1]); PIN(O[2]); PIN(O[3]); PIN(Sb); PIN(Pb); PIN(A3.v1);
    poll(ws, WS_FP + (lane < 8 ? (wave * 8 + lane) * 8 + 7 : c * 8 + ((56 * j + (lane & 1) * 55) >> 6)) * 128, tag);
    const __amdgpu_buffer_rsrc_t rs_kv = __builtin_amdgcn_make_buffer_rsrc(ws + WS_KV + wave * 65536, 0, 65536, 0x00020000);
#define LDKV(B, T) do { const unsigned so_ = (((T) + loc) & 15) * 4096; \
                        B.k0 = BF(BLD(rs_kv, lo16, so_)); B.k1 = BF(BLD(rs_kv, lo16, so_ + 1024)); \
                        B.v0 = BF(BLD(rs_kv, lo16, so_ + 2048)); B.v1 = BF(BLD(rs_kv, lo16, so_ + 3072)); } while (0)
#define LDK(B, T) do { const unsigned so_ = (((T) + loc) & 15) * 4096; B.k0 = BF(BLD(rs_kv, lo16, so_)); B.k1 = BF(BLD(rs_kv, lo16, so_ + 1024)); } while (0)
#define LDV(B, T) do { const unsigned so_ = (((T) + loc) & 15) * 4096; B.v0 = BF(BLD(rs_kv, lo16, so_ + 2048)); B.v1 = BF(BLD(rs_kv, lo16, so_ + 3072)); } while (0)
    bf16x8 Q0[4], Q1[4];
    u32x2 qp[4][4];
    const unsigned q_e = r32 * 16 + hi * 8, q_o = r32 * 16 + (hi ? 512 : 8);
#define LDQ(i) _Pragma("unroll") for (int u = 0; u < 4; ++u) qp[i][u] = __builtin_amdgcn_raw_buffer_load_b64(rs_ws, (((wave ^ (3 - (i))) & 1) ? q_o : q_e) + u * 512, \
                 WS_Q + ((2 * c + ((wave ^ (3 - (i))) >> 1)) * 56 + 7 * j + 3 * ((wave ^ (3 - (i))) & 1)) * 512, SC1)
    LDQ(0); LDKV(A0, 0); LDQ(1); LDQ(2); LDQ(3); LDKV(A1, 1); LDKV(A2, 2); LDK(A3, 3);
    SBAR();
#pragma unroll
    for (int i = 0; i < 4; ++i) { Q0[i] = BF(((u32x4){qp[i][0][0], qp[i][0][1], qp[i][1][0], qp[i][1][1]}));
      Q1[i] = BF(((u32x4){qp[i][2][0], qp[i][2][1], hi ? 0u : qp[i][3][0], hi ? 0u : qp[i][3][1]})); }
    Sa = MFMA(A0.k0, Q0[0], zero16); Sa = MFMA(A0.k1, Q1[0], Sa);
#define UNIT(S, Sn, KN, QN, VP, OP, VC, OC, X) do { \
      Pb[2] = pk(Sn[12], Sn[13]); Pb[3] = pk(Sn[14], Sn[15]); \
      if ((QN) >= 0) Sn = MFMA(KN.k0, Q0[(QN) & 3], zero16); S[0] = EX(S[0]); S[1] = EX(S[1]); S[2] = EX(S[2]); S[3] = EX(S[3]); SBAR(); \
      if ((QN) >= 0) Sn = MFMA(KN.k1, Q1[(QN) & 3], Sn); Pa[0] = pk(S[0], S[1]); Pa[1] = pk(S[2], S[3]); S[4] = EX(S[4]); S[5] = EX(S[5]); S[6] = EX(S[6]); S[7] = EX(S[7]); SBAR(); \
      X; SBAR(); \
      OP = MFMA(VP.v1, BF(Pb), OP); Pa[2] = pk(S[4], S[5]); Pa[3] = pk(S[6], S[7]); S[8] = EX(S[8]); S[9] = EX(S[9]); S[10] = EX(S[10]); S[11] = EX(S[11]); SBAR(); \
      OC = MFMA(VC.v0, BF(Pa), OC); Pb[0] = pk(S[8], S[9]); Pb[1] = pk(S[10], S[11]); S[12] = EX(S[12]); S[13] = EX(S[13]); S[14] = EX(S[14]); S[15] = EX(S[15]); SBAR(); \
    } while (0)
#define TILE(CUR, NXT, PRV, T, LD) do { \
      UNIT(Sa, Sb, CUR, 1, PRV, O[3], CUR, O[0], ); \
      LDV(PRV, (T) + 3); SBAR(); \
      UNIT(Sb, Sa, CUR, 2, CUR, O[0], CUR, O[1], ); \
      UNIT(Sa, Sb, CUR, 3, CUR, O[1], CUR, O[2], if (LD) LDK(CUR, (T) + 4)); \
      UNIT(Sb, Sa, NXT, 0, CUR, O[2], CUR, O[3], ); } while (0)
#pragma unroll 1
    for (int t = 0; t < 12; t += 4) {
      TILE(A0, A1, A3, t, 1); TILE(A1, A2, A0, t + 1, 1); TILE(A2, A3, A1, t + 2, 1); TILE(A3, A0, A2, t + 3, 1);
    }
    TILE(A0, A1, A3, 12, 0);
    const lptr oc = lds + L_OC + lo16;
#define OUTP(T, I) _Pragma("unroll") for (int q4 = 0; q4 < 4; ++q4) *(LAS f32x4*)(oc + ((wave ^ I) * 3 + I - 1) * 4096 + q4 * 1024) = (f32x4){T[4 * q4], T[4 * q4 + 1], T[4 * q4 + 2], T[4 * q4 + 3]}
    f32x4 pv[12];
    UNIT(Sa, Sb, A1, 1, A0, O[3], A1, O[0], ); UNIT(Sb, Sa, A1, 2, A1, O[0], A1, O[1], ); UNIT(Sa, Sb, A2, 0, A1, O[1], A1, O[2], );
    UNIT(Sb, Sa, A2, 1, A1, O[2], A2, O[0], ); UNIT(Sa, Sb, A2, 2, A2, O[0], A2, O[1], ); UNIT(Sb, Sa, A3, 0, A2, O[1], A2, O[2], );
    UNIT(Sa, Sb, A3, 1, A2, O[2], A3, O[0], ); UNIT(Sb, Sa, A3, 2, A3, O[0], A3, O[1], lds_wait(j < 7 ? cCR : cPB, 4));
    UNIT(Sa, Sb, A1, 3, A3, O[1], A3, O[2], OUTP(O[0], 3));
    UNIT(Sb, Sa, A2, 3, A3, O[2], A1, O[3], OUTP(O[1], 2));
    UNIT(Sa, Sb, A3, 3, A1, O[3], A2, O[3], OUTP(O[2], 1); lds_signal(cBB));
    UNIT(Sb, Sa, A3, -1, A2, O[3], A3, O[3], lds_wait(cBB, 4); _Pragma("unroll") for (int s2 = 0; s2 < 12; ++s2) pv[s2] = *(LAS f32x4*)(oc + wave * 12288 + s2 * 1024));
    Pb[2] = pk(Sb[12], Sb[13]); Pb[3] = pk(Sb[14], Sb[15]);
    O[3] = MFMA(A3.v1, BF(Pb), O[3]);
    __builtin_amdgcn_s_setprio(2);
    lds_signal(cGO + 4 * wave);
#pragma unroll
    for (int s2 = 0; s2 < 4; ++s2) pv[s2] = pv[s2] + pv[s2 + 4] + pv[s2 + 8];
    unsigned lo16b = lo16; PIN(lo16b);
    const int lane = lo16b >> 4, r32 = lane & 31, hi = lane >> 5, g4 = lane >> 4, tq = (lane & 15) >> 2, tp = lane & 3;
    f32x16 o = O[3];
#pragma unroll
    for (int r = 0; r < 16; ++r) o[r] += pv[r >> 2][r & 3];
    {
      float l = swap_hi(o[12]);
      float am = 0.f;
#pragma unroll
      for (int r = 0; r < 16; r += 2) am = __builtin_fmaxf(__builtin_fmaxf(am, __builtin_fabsf(o[r])), __builtin_fabsf(o[r + 1]));
      const bool ok = (l >= 0x1p-90f) && (l <= 0x1p110f) && (am <= 0x1p126f);
      const bool slow = !wall(ok);
      if (slow) {
        const __amdgpu_buffer_rsrc_t rk = __builtin_amdgcn_make_buffer_rsrc(ws + WS_KV, 0, 262144, 0x00020000);
        poll(ws, WS_FP + (r32 * 8 + 7) * 128, tag);
        float m = -__builtin_inff();
#pragma unroll 1
        for (int t = 0; t < 64; ++t) {
          f32x16 s = MFMA(BF(BLD(rk, lo16, t * 4096)), Q0[3], zero16); s = MFMA(BF(BLD(rk, lo16 + 1024, t * 4096)), Q1[3], s);
#pragma unroll
          for (int r = 0; r < 16; ++r) m = __builtin_fmaxf(m, s[r]);
        }
        m = __builtin_fmaxf(swap_lo(m), swap_hi(m));
        o = zero16;
#pragma unroll 1
        for (int t = 0; t < 64; ++t) {
          f32x16 s = MFMA(BF(BLD(rk, lo16, t * 4096)), Q0[3], zero16); s = MFMA(BF(BLD(rk, lo16 + 1024, t * 4096)), Q1[3], s);
#pragma unroll
          for (int r = 0; r < 16; ++r) s[r] = EX(s[r] - m);
          o = MFMA(BF(BLD(rk, lo16 + 2048, t * 4096)), pack8(s, 0), o); o = MFMA(BF(BLD(rk, lo16 + 3072, t * 4096)), pack8(s, 1), o);
        }
        l = swap_hi(o[12]);
      }
      const float inv = __builtin_amdgcn_rcpf(l);
#pragma unroll
      for (int r = 0; r < 16; ++r) o[r] *= inv;
      if (hi) { o[12] = 0.f; o[13] = 0.f; o[14] = 0.f; o[15] = 0.f; }
      u32x4 f0 = U4(pack8(o, 0)), f1 = U4(pack8(o, 1));
      if (slow) {
#pragma unroll
        for (int k = 0; k < 4; ++k) { f0[k] = umin(f0[k], 0xfffffffeu); f1[k] = umin(f1[k], 0xfffffffeu); }
      }
      BST(f0, rs_ws, lo16, o_own); BST(f1, rs_ws, lo16 + 1024, o_own);
    }
    lds_signal(cOC);
    __builtin_amdgcn_s_setprio(0);
    if (j == 7) return;
    {
      const CAS Params* kp = KARGS();
      const __amdgpu_buffer_rsrc_t rs_out = __builtin_amdgcn_make_buffer_rsrc(kp->out, 0, 2048 * 896 * 4, 0x00020000);
      lds_wait(cWO, 4);
      u32x4 of[16];
      const unsigned ob = WS_O + (((2 * c + th) * 16 + 8 * chh) * 2) * 1024;
#define LDO(k) of[k] = BLD(rs_ws, lo16 + ((k) & 3) * 1024, ob + ((k) >> 2) * 4096)
      const int rowb = 224 * chh + 4 * (g4 >> 1) + tq;
      const unsigned wb = (L_WO + rowb * 128 + ((32 * (g4 & 1) + 8 * tp) ^ ((tq & 2) << 5))) ^ (chh << 6);
      bf16x8 w0[16], w1[16];
#pragma unroll
      for (int k = 0; k < 16; ++k) w1[k] = ldstr2(lds + (wb ^ 64) + (28 * (k >> 1) + 16 * (k & 1)) * 128, 1024);
      SBAR();
      {
        unsigned have = 0;
#pragma unroll 1
        for (unsigned s = 0; s < SPIN_CAP; ++s) {
          const unsigned w = __builtin_amdgcn_raw_buffer_load_b32(rs_ws, (lane & 7) * 2048 + 2044, ob, SC1);
          const unsigned rdy = (s + 1 < SPIN_CAP ? (unsigned)__builtin_amdgcn_ballot_w64(w != ~0u) : ~0u) & 0xffu & ~have;
#pragma unroll
          for (int h = 0; h < 8; ++h) if (rdy >> h & 1) { LDO(2 * h); LDO(2 * h + 1); }
          have |= rdy;
          if (have == 0xffu) break;
          __builtin_amdgcn_s_sleep(2);
        }
      }
      __builtin_amdgcn_s_setprio(2);
      f32x16 acc0 = zero16, acc1 = zero16;
#define OBAD(h) (!wall(umax(umax(umax(umax(of[2 * (h)][0], of[2 * (h)][1]), umax(of[2 * (h)][2], of[2 * (h)][3])), umax(of[2 * (h) + 1][0], of[2 * (h) + 1][1])), umax(of[2 * (h) + 1][2], of[2 * (h) + 1][3])) != ~0u))
#define OHEAD(h) do { \
        if (OBAD(h)) { \
          _Pragma("unroll 1") for (unsigned s = 0; s < SPIN_CAP; ++s) { LDO(2 * h); LDO(2 * h + 1); if (!OBAD(h)) break; __builtin_amdgcn_s_sleep(2); } \
        } \
        acc1 = MFMA(BF(of[2 * h]), w1[2 * h], acc1); acc1 = MFMA(BF(of[2 * h + 1]), w1[2 * h + 1], acc1); PIN(acc1); \
        w0[2 * h] = ldstr2(lds + wb + 28 * h * 128, 1024); w0[2 * h + 1] = ldstr2(lds + wb + (28 * h + 16) * 128, 1024); } while (0)
      OHEAD(0); OHEAD(1); OHEAD(2); OHEAD(3); OHEAD(4); OHEAD(5); OHEAD(6); OHEAD(7);
      lds_wait(cOC, 4);
#pragma unroll
      for (int k = 0; k < 4; ++k) acc0 = MFMA(BF(of[k]), w0[k], acc0);
      SBAR();
      const lptr tw = lds + L_OC + hi * 512 + r32 * 4;
#pragma unroll
      for (int r = 0; r < 16; ++r) *(LAS float*)(tw + (wave ^ 1) * 4096 + ((r & 3) + 8 * (r >> 2)) * 128) = acc1[r];
      SBAR();
#pragma unroll
      for (int k = 4; k < 8; ++k) acc0 = MFMA(BF(of[k]), w0[k], acc0);
      PIN(acc0);
      lds_signal(cPR + 4 * th);
#pragma unroll
      for (int k = 8; k < 16; ++k) acc0 = MFMA(BF(of[k]), w0[k], acc0);
      PIN(acc0);
      lds_wait(cPR + 4 * th, 2);
      f32x4 pa[4];
#pragma unroll
      for (int k = 0; k < 4; ++k) pa[k] = *(LAS f32x4*)(lds + L_OC + wave * 4096 + k * 1024 + lo16);
#pragma unroll
      for (int r = 0; r < 16; ++r) *(LAS float*)(tw + 16384 + wave * 4096 + ((r & 3) + 8 * (r >> 2)) * 128) = acc0[r];
      MEMBAR();
      const unsigned oo = ((64 * c + 32 * th + (lane >> 3)) * 896 + 448 + 64 * j + 32 * chh + (lane & 7) * 4) * 4;
#pragma unroll
      for (int k = 0; k < 4; ++k)
        __builtin_amdgcn_raw_buffer_store_b128(U4(pa[k] + *(LAS f32x4*)(lds + L_OC + 16384 + wave * 4096 + k * 1024 + lo16)), rs_out, oo + k * 28672, 0, 17);
    }
  } else {
    if (j == 7) {
      VMW(6); BAR(); VMW(0); BAR();
      lds_wait_<4>(cPQ, 4);
      { const u32x2 t2 = *(LAS u32x2*)(lds + L_TAG);
        tag = ((u64)(unsigned)__builtin_amdgcn_readfirstlane(t2[1]) << 32) | (unsigned)__builtin_amdgcn_readfirstlane(t2[0]); }
      __builtin_amdgcn_s_sleep(64);
      MARK();
      publish4(ws, WS_FH + (c * 8 + 7) * 128, tag, lane, cPB);
      if (c == 0 && wa == 0) {
        __builtin_amdgcn_s_sleep(127);
#pragma unroll 1
        for (int k = 0; k < 4; ++k) poll(ws, WS_FP + (k * 64 + lane) * 128, tag);
        if (lane == 0) __hip_atomic_store((GAS u64*)(ws + WS_EPOCH), tag, RLX_AGENT);
      }
      return;
    }
    VMW(6); BAR(); LDCH(L_SA); VMW(6); BAR();
    cf lam, coef, l16, l64; f32x16 ur = zero16, ui = zero16;
    f32x16 acc[4] = {zero16, zero16, zero16, zero16};
    const unsigned cs0 = wa * 16384u, cs1 = L_R1 + wa * 12288u;
    unsigned a_htw = L_HT + wa * 4096 + r32 * 64 + hi * 32, a_htr = L_HT + wa * 4096 + (8 * (g4 >> 1) + tq) * 64 + (g4 & 1) * 32 + tp * 8;
    unsigned a_hst = WS_H + ((2 * c + th) * 56 + 4 * j + 2 * chh) * 1024 + lo16;
    unsigned a_c[4], a_cn[2], a_x[2], a_ex, e_want; u32x4 dg[2]; cf l256, lw, lo_;
    const int seg = 2 * th + hi;
#pragma unroll
    for (int f = 0; f < 4; ++f) a_c[f] = cs0 + (32 * chh + r32) * 128 + (((2 * f + hi) ^ xsw) << 4);
#pragma unroll
    for (int f = 0; f < 2; ++f) a_cn[f] = cs1 + 8192 + (32 * chh + r32) * 64 + (((2 * f + hi) ^ ((r32 >> 2) & 3)) << 4);
    unsigned a_vh = (lane >> 2) * 896 + (((lane & 3) ^ ((lane >> 4) & 3)) << 4) + 384 + chh * 64, a_vo = ve ^ 64;
    unsigned a_vw = lrow * 896 + (((lane & 7) ^ ((lrow & 2) << 1)) << 4);
    unsigned a_fl = WS_FH + (c * 8 + (lane & 7)) * 128;
    unsigned a_el = (31 - umin(r32, c - 1)) * 512 + 4, a_eo = (32 - c + 2 * th + hi) * 512 + r32 * 16, a_hx = L_EX + 2048 + (32 * chh + r32) * 8;
    {
      LDCH(0); LDCH(CH);
      { const f32x4 c0 = *(LAS f32x4*)(lds + L_EX + (32 * chh + r32) * 16), c1 = *(LAS f32x4*)(lds + L_EX + 1024 + (32 * chh + r32) * 16);
        lam = {c0[0], c0[1]}; coef = {c0[2], c0[3]}; l16 = {c1[0], c1[1]}; l64 = {c1[2], c1[3]}; ud = *(LAS unsigned*)(lds + L_EX + 2048 + (32 * chh + r32) * 4); }
      { const int e = r32 - 8 * hi; const unsigned dv = ud << ((e & 1) * 16);
#pragma unroll
        for (int s2 = 0; s2 < 2; ++s2) {
          dg[s2] = (u32x4){(e >> 1) == 8 * s2 ? dv : 0u, (e >> 1) == 8 * s2 + 1 ? dv : 0u, (e >> 1) == 8 * s2 + 2 ? dv : 0u, (e >> 1) == 8 * s2 + 3 ? dv : 0u};
          a_x[s2] = L_SA + (32 * th + r32) * 128 + (((4 * chh + 2 * s2 + hi) ^ xsw) << 4); PIN(dg[s2]); PIN(a_x[s2]); }
        cf lseg = hi ? l16 : (cf){1.f, 0.f};
        if (th) lseg = cmul(lseg, cmul(l16, l16));
        const cf l128 = cmul(l64, l64), ls128 = cmul(lseg, l128);
        l256 = cmul(l128, l128); lw = th ? ls128 : lseg; lo_ = th ? lseg : ls128; e_want = (c - seg + 3) >> 2; a_ex = L_EX + (32 * chh + r32) * 8;
        PIN(l256.r); PIN(l256.i); PIN(lw.r); PIN(lw.i); PIN(lo_.r); PIN(lo_.i); PIN(e_want); PIN(a_ex); }
      PIN(a_htw); PIN(a_htr); PIN(a_hst); PIN(a_c[0]); PIN(a_c[1]); PIN(a_c[2]); PIN(a_c[3]); PIN(a_cn[0]); PIN(a_cn[1]); PIN(a_vh); PIN(a_vw); PIN(a_fl); PIN(a_el); PIN(a_eo); PIN(a_hx); PIN(a_vo);
      const int trow = 32 * th + 16 * ((r32 >> 2) & 1) + (r32 & 3) + 4 * (r32 >> 3);
      const unsigned xa_off = trow * 128, xa_sw = (trow >> 1) & 7, b_off = 8192 + (32 * chh + r32) * 128;
      bf16x8 gax[4], gar[4], gai[4], gbx[4], gbr[4], gbi[4];
      unsigned gx[3][4], gw[3][4];
#define GB(s_) ((s_) < 49152 ? 0u : (s_) < 110592 ? 49152u : 110592u)
#define GT(s_) ((s_) < 49152 ? 0 : (s_) < 110592 ? 1 : 2)
#pragma unroll
      for (int t = 0; t < 3; ++t)
#pragma unroll
        for (int ks = 0; ks < 4; ++ks) { gx[t][ks] = GB(t * 60000) + xa_off + (((2 * ks + hi) ^ xa_sw) << 4); gw[t][ks] = GB(t * 60000) + b_off + (((2 * ks + hi) ^ xsw) << 4); PIN(gx[t][ks]); PIN(gw[t][ks]); }
#define GRD(B, s_) _Pragma("unroll") for (int ks = 0; ks < 4; ++ks) { B##x[ks] = ldsf(lds + gx[GT(s_)][ks] + ((s_) - GB(s_))); \
        B##r[ks] = ldsf(lds + gw[GT(s_)][ks] + ((s_) - GB(s_))); B##i[ks] = ldsf(lds + gw[GT(s_)][ks] + ((s_) - GB(s_) + 8192)); } SBAR()
#define GMM(B) _Pragma("unroll") for (int ks = 0; ks < 4; ++ks) { ur = MFMA(B##x[ks], B##r[ks], ur); ui = MFMA(B##x[ks], B##i[ks], ui); } PIN(ur); PIN(ui); SBAR()
#define GAP() do { SBAR(); asm volatile("s_nop %0" :: "i"(7)); SBAR(); } while (0)
#define GMS(B) _Pragma("unroll") for (int ks = 0; ks < 4; ++ks) { ur = MFMA(B##x[ks], B##r[ks], ur); GAP(); ui = MFMA(B##x[ks], B##i[ks], ui); GAP(); } PIN(ur); PIN(ui); SBAR()
      __builtin_amdgcn_s_setprio(1);
      lds_wait_<1>(cPQ, 4);
      LDCH(2 * CH); LDCH(3 * CH); LDCH(4 * CH);
      VMW(18); lds_barrierA(cAB, 4); GRD(ga, L_SA); GRD(gb, 0); GMM(ga); GRD(ga, CH); GMM(gb);
      VMW(6); lds_barrierA(cAB, 8);
      GRD(gb, 2 * CH); LDCH(L_SA); GMM(ga); GRD(ga, 3 * CH); GMS(gb);
      VMW(6); lds_barrierA(cAB, 12);
      GRD(gb, 4 * CH); GMS(ga);
      VMW(0); lds_barrierA(cAB, 16);
      GRD(ga, L_SA); GMS(gb); GMS(ga);
      { const u32x2 t2 = *(LAS u32x2*)(lds + L_TAG);
        tag = ((u64)(unsigned)__builtin_amdgcn_readfirstlane(t2[1]) << 32) | (unsigned)__builtin_amdgcn_readfirstlane(t2[0]); }
    }
    acc[0] = MFMA(ldsf(lds + a_x[0]), BF(dg[0]), acc[0]); acc[0] = MFMA(ldsf(lds + a_x[1]), BF(dg[1]), acc[0]);
    PIN(acc[0]);
    cf e = {0.f, 0.f};
#pragma unroll
    for (int r = 0; r < 16; ++r) {
      const float a = coef.r * ur[r] - coef.i * ui[r], b2 = -(coef.r * ui[r] + coef.i * ur[r]);
      ur[r] = a; ui[r] = b2; e = r ? cfma(lam, e, (cf){a, b2}) : (cf){a, b2};
    }
    *(LAS f32x2*)(lds + a_ex + th * 1024 + hi * 512) = (f32x2){e.r, e.i};
    __builtin_amdgcn_s_setprio(0);
    lds_barrierA(cAB, 20);
    cf g0;
    {
      const f32x2 e0 = *(LAS f32x2*)(lds + a_ex), e1 = *(LAS f32x2*)(lds + a_ex + 512), e2 = *(LAS f32x2*)(lds + a_ex + 1024), e3 = *(LAS f32x2*)(lds + a_ex + 1536);
      __builtin_amdgcn_s_setprio(1);
      const cf E2 = cfma(l16, (cf){e0[0], e0[1]}, (cf){e1[0], e1[1]}), E3 = cfma(l16, E2, (cf){e2[0], e2[1]}), E = cfma(l16, E3, (cf){e3[0], e3[1]});
      if (th) g0 = hi ? E3 : E2; else g0 = hi ? (cf){e0[0], e0[1]} : (cf){0.f, 0.f};
      PIN(g0.r); PIN(g0.i);
      if (th == 0 && hi == 0 && c < 31)
        BST(((u32x4){f2u(E.r), (unsigned)tag, f2u(E.i), (unsigned)tag}), rs_ws, r32 * 16, WS_E + ((2 * j + chh) * 32 + 31 - c) * 512);
    }
    __builtin_amdgcn_s_setprio(0);
    const CAS Params* kp = KARGS();
    {
      const u32x4 rc = mkrs((const char*)(th ? kp->cim : kp->cre) + (size_t)j * 57344, 57344);
      int n2 = (6 * cl + 2 * ((b & 7) % 3)) % 24;
#pragma unroll
      for (int n = 0; n < 24; ++n) {
        DMA(rc, (n & 1) ? a_vo : ve, (n2 & 7) * 7168 + (n2 >> 3) * 128 + chh * 512, (n2 < 16 ? cs0 : cs1 - 16384) + n2 * 1024, 0);
        n2 = n2 == 23 ? 0 : n2 + 1; }
#pragma unroll
      for (int n = 0; n < 4; ++n) DMA(rc, a_vh, n * 14336, cs1, 8192 + n * 1024);
    }
    cf G = g0;
    if (c > 0) {
      const __amdgpu_buffer_rsrc_t re = __builtin_amdgcn_make_buffer_rsrc(ws + WS_E + (2 * j + chh) * 16384, 0, 16384, 0x00020000);
      u32x4 ev[8];
#pragma unroll 1
      for (unsigned s = 0; s < SPIN_CAP; ++s) {
        const unsigned t1 = __builtin_amdgcn_raw_buffer_load_b32(re, a_el, 0, SC1);
        if (wall(t1 == (unsigned)tag)) break;
        __builtin_amdgcn_s_sleep(2);
      }
      MEMBAR();
#pragma unroll 1
      for (unsigned s = 0; s < SPIN_CAP; ++s) {
        unsigned got = 0;
#pragma unroll
        for (int m = 0; m < 8; ++m) ev[m] = BLD(re, a_eo, m * 2048);
        VMW(0);
        __builtin_amdgcn_s_setprio(1);
#pragma unroll
        for (int m = 0; m < 8; ++m) got += ((ev[m][1] ^ (unsigned)tag) | (ev[m][3] ^ (unsigned)tag)) == 0;
        if (wall(got == e_want)) break;
        __builtin_amdgcn_s_setprio(0);
        __builtin_amdgcn_s_sleep(4);
      }
      cf H = {u2f(ev[7][0]), u2f(ev[7][2])};
#pragma unroll
      for (int m = 6; m >= 0; --m) H = cfma(l256, H, (cf){u2f(ev[m][0]), u2f(ev[m][2])});
      H = cfma(l64, (cf){swap_hi(H.r), swap_hi(H.i)}, (cf){swap_lo(H.r), swap_lo(H.i)});
      *(LAS f32x2*)(lds + a_hx + th * 512) = (f32x2){H.r, H.i};
      G = cfma(lw, H, g0);
      __builtin_amdgcn_s_setprio(0);
      lds_barrierA(cPE + 4 * chh, 2);
      const f32x2 ho = *(LAS f32x2*)(lds + a_hx + (th ^ 1) * 512);
      __builtin_amdgcn_s_setprio(1);
      G = cfma(lo_, (cf){ho[0], ho[1]}, G);
    }
    __builtin_amdgcn_s_setprio(1);
    {
#pragma unroll
      for (int r = 0; r < 16; ++r) { G = cfma(lam, G, (cf){ur[r], ui[r]}); ur[r] = G.r; ui[r] = G.i; }
      *(LAS bf16x8*)(lds + a_htw) = pack8(ur, 0); *(LAS bf16x8*)(lds + a_htw + 16) = pack8(ur, 1);
      *(LAS bf16x8*)(lds + a_htw + 2048) = pack8(ui, 0); *(LAS bf16x8*)(lds + a_htw + 2064) = pack8(ui, 1);
      unsigned a_hs2 = a_hst + 28 * 1024; PIN(a_hs2);
      __builtin_amdgcn_s_setprio(0);
      asm volatile("s_waitcnt lgkmcnt(0)" ::: "memory");
#pragma unroll
      for (int k = 0; k < 4; ++k) BST(U4(ldstr2(lds + a_htr + (k >> 1) * 2048 + (k & 1) * 1024, 256)), rs_ws, (k >> 1) ? a_hs2 : a_hst, (k & 1) * 1024);
      MARK();
      publish4(ws, WS_FH + (c * 8 + j) * 128, tag, lane, cPB);
    }
    bf16x8 cfr[14][2];
    const int cflip = chh ? -1 : 1;
#define LDC(f) do { if ((f) < 12) { const unsigned a_ = a_c[(f) & 3] + ((f) < 8 ? ((f) >> 2) * 8192 : cs1 - cs0); cfr[f][0] = ldsf(lds + a_); cfr[f][1] = ldsf(lds + a_ + cflip * 4096); } \
                    else { cfr[f][0] = ldsf(lds + a_cn[(f) & 1]); cfr[f][1] = ldsf(lds + a_cn[(f) & 1] + cflip * 2048); } } while (0)
#pragma unroll
    for (int f = 0; f < 8; ++f) LDC(f);
    lds_signal(cCR);
    lds_wait_<1>(cCR, 4);
    {
      const u32x4 rw = mkrs((const char*)kp->wo + j * 128 + wa * 7168, 401408 - j * 128 - wa * 7168);
      int m2 = (4 * cl + (b & 7)) % 14;
#pragma unroll
      for (int m = 0; m < 14; ++m) { DMA(rw, a_vw, m2 * 28672, wa * 1024 + m2 * 4096, L_WO); m2 = m2 == 13 ? 0 : m2 + 1; }
      if (wa == 0 && lane < 32) *(LAS u32x4*)(lds + L_WO + 57344 + lo16) = (u32x4){0, 0, 0, 0};
    }
    poll(ws, a_fl, tag);
    VMW(0);
    lds_signal(cWO);
    {
      const unsigned hb = WS_H + (2 * c * 56 + 14 * wa) * 1024, h0 = hb + th * 57344, h1 = hb + (th ^ 1) * 57344;
      const unsigned fa = chh * 2048, fb = chh ? 0u - 12288u : 0u;
      u32x4 hf[14][2];
#define LDH(f) do { hf[f][0] = BLD(rs_ws, lo16, h0 + (f) * 1024 + ((f) < 12 ? fa : fb)); hf[f][1] = BLD(rs_ws, lo16, h1 + (f) * 1024 + ((f) < 12 ? fa : fb)); } while (0)
#define MMH(f) do { acc[0] = MFMA(BF(hf[f][0]), cfr[f][0], acc[0]); acc[1] = MFMA(BF(hf[f][0]), cfr[f][1], acc[1]); \
                    acc[2] = MFMA(BF(hf[f][1]), cfr[f][0], acc[2]); acc[3] = MFMA(BF(hf[f][1]), cfr[f][1], acc[3]); \
                    PIN(acc[0]); PIN(acc[1]); PIN(acc[2]); PIN(acc[3]); } while (0)
#pragma unroll
      for (int f = 0; f < 14; ++f) LDH(f);
      SBAR();
      lds_wait_<4>(cGO + 4 * wa, 1);
      __builtin_amdgcn_s_setprio(1);
#pragma unroll
      for (int f = 0; f < 8; ++f) { MMH(f); SBAR(); if (f >= 1 && f < 7) LDC(f + 7); SBAR(); }
      lds_signal(cP4);
#pragma unroll
      for (int f = 8; f < 14; ++f) MMH(f);
    }
    lds_wait(cP4, 4);
    rs_write(lds + L_G2, wa, lane, acc[1], acc[2], acc[3]);
    lds_barrierA(cAB, 24);
    f32x16 y = rs_read(lds + L_G2, wa, lane, acc[0]);
    const __amdgpu_buffer_rsrc_t rs_out = __builtin_amdgcn_make_buffer_rsrc(kp->out, 0, 2048 * 896 * 4, 0x00020000);
    MEMBAR();
#pragma unroll
    for (int r = 0; r < 16; ++r) *(LAS float*)(lds + L_G2 + wa * 12288 + hi * 512 + r32 * 4 + ((r & 3) + 8 * (r >> 2)) * 128) = y[r];
    MEMBAR();
    const unsigned oo = ((64 * c + 32 * th + (lane >> 3)) * 896 + 64 * j + 32 * chh + (lane & 7) * 4) * 4;
#pragma unroll
    for (int k = 0; k < 4; ++k) __builtin_amdgcn_raw_buffer_store_b128(*(LAS u32x4*)(lds + L_G2 + wa * 12288 + k * 1024 + lo16), rs_out, oo + k * 28672, 0, 17);
  }
}

extern "C" void kernel_launch(void* const* d_in, const int* in_sizes, int n_in, void* d_out, int out_size, void* d_ws, size_t ws_size,
                              hipStream_t stream) {
  Params p;
  p.xa = (const u16*)d_in[0]; p.lre = (const u16*)d_in[1]; p.lim = (const u16*)d_in[2]; p.bre = (const u16*)d_in[3]; p.bim = (const u16*)d_in[4];
  p.cre = (const u16*)d_in[5]; p.cim = (const u16*)d_in[6]; p.ldt = (const u16*)d_in[7]; p.dsk = (const u16*)d_in[8];
  p.xb = (const u16*)d_in[9]; p.wq = (const u16*)d_in[10]; p.wk = (const u16*)d_in[11]; p.wv = (const u16*)d_in[12]; p.wo = (const u16*)d_in[13];
  p.out = (float*)d_out; p.ws = (char*)d_ws;
  fused_kernel<<<256, 512, 0, stream>>>(p);
}
```

```cpp
#include <hip/hip_runtime.h>
#include <stdint.h>

#define DI __device__ __forceinline__
#define GAS __attribute__((address_space(1)))
#define LAS __attribute__((address_space(3)))
#define CAS __attribute__((address_space(4)))
typedef short bf16x8 __attribute__((ext_vector_type(8)));
typedef short s16x4 __attribute__((ext_vector_type(4)));
typedef float f32x16 __attribute__((ext_vector_type(16)));
typedef float f32x4 __attribute__((ext_vector_type(4)));
typedef float f32x2 __attribute__((ext_vector_type(2)));
typedef unsigned u32x4 __attribute__((ext_vector_type(4)));
typedef unsigned u32x2 __attribute__((ext_vector_type(2)));
typedef __bf16 bf16x2_t __attribute__((ext_vector_type(2)));
typedef unsigned long long u64;
typedef unsigned short u16;
typedef LAS char* lptr;
#define RLX_AGENT __ATOMIC_RELAXED, __HIP_MEMORY_SCOPE_AGENT
#define MFMA(a, b, c) __builtin_amdgcn_mfma_f32_32x32x16_bf16((a), (b), (c), 0, 0, 0)
#define SBAR() __builtin_amdgcn_sched_barrier(0)
#define PIN(x) asm volatile("" : "+v"(x))
#define MEMBAR() asm volatile("" ::: "memory")
#define SC1 16

constexpr unsigned WS_EPOCH = 0, WS_FH = 8192, WS_E = 0x10000, WS_FP = 0x48000,
                   WS_KV = 0x50000, WS_O = 0x100000, WS_H = 0x300000, WS_Q = 0x680000, WS_END = 0x880000, WS_DBG = 0x900000;
constexpr unsigned SK = 16384, K_W = 8192;
constexpr unsigned CH = 24576, L_SA = 135168;
constexpr unsigned L_R1 = 107520, L_HT = 65536, L_WO = 0, L_OC = 57856, L_G2 = 107520, L_MISC = 159744, L_EX = L_MISC + 128, L_TOTAL = 163840;
constexpr unsigned cBB = L_MISC, cAB = L_MISC + 4, cCR = L_MISC + 8, cWO = L_MISC + 12, cOC = L_MISC + 16, cPB = L_MISC + 20, cPR = L_MISC + 24, cP4 = L_MISC + 32, cPQ = L_MISC + 52, cGO = L_MISC + 36, cPE = L_MISC + 56, L_TAG = L_MISC + 64;
constexpr unsigned SPIN_CAP = 1u << 18;

struct Params {
  const u16 *xb, *wq, *wk, *wv; char* ws; const u16 *lre, *lim, *ldt, *dsk, *xa, *bre, *bim, *cre, *cim, *wo;
  float* out;
};

DI float u2f(unsigned v) { return __builtin_bit_cast(float, v); }
DI unsigned f2u(float v) { return __builtin_bit_cast(unsigned, v); }
DI unsigned umin(unsigned a, unsigned b) { return a < b ? a : b; }
DI unsigned umax(unsigned a, unsigned b) { return a > b ? a : b; }
DI bool wall(bool x) { return __builtin_amdgcn_ballot_w64(!x) == 0; }
DI float bf2f(unsigned v) { return u2f(v << 16); }
DI unsigned pk(float a, float b) { f32x2 v = {a, b}; return __builtin_bit_cast(unsigned, __builtin_convertvector(v, bf16x2_t)); }
DI float rbf(float a) { return u2f(pk(0.f, a)); }
DI bf16x8 pack8(const f32x16& x, int s) {
  u32x4 p = {pk(x[8 * s], x[8 * s + 1]), pk(x[8 * s + 2], x[8 * s + 3]), pk(x[8 * s + 4], x[8 * s + 5]), pk(x[8 * s + 6], x[8 * s + 7])};
  return __builtin_bit_cast(bf16x8, p);
}
DI u32x4 mkrs(const void* base, unsigned bytes) { const u64 a = (u64)base; return (u32x4){(unsigned)a, (unsigned)(a >> 32) & 0xffffu, bytes, 0x20000u}; }
#define DMA(rs, voff, soff, ldsb, IMM) asm volatile("s_add_u32 m0, %3, %4\n\ts_nop 0\n\tbuffer_load_dwordx4 %0, %1, %2 offen lds" \
                                                    :: "v"(voff), "s"(rs), "s"(soff), "s"(ldsb), "i"(IMM) : "memory", "scc")
#define DMAX(rs, voff, soff, ldsb, IMM) asm volatile("s_add_u32 m0, %3, %4\n\ts_nop 0\n\tbuffer_load_dwordx4 %0, %1, %2 offen lds" \
                                                     :: "v"(voff), "s"(rs), "s"(soff), "s"(ldsb), "i"(IMM) : "memory", "scc")
DI bf16x8 ldsf(lptr p) { return *(LAS bf16x8*)p; }
DI s16x4 ldstr(lptr p) { return __builtin_amdgcn_ds_read_tr16_b64_v4i16((LAS s16x4*)p); }
DI bf16x8 ldstr2(lptr p, int second) { s16x4 lo = ldstr(p), hi = ldstr(p + second); return __builtin_shufflevector(lo, hi, 0, 1, 2, 3, 4, 5, 6, 7); }
DI void lds_signal(unsigned c) {
  asm volatile("s_waitcnt lgkmcnt(0)\n\ts_mov_b64 exec, 1\n\tds_add_u32 %0, %1\n\ts_mov_b64 exec, -1" :: "v"(c), "v"(1u) : "memory");
}
template <int SLEEP> DI void lds_wait_(unsigned c, unsigned target) {
  unsigned v, s, n;
  asm volatile("s_mov_b32 %2, 0x400000\n1:\n\tds_read_b32 %0, %3\n\ts_waitcnt lgkmcnt(0)\n\tv_readfirstlane_b32 %1, %0\n\ts_cmp_ge_u32 %1, %4\n\ts_cbranch_scc1 2f\n\t"
               "s_sleep %5\n\ts_sub_u32 %2, %2, 1\n\ts_cbranch_scc0 1b\n2:" : "=&v"(v), "=&s"(s), "=&s"(n) : "v"(c), "i"(target), "i"(SLEEP) : "memory", "scc");
}
#define lds_wait(c, t) lds_wait_<0>(c, t)
#define lds_barrier(c, t) do { lds_signal(c); lds_wait(c, t); } while (0)
#define lds_barrierA(c, t) do { lds_signal(c); lds_wait_<1>(c, t); } while (0)
DI void poll(const char* ws, unsigned off, u64 tag) {
#pragma unroll 1
  for (unsigned s = 0; s < SPIN_CAP; ++s) {
    u64 v = __hip_atomic_load((GAS u64*)(ws + off), RLX_AGENT);
    if (wall(v == tag)) break;
    __builtin_amdgcn_s_sleep(2);
  }
  MEMBAR();
}
DI void publish4(char* ws, unsigned off, u64 tag, int lane, unsigned cnt) {
  unsigned old;
  asm volatile("s_waitcnt vmcnt(0)\n\ts_mov_b64 exec, 1\n\tds_add_rtn_u32 %0, %1, %2\n\ts_waitcnt lgkmcnt(0)\n\ts_mov_b64 exec, -1" : "=&v"(old) : "v"(cnt), "v"(1u) : "memory");
  if (__builtin_amdgcn_readfirstlane(old) == 3 && lane == 0) __hip_atomic_store((GAS u64*)(ws + off), tag, RLX_AGENT);
}
DI float swap_lo(float x) {
  auto r = __builtin_amdgcn_permlane32_swap(f2u(x), f2u(x), false, false); return u2f(r[0]); }
DI float swap_hi(float x) {
  auto r = __builtin_amdgcn_permlane32_swap(f2u(x), f2u(x), false, false); return u2f(r[1]); }
struct cf { float r, i; };
DI cf cmul(cf a, cf b) { return {a.r * b.r - a.i * b.i, a.r * b.i + a.i * b.r}; }
DI cf cfma(cf a, cf b, cf c) { return {__builtin_fmaf(a.r, b.r, __builtin_fmaf(-a.i, b.i, c.r)), __builtin_fmaf(a.r, b.i, __builtin_fmaf(a.i, b.r, c.i))}; }

#define STAMP(k) do {} while (0)
#define STAMP_OUT() do {} while (0)

struct KV { bf16x8 k0, k1, v0, v1; };

DI void rs_write(lptr base, int w, int lane, const f32x16& t1, const f32x16& t2, const f32x16& t3) {
#define RSW(T, I) _Pragma("unroll") for (int q = 0; q < 4; ++q) *(LAS f32x4*)(base + ((w ^ I) * 3 + I - 1) * 4096 + q * 1024 + lane * 16) = (f32x4){T[4 * q], T[4 * q + 1], T[4 * q + 2], T[4 * q + 3]}
  RSW(t1, 1); RSW(t2, 2); RSW(t3, 3);
}
DI f32x16 rs_read(lptr base, int w, int lane, f32x16 a) {
  f32x4 v[12];
#pragma unroll
  for (int s = 0; s < 12; ++s) v[s] = *(LAS f32x4*)(base + w * 12288 + s * 1024 + lane * 16);
  SBAR();
#pragma unroll
  for (int s = 0; s < 12; ++s) { const int q = s & 3; a[4 * q] += v[s][0]; a[4 * q + 1] += v[s][1]; a[4 * q + 2] += v[s][2]; a[4 * q + 3] += v[s][3]; }
  return a;
}

#define VMW_(n) asm volatile("s_waitcnt vmcnt(" #n ")" ::: "memory")
#define VMW(n) VMW_(n)
#define BAR() asm volatile("s_waitcnt lgkmcnt(0)\n\ts_barrier" ::: "memory")
#define BLD(R, V, S) __builtin_amdgcn_raw_buffer_load_b128(R, V, S, SC1)
#define BST(D, R, V, S) __builtin_amdgcn_raw_buffer_store_b128(D, R, (V) + (S), 0, SC1)
#define BF(x) __builtin_bit_cast(bf16x8, x)
#define U4(x) __builtin_bit_cast(u32x4, x)
#define EX(x) __builtin_amdgcn_exp2f(x)
#define KARGS() ({ const CAS Params* kp_ = (const CAS Params*)__builtin_amdgcn_kernarg_segment_ptr(); asm volatile("" : "+s"(kp_)); kp_; })
#define NEXTK() do { k += step; k = k >= 7 ? k - 7 : k; } while (0)

#if defined(__HIP_DEVICE_COMPILE__)
#define NO_PK __attribute__((target("no-packed-fp32-ops")))
#else
#define NO_PK
#endif
__global__ void __launch_bounds__(512, 2) NO_PK fused_kernel(Params p) {
  __shared__ __attribute__((aligned(128))) char smem[L_TOTAL];
  const lptr lds = (lptr)smem;
  const int tid = threadIdx.x, lane = tid & 63, wave = __builtin_amdgcn_readfirstlane(tid >> 6);
  const int b = blockIdx.x, loc = b >> 3, cl = loc >> 3, c = (b & 7) * 4 + cl, j = loc & 7;
  const int lrow = lane >> 3;
  const unsigned ve = lrow * 896 + (((lane & 7) ^ (lrow >> 1)) << 4);
  const int step = 1 + (cl + (b & 7) * 4) % 6, k0 = j == 7 ? 0 : j;
  const bool isB = wave < 4;
  const int wa = wave & 3;
  char* const ws = p.ws;
  STAMP(0);
  unsigned ua = 0, ub = 0, uc = 0, ud = 0;
  u32x4 rxa, rbr, rbi; int ka = k0;
  const unsigned vo = (ve ^ 64) + 7168, lba = wa * 2048;
#define NEXTKA() do { ka += step; ka = ka >= 7 ? ka - 7 : ka; } while (0)
#define LDCH(slot) do { NEXTKA(); const unsigned k_ = ka * 128; DMAX(rxa, ve, k_, lba, (slot)); DMAX(rxa, vo, k_, lba, (slot) + 1024); \
      DMA(rbr, ve, k_, lba, (slot) + 8192); DMA(rbr, vo, k_, lba, (slot) + 9216); DMA(rbi, ve, k_, lba, (slot) + 16384); DMA(rbi, vo, k_, lba, (slot) + 17408); } while (0)
  {
    const u32x4 rx = mkrs((const char*)p.xb + (size_t)c * 57344 + wave * 7168, 7168);
    const unsigned vx = ve ^ ((wave & 1) << 6), lb = wave * 1024;
    int k = k0;
    if (j == 7) {
      const u32x4 rw = mkrs(isB ? p.wk : p.wv, 25088);
      const unsigned vw = wa * 1024 + lane * 16, lw = K_W + (isB ? 0 : 3584) + wa * 1024; const bool half = wa < 3 || lane < 32;
#pragma unroll
      for (int i = 0; i < 7; ++i) { NEXTK(); DMAX(rx, vx, k * 128, lb, i * SK); if (half) DMA(rw, vw, k * 3584, lw, i * SK); }
    } else {
      const u32x4 rw = mkrs((const char*)p.wq + j * 128 + wave * 7168, 401408 - j * 128 - wave * 7168);
      const unsigned vw = lrow * 896 + (((lane & 7) ^ ((lrow & 2) << 1)) << 4);
      if (wave == 0) { const unsigned co = (64 * j + lane) * 2;
        asm volatile("global_load_ushort %0, %4, %5\n\tglobal_load_ushort %1, %4, %6\n\tglobal_load_ushort %2, %4, %7\n\tglobal_load_ushort %3, %4, %8"
                     : "=&v"(ua), "=&v"(ub), "=&v"(uc), "=&v"(ud) : "v"(co), "s"(p.lre), "s"(p.lim), "s"(p.ldt), "s"(p.dsk) : "memory"); }
#pragma unroll
      for (int i = 0; i < 7; ++i) { NEXTK(); DMAX(rx, vx, k * 128, lb, i * SK); DMA(rw, vw, k * 57344, lb, i * SK + K_W); }
      rxa = mkrs((const char*)p.xa + (size_t)c * 57344 + wa * 14336, 14336);
      rbr = mkrs((const char*)p.bre + (size_t)j * 57344 + wa * 14336, 14336);
      rbi = mkrs((const char*)p.bim + (size_t)j * 57344 + wa * 14336, 14336);
    }
  }
  STAMP(8);
  if (tid < 16) ((LAS unsigned*)(lds + L_MISC))[tid] = 0;
  const int r32 = lane & 31, hi = lane >> 5;
  const int g4 = lane >> 4, tq = (lane & 15) >> 2, tp = lane & 3;
  const unsigned xsw = (r32 >> 1) & 7, lo16 = lane * 16;
  const int th = wa >> 1, chh = wa & 1;
  u64 tag;
  const __amdgpu_buffer_rsrc_t rs_ws = __builtin_amdgcn_make_buffer_rsrc(ws, 0, WS_END, 0x00020000);
  const f32x16 zero16 = {};
  const unsigned o_own = WS_O + (((2 * c + th) * 16 + 2 * j + chh) * 2) * 1024;
#define MARK() do { const u32x4 ones = {~0u, ~0u, ~0u, ~0u};       \
    BST(ones, rs_ws, lo16, o_own); BST(ones, rs_ws, lo16 + 1024, o_own); } while (0)
#define LDEPOCH() asm volatile("s_load_dwordx2 %0, %1, 0x0\n\ts_waitcnt lgkmcnt(0)" : "=s"(tag) : "s"(ws) : "memory"); tag = (tag | 1) + 2

  if (isB) {
    if (wave == 0 && j < 7) {
      asm volatile("s_waitcnt vmcnt(14)" : "+v"(ua), "+v"(ub), "+v"(uc), "+v"(ud) :: "memory");
      const float pa = bf2f(ua), pb = bf2f(ub), dt = rbf(expf(bf2f(uc)));
      const float er = expf(dt * pa); float sn, cs; sincosf(dt * pb, &sn, &cs);
      const float nr = er * cs - 1.f, ni = er * sn, den = pa * pa + pb * pb;
      const cf lam = {er * cs, -ni};
      cf l16 = cmul(lam, lam); l16 = cmul(l16, l16); l16 = cmul(l16, l16); l16 = cmul(l16, l16);
      cf l64 = cmul(l16, l16); l64 = cmul(l64, l64);
      *(LAS f32x4*)(lds + L_EX + lo16) = (f32x4){lam.r, lam.i, 2.f * (nr * pa + ni * pb) / den, 2.f * (ni * pa - nr * pb) / den};
      *(LAS f32x4*)(lds + L_EX + 1024 + lo16) = (f32x4){l16.r, l16.i, l64.r, l64.i};
      *(LAS unsigned*)(lds + L_EX + 2048 + lane * 4) = ud;
    }
    LDEPOCH();
    if (tid == 0) *(LAS u64*)(lds + L_TAG) = tag;
    {
      const unsigned x_off = (32 * th + r32) * 128;
      const int wrow = 8 * (g4 >> 1) + tq;
      bf16x8 fax[4], faw[4], fbx[4], fbw[4];
      f32x16 q = zero16;
#define PRD(B, i, P) _Pragma("unroll") for (int ks = 0; ks < 4; ++ks) { B##x[ks] = ldsf(lds + (i) * SK + x_off + (((2 * ks + hi) ^ xsw) << 4)); B##w[ks] = ldstr2(lds + (i) * SK + w_off + ks * 16 * (P), 4 * (P)); } SBAR()
#define PMM(B, SW) _Pragma("unroll") for (int ks = 0; ks < 4; ++ks) q = (SW) ? MFMA(B##x[ks], B##w[ks], q) : MFMA(B##w[ks], B##x[ks], q); PIN(q); SBAR()
#define PROJ(P, SW) do { VMW(6); BAR(); PRD(fa, 0, P); PRD(fb, 1, P); PMM(fa, SW); PRD(fa, 2, P); PMM(fb, SW); PRD(fb, 3, P); PMM(fa, SW); \
        VMW(0); BAR(); PRD(fa, 4, P); PMM(fb, SW); PRD(fb, 5, P); PMM(fa, SW); PRD(fa, 6, P); PMM(fb, SW); PMM(fa, SW); } while (0)
      if (j < 7) {
        const unsigned w_off = K_W + wrow * 128 + ((64 * chh + 32 * (g4 & 1) + 8 * tp) ^ ((tq & 2) << 5));
        PROJ(128, false);
        STAMP(1);
        const float qs = 0.18898223650461361f * 1.4426950408889634f;
#pragma unroll
        for (int g = 0; g < 4; g += 2) {
          const auto s0 = __builtin_amdgcn_permlane32_swap(pk(q[4 * g] * qs, q[4 * g + 1] * qs), pk(q[4 * g + 4] * qs, q[4 * g + 5] * qs), false, false);
          const auto s1 = __builtin_amdgcn_permlane32_swap(pk(q[4 * g + 2] * qs, q[4 * g + 3] * qs), pk(q[4 * g + 6] * qs, q[4 * g + 7] * qs), false, false);
          BST(((u32x4){s0[0], s1[0], s0[1], s1[1]}), rs_ws, lo16 + g * 512, WS_Q + ((2 * c + th) * 56 + 8 * j + 4 * chh) * 512);
        }
      } else {
        const unsigned w_off = K_W + chh * 3584 + wrow * 56 + (16 * (g4 & 1) + 4 * tp) * 2;
        if (chh) { PROJ(56, true);
#pragma unroll
          for (int r = 0; r < 16; ++r) q[r] = r32 < 28 ? q[r] : (r32 == 28 ? 1.f : 0.f);
        } else { PROJ(56, false); if (hi) { q[12] = 0.f; q[13] = 0.f; q[14] = 0.f; q[15] = 0.f; } }
        STAMP(1);
        const unsigned ko = WS_KV + (2 * c + th) * 4096 + chh * 2048;
        BST(U4(pack8(q, 0)), rs_ws, lo16, ko); BST(U4(pack8(q, 1)), rs_ws, lo16 + 1024, ko);
      }
      publish4(ws, WS_FP + (c * 8 + j) * 128, tag, lane, cPQ);
    }
    KV A0, A1, A2, A3;
    A3.v1 = (bf16x8){};
    f32x16 O[4] = {zero16, zero16, zero16, zero16};
    f32x16 Sa, Sb = zero16; u32x4 Pa, Pb = {};
    PIN(O[0]); PIN(O[1]); PIN(O[2]); PIN(O[3]); PIN(Sb); PIN(Pb); PIN(A3.v1);
    STAMP(2);
    poll(ws, WS_FP + (lane < 8 ? (wave * 8 + lane) * 8 + 7 : c * 8 + ((56 * j + (lane & 1) * 55) >> 6)) * 128, tag);
    STAMP(3);
    const __amdgpu_buffer_rsrc_t rs_kv = __builtin_amdgcn_make_buffer_rsrc(ws + WS_KV + wave * 65536, 0, 65536, 0x00020000);
#define LDKV(B, T) do { const unsigned so_ = (((T) + loc) & 15) * 4096; \
                        B.k0 = BF(BLD(rs_kv, lo16, so_)); B.k1 = BF(BLD(rs_kv, lo16, so_ + 1024)); \
                        B.v0 = BF(BLD(rs_kv, lo16, so_ + 2048)); B.v1 = BF(BLD(rs_kv, lo16, so_ + 3072)); } while (0)
#define LDK(B, T) do { const unsigned so_ = (((T) + loc) & 15) * 4096; B.k0 = BF(BLD(rs_kv, lo16, so_)); B.k1 = BF(BLD(rs_kv, lo16, so_ + 1024)); } while (0)
#define LDV(B, T) do { const unsigned so_ = (((T) + loc) & 15) * 4096; B.v0 = BF(BLD(rs_kv, lo16, so_ + 2048)); B.v1 = BF(BLD(rs_kv, lo16, so_ + 3072)); } while (0)
    bf16x8 Q0[4], Q1[4];
    u32x2 qp[4][4];
    const unsigned q_e = r32 * 16 + hi * 8, q_o = r32 * 16 + (hi ? 512 : 8);
#define LDQ(i) _Pragma("unroll") for (int u = 0; u < 4; ++u) qp[i][u] = __builtin_amdgcn_raw_buffer_load_b64(rs_ws, (((wave ^ (3 - (i))) & 1) ? q_o : q_e) + u * 512, \
                 WS_Q + ((2 * c + ((wave ^ (3 - (i))) >> 1)) * 56 + 7 * j + 3 * ((wave ^ (3 - (i))) & 1)) * 512, SC1)
    LDQ(0); LDKV(A0, 0); LDQ(1); LDQ(2); LDQ(3); LDKV(A1, 1); LDKV(A2, 2); LDK(A3, 3);
    SBAR();
#pragma unroll
    for (int i = 0; i < 4; ++i) { Q0[i] = BF(((u32x4){qp[i][0][0], qp[i][0][1], qp[i][1][0], qp[i][1][1]}));
      Q1[i] = BF(((u32x4){qp[i][2][0], qp[i][2][1], hi ? 0u : qp[i][3][0], hi ? 0u : qp[i][3][1]})); }
    Sa = MFMA(A0.k0, Q0[0], zero16); Sa = MFMA(A0.k1, Q1[0], Sa);
    STAMP(9);
#define UNIT(S, Sn, KN, QN, VP, OP, VC, OC, X) do { \
      Pb[2] = pk(Sn[12], Sn[13]); Pb[3] = pk(Sn[14], Sn[15]); \
      if ((QN) >= 0) Sn = MFMA(KN.k0, Q0[(QN) & 3], zero16); S[0] = EX(S[0]); S[1] = EX(S[1]); S[2] = EX(S[2]); S[3] = EX(S[3]); SBAR(); \
      if ((QN) >= 0) Sn = MFMA(KN.k1, Q1[(QN) & 3], Sn); Pa[0] = pk(S[0], S[1]); Pa[1] = pk(S[2], S[3]); S[4] = EX(S[4]); S[5] = EX(S[5]); S[6] = EX(S[6]); S[7] = EX(S[7]); SBAR(); \
      X; SBAR(); \
      OP = MFMA(VP.v1, BF(Pb), OP); Pa[2] = pk(S[4], S[5]); Pa[3] = pk(S[6], S[7]); S[8] = EX(S[8]); S[9] = EX(S[9]); S[10] = EX(S[10]); S[11] = EX(S[11]); SBAR(); \
      OC = MFMA(VC.v0, BF(Pa), OC); Pb[0] = pk(S[8], S[9]); Pb[1] = pk(S[10], S[11]); S[12] = EX(S[12]); S[13] = EX(S[13]); S[14] = EX(S[14]); S[15] = EX(S[15]); SBAR(); \
    } while (0)
#define TILE(CUR, NXT, PRV, T, LD) do { \
      UNIT(Sa, Sb, CUR, 1, PRV, O[3], CUR, O[0], ); \
      LDV(PRV, (T) + 3); SBAR(); \
      UNIT(Sb, Sa, CUR, 2, CUR, O[0], CUR, O[1], ); \
      UNIT(Sa, Sb, CUR, 3, CUR, O[1], CUR, O[2], if (LD) LDK(CUR, (T) + 4)); \
      UNIT(Sb, Sa, NXT, 0, CUR, O[2], CUR, O[3], ); } while (0)
#pragma unroll 1
    for (int t = 0; t < 12; t += 4) {
      TILE(A0, A1, A3, t, 1); TILE(A1, A2, A0, t + 1, 1); TILE(A2, A3, A1, t + 2, 1); TILE(A3, A0, A2, t + 3, 1);
    }
    TILE(A0, A1, A3, 12, 0);
    const lptr oc = lds + L_OC + lo16;
#define OUTP(T, I) _Pragma("unroll") for (int q4 = 0; q4 < 4; ++q4) *(LAS f32x4*)(oc + ((wave ^ I) * 3 + I - 1) * 4096 + q4 * 1024) = (f32x4){T[4 * q4], T[4 * q4 + 1], T[4 * q4 + 2], T[4 * q4 + 3]}
    f32x4 pv[12];
    UNIT(Sa, Sb, A1, 1, A0, O[3], A1, O[0], ); UNIT(Sb, Sa, A1, 2, A1, O[0], A1, O[1], ); UNIT(Sa, Sb, A2, 0, A1, O[1], A1, O[2], );
    UNIT(Sb, Sa, A2, 1, A1, O[2], A2, O[0], ); UNIT(Sa, Sb, A2, 2, A2, O[0], A2, O[1], ); UNIT(Sb, Sa, A3, 0, A2, O[1], A2, O[2], );
    UNIT(Sa, Sb, A3, 1, A2, O[2], A3, O[0], ); UNIT(Sb, Sa, A3, 2, A3, O[0], A3, O[1], lds_wait(j < 7 ? cCR : cPB, 4));
    UNIT(Sa, Sb, A1, 3, A3, O[1], A3, O[2], OUTP(O[0], 3));
    UNIT(Sb, Sa, A2, 3, A3, O[2], A1, O[3], OUTP(O[1], 2));
    UNIT(Sa, Sb, A3, 3, A1, O[3], A2, O[3], OUTP(O[2], 1); lds_signal(cBB));
    UNIT(Sb, Sa, A3, -1, A2, O[3], A3, O[3], lds_wait(cBB, 4); _Pragma("unroll") for (int s2 = 0; s2 < 12; ++s2) pv[s2] = *(LAS f32x4*)(oc + wave * 12288 + s2 * 1024));
    Pb[2] = pk(Sb[12], Sb[13]); Pb[3] = pk(Sb[14], Sb[15]);
    O[3] = MFMA(A3.v1, BF(Pb), O[3]);
    STAMP(4);
    __builtin_amdgcn_s_setprio(2);
    lds_signal(cGO + 4 * wave);
#pragma unroll
    for (int s2 = 0; s2 < 4; ++s2) pv[s2] = pv[s2] + pv[s2 + 4] + pv[s2 + 8];
    STAMP(10);
    unsigned lo16b = lo16; PIN(lo16b);
    const int lane = lo16b >> 4, r32 = lane & 31, hi = lane >> 5, g4 = lane >> 4, tq = (lane & 15) >> 2, tp = lane & 3;
    f32x16 o = O[3];
#pragma unroll
    for (int r = 0; r < 16; ++r) o[r] += pv[r >> 2][r & 3];
    {
      float l = swap_hi(o[12]);
      float am = 0.f;
#pragma unroll
      for (int r = 0; r < 16; r += 2) am = __builtin_fmaxf(__builtin_fmaxf(am, __builtin_fabsf(o[r])), __builtin_fabsf(o[r + 1]));
      const bool ok = (l >= 0x1p-90f) && (l <= 0x1p110f) && (am <= 0x1p126f);
      const bool slow = !wall(ok);
      if (slow) {
        const __amdgpu_buffer_rsrc_t rk = __builtin_amdgcn_make_buffer_rsrc(ws + WS_KV, 0, 262144, 0x00020000);
        poll(ws, WS_FP + (r32 * 8 + 7) * 128, tag);
        float m = -__builtin_inff();
#pragma unroll 1
        for (int t = 0; t < 64; ++t) {
          f32x16 s = MFMA(BF(BLD(rk, lo16, t * 4096)), Q0[3], zero16); s = MFMA(BF(BLD(rk, lo16 + 1024, t * 4096)), Q1[3], s);
#pragma unroll
          for (int r = 0; r < 16; ++r) m = __builtin_fmaxf(m, s[r]);
        }
        m = __builtin_fmaxf(swap_lo(m), swap_hi(m));
        o = zero16;
#pragma unroll 1
        for (int t = 0; t < 64; ++t) {
          f32x16 s = MFMA(BF(BLD(rk, lo16, t * 4096)), Q0[3], zero16); s = MFMA(BF(BLD(rk, lo16 + 1024, t * 4096)), Q1[3], s);
#pragma unroll
          for (int r = 0; r < 16; ++r) s[r] = EX(s[r] - m);
          o = MFMA(BF(BLD(rk, lo16 + 2048, t * 4096)), pack8(s, 0), o); o = MFMA(BF(BLD(rk, lo16 + 3072, t * 4096)), pack8(s, 1), o);
        }
        l = swap_hi(o[12]);
      }
      const float inv = __builtin_amdgcn_rcpf(l);
#pragma unroll
      for (int r = 0; r < 16; ++r) o[r] *= inv;
      if (hi) { o[12] = 0.f; o[13] = 0.f; o[14] = 0.f; o[15] = 0.f; }
      u32x4 f0 = U4(pack8(o, 0)), f1 = U4(pack8(o, 1));
      if (slow) {
#pragma unroll
        for (int k = 0; k < 4; ++k) { f0[k] = umin(f0[k], 0xfffffffeu); f1[k] = umin(f1[k], 0xfffffffeu); }
      }
      BST(f0, rs_ws, lo16, o_own); BST(f1, rs_ws, lo16 + 1024, o_own);
    }
    lds_signal(cOC);
    __builtin_amdgcn_s_setprio(0);
    STAMP(5);
    if (j == 7) { STAMP_OUT(); return; }
    {
      const CAS Params* kp = KARGS();
      const __amdgpu_buffer_rsrc_t rs_out = __builtin_amdgcn_make_buffer_rsrc(kp->out, 0, 2048 * 896 * 4, 0x00020000);
      lds_wait(cWO, 4);
      u32x4 of[16];
      const unsigned ob = WS_O + (((2 * c + th) * 16 + 8 * chh) * 2) * 1024;
#define LDO(k) of[k] = BLD(rs_ws, lo16 + ((k) & 3) * 1024, ob + ((k) >> 2) * 4096)
      const int rowb = 224 * chh + 4 * (g4 >> 1) + tq;
      const unsigned wb = (L_WO + rowb * 128 + ((32 * (g4 & 1) + 8 * tp) ^ ((tq & 2) << 5))) ^ (chh << 6);
      bf16x8 w0[16], w1[16];
#pragma unroll
      for (int k = 0; k < 16; ++k) w1[k] = ldstr2(lds + (wb ^ 64) + (28 * (k >> 1) + 16 * (k & 1)) * 128, 1024);
      SBAR();
      {
        unsigned have = 0;
#pragma unroll 1
        for (unsigned s = 0; s < SPIN_CAP; ++s) {
          const unsigned w = __builtin_amdgcn_raw_buffer_load_b32(rs_ws, (lane & 7) * 2048 + 2044, ob, SC1);
          const unsigned rdy = (s + 1 < SPIN_CAP ? (unsigned)__builtin_amdgcn_ballot_w64(w != ~0u) : ~0u) & 0xffu & ~have;
#pragma unroll
          for (int h = 0; h < 8; ++h) if (rdy >> h & 1) { LDO(2 * h); LDO(2 * h + 1); }
          have |= rdy;
          if (have == 0xffu) break;
          __builtin_amdgcn_s_sleep(2);
        }
      }
      __builtin_amdgcn_s_setprio(2);
      f32x16 acc0 = zero16, acc1 = zero16;
#define OBAD(h) (!wall(umax(umax(umax(umax(of[2 * (h)][0], of[2 * (h)][1]), umax(of[2 * (h)][2], of[2 * (h)][3])), umax(of[2 * (h) + 1][0], of[2 * (h) + 1][1])), umax(of[2 * (h) + 1][2], of[2 * (h) + 1][3])) != ~0u))
#define OHEAD(h) do { \
        if (OBAD(h)) { \
          _Pragma("unroll 1") for (unsigned s = 0; s < SPIN_CAP; ++s) { LDO(2 * h); LDO(2 * h + 1); if (!OBAD(h)) break; __builtin_amdgcn_s_sleep(2); } \
        } \
        acc1 = MFMA(BF(of[2 * h]), w1[2 * h], acc1); acc1 = MFMA(BF(of[2 * h + 1]), w1[2 * h + 1], acc1); PIN(acc1); \
        w0[2 * h] = ldstr2(lds + wb + 28 * h * 128, 1024); w0[2 * h + 1] = ldstr2(lds + wb + (28 * h + 16) * 128, 1024); } while (0)
      OHEAD(0); STAMP(6); OHEAD(1); OHEAD(2); OHEAD(3); OHEAD(4); OHEAD(5); OHEAD(6); OHEAD(7);
      STAMP(11);
      lds_wait(cOC, 4);
#pragma unroll
      for (int k = 0; k < 4; ++k) acc0 = MFMA(BF(of[k]), w0[k], acc0);
      SBAR();
      const lptr tw = lds + L_OC + hi * 512 + r32 * 4;
#pragma unroll
      for (int r = 0; r < 16; ++r) *(LAS float*)(tw + (wave ^ 1) * 4096 + ((r & 3) + 8 * (r >> 2)) * 128) = acc1[r];
      SBAR();
#pragma unroll
      for (int k = 4; k < 8; ++k) acc0 = MFMA(BF(of[k]), w0[k], acc0);
      PIN(acc0);
      lds_signal(cPR + 4 * th);
#pragma unroll
      for (int k = 8; k < 16; ++k) acc0 = MFMA(BF(of[k]), w0[k], acc0);
      PIN(acc0);
      lds_wait(cPR + 4 * th, 2);
      f32x4 pa[4];
#pragma unroll
      for (int k = 0; k < 4; ++k) pa[k] = *(LAS f32x4*)(lds + L_OC + wave * 4096 + k * 1024 + lo16);
#pragma unroll
      for (int r = 0; r < 16; ++r) *(LAS float*)(tw + 16384 + wave * 4096 + ((r & 3) + 8 * (r >> 2)) * 128) = acc0[r];
      MEMBAR();
      STAMP(12);
      const unsigned oo = ((64 * c + 32 * th + (lane >> 3)) * 896 + 448 + 64 * j + 32 * chh + (lane & 7) * 4) * 4;
#pragma unroll
      for (int k = 0; k < 4; ++k)
        __builtin_amdgcn_raw_buffer_store_b128(U4(pa[k] + *(LAS f32x4*)(lds + L_OC + 16384 + wave * 4096 + k * 1024 + lo16)), rs_out, oo + k * 28672, 0, 17);
    }
    STAMP(7);
    STAMP_OUT();
  } else {
    if (j == 7) {
      VMW(6); BAR(); VMW(0); BAR();
      lds_wait_<4>(cPQ, 4);
      { const u32x2 t2 = *(LAS u32x2*)(lds + L_TAG);
        tag = ((u64)(unsigned)__builtin_amdgcn_readfirstlane(t2[1]) << 32) | (unsigned)__builtin_amdgcn_readfirstlane(t2[0]); }
      __builtin_amdgcn_s_sleep(64);
      MARK();
      publish4(ws, WS_FH + (c * 8 + 7) * 128, tag, lane, cPB);
      if (c == 0 && wa == 0) {
        __builtin_amdgcn_s_sleep(127);
#pragma unroll 1
        for (int k = 0; k < 4; ++k) poll(ws, WS_FP + (k * 64 + lane) * 128, tag);
        if (lane == 0) __hip_atomic_store((GAS u64*)(ws + WS_EPOCH), tag, RLX_AGENT);
      }
      STAMP_OUT();
      return;
    }
    VMW(6); BAR(); LDCH(L_SA); VMW(6); BAR();
    cf lam, coef, l16, l64; f32x16 ur = zero16, ui = zero16;
    f32x16 acc[4] = {zero16, zero16, zero16, zero16};
    const unsigned cs0 = wa * 16384u, cs1 = L_R1 + wa * 12288u;
    unsigned a_htw = L_HT + wa * 4096 + r32 * 64 + hi * 32, a_htr = L_HT + wa * 4096 + (8 * (g4 >> 1) + tq) * 64 + (g4 & 1) * 32 + tp * 8;
    unsigned a_hst = WS_H + ((2 * c + th) * 56 + 4 * j + 2 * chh) * 1024 + lo16;
    unsigned a_c[4], a_cn[2], a_x[2], a_ex, e_want; u32x4 dg[2]; cf l256, lw, lo_;
    const int seg = 2 * th + hi;
#pragma unroll
    for (int f = 0; f < 4; ++f) a_c[f] = cs0 + (32 * chh + r32) * 128 + (((2 * f + hi) ^ xsw) << 4);
#pragma unroll
    for (int f = 0; f < 2; ++f) a_cn[f] = cs1 + 8192 + (32 * chh + r32) * 64 + (((2 * f + hi) ^ ((r32 >> 2) & 3)) << 4);
    unsigned a_vh = (lane >> 2) * 896 + (((lane & 3) ^ ((lane >> 4) & 3)) << 4) + 384 + chh * 64, a_vo = ve ^ 64;
    unsigned a_vw = lrow * 896 + (((lane & 7) ^ ((lrow & 2) << 1)) << 4);
    unsigned a_fl = WS_FH + (c * 8 + (lane & 7)) * 128;
    unsigned a_el = (31 - umin(r32, c - 1)) * 512 + 4, a_eo = (32 - c + 2 * th + hi) * 512 + r32 * 16, a_hx = L_EX + 2048 + (32 * chh + r32) * 8;
    {
      LDCH(0); LDCH(CH);
      { const f32x4 c0 = *(LAS f32x4*)(lds + L_EX + (32 * chh + r32) * 16), c1 = *(LAS f32x4*)(lds + L_EX + 1024 + (32 * chh + r32) * 16);
        lam = {c0[0], c0[1]}; coef = {c0[2], c0[3]}; l16 = {c1[0], c1[1]}; l64 = {c1[2], c1[3]}; ud = *(LAS unsigned*)(lds + L_EX + 2048 + (32 * chh + r32) * 4); }
      { const int e = r32 - 8 * hi; const unsigned dv = ud << ((e & 1) * 16);
#pragma unroll
        for (int s2 = 0; s2 < 2; ++s2) {
          dg[s2] = (u32x4){(e >> 1) == 8 * s2 ? dv : 0u, (e >> 1) == 8 * s2 + 1 ? dv : 0u, (e >> 1) == 8 * s2 + 2 ? dv : 0u, (e >> 1) == 8 * s2 + 3 ? dv : 0u};
          a_x[s2] = L_SA + (32 * th + r32) * 128 + (((4 * chh + 2 * s2 + hi) ^ xsw) << 4); PIN(dg[s2]); PIN(a_x[s2]); }
        cf lseg = hi ? l16 : (cf){1.f, 0.f};
        if (th) lseg = cmul(lseg, cmul(l16, l16));
        const cf l128 = cmul(l64, l64), ls128 = cmul(lseg, l128);
        l256 = cmul(l128, l128); lw = th ? ls128 : lseg; lo_ = th ? lseg : ls128; e_want = (c - seg + 3) >> 2; a_ex = L_EX + (32 * chh + r32) * 8;
        PIN(l256.r); PIN(l256.i); PIN(lw.r); PIN(lw.i); PIN(lo_.r); PIN(lo_.i); PIN(e_want); PIN(a_ex); }
      PIN(a_htw); PIN(a_htr); PIN(a_hst); PIN(a_c[0]); PIN(a_c[1]); PIN(a_c[2]); PIN(a_c[3]); PIN(a_cn[0]); PIN(a_cn[1]); PIN(a_vh); PIN(a_vw); PIN(a_fl); PIN(a_el); PIN(a_eo); PIN(a_hx); PIN(a_vo);
      const int trow = 32 * th + 16 * ((r32 >> 2) & 1) + (r32 & 3) + 4 * (r32 >> 3);
      const unsigned xa_off = trow * 128, xa_sw = (trow >> 1) & 7, b_off = 8192 + (32 * chh + r32) * 128;
      bf16x8 gax[4], gar[4], gai[4], gbx[4], gbr[4], gbi[4];
      unsigned gx[3][4], gw[3][4];
#define GB(s_) ((s_) < 49152 ? 0u : (s_) < 110592 ? 49152u : 110592u)
#define GT(s_) ((s_) < 49152 ? 0 : (s_) < 110592 ? 1 : 2)
#pragma unroll
      for (int t = 0; t < 3; ++t)
#pragma unroll
        for (int ks = 0; ks < 4; ++ks) { gx[t][ks] = GB(t * 60000) + xa_off + (((2 * ks + hi) ^ xa_sw) << 4); gw[t][ks] = GB(t * 60000) + b_off + (((2 * ks + hi) ^ xsw) << 4); PIN(gx[t][ks]); PIN(gw[t][ks]); }
#define GRD(B, s_) _Pragma("unroll") for (int ks = 0; ks < 4; ++ks) { B##x[ks] = ldsf(lds + gx[GT(s_)][ks] + ((s_) - GB(s_))); \
        B##r[ks] = ldsf(lds + gw[GT(s_)][ks] + ((s_) - GB(s_))); B##i[ks] = ldsf(lds + gw[GT(s_)][ks] + ((s_) - GB(s_) + 8192)); } SBAR()
#define GMM(B) _Pragma("unroll") for (int ks = 0; ks < 4; ++ks) { ur = MFMA(B##x[ks], B##r[ks], ur); ui = MFMA(B##x[ks], B##i[ks], ui); } PIN(ur); PIN(ui); SBAR()
#define GAP() do { SBAR(); asm volatile("s_nop %0" :: "i"(7)); SBAR(); } while (0)
#define GMS(B) _Pragma("unroll") for (int ks = 0; ks < 4; ++ks) { ur = MFMA(B##x[ks], B##r[ks], ur); GAP(); ui = MFMA(B##x[ks], B##i[ks], ui); GAP(); } PIN(ur); PIN(ui); SBAR()
      __builtin_amdgcn_s_setprio(1);
      STAMP(12);
      lds_wait_<1>(cPQ, 4);
      LDCH(2 * CH); LDCH(3 * CH); LDCH(4 * CH);
      VMW(18); lds_barrierA(cAB, 4); GRD(ga, L_SA); GRD(gb, 0); GMM(ga); GRD(ga, CH); GMM(gb);
      VMW(6); lds_barrierA(cAB, 8);
      STAMP(13);
      GRD(gb, 2 * CH); LDCH(L_SA); GMM(ga); GRD(ga, 3 * CH); GMS(gb);
      VMW(6); lds_barrierA(cAB, 12);
      STAMP(14);
      GRD(gb, 4 * CH); GMS(ga);
      VMW(0); lds_barrierA(cAB, 16);
      STAMP(15);
      GRD(ga, L_SA); GMS(gb); GMS(ga);
      { const u32x2 t2 = *(LAS u32x2*)(lds + L_TAG);
        tag = ((u64)(unsigned)__builtin_amdgcn_readfirstlane(t2[1]) << 32) | (unsigned)__builtin_amdgcn_readfirstlane(t2[0]); }
    }
    acc[0] = MFMA(ldsf(lds + a_x[0]), BF(dg[0]), acc[0]); acc[0] = MFMA(ldsf(lds + a_x[1]), BF(dg[1]), acc[0]);
    PIN(acc[0]);
    STAMP(1);
    cf e = {0.f, 0.f};
#pragma unroll
    for (int r = 0; r < 16; ++r) {
      const float a = coef.r * ur[r] - coef.i * ui[r], b2 = -(coef.r * ui[r] + coef.i * ur[r]);
      ur[r] = a; ui[r] = b2; e = r ? cfma(lam, e, (cf){a, b2}) : (cf){a, b2};
    }
    *(LAS f32x2*)(lds + a_ex + th * 1024 + hi * 512) = (f32x2){e.r, e.i};
    __builtin_amdgcn_s_setprio(0);
    lds_barrierA(cAB, 20);
    cf g0;
    {
      const f32x2 e0 = *(LAS f32x2*)(lds + a_ex), e1 = *(LAS f32x2*)(lds + a_ex + 512), e2 = *(LAS f32x2*)(lds + a_ex + 1024), e3 = *(LAS f32x2*)(lds + a_ex + 1536);
      __builtin_amdgcn_s_setprio(1);
      const cf E2 = cfma(l16, (cf){e0[0], e0[1]}, (cf){e1[0], e1[1]}), E3 = cfma(l16, E2, (cf){e2[0], e2[1]}), E = cfma(l16, E3, (cf){e3[0], e3[1]});
      if (th) g0 = hi ? E3 : E2; else g0 = hi ? (cf){e0[0], e0[1]} : (cf){0.f, 0.f};
      PIN(g0.r); PIN(g0.i);
      if (th == 0 && hi == 0 && c < 31)
        BST(((u32x4){f2u(E.r), (unsigned)tag, f2u(E.i), (unsigned)tag}), rs_ws, r32 * 16, WS_E + ((2 * j + chh) * 32 + 31 - c) * 512);
    }
    __builtin_amdgcn_s_setprio(0);
    const CAS Params* kp = KARGS();
    {
      const u32x4 rc = mkrs((const char*)(th ? kp->cim : kp->cre) + (size_t)j * 57344, 57344);
      int n2 = (6 * cl + 2 * ((b & 7) % 3)) % 24;
#pragma unroll
      for (int n = 0; n < 24; ++n) {
        DMA(rc, (n & 1) ? a_vo : ve, (n2 & 7) * 7168 + (n2 >> 3) * 128 + chh * 512, (n2 < 16 ? cs0 : cs1 - 16384) + n2 * 1024, 0);
        n2 = n2 == 23 ? 0 : n2 + 1; }
#pragma unroll
      for (int n = 0; n < 4; ++n) DMA(rc, a_vh, n * 14336, cs1, 8192 + n * 1024);
    }
    STAMP(2);
    cf G = g0;
    if (c > 0) {
      const __amdgpu_buffer_rsrc_t re = __builtin_amdgcn_make_buffer_rsrc(ws + WS_E + (2 * j + chh) * 16384, 0, 16384, 0x00020000);
      u32x4 ev[8];
#pragma unroll 1
      for (unsigned s = 0; s < SPIN_CAP; ++s) {
        const unsigned t1 = __builtin_amdgcn_raw_buffer_load_b32(re, a_el, 0, SC1);
        if (wall(t1 == (unsigned)tag)) break;
        __builtin_amdgcn_s_sleep(2);
      }
      MEMBAR();
#pragma unroll 1
      for (unsigned s = 0; s < SPIN_CAP; ++s) {
        unsigned got = 0;
#pragma unroll
        for (int m = 0; m < 8; ++m) ev[m] = BLD(re, a_eo, m * 2048);
        VMW(0);
        __builtin_amdgcn_s_setprio(1);
#pragma unroll
        for (int m = 0; m < 8; ++m) got += ((ev[m][1] ^ (unsigned)tag) | (ev[m][3] ^ (unsigned)tag)) == 0;
        if (wall(got == e_want)) break;
        __builtin_amdgcn_s_setprio(0);
        __builtin_amdgcn_s_sleep(4);
      }
      STAMP(3);
      cf H = {u2f(ev[7][0]), u2f(ev[7][2])};
#pragma unroll
      for (int m = 6; m >= 0; --m) H = cfma(l256, H, (cf){u2f(ev[m][0]), u2f(ev[m][2])});
      H = cfma(l64, (cf){swap_hi(H.r), swap_hi(H.i)}, (cf){swap_lo(H.r), swap_lo(H.i)});
      *(LAS f32x2*)(lds + a_hx + th * 512) = (f32x2){H.r, H.i};
      G = cfma(lw, H, g0);
      __builtin_amdgcn_s_setprio(0);
      lds_barrierA(cPE + 4 * chh, 2);
      const f32x2 ho = *(LAS f32x2*)(lds + a_hx + (th ^ 1) * 512);
      __builtin_amdgcn_s_setprio(1);
      G = cfma(lo_, (cf){ho[0], ho[1]}, G);
    }
    STAMP(4);
    __builtin_amdgcn_s_setprio(1);
    {
#pragma unroll
      for (int r = 0; r < 16; ++r) { G = cfma(lam, G, (cf){ur[r], ui[r]}); ur[r] = G.r; ui[r] = G.i; }
      *(LAS bf16x8*)(lds + a_htw) = pack8(ur, 0); *(LAS bf16x8*)(lds + a_htw + 16) = pack8(ur, 1);
      *(LAS bf16x8*)(lds + a_htw + 2048) = pack8(ui, 0); *(LAS bf16x8*)(lds + a_htw + 2064) = pack8(ui, 1);
      unsigned a_hs2 = a_hst + 28 * 1024; PIN(a_hs2);
      __builtin_amdgcn_s_setprio(0);
      asm volatile("s_waitcnt lgkmcnt(0)" ::: "memory");
#pragma unroll
      for (int k = 0; k < 4; ++k) BST(U4(ldstr2(lds + a_htr + (k >> 1) * 2048 + (k & 1) * 1024, 256)), rs_ws, (k >> 1) ? a_hs2 : a_hst, (k & 1) * 1024);
      MARK();
      publish4(ws, WS_FH + (c * 8 + j) * 128, tag, lane, cPB);
    }
    STAMP(5);
    bf16x8 cfr[14][2];
    const int cflip = chh ? -1 : 1;
#define LDC(f) do { if ((f) < 12) { const unsigned a_ = a_c[(f) & 3] + ((f) < 8 ? ((f) >> 2) * 8192 : cs1 - cs0); cfr[f][0] = ldsf(lds + a_); cfr[f][1] = ldsf(lds + a_ + cflip * 4096); } \
                    else { cfr[f][0] = ldsf(lds + a_cn[(f) & 1]); cfr[f][1] = ldsf(lds + a_cn[(f) & 1] + cflip * 2048); } } while (0)
#pragma unroll
    for (int f = 0; f < 8; ++f) LDC(f);
    lds_signal(cCR);
    STAMP(10);
    lds_wait_<1>(cCR, 4);
    {
      const u32x4 rw = mkrs((const char*)kp->wo + j * 128 + wa * 7168, 401408 - j * 128 - wa * 7168);
      int m2 = (4 * cl + (b & 7)) % 14;
#pragma unroll
      for (int m = 0; m < 14; ++m) { DMA(rw, a_vw, m2 * 28672, wa * 1024 + m2 * 4096, L_WO); m2 = m2 == 13 ? 0 : m2 + 1; }
      if (wa == 0 && lane < 32) *(LAS u32x4*)(lds + L_WO + 57344 + lo16) = (u32x4){0, 0, 0, 0};
    }
    poll(ws, a_fl, tag);
    VMW(0);
    lds_signal(cWO);
    STAMP(6);
    {
      const unsigned hb = WS_H + (2 * c * 56 + 14 * wa) * 1024, h0 = hb + th * 57344, h1 = hb + (th ^ 1) * 57344;
      const unsigned fa = chh * 2048, fb = chh ? 0u - 12288u : 0u;
      u32x4 hf[14][2];
#define LDH(f) do { hf[f][0] = BLD(rs_ws, lo16, h0 + (f) * 1024 + ((f) < 12 ? fa : fb)); hf[f][1] = BLD(rs_ws, lo16, h1 + (f) * 1024 + ((f) < 12 ? fa : fb)); } while (0)
#define MMH(f) do { acc[0] = MFMA(BF(hf[f][0]), cfr[f][0], acc[0]); acc[1] = MFMA(BF(hf[f][0]), cfr[f][1], acc[1]); \
                    acc[2] = MFMA(BF(hf[f][1]), cfr[f][0], acc[2]); acc[3] = MFMA(BF(hf[f][1]), cfr[f][1], acc[3]); \
                    PIN(acc[0]); PIN(acc[1]); PIN(acc[2]); PIN(acc[3]); } while (0)
#pragma unroll
      for (int f = 0; f < 14; ++f) LDH(f);
      SBAR();
      lds_wait_<4>(cGO + 4 * wa, 1);
      STAMP(9);
      __builtin_amdgcn_s_setprio(1);
#pragma unroll
      for (int f = 0; f < 8; ++f) { MMH(f); SBAR(); if (f >= 1 && f < 7) LDC(f + 7); SBAR(); }
      lds_signal(cP4);
#pragma unroll
      for (int f = 8; f < 14; ++f) MMH(f);
    }
    lds_wait(cP4, 4);
    STAMP(11);
    rs_write(lds + L_G2, wa, lane, acc[1], acc[2], acc[3]);
    lds_barrierA(cAB, 24);
    f32x16 y = rs_read(lds + L_G2, wa, lane, acc[0]);
    const __amdgpu_buffer_rsrc_t rs_out = __builtin_amdgcn_make_buffer_rsrc(kp->out, 0, 2048 * 896 * 4, 0x00020000);
    MEMBAR();
#pragma unroll
    for (int r = 0; r < 16; ++r) *(LAS float*)(lds + L_G2 + wa * 12288 + hi * 512 + r32 * 4 + ((r & 3) + 8 * (r >> 2)) * 128) = y[r];
    MEMBAR();
    const unsigned oo = ((64 * c + 32 * th + (lane >> 3)) * 896 + 64 * j + 32 * chh + (lane & 7) * 4) * 4;
#pragma unroll
    for (int k = 0; k < 4; ++k) __builtin_amdgcn_raw_buffer_store_b128(*(LAS u32x4*)(lds + L_G2 + wa * 12288 + k * 1024 + lo16), rs_out, oo + k * 28672, 0, 17);
    STAMP(7);
    STAMP_OUT();
  }
}

extern "C" void kernel_launch(void* const* d_in, const int* in_sizes, int n_in, void* d_out, int out_size, void* d_ws, size_t ws_size,
                              hipStream_t stream) {
  Params p;
  p.xa = (const u16*)d_in[0]; p.lre = (const u16*)d_in[1]; p.lim = (const u16*)d_in[2]; p.bre = (const u16*)d_in[3]; p.bim = (const u16*)d_in[4];
  p.cre = (const u16*)d_in[5]; p.cim = (const u16*)d_in[6]; p.ldt = (const u16*)d_in[7]; p.dsk = (const u16*)d_in[8];
  p.xb = (const u16*)d_in[9]; p.wq = (const u16*)d_in[10]; p.wk = (const u16*)d_in[11]; p.wv = (const u16*)d_in[12]; p.wo = (const u16*)d_in[13];
  p.out = (float*)d_out; p.ws = (char*)d_ws;
  fused_kernel<<<256, 512, 0, stream>>>(p);
}
```

```cpp
#include <hip/hip_runtime.h>
#include <stdint.h>

#define DI __device__ __forceinline__
#define GAS __attribute__((address_space(1)))
#define LAS __attribute__((address_space(3)))
#define CAS __attribute__((address_space(4)))
typedef short bf16x8 __attribute__((ext_vector_type(8)));
typedef short s16x4 __attribute__((ext_vector_type(4)));
typedef float f32x16 __attribute__((ext_vector_type(16)));
typedef float f32x4 __attribute__((ext_vector_type(4)));
typedef float f32x2 __attribute__((ext_vector_type(2)));
typedef unsigned u32x4 __attribute__((ext_vector_type(4)));
typedef unsigned u32x2 __attribute__((ext_vector_type(2)));
typedef __bf16 bf16x2_t __attribute__((ext_vector_type(2)));
typedef unsigned long long u64;
typedef unsigned short u16;
typedef LAS char* lptr;
#define RLX_AGENT __ATOMIC_RELAXED, __HIP_MEMORY_SCOPE_AGENT
#define MFMA(a, b, c) __builtin_amdgcn_mfma_f32_32x32x16_bf16((a), (b), (c), 0, 0, 0)
#define SBAR() __builtin_amdgcn_sched_barrier(0)
#define PIN(x) asm volatile("" : "+v"(x))
#define MEMBAR() asm volatile("" ::: "memory")
#define SC1 16

constexpr unsigned WS_EPOCH = 0, WS_FH = 8192, WS_E = 0x10000, WS_FP = 0x48000,
                   WS_KV = 0x50000, WS_O = 0x100000, WS_H = 0x300000, WS_Q = 0x680000, WS_END = 0x880000;
constexpr unsigned SK = 16384, K_W = 8192;
constexpr unsigned CH = 24576, L_SA = 135168;
constexpr unsigned L_R1 = 107520, L_HT = 65536, L_WO = 0, L_OC = 57856, L_G2 = 107520, L_MISC = 159744, L_EX = L_MISC + 128, L_TOTAL = 163840;
constexpr unsigned cBB = L_MISC, cAB = L_MISC + 4, cCR = L_MISC + 8, cWO = L_MISC + 12, cOC = L_MISC + 16, cPB = L_MISC + 20, cPR = L_MISC + 24, cP4 = L_MISC + 32, cPQ = L_MISC + 52, cGO = L_MISC + 36, cPE = L_MISC + 56, L_TAG = L_MISC + 64;
constexpr unsigned SPIN_CAP = 1u << 18;

struct Params {
  const u16 *xb, *wq, *wk, *wv; char* ws; const u16 *lre, *lim, *ldt, *dsk, *xa, *bre, *bim, *cre, *cim, *wo;
  float* out;
};

DI float u2f(unsigned v) { return __builtin_bit_cast(float, v); }
DI unsigned f2u(float v) { return __builtin_bit_cast(unsigned, v); }
DI unsigned umin(unsigned a, unsigned b) { return a < b ? a : b; }
DI unsigned umax(unsigned a, unsigned b) { return a > b ? a : b; }
DI bool wall(bool x) { return __builtin_amdgcn_ballot_w64(!x) == 0; }
DI float bf2f(unsigned v) { return u2f(v << 16); }
DI unsigned pk(float a, float b) { f32x2 v = {a, b}; return __builtin_bit_cast(unsigned, __builtin_convertvector(v, bf16x2_t)); }
DI float rbf(float a) { return u2f(pk(0.f, a)); }
DI bf16x8 pack8(const f32x16& x, int s) {
  u32x4 p = {pk(x[8 * s], x[8 * s + 1]), pk(x[8 * s + 2], x[8 * s + 3]), pk(x[8 * s + 4], x[8 * s + 5]), pk(x[8 * s + 6], x[8 * s + 7])};
  return __builtin_bit_cast(bf16x8, p);
}
DI u32x4 mkrs(const void* base, unsigned bytes) { const u64 a = (u64)base; return (u32x4){(unsigned)a, (unsigned)(a >> 32) & 0xffffu, bytes, 0x20000u}; }
#define DMA(rs, voff, soff, ldsb, IMM) asm volatile("s_add_u32 m0, %3, %4\n\ts_nop 0\n\tbuffer_load_dwordx4 %0, %1, %2 offen lds" \
                                                    :: "v"(voff), "s"(rs), "s"(soff), "s"(ldsb), "i"(IMM) : "memory", "scc")
DI bf16x8 ldsf(lptr p) { return *(LAS bf16x8*)p; }
DI s16x4 ldstr(lptr p) { return __builtin_amdgcn_ds_read_tr16_b64_v4i16((LAS s16x4*)p); }
DI bf16x8 ldstr2(lptr p, int second) { s16x4 lo = ldstr(p), hi = ldstr(p + second); return __builtin_shufflevector(lo, hi, 0, 1, 2, 3, 4, 5, 6, 7); }
DI void lds_signal(unsigned c) {
  asm volatile("s_waitcnt lgkmcnt(0)\n\ts_mov_b64 exec, 1\n\tds_add_u32 %0, %1\n\ts_mov_b64 exec, -1" :: "v"(c), "v"(1u) : "memory");
}
template <int SLEEP> DI void lds_wait_(unsigned c, unsigned target) {
  unsigned v, s, n;
  asm volatile("s_mov_b32 %2, 0x400000\n1:\n\tds_read_b32 %0, %3\n\ts_waitcnt lgkmcnt(0)\n\tv_readfirstlane_b32 %1, %0\n\ts_cmp_ge_u32 %1, %4\n\ts_cbranch_scc1 2f\n\t"
               "s_sleep %5\n\ts_sub_u32 %2, %2, 1\n\ts_cbranch_scc0 1b\n2:" : "=&v"(v), "=&s"(s), "=&s"(n) : "v"(c), "i"(target), "i"(SLEEP) : "memory", "scc");
}
#define lds_wait(c, t) lds_wait_<0>(c, t)
#define lds_barrier(c, t) do { lds_signal(c); lds_wait(c, t); } while (0)
#define lds_barrierA(c, t) do { lds_signal(c); lds_wait_<1>(c, t); } while (0)
DI void poll(const char* ws, unsigned off, u64 tag) {
#pragma unroll 1
  for (unsigned s = 0; s < SPIN_CAP; ++s) {
    u64 v = __hip_atomic_load((GAS u64*)(ws + off), RLX_AGENT);
    if (wall(v == tag)) break;
    __builtin_amdgcn_s_sleep(2);
  }
  MEMBAR();
}
DI void publish4(char* ws, unsigned off, u64 tag, int lane, unsigned cnt) {
  unsigned old;
  asm volatile("s_waitcnt vmcnt(0)\n\ts_mov_b64 exec, 1\n\tds_add_rtn_u32 %0, %1, %2\n\ts_waitcnt lgkmcnt(0)\n\ts_mov_b64 exec, -1" : "=&v"(old) : "v"(cnt), "v"(1u) : "memory");
  if (__builtin_amdgcn_readfirstlane(old) == 3 && lane == 0) __hip_atomic_store((GAS u64*)(ws + off), tag, RLX_AGENT);
}
DI float swap_lo(float x) {
  auto r = __builtin_amdgcn_permlane32_swap(f2u(x), f2u(x), false, false); return u2f(r[0]); }
DI float swap_hi(float x) {
  auto r = __builtin_amdgcn_permlane32_swap(f2u(x), f2u(x), false, false); return u2f(r[1]); }
struct cf { float r, i; };
DI cf cmul(cf a, cf b) { return {a.r * b.r - a.i * b.i, a.r * b.i + a.i * b.r}; }
DI cf cfma(cf a, cf b, cf c) { return {__builtin_fmaf(a.r, b.r, __builtin_fmaf(-a.i, b.i, c.r)), __builtin_fmaf(a.r, b.i, __builtin_fmaf(a.i, b.r, c.i))}; }

struct KV { bf16x8 k0, k1, v0, v1; };

DI void rs_write(lptr base, int w, int lane, const f32x16& t1, const f32x16& t2, const f32x16& t3) {
#define RSW(T, I) _Pragma("unroll") for (int q = 0; q < 4; ++q) *(LAS f32x4*)(base + ((w ^ I) * 3 + I - 1) * 4096 + q * 1024 + lane * 16) = (f32x4){T[4 * q], T[4 * q + 1], T[4 * q + 2], T[4 * q + 3]}
  RSW(t1, 1); RSW(t2, 2); RSW(t3, 3);
}
DI f32x16 rs_read(lptr base, int w, int lane, f32x16 a) {
  f32x4 v[12];
#pragma unroll
  for (int s = 0; s < 12; ++s) v[s] = *(LAS f32x4*)(base + w * 12288 + s * 1024 + lane * 16);
  SBAR();
#pragma unroll
  for (int s = 0; s < 12; ++s) { const int q = s & 3; a[4 * q] += v[s][0]; a[4 * q + 1] += v[s][1]; a[4 * q + 2] += v[s][2]; a[4 * q + 3] += v[s][3]; }
  return a;
}

#define VMW_(n) asm volatile("s_waitcnt vmcnt(" #n ")" ::: "memory")
#define VMW(n) VMW_(n)
#define BAR() asm volatile("s_waitcnt lgkmcnt(0)\n\ts_barrier" ::: "memory")
#define BLD(R, V, S) __builtin_amdgcn_raw_buffer_load_b128(R, V, S, SC1)
#define BST(D, R, V, S) __builtin_amdgcn_raw_buffer_store_b128(D, R, (V) + (S), 0, SC1)
#define BF(x) __builtin_bit_cast(bf16x8, x)
#define U4(x) __builtin_bit_cast(u32x4, x)
#define EX(x) __builtin_amdgcn_exp2f(x)
#define KARGS() ({ const CAS Params* kp_ = (const CAS Params*)__builtin_amdgcn_kernarg_segment_ptr(); asm volatile("" : "+s"(kp_)); kp_; })
#define NEXTK() do { k += step; k = k >= 7 ? k - 7 : k; } while (0)

#if defined(__HIP_DEVICE_COMPILE__)
#define NO_PK __attribute__((target("no-packed-fp32-ops")))
#else
#define NO_PK
#endif
__global__ void __launch_bounds__(512, 2) NO_PK fused_kernel(Params p) {
  __shared__ __attribute__((aligned(128))) char smem[L_TOTAL];
  const lptr lds = (lptr)smem;
  const int tid = threadIdx.x, lane = tid & 63, wave = __builtin_amdgcn_readfirstlane(tid >> 6);
  const int b = blockIdx.x, loc = b >> 3, cl = loc >> 3, c = (b & 7) * 4 + cl, j = loc & 7;
  const int lrow = lane >> 3;
  const unsigned ve = lrow * 896 + (((lane & 7) ^ (lrow >> 1)) << 4);
  const int step = 1 + (cl + (b & 7) * 4) % 6, k0 = j == 7 ? 0 : j;
  const bool isB = wave < 4;
  const int wa = wave & 3;
  char* const ws = p.ws;
  unsigned ua = 0, ub = 0, uc = 0, ud = 0;
  u32x4 rxa, rbr, rbi; int ka = k0;
  const unsigned vo = (ve ^ 64) + 7168, lba = wa * 2048;
#define NEXTKA() do { ka += step; ka = ka >= 7 ? ka - 7 : ka; } while (0)
#define LDCH(slot) do { NEXTKA(); const unsigned k_ = ka * 128; DMA(rxa, ve, k_, lba, (slot)); DMA(rxa, vo, k_, lba, (slot) + 1024); \
      DMA(rbr, ve, k_, lba, (slot) + 8192); DMA(rbr, vo, k_, lba, (slot) + 9216); DMA(rbi, ve, k_, lba, (slot) + 16384); DMA(rbi, vo, k_, lba, (slot) + 17408); } while (0)
  {
    const u32x4 rx = mkrs((const char*)p.xb + (size_t)c * 57344 + wave * 7168, 7168);
    const unsigned vx = ve ^ ((wave & 1) << 6), lb = wave * 1024;
    int k = k0;
    if (j == 7) {
      const u32x4 rw = mkrs(isB ? p.wk : p.wv, 25088);
      const unsigned vw = wa * 1024 + lane * 16, lw = K_W + (isB ? 0 : 3584) + wa * 1024; const bool half = wa < 3 || lane < 32;
#pragma unroll
      for (int i = 0; i < 7; ++i) { NEXTK(); DMA(rx, vx, k * 128, lb, i * SK); if (half) DMA(rw, vw, k * 3584, lw, i * SK); }
    } else {
      const u32x4 rw = mkrs((const char*)p.wq + j * 128 + wave * 7168, 401408 - j * 128 - wave * 7168);
      const unsigned vw = lrow * 896 + (((lane & 7) ^ ((lrow & 2) << 1)) << 4);
      if (wave == 0) { const unsigned co = (64 * j + lane) * 2;
        asm volatile("global_load_ushort %0, %4, %5\n\tglobal_load_ushort %1, %4, %6\n\tglobal_load_ushort %2, %4, %7\n\tglobal_load_ushort %3, %4, %8"
                     : "=&v"(ua), "=&v"(ub), "=&v"(uc), "=&v"(ud) : "v"(co), "s"(p.lre), "s"(p.lim), "s"(p.ldt), "s"(p.dsk) : "memory"); }
#pragma unroll
      for (int i = 0; i < 7; ++i) { NEXTK(); DMA(rx, vx, k * 128, lb, i * SK); DMA(rw, vw, k * 57344, lb, i * SK + K_W); }
      rxa = mkrs((const char*)p.xa + (size_t)c * 57344 + wa * 14336, 14336);
      rbr = mkrs((const char*)p.bre + (size_t)j * 57344 + wa * 14336, 14336);
      rbi = mkrs((const char*)p.bim + (size_t)j * 57344 + wa * 14336, 14336);
    }
  }
  if (tid < 16) ((LAS unsigned*)(lds + L_MISC))[tid] = 0;
  const int r32 = lane & 31, hi = lane >> 5;
  const int g4 = lane >> 4, tq = (lane & 15) >> 2, tp = lane & 3;
  const unsigned xsw = (r32 >> 1) & 7, lo16 = lane * 16;
  const int th = wa >> 1, chh = wa & 1;
  u64 tag;
  const __amdgpu_buffer_rsrc_t rs_ws = __builtin_amdgcn_make_buffer_rsrc(ws, 0, WS_END, 0x00020000);
  const f32x16 zero16 = {};
  const unsigned o_own = WS_O + (((2 * c + th) * 16 + 2 * j + chh) * 2) * 1024;
#define MARK() do { const u32x4 ones = {~0u, ~0u, ~0u, ~0u};       \
    BST(ones, rs_ws, lo16, o_own); BST(ones, rs_ws, lo16 + 1024, o_own); } while (0)
#define LDEPOCH() asm volatile("s_load_dwordx2 %0, %1, 0x0\n\ts_waitcnt lgkmcnt(0)" : "=s"(tag) : "s"(ws) : "memory"); tag = (tag | 1) + 2

  if (isB) {
    if (wave == 0 && j < 7) {
      asm volatile("s_waitcnt vmcnt(14)" : "+v"(ua), "+v"(ub), "+v"(uc), "+v"(ud) :: "memory");
      const float pa = bf2f(ua), pb = bf2f(ub), dt = rbf(expf(bf2f(uc)));
      const float er = expf(dt * pa); float sn, cs; sincosf(dt * pb, &sn, &cs);
      const float nr = er * cs - 1.f, ni = er * sn, den = pa * pa + pb * pb;
      const cf lam = {er * cs, -ni};
      cf l16 = cmul(lam, lam); l16 = cmul(l16, l16); l16 = cmul(l16, l16); l16 = cmul(l16, l16);
      cf l64 = cmul(l16, l16); l64 = cmul(l64, l64);
      *(LAS f32x4*)(lds + L_EX + lo16) = (f32x4){lam.r, lam.i, 2.f * (nr * pa + ni * pb) / den, 2.f * (ni * pa - nr * pb) / den};
      *(LAS f32x4*)(lds + L_EX + 1024 + lo16) = (f32x4){l16.r, l16.i, l64.r, l64.i};
      *(LAS unsigned*)(lds + L_EX + 2048 + lane * 4) = ud;
    }
    LDEPOCH();
    if (tid == 0) *(LAS u64*)(lds + L_TAG) = tag;
    {
      const unsigned x_off = (32 * th + r32) * 128;
      const int wrow = 8 * (g4 >> 1) + tq;
      bf16x8 fax[4], faw[4], fbx[4], fbw[4];
      f32x16 q = zero16;
#define PRD(B, i, P) _Pragma("unroll") for (int ks = 0; ks < 4; ++ks) { B##x[ks] = ldsf(lds + (i) * SK + x_off + (((2 * ks + hi) ^ xsw) << 4)); B##w[ks] = ldstr2(lds + (i) * SK + w_off + ks * 16 * (P), 4 * (P)); } SBAR()
#define PMM(B, SW) _Pragma("unroll") for (int ks = 0; ks < 4; ++ks) q = (SW) ? MFMA(B##x[ks], B##w[ks], q) : MFMA(B##w[ks], B##x[ks], q); PIN(q); SBAR()
#define PROJ(P, SW) do { VMW(6); BAR(); PRD(fa, 0, P); PRD(fb, 1, P); PMM(fa, SW); PRD(fa, 2, P); PMM(fb, SW); PRD(fb, 3, P); PMM(fa, SW); \
        VMW(0); BAR(); PRD(fa, 4, P); PMM(fb, SW); PRD(fb, 5, P); PMM(fa, SW); PRD(fa, 6, P); PMM(fb, SW); PMM(fa, SW); } while (0)
      if (j < 7) {
        const unsigned w_off = K_W + wrow * 128 + ((64 * chh + 32 * (g4 & 1) + 8 * tp) ^ ((tq & 2) << 5));
        PROJ(128, false);
        const float qs = 0.18898223650461361f * 1.4426950408889634f;
#pragma unroll
        for (int g = 0; g < 4; g += 2) {
          const auto s0 = __builtin_amdgcn_permlane32_swap(pk(q[4 * g] * qs, q[4 * g + 1] * qs), pk(q[4 * g + 4] * qs, q[4 * g + 5] * qs), false, false);
          const auto s1 = __builtin_amdgcn_permlane32_swap(pk(q[4 * g + 2] * qs, q[4 * g + 3] * qs), pk(q[4 * g + 6] * qs, q[4 * g + 7] * qs), false, false);
          BST(((u32x4){s0[0], s1[0], s0[1], s1[1]}), rs_ws, lo16 + g * 512, WS_Q + ((2 * c + th) * 56 + 8 * j + 4 * chh) * 512);
        }
      } else {
        const unsigned w_off = K_W + chh * 3584 + wrow * 56 + (16 * (g4 & 1) + 4 * tp) * 2;
        if (chh) { PROJ(56, true);
#pragma unroll
          for (int r = 0; r < 16; ++r) q[r] = r32 < 28 ? q[r] : (r32 == 28 ? 1.f : 0.f);
        } else { PROJ(56, false); if (hi) { q[12] = 0.f; q[13] = 0.f; q[14] = 0.f; q[15] = 0.f; } }
        const unsigned ko = WS_KV + (2 * c + th) * 4096 + chh * 2048;
        BST(U4(pack8(q, 0)), rs_ws, lo16, ko); BST(U4(pack8(q, 1)), rs_ws, lo16 + 1024, ko);
      }
      publish4(ws, WS_FP + (c * 8 + j) * 128, tag, lane, cPQ);
    }
    KV A0, A1, A2, A3;
    A3.v1 = (bf16x8){};
    f32x16 O[4] = {zero16, zero16, zero16, zero16};
    f32x16 Sa, Sb = zero16; u32x4 Pa, Pb = {};
    PIN(O[0]); PIN(O[1]); PIN(O[2]); PIN(O[3]); PIN(Sb); PIN(Pb); PIN(A3.v1);
    poll(ws, WS_FP + (lane < 8 ? (wave * 8 + lane) * 8 + 7 : c * 8 + ((56 * j + (lane & 1) * 55) >> 6)) * 128, tag);
    const __amdgpu_buffer_rsrc_t rs_kv = __builtin_amdgcn_make_buffer_rsrc(ws + WS_KV + wave * 65536, 0, 65536, 0x00020000);
#define LDKV(B, T) do { const unsigned so_ = (((T) + loc) & 15) * 4096; \
                        B.k0 = BF(BLD(rs_kv, lo16, so_)); B.k1 = BF(BLD(rs_kv, lo16, so_ + 1024)); \
                        B.v0 = BF(BLD(rs_kv, lo16, so_ + 2048)); B.v1 = BF(BLD(rs_kv, lo16, so_ + 3072)); } while (0)
#define LDK(B, T) do { const unsigned so_ = (((T) + loc) & 15) * 4096; B.k0 = BF(BLD(rs_kv, lo16, so_)); B.k1 = BF(BLD(rs_kv, lo16, so_ + 1024)); } while (0)
#define LDV(B, T) do { const unsigned so_ = (((T) + loc) & 15) * 4096; B.v0 = BF(BLD(rs_kv, lo16, so_ + 2048)); B.v1 = BF(BLD(rs_kv, lo16, so_ + 3072)); } while (0)
    bf16x8 Q0[4], Q1[4];
    u32x2 qp[4][4];
    const unsigned q_e = r32 * 16 + hi * 8, q_o = r32 * 16 + (hi ? 512 : 8);
#define LDQ(i) _Pragma("unroll") for (int u = 0; u < 4; ++u) qp[i][u] = __builtin_amdgcn_raw_buffer_load_b64(rs_ws, (((wave ^ (3 - (i))) & 1) ? q_o : q_e) + u * 512, \
                 WS_Q + ((2 * c + ((wave ^ (3 - (i))) >> 1)) * 56 + 7 * j + 3 * ((wave ^ (3 - (i))) & 1)) * 512, SC1)
    LDQ(0); LDKV(A0, 0); LDQ(1); LDQ(2); LDQ(3); LDKV(A1, 1); LDKV(A2, 2); LDK(A3, 3);
    SBAR();
#pragma unroll
    for (int i = 0; i < 4; ++i) { Q0[i] = BF(((u32x4){qp[i][0][0], qp[i][0][1], qp[i][1][0], qp[i][1][1]}));
      Q1[i] = BF(((u32x4){qp[i][2][0], qp[i][2][1], hi ? 0u : qp[i][3][0], hi ? 0u : qp[i][3][1]})); }
    Sa = MFMA(A0.k0, Q0[0], zero16); Sa = MFMA(A0.k1, Q1[0], Sa);
#define UNIT(S, Sn, KN, QN, VP, OP, VC, OC, X) do { \
      Pb[2] = pk(Sn[12], Sn[13]); Pb[3] = pk(Sn[14], Sn[15]); \
      if ((QN) >= 0) Sn = MFMA(KN.k0, Q0[(QN) & 3], zero16); S[0] = EX(S[0]); S[1] = EX(S[1]); S[2] = EX(S[2]); S[3] = EX(S[3]); SBAR(); \
      if ((QN) >= 0) Sn = MFMA(KN.k1, Q1[(QN) & 3], Sn); Pa[0] = pk(S[0], S[1]); Pa[1] = pk(S[2], S[3]); S[4] = EX(S[4]); S[5] = EX(S[5]); S[6] = EX(S[6]); S[7] = EX(S[7]); SBAR(); \
      X; SBAR(); \
      OP = MFMA(VP.v1, BF(Pb), OP); Pa[2] = pk(S[4], S[5]); Pa[3] = pk(S[6], S[7]); S[8] = EX(S[8]); S[9] = EX(S[9]); S[10] = EX(S[10]); S[11] = EX(S[11]); SBAR(); \
      OC = MFMA(VC.v0, BF(Pa), OC); Pb[0] = pk(S[8], S[9]); Pb[1] = pk(S[10], S[11]); S[12] = EX(S[12]); S[13] = EX(S[13]); S[14] = EX(S[14]); S[15] = EX(S[15]); SBAR(); \
    } while (0)
#define TILE(CUR, NXT, PRV, T, LD) do { \
      UNIT(Sa, Sb, CUR, 1, PRV, O[3], CUR, O[0], ); \
      LDV(PRV, (T) + 3); SBAR(); \
      UNIT(Sb, Sa, CUR, 2, CUR, O[0], CUR, O[1], ); \
      UNIT(Sa, Sb, CUR, 3, CUR, O[1], CUR, O[2], if (LD) LDK(CUR, (T) + 4)); \
      UNIT(Sb, Sa, NXT, 0, CUR, O[2], CUR, O[3], ); } while (0)
#pragma unroll 1
    for (int t = 0; t < 12; t += 4) {
      TILE(A0, A1, A3, t, 1); TILE(A1, A2, A0, t + 1, 1); TILE(A2, A3, A1, t + 2, 1); TILE(A3, A0, A2, t + 3, 1);
    }
    TILE(A0, A1, A3, 12, 0);
    const lptr oc = lds + L_OC + lo16;
#define OUTP(T, I) _Pragma("unroll") for (int q4 = 0; q4 < 4; ++q4) *(LAS f32x4*)(oc + ((wave ^ I) * 3 + I - 1) * 4096 + q4 * 1024) = (f32x4){T[4 * q4], T[4 * q4 + 1], T[4 * q4 + 2], T[4 * q4 + 3]}
    f32x4 pv[12];
    UNIT(Sa, Sb, A1, 1, A0, O[3], A1, O[0], ); UNIT(Sb, Sa, A1, 2, A1, O[0], A1, O[1], ); UNIT(Sa, Sb, A2, 0, A1, O[1], A1, O[2], );
    UNIT(Sb, Sa, A2, 1, A1, O[2], A2, O[0], ); UNIT(Sa, Sb, A2, 2, A2, O[0], A2, O[1], ); UNIT(Sb, Sa, A3, 0, A2, O[1], A2, O[2], );
    UNIT(Sa, Sb, A3, 1, A2, O[2], A3, O[0], ); UNIT(Sb, Sa, A3, 2, A3, O[0], A3, O[1], lds_wait(j < 7 ? cCR : cPB, 4));
    UNIT(Sa, Sb, A1, 3, A3, O[1], A3, O[2], OUTP(O[0], 3));
    UNIT(Sb, Sa, A2, 3, A3, O[2], A1, O[3], OUTP(O[1], 2));
    UNIT(Sa, Sb, A3, 3, A1, O[3], A2, O[3], OUTP(O[2], 1); lds_signal(cBB));
    UNIT(Sb, Sa, A3, -1, A2, O[3], A3, O[3], lds_wait(cBB, 4); _Pragma("unroll") for (int s2 = 0; s2 < 12; ++s2) pv[s2] = *(LAS f32x4*)(oc + wave * 12288 + s2 * 1024));
    Pb[2] = pk(Sb[12], Sb[13]); Pb[3] = pk(Sb[14], Sb[15]);
    O[3] = MFMA(A3.v1, BF(Pb), O[3]);
    __builtin_amdgcn_s_setprio(2);
    lds_signal(cGO + 4 * wave);
#pragma unroll
    for (int s2 = 0; s2 < 4; ++s2) pv[s2] = pv[s2] + pv[s2 + 4] + pv[s2 + 8];
    unsigned lo16b = lo16; PIN(lo16b);
    const int lane = lo16b >> 4, r32 = lane & 31, hi = lane >> 5, g4 = lane >> 4, tq = (lane & 15) >> 2, tp = lane & 3;
    f32x16 o = O[3];
#pragma unroll
    for (int r = 0; r < 16; ++r) o[r] += pv[r >> 2][r & 3];
    {
      float l = swap_hi(o[12]);
      float am = 0.f;
#pragma unroll
      for (int r = 0; r < 16; r += 2) am = __builtin_fmaxf(__builtin_fmaxf(am, __builtin_fabsf(o[r])), __builtin_fabsf(o[r + 1]));
      const bool ok = (l >= 0x1p-90f) && (l <= 0x1p110f) && (am <= 0x1p126f);
      const bool slow = !wall(ok);
      if (slow) {
        const __amdgpu_buffer_rsrc_t rk = __builtin_amdgcn_make_buffer_rsrc(ws + WS_KV, 0, 262144, 0x00020000);
        poll(ws, WS_FP + (r32 * 8 + 7) * 128, tag);
        float m = -__builtin_inff();
#pragma unroll 1
        for (int t = 0; t < 64; ++t) {
          f32x16 s = MFMA(BF(BLD(rk, lo16, t * 4096)), Q0[3], zero16); s = MFMA(BF(BLD(rk, lo16 + 1024, t * 4096)), Q1[3], s);
#pragma unroll
          for (int r = 0; r < 16; ++r) m = __builtin_fmaxf(m, s[r]);
        }
        m = __builtin_fmaxf(swap_lo(m), swap_hi(m));
        o = zero16;
#pragma unroll 1
        for (int t = 0; t < 64; ++t) {
          f32x16 s = MFMA(BF(BLD(rk, lo16, t * 4096)), Q0[3], zero16); s = MFMA(BF(BLD(rk, lo16 + 1024, t * 4096)), Q1[3], s);
#pragma unroll
          for (int r = 0; r < 16; ++r) s[r] = EX(s[r] - m);
          o = MFMA(BF(BLD(rk, lo16 + 2048, t * 4096)), pack8(s, 0), o); o = MFMA(BF(BLD(rk, lo16 + 3072, t * 4096)), pack8(s, 1), o);
        }
        l = swap_hi(o[12]);
      }
      const float inv = __builtin_amdgcn_rcpf(l);
#pragma unroll
      for (int r = 0; r < 16; ++r) o[r] *= inv;
      if (hi) { o[12] = 0.f; o[13] = 0.f; o[14] = 0.f; o[15] = 0.f; }
      u32x4 f0 = U4(pack8(o, 0)), f1 = U4(pack8(o, 1));
      if (slow) {
#pragma unroll
        for (int k = 0; k < 4; ++k) { f0[k] = umin(f0[k], 0xfffffffeu); f1[k] = umin(f1[k], 0xfffffffeu); }
      }
      BST(f0, rs_ws, lo16, o_own); BST(f1, rs_ws, lo16 + 1024, o_own);
    }
    lds_signal(cOC);
    __builtin_amdgcn_s_setprio(0);
    if (j == 7) return;
    {
      const CAS Params* kp = KARGS();
      const __amdgpu_buffer_rsrc_t rs_out = __builtin_amdgcn_make_buffer_rsrc(kp->out, 0, 2048 * 896 * 4, 0x00020000);
      lds_wait(cWO, 4);
      u32x4 of[16];
      const unsigned ob = WS_O + (((2 * c + th) * 16 + 8 * chh) * 2) * 1024;
#define LDO(k) of[k] = BLD(rs_ws, lo16 + ((k) & 3) * 1024, ob + ((k) >> 2) * 4096)
      const int rowb = 224 * chh + 4 * (g4 >> 1) + tq;
      const unsigned wb = (L_WO + rowb * 128 + ((32 * (g4 & 1) + 8 * tp) ^ ((tq & 2) << 5))) ^ (chh << 6);
      bf16x8 w0[16], w1[16];
#pragma unroll
      for (int k = 0; k < 16; ++k) w1[k] = ldstr2(lds + (wb ^ 64) + (28 * (k >> 1) + 16 * (k & 1)) * 128, 1024);
      SBAR();
      {
        unsigned have = 0;
#pragma unroll 1
        for (unsigned s = 0; s < SPIN_CAP; ++s) {
          const unsigned w = __builtin_amdgcn_raw_buffer_load_b32(rs_ws, (lane & 7) * 2048 + 2044, ob, SC1);
          const unsigned rdy = (s + 1 < SPIN_CAP ? (unsigned)__builtin_amdgcn_ballot_w64(w != ~0u) : ~0u) & 0xffu & ~have;
#pragma unroll
          for (int h = 0; h < 8; ++h) if (rdy >> h & 1) { LDO(2 * h); LDO(2 * h + 1); }
          have |= rdy;
          if (have == 0xffu) break;
          __builtin_amdgcn_s_sleep(2);
        }
      }
      __builtin_amdgcn_s_setprio(2);
      f32x16 acc0 = zero16, acc1 = zero16;
#define OBAD(h) (!wall(umax(umax(umax(umax(of[2 * (h)][0], of[2 * (h)][1]), umax(of[2 * (h)][2], of[2 * (h)][3])), umax(of[2 * (h) + 1][0], of[2 * (h) + 1][1])), umax(of[2 * (h) + 1][2], of[2 * (h) + 1][3])) != ~0u))
#define OHEAD(h) do { \
        if (OBAD(h)) { \
          _Pragma("unroll 1") for (unsigned s = 0; s < SPIN_CAP; ++s) { LDO(2 * h); LDO(2 * h + 1); if (!OBAD(h)) break; __builtin_amdgcn_s_sleep(2); } \
        } \
        acc1 = MFMA(BF(of[2 * h]), w1[2 * h], acc1); acc1 = MFMA(BF(of[2 * h + 1]), w1[2 * h + 1], acc1); PIN(acc1); \
        w0[2 * h] = ldstr2(lds + wb + 28 * h * 128, 1024); w0[2 * h + 1] = ldstr2(lds + wb + (28 * h + 16) * 128, 1024); } while (0)
      OHEAD(0); OHEAD(1); OHEAD(2); OHEAD(3); OHEAD(4); OHEAD(5); OHEAD(6); OHEAD(7);
      lds_wait(cOC, 4);
#pragma unroll
      for (int k = 0; k < 4; ++k) acc0 = MFMA(BF(of[k]), w0[k], acc0);
      SBAR();
      const lptr tw = lds + L_OC + hi * 512 + r32 * 4;
#pragma unroll
      for (int r = 0; r < 16; ++r) *(LAS float*)(tw + (wave ^ 1) * 4096 + ((r & 3) + 8 * (r >> 2)) * 128) = acc1[r];
      SBAR();
#pragma unroll
      for (int k = 4; k < 8; ++k) acc0 = MFMA(BF(of[k]), w0[k], acc0);
      PIN(acc0);
      lds_signal(cPR + 4 * th);
#pragma unroll
      for (int k = 8; k < 16; ++k) acc0 = MFMA(BF(of[k]), w0[k], acc0);
      PIN(acc0);
      lds_wait(cPR + 4 * th, 2);
      f32x4 pa[4];
#pragma unroll
      for (int k = 0; k < 4; ++k) pa[k] = *(LAS f32x4*)(lds + L_OC + wave * 4096 + k * 1024 + lo16);
#pragma unroll
      for (int r = 0; r < 16; ++r) *(LAS float*)(tw + 16384 + wave * 4096 + ((r & 3) + 8 * (r >> 2)) * 128) = acc0[r];
      MEMBAR();
      const unsigned oo = ((64 * c + 32 * th + (lane >> 3)) * 896 + 448 + 64 * j + 32 * chh + (lane & 7) * 4) * 4;
#pragma unroll
      for (int k = 0; k < 4; ++k)
        __builtin_amdgcn_raw_buffer_store_b128(U4(pa[k] + *(LAS f32x4*)(lds + L_OC + 16384 + wave * 4096 + k * 1024 + lo16)), rs_out, oo + k * 28672, 0, 17);
    }
  } else {
    if (j == 7) {
      VMW(6); BAR(); VMW(0); BAR();
      lds_wait_<4>(cPQ, 4);
      { const u32x2 t2 = *(LAS u32x2*)(lds + L_TAG);
        tag = ((u64)(unsigned)__builtin_amdgcn_readfirstlane(t2[1]) << 32) | (unsigned)__builtin_amdgcn_readfirstlane(t2[0]); }
      __builtin_amdgcn_s_sleep(64);
      MARK();
      publish4(ws, WS_FH + (c * 8 + 7) * 128, tag, lane, cPB);
      if (c == 0 && wa == 0) {
        __builtin_amdgcn_s_sleep(127);
#pragma unroll 1
        for (int k = 0; k < 4; ++k) poll(ws, WS_FP + (k * 64 + lane) * 128, tag);
        if (lane == 0) __hip_atomic_store((GAS u64*)(ws + WS_EPOCH), tag, RLX_AGENT);
      }
      return;
    }
    VMW(6); BAR(); LDCH(L_SA); VMW(6); BAR();
    cf lam, coef, l16, l64; f32x16 ur = zero16, ui = zero16;
    f32x16 acc[4] = {zero16, zero16, zero16, zero16};
    const unsigned cs0 = wa * 16384u, cs1 = L_R1 + wa * 12288u;
    unsigned a_htw = L_HT + wa * 4096 + r32 * 64 + hi * 32, a_htr = L_HT + wa * 4096 + (8 * (g4 >> 1) + tq) * 64 + (g4 & 1) * 32 + tp * 8;
    unsigned a_hst = WS_H + ((2 * c + th) * 56 + 4 * j + 2 * chh) * 1024 + lo16;
    unsigned a_c[4], a_cn[2], a_x[2], a_ex, e_want; u32x4 dg[2]; cf l256, lw, lo_;
    const int seg = 2 * th + hi;
#pragma unroll
    for (int f = 0; f < 4; ++f) a_c[f] = cs0 + (32 * chh + r32) * 128 + (((2 * f + hi) ^ xsw) << 4);
#pragma unroll
    for (int f = 0; f < 2; ++f) a_cn[f] = cs1 + 8192 + (32 * chh + r32) * 64 + (((2 * f + hi) ^ ((r32 >> 2) & 3)) << 4);
    unsigned a_vh = (lane >> 2) * 896 + (((lane & 3) ^ ((lane >> 4) & 3)) << 4) + 384 + chh * 64, a_vo = ve ^ 64;
    unsigned a_vw = lrow * 896 + (((lane & 7) ^ ((lrow & 2) << 1)) << 4);
    unsigned a_fl = WS_FH + (c * 8 + (lane & 7)) * 128;
    unsigned a_el = (31 - umin(r32, c - 1)) * 512 + 4, a_eo = (32 - c + 2 * th + hi) * 512 + r32 * 16, a_hx = L_EX + 2048 + (32 * chh + r32) * 8;
    {
      LDCH(0); LDCH(CH);
      { const f32x4 c0 = *(LAS f32x4*)(lds + L_EX + (32 * chh + r32) * 16), c1 = *(LAS f32x4*)(lds + L_EX + 1024 + (32 * chh + r32) * 16);
        lam = {c0[0], c0[1]}; coef = {c0[2], c0[3]}; l16 = {c1[0], c1[1]}; l64 = {c1[2], c1[3]}; ud = *(LAS unsigned*)(lds + L_EX + 2048 + (32 * chh + r32) * 4); }
      { const int e = r32 - 8 * hi; const unsigned dv = ud << ((e & 1) * 16);
#pragma unroll
        for (int s2 = 0; s2 < 2; ++s2) {
          dg[s2] = (u32x4){(e >> 1) == 8 * s2 ? dv : 0u, (e >> 1) == 8 * s2 + 1 ? dv : 0u, (e >> 1) == 8 * s2 + 2 ? dv : 0u, (e >> 1) == 8 * s2 + 3 ? dv : 0u};
          a_x[s2] = L_SA + (32 * th + r32) * 128 + (((4 * chh + 2 * s2 + hi) ^ xsw) << 4); PIN(dg[s2]); PIN(a_x[s2]); }
        cf lseg = hi ? l16 : (cf){1.f, 0.f};
        if (th) lseg = cmul(lseg, cmul(l16, l16));
        const cf l128 = cmul(l64, l64), ls128 = cmul(lseg, l128);
        l256 = cmul(l128, l128); lw = th ? ls128 : lseg; lo_ = th ? lseg : ls128; e_want = (c - seg + 3) >> 2; a_ex = L_EX + (32 * chh + r32) * 8;
        PIN(l256.r); PIN(l256.i); PIN(lw.r); PIN(lw.i); PIN(lo_.r); PIN(lo_.i); PIN(e_want); PIN(a_ex); }
      PIN(a_htw); PIN(a_htr); PIN(a_hst); PIN(a_c[0]); PIN(a_c[1]); PIN(a_c[2]); PIN(a_c[3]); PIN(a_cn[0]); PIN(a_cn[1]); PIN(a_vh); PIN(a_vw); PIN(a_fl); PIN(a_el); PIN(a_eo); PIN(a_hx); PIN(a_vo);
      const int trow = 32 * th + 16 * ((r32 >> 2) & 1) + (r32 & 3) + 4 * (r32 >> 3);
      const unsigned xa_off = trow * 128, xa_sw = (trow >> 1) & 7, b_off = 8192 + (32 * chh + r32) * 128;
      bf16x8 gax[4], gar[4], gai[4], gbx[4], gbr[4], gbi[4];
      unsigned gx[3][4], gw[3][4];
#define GB(s_) ((s_) < 49152 ? 0u : (s_) < 110592 ? 49152u : 110592u)
#define GT(s_) ((s_) < 49152 ? 0 : (s_) < 110592 ? 1 : 2)
#pragma unroll
      for (int t = 0; t < 3; ++t)
#pragma unroll
        for (int ks = 0; ks < 4; ++ks) { gx[t][ks] = GB(t * 60000) + xa_off + (((2 * ks + hi) ^ xa_sw) << 4); gw[t][ks] = GB(t * 60000) + b_off + (((2 * ks + hi) ^ xsw) << 4); PIN(gx[t][ks]); PIN(gw[t][ks]); }
#define GRD(B, s_) _Pragma("unroll") for (int ks = 0; ks < 4; ++ks) { B##x[ks] = ldsf(lds + gx[GT(s_)][ks] + ((s_) - GB(s_))); \
        B##r[ks] = ldsf(lds + gw[GT(s_)][ks] + ((s_) - GB(s_))); B##i[ks] = ldsf(lds + gw[GT(s_)][ks] + ((s_) - GB(s_) + 8192)); } SBAR()
#define GMM(B) _Pragma("unroll") for (int ks = 0; ks < 4; ++ks) { ur = MFMA(B##x[ks], B##r[ks], ur); ui = MFMA(B##x[ks], B##i[ks], ui); } PIN(ur); PIN(ui); SBAR()
#define GAP() do { SBAR(); asm volatile("s_nop %0" :: "i"(7)); SBAR(); } while (0)
#define GMS(B) _Pragma("unroll") for (int ks = 0; ks < 4; ++ks) { ur = MFMA(B##x[ks], B##r[ks], ur); GAP(); ui = MFMA(B##x[ks], B##i[ks], ui); GAP(); } PIN(ur); PIN(ui); SBAR()
      __builtin_amdgcn_s_setprio(1);
      lds_wait_<1>(cPQ, 4);
      LDCH(2 * CH); LDCH(3 * CH); LDCH(4 * CH);
      VMW(18); lds_barrierA(cAB, 4); GRD(ga, L_SA); GRD(gb, 0); GMM(ga); GRD(ga, CH); GMM(gb);
      VMW(6); lds_barrierA(cAB, 8);
      GRD(gb, 2 * CH); LDCH(L_SA); GMM(ga); GRD(ga, 3 * CH); GMS(gb);
      VMW(6); lds_barrierA(cAB, 12);
      GRD(gb, 4 * CH); GMS(ga);
      VMW(0); lds_barrierA(cAB, 16);
      GRD(ga, L_SA); GMS(gb); GMS(ga);
      { const u32x2 t2 = *(LAS u32x2*)(lds + L_TAG);
        tag = ((u64)(unsigned)__builtin_amdgcn_readfirstlane(t2[1]) << 32) | (unsigned)__builtin_amdgcn_readfirstlane(t2[0]); }
    }
    acc[0] = MFMA(ldsf(lds + a_x[0]), BF(dg[0]), acc[0]); acc[0] = MFMA(ldsf(lds + a_x[1]), BF(dg[1]), acc[0]);
    PIN(acc[0]);
    cf e = {0.f, 0.f};
#pragma unroll
    for (int r = 0; r < 16; ++r) {
      const float a = coef.r * ur[r] - coef.i * ui[r], b2 = -(coef.r * ui[r] + coef.i * ur[r]);
      ur[r] = a; ui[r] = b2; e = r ? cfma(lam, e, (cf){a, b2}) : (cf){a, b2};
    }
    *(LAS f32x2*)(lds + a_ex + th * 1024 + hi * 512) = (f32x2){e.r, e.i};
    __builtin_amdgcn_s_setprio(0);
    lds_barrierA(cAB, 20);
    cf g0;
    {
      const f32x2 e0 = *(LAS f32x2*)(lds + a_ex), e1 = *(LAS f32x2*)(lds + a_ex + 512), e2 = *(LAS f32x2*)(lds + a_ex + 1024), e3 = *(LAS f32x2*)(lds + a_ex + 1536);
      __builtin_amdgcn_s_setprio(1);
      const cf E2 = cfma(l16, (cf){e0[0], e0[1]}, (cf){e1[0], e1[1]}), E3 = cfma(l16, E2, (cf){e2[0], e2[1]}), E = cfma(l16, E3, (cf){e3[0], e3[1]});
      if (th) g0 = hi ? E3 : E2; else g0 = hi ? (cf){e0[0], e0[1]} : (cf){0.f, 0.f};
      PIN(g0.r); PIN(g0.i);
      if (th == 0 && hi == 0 && c < 31)
        BST(((u32x4){f2u(E.r), (unsigned)tag, f2u(E.i), (unsigned)tag}), rs_ws, r32 * 16, WS_E + ((2 * j + chh) * 32 + 31 - c) * 512);
    }
    __builtin_amdgcn_s_setprio(0);
    const CAS Params* kp = KARGS();
    {
      const u32x4 rc = mkrs((const char*)(th ? kp->cim : kp->cre) + (size_t)j * 57344, 57344);
      int n2 = (6 * cl + 2 * ((b & 7) % 3)) % 24;
#pragma unroll
      for (int n = 0; n < 24; ++n) {
        DMA(rc, (n & 1) ? a_vo : ve, (n2 & 7) * 7168 + (n2 >> 3) * 128 + chh * 512, (n2 < 16 ? cs0 : cs1 - 16384) + n2 * 1024, 0);
        n2 = n2 == 23 ? 0 : n2 + 1; }
#pragma unroll
      for (int n = 0; n < 4; ++n) DMA(rc, a_vh, n * 14336, cs1, 8192 + n * 1024);
    }
    cf G = g0;
    if (c > 0) {
      const __amdgpu_buffer_rsrc_t re = __builtin_amdgcn_make_buffer_rsrc(ws + WS_E + (2 * j + chh) * 16384, 0, 16384, 0x00020000);
      u32x4 ev[8];
#pragma unroll 1
      for (unsigned s = 0; s < SPIN_CAP; ++s) {
        const unsigned t1 = __builtin_amdgcn_raw_buffer_load_b32(re, a_el, 0, SC1);
        if (wall(t1 == (unsigned)tag)) break;
        __builtin_amdgcn_s_sleep(2);
      }
      MEMBAR();
#pragma unroll 1
      for (unsigned s = 0; s < SPIN_CAP; ++s) {
        unsigned got = 0;
#pragma unroll
        for (int m = 0; m < 8; ++m) ev[m] = BLD(re, a_eo, m * 2048);
        VMW(0);
        __builtin_amdgcn_s_setprio(1);
#pragma unroll
        for (int m = 0; m < 8; ++m) got += ((ev[m][1] ^ (unsigned)tag) | (ev[m][3] ^ (unsigned)tag)) == 0;
        if (wall(got == e_want)) break;
        __builtin_amdgcn_s_setprio(0);
        __builtin_amdgcn_s_sleep(4);
      }
      cf H = {u2f(ev[7][0]), u2f(ev[7][2])};
#pragma unroll
      for (int m = 6; m >= 0; --m) H = cfma(l256, H, (cf){u2f(ev[m][0]), u2f(ev[m][2])});
      H = cfma(l64, (cf){swap_hi(H.r), swap_hi(H.i)}, (cf){swap_lo(H.r), swap_lo(H.i)});
      *(LAS f32x2*)(lds + a_hx + th * 512) = (f32x2){H.r, H.i};
      G = cfma(lw, H, g0);
      __builtin_amdgcn_s_setprio(0);
      lds_barrierA(cPE + 4 * chh, 2);
      const f32x2 ho = *(LAS f32x2*)(lds + a_hx + (th ^ 1) * 512);
      __builtin_amdgcn_s_setprio(1);
      G = cfma(lo_, (cf){ho[0], ho[1]}, G);
    }
    __builtin_amdgcn_s_setprio(1);
    {
#pragma unroll
      for (int r = 0; r < 16; ++r) { G = cfma(lam, G, (cf){ur[r], ui[r]}); ur[r] = G.r; ui[r] = G.i; }
      *(LAS bf16x8*)(lds + a_htw) = pack8(ur, 0); *(LAS bf16x8*)(lds + a_htw + 16) = pack8(ur, 1);
      *(LAS bf16x8*)(lds + a_htw + 2048) = pack8(ui, 0); *(LAS bf16x8*)(lds + a_htw + 2064) = pack8(ui, 1);
      unsigned a_hs2 = a_hst + 28 * 1024; PIN(a_hs2);
      __builtin_amdgcn_s_setprio(0);
      asm volatile("s_waitcnt lgkmcnt(0)" ::: "memory");
#pragma unroll
      for (int k = 0; k < 4; ++k) BST(U4(ldstr2(lds + a_htr + (k >> 1) * 2048 + (k & 1) * 1024, 256)), rs_ws, (k >> 1) ? a_hs2 : a_hst, (k & 1) * 1024);
      MARK();
      publish4(ws, WS_FH + (c * 8 + j) * 128, tag, lane, cPB);
    }
    bf16x8 cfr[14][2];
    const int cflip = chh ? -1 : 1;
#define LDC(f) do { if ((f) < 12) { const unsigned a_ = a_c[(f) & 3] + ((f) < 8 ? ((f) >> 2) * 8192 : cs1 - cs0); cfr[f][0] = ldsf(lds + a_); cfr[f][1] = ldsf(lds + a_ + cflip * 4096); } \
                    else { cfr[f][0] = ldsf(lds + a_cn[(f) & 1]); cfr[f][1] = ldsf(lds + a_cn[(f) & 1] + cflip * 2048); } } while (0)
#pragma unroll
    for (int f = 0; f < 8; ++f) LDC(f);
    lds_signal(cCR);
    lds_wait_<1>(cCR, 4);
    {
      const u32x4 rw = mkrs((const char*)kp->wo + j * 128 + wa * 7168, 401408 - j * 128 - wa * 7168);
      int m2 = (4 * cl + (b & 7)) % 14;
#pragma unroll
      for (int m = 0; m < 14; ++m) { DMA(rw, a_vw, m2 * 28672, wa * 1024 + m2 * 4096, L_WO); m2 = m2 == 13 ? 0 : m2 + 1; }
      if (wa == 0 && lane < 32) *(LAS u32x4*)(lds + L_WO + 57344 + lo16) = (u32x4){0, 0, 0, 0};
    }
    poll(ws, a_fl, tag);
    VMW(0);
    lds_signal(cWO);
    {
      const unsigned hb = WS_H + (2 * c * 56 + 14 * wa) * 1024, h0 = hb + th * 57344, h1 = hb + (th ^ 1) * 57344;
      const unsigned fa = chh * 2048, fb = chh ? 0u - 12288u : 0u;
      u32x4 hf[14][2];
#define LDH(f) do { hf[f][0] = BLD(rs_ws, lo16, h0 + (f) * 1024 + ((f) < 12 ? fa : fb)); hf[f][1] = BLD(rs_ws, lo16, h1 + (f) * 1024 + ((f) < 12 ? fa : fb)); } while (0)
#define MMH(f) do { acc[0] = MFMA(BF(hf[f][0]), cfr[f][0], acc[0]); acc[1] = MFMA(BF(hf[f][0]), cfr[f][1], acc[1]); \
                    acc[2] = MFMA(BF(hf[f][1]), cfr[f][0], acc[2]); acc[3] = MFMA(BF(hf[f][1]), cfr[f][1], acc[3]); \
                    PIN(acc[0]); PIN(acc[1]); PIN(acc[2]); PIN(acc[3]); } while (0)
#pragma unroll
      for (int f = 0; f < 14; ++f) LDH(f);
      SBAR();
      lds_wait_<4>(cGO + 4 * wa, 1);
      __builtin_amdgcn_s_setprio(1);
#pragma unroll
      for (int f = 0; f < 8; ++f) { MMH(f); SBAR(); if (f >= 1 && f < 7) LDC(f + 7); SBAR(); }
      lds_signal(cP4);
#pragma unroll
      for (int f = 8; f < 14; ++f) MMH(f);
    }
    lds_wait(cP4, 4);
    rs_write(lds + L_G2, wa, lane, acc[1], acc[2], acc[3]);
    lds_barrierA(cAB, 24);
    f32x16 y = rs_read(lds + L_G2, wa, lane, acc[0]);
    const __amdgpu_buffer_rsrc_t rs_out = __builtin_amdgcn_make_buffer_rsrc(kp->out, 0, 2048 * 896 * 4, 0x00020000);
    MEMBAR();
#pragma unroll
    for (int r = 0; r < 16; ++r) *(LAS float*)(lds + L_G2 + wa * 12288 + hi * 512 + r32 * 4 + ((r & 3) + 8 * (r >> 2)) * 128) = y[r];
    MEMBAR();
    const unsigned oo = ((64 * c + 32 * th + (lane >> 3)) * 896 + 64 * j + 32 * chh + (lane & 7) * 4) * 4;
#pragma unroll
    for (int k = 0; k < 4; ++k) __builtin_amdgcn_raw_buffer_store_b128(*(LAS u32x4*)(lds + L_G2 + wa * 12288 + k * 1024 + lo16), rs_out, oo + k * 28672, 0, 17);
  }
}

extern "C" void kernel_launch(void* const* d_in, const int* in_sizes, int n_in, void* d_out, int out_size, void* d_ws, size_t ws_size,
                              hipStream_t stream) {
  Params p;
  p.xa = (const u16*)d_in[0]; p.lre = (const u16*)d_in[1]; p.lim = (const u16*)d_in[2]; p.bre = (const u16*)d_in[3]; p.bim = (const u16*)d_in[4];
  p.cre = (const u16*)d_in[5]; p.cim = (const u16*)d_in[6]; p.ldt = (const u16*)d_in[7]; p.dsk = (const u16*)d_in[8];
  p.xb = (const u16*)d_in[9]; p.wq = (const u16*)d_in[10]; p.wk = (const u16*)d_in[11]; p.wv = (const u16*)d_in[12]; p.wo = (const u16*)d_in[13];
  p.out = (float*)d_out; p.ws = (char*)d_ws;
  fused_kernel<<<256, 512, 0, stream>>>(p);
}
```

```cpp
#include <hip/hip_runtime.h>
#include <stdint.h>

#define DI __device__ __forceinline__
#define GAS __attribute__((address_space(1)))
#define LAS __attribute__((address_space(3)))
#define CAS __attribute__((address_space(4)))
typedef short bf16x8 __attribute__((ext_vector_type(8)));
typedef short s16x4 __attribute__((ext_vector_type(4)));
typedef float f32x16 __attribute__((ext_vector_type(16)));
typedef float f32x4 __attribute__((ext_vector_type(4)));
typedef float f32x2 __attribute__((ext_vector_type(2)));
typedef unsigned u32x4 __attribute__((ext_vector_type(4)));
typedef unsigned u32x2 __attribute__((ext_vector_type(2)));
typedef __bf16 bf16x2_t __attribute__((ext_vector_type(2)));
typedef unsigned long long u64;
typedef unsigned short u16;
typedef LAS char* lptr;
#define RLX_AGENT __ATOMIC_RELAXED, __HIP_MEMORY_SCOPE_AGENT
#define MFMA(a, b, c) __builtin_amdgcn_mfma_f32_32x32x16_bf16((a), (b), (c), 0, 0, 0)
#define SBAR() __builtin_amdgcn_sched_barrier(0)
#define PIN(x) asm volatile("" : "+v"(x))
#define MEMBAR() asm volatile("" ::: "memory")
#define SC1 16
#define KVW 1

constexpr unsigned WS_EPOCH = 0, WS_FKV = 4096, WS_FH = 8192, WS_E = 0x10000, WS_FP = 0x48000,
                   WS_KV = 0x50000, WS_O = 0x100000, WS_H = 0x300000, WS_END = 0x680000, WS_DBG = 0x900000;
constexpr unsigned SK = 15360, K_W = 8192, K_WQ = 107520;
constexpr unsigned CH = 24576, L_SA = 107520, L_QS = 99328;
constexpr unsigned L_R1 = 107520, L_HT = 65536, L_WO = 0, L_OC = 57856, L_G2 = 107520, L_MISC = 159744, L_EX = L_MISC + 128, L_TOTAL = 163840;
constexpr unsigned cBB = L_MISC, cAB = L_MISC + 4, cCR = L_MISC + 8, cWO = L_MISC + 12, cOC = L_MISC + 16, cPB = L_MISC + 20, cPR = L_MISC + 24, cP4 = L_MISC + 32, cPQ = L_MISC + 52, cGO = L_MISC + 36, cPE = L_MISC + 56, cQD = L_MISC + 72, L_TAG = L_MISC + 64;
constexpr unsigned SPIN_CAP = 1u << 18;

struct Params {
  const u16 *xb, *wq, *wk, *wv; char* ws; const u16 *lre, *lim, *ldt, *dsk, *xa, *bre, *bim, *cre, *cim, *wo;
  float* out;
};

DI float u2f(unsigned v) { return __builtin_bit_cast(float, v); }
DI unsigned f2u(float v) { return __builtin_bit_cast(unsigned, v); }
DI unsigned umin(unsigned a, unsigned b) { return a < b ? a : b; }
DI unsigned umax(unsigned a, unsigned b) { return a > b ? a : b; }
DI bool wall(bool x) { return __builtin_amdgcn_ballot_w64(!x) == 0; }
DI float bf2f(unsigned v) { return u2f(v << 16); }
DI unsigned pk(float a, float b) { f32x2 v = {a, b}; return __builtin_bit_cast(unsigned, __builtin_convertvector(v, bf16x2_t)); }
DI float rbf(float a) { return u2f(pk(0.f, a)); }
DI bf16x8 pack8(const f32x16& x, int s) {
  u32x4 p = {pk(x[8 * s], x[8 * s + 1]), pk(x[8 * s + 2], x[8 * s + 3]), pk(x[8 * s + 4], x[8 * s + 5]), pk(x[8 * s + 6], x[8 * s + 7])};
  return __builtin_bit_cast(bf16x8, p);
}
DI u32x4 mkrs(const void* base, unsigned bytes) { const u64 a = (u64)base; return (u32x4){(unsigned)a, (unsigned)(a >> 32) & 0xffffu, bytes, 0x20000u}; }
#define DMA(rs, voff, soff, ldsb, IMM) asm volatile("s_add_u32 m0, %3, %4\n\ts_nop 0\n\tbuffer_load_dwordx4 %0, %1, %2 offen lds" \
                                                    :: "v"(voff), "s"(rs), "s"(soff), "s"(ldsb), "i"(IMM) : "memory", "scc")
#define DMAX(rs, voff, soff, ldsb, IMM) asm volatile("s_add_u32 m0, %3, %4\n\ts_nop 0\n\tbuffer_load_dwordx4 %0, %1, %2 offen lds" \
                                                     :: "v"(voff), "s"(rs), "s"(soff), "s"(ldsb), "i"(IMM) : "memory", "scc")
DI bf16x8 ldsf(lptr p) { return *(LAS bf16x8*)p; }
DI s16x4 ldstr(lptr p) { return __builtin_amdgcn_ds_read_tr16_b64_v4i16((LAS s16x4*)p); }
DI bf16x8 ldstr2(lptr p, int second) { s16x4 lo = ldstr(p), hi = ldstr(p + second); return __builtin_shufflevector(lo, hi, 0, 1, 2, 3, 4, 5, 6, 7); }
DI void lds_signal(unsigned c) {
  asm volatile("s_waitcnt lgkmcnt(0)\n\ts_mov_b64 exec, 1\n\tds_add_u32 %0, %1\n\ts_mov_b64 exec, -1" :: "v"(c), "v"(1u) : "memory");
}
template <int SLEEP> DI void lds_wait_(unsigned c, unsigned target) {
  unsigned v, s, n;
  asm volatile("s_mov_b32 %2, 0x400000\n1:\n\tds_read_b32 %0, %3\n\ts_waitcnt lgkmcnt(0)\n\tv_readfirstlane_b32 %1, %0\n\ts_cmp_ge_u32 %1, %4\n\ts_cbranch_scc1 2f\n\t"
               "s_sleep %5\n\ts_sub_u32 %2, %2, 1\n\ts_cbranch_scc0 1b\n2:" : "=&v"(v), "=&s"(s), "=&s"(n) : "v"(c), "i"(target), "i"(SLEEP) : "memory", "scc");
}
#define lds_wait(c, t) lds_wait_<0>(c, t)
#define lds_barrier(c, t) do { lds_signal(c); lds_wait(c, t); } while (0)
#define lds_barrierA(c, t) do { lds_signal(c); lds_wait_<1>(c, t); } while (0)
DI void poll(const char* ws, unsigned off, u64 tag) {
#pragma unroll 1
  for (unsigned s = 0; s < SPIN_CAP; ++s) {
    u64 v = __hip_atomic_load((GAS u64*)(ws + off), RLX_AGENT);
    if (wall(v == tag)) break;
    __builtin_amdgcn_s_sleep(2);
  }
  MEMBAR();
}
DI void publish4(char* ws, unsigned off, u64 tag, int lane, unsigned cnt) {
  unsigned old;
  asm volatile("s_waitcnt vmcnt(0)\n\ts_mov_b64 exec, 1\n\tds_add_rtn_u32 %0, %1, %2\n\ts_waitcnt lgkmcnt(0)\n\ts_mov_b64 exec, -1" : "=&v"(old) : "v"(cnt), "v"(1u) : "memory");
  if (__builtin_amdgcn_readfirstlane(old) == 3 && lane == 0) __hip_atomic_store((GAS u64*)(ws + off), tag, RLX_AGENT);
}
DI float swap_lo(float x) {
  auto r = __builtin_amdgcn_permlane32_swap(f2u(x), f2u(x), false, false); return u2f(r[0]); }
DI float swap_hi(float x) {
  auto r = __builtin_amdgcn_permlane32_swap(f2u(x), f2u(x), false, false); return u2f(r[1]); }
struct cf { float r, i; };
DI cf cmul(cf a, cf b) { return {a.r * b.r - a.i * b.i, a.r * b.i + a.i * b.r}; }
DI cf cfma(cf a, cf b, cf c) { return {__builtin_fmaf(a.r, b.r, __builtin_fmaf(-a.i, b.i, c.r)), __builtin_fmaf(a.r, b.i, __builtin_fmaf(a.i, b.r, c.i))}; }

#define STAMP(k) do {} while (0)
#define STAMP_OUT() do {} while (0)

struct KV { bf16x8 k0, k1, v0, v1; };

DI void rs_write(lptr base, int w, int lane, const f32x16& t1, const f32x16& t2, const f32x16& t3) {
#define RSW(T, I) _Pragma("unroll") for (int q = 0; q < 4; ++q) *(LAS f32x4*)(base + ((w ^ I) * 3 + I - 1) * 4096 + q * 1024 + lane * 16) = (f32x4){T[4 * q], T[4 * q + 1], T[4 * q + 2], T[4 * q + 3]}
  RSW(t1, 1); RSW(t2, 2); RSW(t3, 3);
}
DI f32x16 rs_read(lptr base, int w, int lane, f32x16 a) {
  f32x4 v[12];
#pragma unroll
  for (int s = 0; s < 12; ++s) v[s] = *(LAS f32x4*)(base + w * 12288 + s * 1024 + lane * 16);
  SBAR();
#pragma unroll
  for (int s = 0; s < 12; ++s) { const int q = s & 3; a[4 * q] += v[s][0]; a[4 * q + 1] += v[s][1]; a[4 * q + 2] += v[s][2]; a[4 * q + 3] += v[s][3]; }
  return a;
}

#define VMW_(n) asm volatile("s_waitcnt vmcnt(" #n ")" ::: "memory")
#define VMW(n) VMW_(n)
#define BAR() asm volatile("s_waitcnt lgkmcnt(0)\n\ts_barrier" ::: "memory")
#define BLD(R, V, S) __builtin_amdgcn_raw_buffer_load_b128(R, V, S, SC1)
#define BST(D, R, V, S) __builtin_amdgcn_raw_buffer_store_b128(D, R, (V) + (S), 0, SC1)
#define BF(x) __builtin_bit_cast(bf16x8, x)
#define U4(x) __builtin_bit_cast(u32x4, x)
#define EX(x) __builtin_amdgcn_exp2f(x)
#define KARGS() ({ const CAS Params* kp_ = (const CAS Params*)__builtin_amdgcn_kernarg_segment_ptr(); asm volatile("" : "+s"(kp_)); kp_; })
#define NEXTK() do { k += step; k = k >= 7 ? k - 7 : k; } while (0)

#if defined(__HIP_DEVICE_COMPILE__)
#define NO_PK __attribute__((target("no-packed-fp32-ops")))
#else
#define NO_PK
#endif
__global__ void __launch_bounds__(512, 2) NO_PK fused_kernel(Params p) {
  __shared__ __attribute__((aligned(128))) char smem[L_TOTAL];
  const lptr lds = (lptr)smem;
  const int tid = threadIdx.x, lane = tid & 63, wave = __builtin_amdgcn_readfirstlane(tid >> 6);
  const int b = blockIdx.x, loc = b >> 3, cl = loc >> 3, c = (b & 7) * 4 + cl, j = loc & 7;
  const int lrow = lane >> 3;
  const unsigned ve = lrow * 896 + (((lane & 7) ^ (lrow >> 1)) << 4);
  const int step = cl + 1, k0 = j == 7 ? 0 : j;
  const bool isB = wave < 4;
  const int wa = wave & 3;
  char* const ws = p.ws;
  STAMP(0);
  unsigned ua = 0, ub = 0, uc = 0, ud = 0;
  u32x4 rxa, rbr, rbi; int ka = k0;
  const unsigned vo = (ve ^ 64) + 7168, lba = wa * 2048;
#define NEXTKA() do { ka += step; ka = ka >= 7 ? ka - 7 : ka; } while (0)
#define LDCH(slot) do { NEXTKA(); const unsigned k_ = ka * 128; DMAX(rxa, ve, k_, lba, (slot)); DMAX(rxa, vo, k_, lba, (slot) + 1024); \
      DMA(rbr, ve, k_, lba, (slot) + 8192); DMA(rbr, vo, k_, lba, (slot) + 9216); DMA(rbi, ve, k_, lba, (slot) + 16384); DMA(rbi, vo, k_, lba, (slot) + 17408); } while (0)
  {
    const u32x4 rx = mkrs((const char*)p.xb + (size_t)c * 57344 + wave * 7168, 7168);
    const unsigned vx = ve ^ ((wave & 1) << 6), lb = wave * 1024;
    int k = k0;
    if (j == 7) {
      const u32x4 rw = mkrs(isB ? p.wk : p.wv, 25088);
      const unsigned vw = wa * 1024 + lane * 16, lw = K_W + (isB ? 0 : 3584) + wa * 1024; const bool half = wa < 3 || lane < 32;
#pragma unroll
      for (int i = 0; i < 7; ++i) { NEXTK(); DMAX(rx, vx, k * 128, lb, i * SK); if (half) DMA(rw, vw, k * 3584, lw, i * SK); }
    } else {
      const int iw = wave * 64 + lane, wrow = (iw * 9363) >> 16;
      const unsigned vwq = wrow * 896 + (iw - wrow * 7) * 16;
      const u32x4 rw = mkrs((const char*)p.wq + j * 112, 401408 - j * 112);
      if (wave == 0) { const unsigned co = (64 * j + lane) * 2;
        asm volatile("global_load_ushort %0, %4, %5\n\tglobal_load_ushort %1, %4, %6\n\tglobal_load_ushort %2, %4, %7\n\tglobal_load_ushort %3, %4, %8"
                     : "=&v"(ua), "=&v"(ub), "=&v"(uc), "=&v"(ud) : "v"(co), "s"(p.lre), "s"(p.lim), "s"(p.ldt), "s"(p.dsk) : "memory"); }
      if (wave < 7) {
#pragma unroll
        for (int i = 0; i < 7; ++i) { NEXTK(); DMAX(rx, vx, k * 128, lb, i * SK); DMA(rw, vwq, k * 57344, lb, i * SK + K_W); }
      } else {
#pragma unroll
        for (int i = 0; i < 7; ++i) { NEXTK(); DMAX(rx, vx, k * 128, lb, i * SK); }
      }
      rxa = mkrs((const char*)p.xa + (size_t)c * 57344 + wa * 14336, 14336);
      rbr = mkrs((const char*)p.bre + (size_t)j * 57344 + wa * 14336, 14336);
      rbi = mkrs((const char*)p.bim + (size_t)j * 57344 + wa * 14336, 14336);
    }
  }
  STAMP(8);
  if (tid < 32 && (tid & ~1) != 16) ((LAS unsigned*)(lds + L_MISC))[tid] = 0;
  const int r32 = lane & 31, hi = lane >> 5;
  const int g4 = lane >> 4, tq = (lane & 15) >> 2, tp = lane & 3;
  const unsigned xsw = (r32 >> 1) & 7, lo16 = lane * 16;
  const int th = wa >> 1, chh = wa & 1;
  u64 tag;
  const __amdgpu_buffer_rsrc_t rs_ws = __builtin_amdgcn_make_buffer_rsrc(ws, 0, WS_END, 0x00020000);
  const f32x16 zero16 = {};
  const unsigned o_own = WS_O + (((2 * c + th) * 16 + 2 * j + chh) * 2) * 1024;
#define MARK() do { const u32x4 ones = {~0u, ~0u, ~0u, ~0u};       \
    BST(ones, rs_ws, lo16, o_own); BST(ones, rs_ws, lo16 + 1024, o_own); } while (0)
#define LDEPOCH() asm volatile("s_load_dwordx2 %0, %1, 0x0\n\ts_waitcnt lgkmcnt(0)" : "=s"(tag) : "s"(ws) : "memory"); tag = (tag | 1) + 2

  if (isB) {
    if (wave == 0 && j < 7) {
      asm volatile("s_waitcnt vmcnt(14)" : "+v"(ua), "+v"(ub), "+v"(uc), "+v"(ud) :: "memory");
      const float pa = bf2f(ua), pb = bf2f(ub), dt = rbf(expf(bf2f(uc)));
      const float er = expf(dt * pa); float sn, cs; sincosf(dt * pb, &sn, &cs);
      const float nr = er * cs - 1.f, ni = er * sn, den = pa * pa + pb * pb;
      const cf lam = {er * cs, -ni};
      cf l16 = cmul(lam, lam); l16 = cmul(l16, l16); l16 = cmul(l16, l16); l16 = cmul(l16, l16);
      cf l64 = cmul(l16, l16); l64 = cmul(l64, l64);
      *(LAS f32x4*)(lds + L_EX + lo16) = (f32x4){lam.r, lam.i, 2.f * (nr * pa + ni * pb) / den, 2.f * (ni * pa - nr * pb) / den};
      *(LAS f32x4*)(lds + L_EX + 1024 + lo16) = (f32x4){l16.r, l16.i, l64.r, l64.i};
      *(LAS unsigned*)(lds + L_EX + 2048 + lane * 4) = ud;
    }
    LDEPOCH();
    if (tid == 0) *(LAS u64*)(lds + L_TAG) = tag;
    bf16x8 Q0[4], Q1[4];
    {
      const unsigned xq_off = (32 * th + r32) * 128, wq_off = (8 * (g4 >> 1) + tq) * 112 + (28 * chh + 16 * (g4 & 1) + 4 * tp) * 2;
      bf16x8 fax[4], faw[4], fbx[4], fbw[4];
      f32x16 q = zero16;
#define QRD(B, X, W) _Pragma("unroll") for (int ks = 0; ks < 4; ++ks) { B##x[ks] = ldsf(lds + (X) + xq_off + (((2 * ks + hi) ^ xsw) << 4)); B##w[ks] = ldstr2(lds + (W) + wq_off + ks * 1792, 448); } SBAR()
#define QMM(B) _Pragma("unroll") for (int ks = 0; ks < 4; ++ks) q = MFMA(B##w[ks], B##x[ks], q); PIN(q); SBAR()
      if (j == 7) {
        VMW(6); BAR(); VMW(0); BAR();
        const int i0 = wa * 128 + lane, r0 = (i0 * 9363) >> 16, r1 = ((i0 + 64) * 9363) >> 16;
        const unsigned vq0 = r0 * 896 + (i0 - r0 * 7) * 16, vq1 = r1 * 896 + (i0 + 64 - r1 * 7) * 16, lb = wave * 2048;
        const u32x4 rw = mkrs((const char*)p.wq + 784, 401408 - 784);
        if (KVW) lds_wait(cPQ, 4);
        int k = k0;
#pragma unroll
        for (int i = 0; i < 7; ++i) { NEXTK(); DMA(rw, vq0, k * 57344, lb, K_WQ + i * 7168); if (wave < 3) DMA(rw, vq1, k * 57344, lb, K_WQ + i * 7168 + 1024); }
        VMW(0);
        lds_barrier(cOC, 4);
        QRD(fa, 0, K_WQ); QRD(fb, SK, K_WQ + 7168); QMM(fa); QRD(fa, 2 * SK, K_WQ + 2 * 7168); QMM(fb); QRD(fb, 3 * SK, K_WQ + 3 * 7168); QMM(fa);
        QRD(fa, 4 * SK, K_WQ + 4 * 7168); QMM(fb); QRD(fb, 5 * SK, K_WQ + 5 * 7168); QMM(fa); QRD(fa, 6 * SK, K_WQ + 6 * 7168); QMM(fb); QMM(fa);
        lds_wait(cPQ, 4);
      } else {
        VMW(6); BAR();
        QRD(fa, 0, K_W); QRD(fb, SK, SK + K_W); QMM(fa); QRD(fa, 2 * SK, 2 * SK + K_W); QMM(fb); QRD(fb, 3 * SK, 3 * SK + K_W); QMM(fa);
        VMW(0); BAR();
        QRD(fa, 4 * SK, 4 * SK + K_W); QMM(fb); QRD(fb, 5 * SK, 5 * SK + K_W); QMM(fa); QRD(fa, 6 * SK, 6 * SK + K_W); QMM(fb); QMM(fa);
      }
      STAMP(1);
      lds_barrier(cQD, 4);
      if (tid == 0) __hip_atomic_store((GAS u64*)(ws + WS_FP + (c * 8 + j) * 128), tag, RLX_AGENT);
      const float qs = 0.18898223650461361f * 1.4426950408889634f;
#pragma unroll
      for (int r = 0; r < 16; ++r) q[r] *= qs;
      if (hi) { q[12] = 0.f; q[13] = 0.f; q[14] = 0.f; q[15] = 0.f; }
      Q0[3] = pack8(q, 0); Q1[3] = pack8(q, 1);
      *(LAS bf16x8*)(lds + L_QS + wave * 2048 + lo16) = Q0[3];
      *(LAS bf16x8*)(lds + L_QS + wave * 2048 + 1024 + lo16) = Q1[3];
      lds_barrier(cQD, 8);
#pragma unroll
      for (int i = 0; i < 3; ++i) { Q0[i] = ldsf(lds + L_QS + (wave ^ (3 - i)) * 2048 + lo16); Q1[i] = ldsf(lds + L_QS + (wave ^ (3 - i)) * 2048 + 1024 + lo16); }
      lds_signal(cQD);
    }
    KV A0, A1, A2, A3;
    A3.v1 = (bf16x8){};
    f32x16 O[4] = {zero16, zero16, zero16, zero16};
    f32x16 Sa, Sb = zero16; u32x4 Pa, Pb = {};
    PIN(O[0]); PIN(O[1]); PIN(O[2]); PIN(O[3]); PIN(Sb); PIN(Pb); PIN(A3.v1);
    STAMP(2);
    poll(ws, WS_FKV + (wave * 8 + (lane & 7)) * 128, tag);
    STAMP(3);
    const __amdgpu_buffer_rsrc_t rs_kv = __builtin_amdgcn_make_buffer_rsrc(ws + WS_KV + wave * 65536, 0, 65536, 0x00020000);
#define LDKV(B, T) do { const unsigned so_ = (((T) + loc) & 15) * 4096; \
                        B.k0 = BF(BLD(rs_kv, lo16, so_)); B.k1 = BF(BLD(rs_kv, lo16, so_ + 1024)); \
                        B.v0 = BF(BLD(rs_kv, lo16, so_ + 2048)); B.v1 = BF(BLD(rs_kv, lo16, so_ + 3072)); } while (0)
#define LDK(B, T) do { const unsigned so_ = (((T) + loc) & 15) * 4096; B.k0 = BF(BLD(rs_kv, lo16, so_)); B.k1 = BF(BLD(rs_kv, lo16, so_ + 1024)); } while (0)
#define LDV(B, T) do { const unsigned so_ = (((T) + loc) & 15) * 4096; B.v0 = BF(BLD(rs_kv, lo16, so_ + 2048)); B.v1 = BF(BLD(rs_kv, lo16, so_ + 3072)); } while (0)
    LDKV(A0, 0); LDKV(A1, 1); LDKV(A2, 2); LDK(A3, 3);
    SBAR();
    Sa = MFMA(A0.k0, Q0[0], zero16); Sa = MFMA(A0.k1, Q1[0], Sa);
    STAMP(9);
#define UNIT(S, Sn, KN, QN, VP, OP, VC, OC, X) do { \
      Pb[2] = pk(Sn[12], Sn[13]); Pb[3] = pk(Sn[14], Sn[15]); \
      if ((QN) >= 0) Sn = MFMA(KN.k0, Q0[(QN) & 3], zero16); S[0] = EX(S[0]); S[1] = EX(S[1]); S[2] = EX(S[2]); S[3] = EX(S[3]); SBAR(); \
      if ((QN) >= 0) Sn = MFMA(KN.k1, Q1[(QN) & 3], Sn); Pa[0] = pk(S[0], S[1]); Pa[1] = pk(S[2], S[3]); S[4] = EX(S[4]); S[5] = EX(S[5]); S[6] = EX(S[6]); S[7] = EX(S[7]); SBAR(); \
      X; SBAR(); \
      OP = MFMA(VP.v1, BF(Pb), OP); Pa[2] = pk(S[4], S[5]); Pa[3] = pk(S[6], S[7]); S[8] = EX(S[8]); S[9] = EX(S[9]); S[10] = EX(S[10]); S[11] = EX(S[11]); SBAR(); \
      OC = MFMA(VC.v0, BF(Pa), OC); Pb[0] = pk(S[8], S[9]); Pb[1] = pk(S[10], S[11]); S[12] = EX(S[12]); S[13] = EX(S[13]); S[14] = EX(S[14]); S[15] = EX(S[15]); SBAR(); \
    } while (0)
#define TILE(CUR, NXT, PRV, T, LD) do { \
      UNIT(Sa, Sb, CUR, 1, PRV, O[3], CUR, O[0], ); \
      LDV(PRV, (T) + 3); SBAR(); \
      UNIT(Sb, Sa, CUR, 2, CUR, O[0], CUR, O[1], ); \
      UNIT(Sa, Sb, CUR, 3, CUR, O[1], CUR, O[2], if (LD) LDK(CUR, (T) + 4)); \
      UNIT(Sb, Sa, NXT, 0, CUR, O[2], CUR, O[3], ); } while (0)
#pragma unroll 1
    for (int t = 0; t < 12; t += 4) {
      TILE(A0, A1, A3, t, 1); TILE(A1, A2, A0, t + 1, 1); TILE(A2, A3, A1, t + 2, 1); TILE(A3, A0, A2, t + 3, 1);
    }
    TILE(A0, A1, A3, 12, 0);
    const lptr oc = lds + L_OC + lo16;
#define OUTP(T, I) _Pragma("unroll") for (int q4 = 0; q4 < 4; ++q4) *(LAS f32x4*)(oc + ((wave ^ I) * 3 + I - 1) * 4096 + q4 * 1024) = (f32x4){T[4 * q4], T[4 * q4 + 1], T[4 * q4 + 2], T[4 * q4 + 3]}
    f32x4 pv[12];
    UNIT(Sa, Sb, A1, 1, A0, O[3], A1, O[0], ); UNIT(Sb, Sa, A1, 2, A1, O[0], A1, O[1], ); UNIT(Sa, Sb, A2, 0, A1, O[1], A1, O[2], );
    UNIT(Sb, Sa, A2, 1, A1, O[2], A2, O[0], ); UNIT(Sa, Sb, A2, 2, A2, O[0], A2, O[1], ); UNIT(Sb, Sa, A3, 0, A2, O[1], A2, O[2], );
    UNIT(Sa, Sb, A3, 1, A2, O[2], A3, O[0], ); UNIT(Sb, Sa, A3, 2, A3, O[0], A3, O[1], lds_wait(j < 7 ? cCR : cPB, 4); lds_wait(cQD, 12));
    UNIT(Sa, Sb, A1, 3, A3, O[1], A3, O[2], OUTP(O[0], 3));
    UNIT(Sb, Sa, A2, 3, A3, O[2], A1, O[3], OUTP(O[1], 2));
    UNIT(Sa, Sb, A3, 3, A1, O[3], A2, O[3], OUTP(O[2], 1); lds_signal(cBB));
    UNIT(Sb, Sa, A3, -1, A2, O[3], A3, O[3], lds_wait(cBB, 4); _Pragma("unroll") for (int s2 = 0; s2 < 12; ++s2) pv[s2] = *(LAS f32x4*)(oc + wave * 12288 + s2 * 1024));
    Pb[2] = pk(Sb[12], Sb[13]); Pb[3] = pk(Sb[14], Sb[15]);
    O[3] = MFMA(A3.v1, BF(Pb), O[3]);
    STAMP(4);
    __builtin_amdgcn_s_setprio(2);
    lds_signal(cGO + 4 * wave);
#pragma unroll
    for (int s2 = 0; s2 < 4; ++s2) pv[s2] = pv[s2] + pv[s2 + 4] + pv[s2 + 8];
    STAMP(10);
    unsigned lo16b = lo16; PIN(lo16b);
    const int lane = lo16b >> 4, r32 = lane & 31, hi = lane >> 5, g4 = lane >> 4, tq = (lane & 15) >> 2, tp = lane & 3;
    f32x16 o = O[3];
#pragma unroll
    for (int r = 0; r < 16; ++r) o[r] += pv[r >> 2][r & 3];
    {
      float l = swap_hi(o[12]);
      float am = 0.f;
#pragma unroll
      for (int r = 0; r < 16; r += 2) am = __builtin_fmaxf(__builtin_fmaxf(am, __builtin_fabsf(o[r])), __builtin_fabsf(o[r + 1]));
      const bool ok = (l >= 0x1p-90f) && (l <= 0x1p110f) && (am <= 0x1p126f);
      const bool slow = !wall(ok);
      if (slow) {
        const __amdgpu_buffer_rsrc_t rk = __builtin_amdgcn_make_buffer_rsrc(ws + WS_KV, 0, 262144, 0x00020000);
        poll(ws, WS_FKV + r32 * 128, tag);
        float m = -__builtin_inff();
#pragma unroll 1
        for (int t = 0; t < 64; ++t) {
          f32x16 s = MFMA(BF(BLD(rk, lo16, t * 4096)), Q0[3], zero16); s = MFMA(BF(BLD(rk, lo16 + 1024, t * 4096)), Q1[3], s);
#pragma unroll
          for (int r = 0; r < 16; ++r) m = __builtin_fmaxf(m, s[r]);
        }
        m = __builtin_fmaxf(swap_lo(m), swap_hi(m));
        o = zero16;
#pragma unroll 1
        for (int t = 0; t < 64; ++t) {
          f32x16 s = MFMA(BF(BLD(rk, lo16, t * 4096)), Q0[3], zero16); s = MFMA(BF(BLD(rk, lo16 + 1024, t * 4096)), Q1[3], s);
#pragma unroll
          for (int r = 0; r < 16; ++r) s[r] = EX(s[r] - m);
          o = MFMA(BF(BLD(rk, lo16 + 2048, t * 4096)), pack8(s, 0), o); o = MFMA(BF(BLD(rk, lo16 + 3072, t * 4096)), pack8(s, 1), o);
        }
        l = swap_hi(o[12]);
      }
      const float inv = __builtin_amdgcn_rcpf(l);
#pragma unroll
      for (int r = 0; r < 16; ++r) o[r] *= inv;
      if (hi) { o[12] = 0.f; o[13] = 0.f; o[14] = 0.f; o[15] = 0.f; }
      u32x4 f0 = U4(pack8(o, 0)), f1 = U4(pack8(o, 1));
      if (slow) {
#pragma unroll
        for (int k = 0; k < 4; ++k) { f0[k] = umin(f0[k], 0xfffffffeu); f1[k] = umin(f1[k], 0xfffffffeu); }
      }
      BST(f0, rs_ws, lo16, o_own); BST(f1, rs_ws, lo16 + 1024, o_own);
    }
    lds_signal(cOC);
    __builtin_amdgcn_s_setprio(0);
    STAMP(5);
    if (j == 7) { STAMP_OUT(); return; }
    {
      const CAS Params* kp = KARGS();
      const __amdgpu_buffer_rsrc_t rs_out = __builtin_amdgcn_make_buffer_rsrc(kp->out, 0, 2048 * 896 * 4, 0x00020000);
      lds_wait(cWO, 4);
      u32x4 of[16];
      const unsigned ob = WS_O + (((2 * c + th) * 16 + 8 * chh) * 2) * 1024;
#define LDO(k) of[k] = BLD(rs_ws, lo16 + ((k) & 3) * 1024, ob + ((k) >> 2) * 4096)
      const int rowb = 224 * chh + 4 * (g4 >> 1) + tq;
      const unsigned wb = (L_WO + rowb * 128 + ((32 * (g4 & 1) + 8 * tp) ^ ((tq & 2) << 5))) ^ (chh << 6);
      bf16x8 w0[16], w1[16];
#pragma unroll
      for (int k = 0; k < 16; ++k) w1[k] = ldstr2(lds + (wb ^ 64) + (28 * (k >> 1) + 16 * (k & 1)) * 128, 1024);
      SBAR();
      {
        unsigned have = 0;
#pragma unroll 1
        for (unsigned s = 0; s < SPIN_CAP; ++s) {
          const unsigned w = __builtin_amdgcn_raw_buffer_load_b32(rs_ws, (lane & 7) * 2048 + 2044, ob, SC1);
          const unsigned rdy = (s + 1 < SPIN_CAP ? (unsigned)__builtin_amdgcn_ballot_w64(w != ~0u) : ~0u) & 0xffu & ~have;
#pragma unroll
          for (int h = 0; h < 8; ++h) if (rdy >> h & 1) { LDO(2 * h); LDO(2 * h + 1); }
          have |= rdy;
          if (have == 0xffu) break;
          __builtin_amdgcn_s_sleep(2);
        }
      }
      __builtin_amdgcn_s_setprio(2);
      f32x16 acc0 = zero16, acc1 = zero16;
#define OBAD(h) (!wall(umax(umax(umax(umax(of[2 * (h)][0], of[2 * (h)][1]), umax(of[2 * (h)][2], of[2 * (h)][3])), umax(of[2 * (h) + 1][0], of[2 * (h) + 1][1])), umax(of[2 * (h) + 1][2], of[2 * (h) + 1][3])) != ~0u))
#define OHEAD(h) do { \
        if (OBAD(h)) { \
          _Pragma("unroll 1") for (unsigned s = 0; s < SPIN_CAP; ++s) { LDO(2 * h); LDO(2 * h + 1); if (!OBAD(h)) break; __builtin_amdgcn_s_sleep(2); } \
        } \
        acc1 = MFMA(BF(of[2 * h]), w1[2 * h], acc1); acc1 = MFMA(BF(of[2 * h + 1]), w1[2 * h + 1], acc1); PIN(acc1); \
        w0[2 * h] = ldstr2(lds + wb + 28 * h * 128, 1024); w0[2 * h + 1] = ldstr2(lds + wb + (28 * h + 16) * 128, 1024); } while (0)
      OHEAD(0); STAMP(6); OHEAD(1); OHEAD(2); OHEAD(3); OHEAD(4); OHEAD(5); OHEAD(6); OHEAD(7);
      STAMP(11);
      lds_wait(cOC, 4);
#pragma unroll
      for (int k = 0; k < 4; ++k) acc0 = MFMA(BF(of[k]), w0[k], acc0);
      SBAR();
      const lptr tw = lds + L_OC + hi * 512 + r32 * 4;
#pragma unroll
      for (int r = 0; r < 16; ++r) *(LAS float*)(tw + (wave ^ 1) * 4096 + ((r & 3) + 8 * (r >> 2)) * 128) = acc1[r];
      SBAR();
#pragma unroll
      for (int k = 4; k < 8; ++k) acc0 = MFMA(BF(of[k]), w0[k], acc0);
      PIN(acc0);
      lds_signal(cPR + 4 * th);
#pragma unroll
      for (int k = 8; k < 16; ++k) acc0 = MFMA(BF(of[k]), w0[k], acc0);
      PIN(acc0);
      lds_wait(cPR + 4 * th, 2);
      f32x4 pa[4];
#pragma unroll
      for (int k = 0; k < 4; ++k) pa[k] = *(LAS f32x4*)(lds + L_OC + wave * 4096 + k * 1024 + lo16);
#pragma unroll
      for (int r = 0; r < 16; ++r) *(LAS float*)(tw + 16384 + wave * 4096 + ((r & 3) + 8 * (r >> 2)) * 128) = acc0[r];
      MEMBAR();
      STAMP(12);
      const unsigned oo = ((64 * c + 32 * th + (lane >> 3)) * 896 + 448 + 64 * j + 32 * chh + (lane & 7) * 4) * 4;
#pragma unroll
      for (int k = 0; k < 4; ++k)
        __builtin_amdgcn_raw_buffer_store_b128(U4(pa[k] + *(LAS f32x4*)(lds + L_OC + 16384 + wave * 4096 + k * 1024 + lo16)), rs_out, oo + k * 28672, 0, 17);
    }
    STAMP(7);
    STAMP_OUT();
  } else {
    if (j == 7) {
      VMW(6); BAR();
      { const u32x2 t2 = *(LAS u32x2*)(lds + L_TAG);
        tag = ((u64)(unsigned)__builtin_amdgcn_readfirstlane(t2[1]) << 32) | (unsigned)__builtin_amdgcn_readfirstlane(t2[0]); }
      {
        const unsigned xk_off = (32 * chh + r32) * 128, wk_off = K_W + th * 3584 + (8 * (g4 >> 1) + tq) * 56 + (16 * (g4 & 1) + 4 * tp) * 2;
        bf16x8 fax[4], faw[4], fbx[4], fbw[4];
        f32x16 a = zero16;
#define KRD(B, i) _Pragma("unroll") for (int ks = 0; ks < 4; ++ks) { B##x[ks] = ldsf(lds + (i) * SK + xk_off + (((2 * ks + hi) ^ xsw) << 4)); B##w[ks] = ldstr2(lds + (i) * SK + wk_off + ks * 896, 224); } SBAR()
#define KMM(B) if (th) { _Pragma("unroll") for (int ks = 0; ks < 4; ++ks) a = MFMA(B##x[ks], B##w[ks], a); } else { _Pragma("unroll") for (int ks = 0; ks < 4; ++ks) a = MFMA(B##w[ks], B##x[ks], a); } PIN(a); SBAR()
        KRD(fa, 0); KRD(fb, 1); KMM(fa); KRD(fa, 2); KMM(fb); KRD(fb, 3); KMM(fa);
        VMW(0); BAR();
        KRD(fa, 4); KMM(fb); KRD(fb, 5); KMM(fa); KRD(fa, 6); KMM(fb); KMM(fa);
        STAMP(1);
        if (th) {
#pragma unroll
          for (int r = 0; r < 16; ++r) a[r] = r32 < 28 ? a[r] : (r32 == 28 ? 1.f : 0.f);
        } else if (hi) { a[12] = 0.f; a[13] = 0.f; a[14] = 0.f; a[15] = 0.f; }
        const unsigned ko = WS_KV + (2 * c + chh) * 4096 + th * 2048;
        BST(U4(pack8(a, 0)), rs_ws, lo16, ko); BST(U4(pack8(a, 1)), rs_ws, lo16 + 1024, ko);
        publish4(ws, WS_FKV + c * 128, tag, lane, cPQ);
        STAMP(2);
      }
      lds_wait_<8>(cQD, 8);
      __builtin_amdgcn_s_sleep(64);
      MARK();
      publish4(ws, WS_FH + (c * 8 + 7) * 128, tag, lane, cPB);
      if (c == 0 && wa == 0) {
        __builtin_amdgcn_s_sleep(127);
#pragma unroll 1
        for (int k = 0; k < 4; ++k) poll(ws, WS_FP + (k * 64 + lane) * 128, tag);
        if (lane == 0) __hip_atomic_store((GAS u64*)(ws + WS_EPOCH), tag, RLX_AGENT);
      }
      STAMP_OUT();
      return;
    }
    if (wave < 7) VMW(6); else VMW(3);
    BAR(); LDCH(L_SA); LDCH(L_SA + CH); VMW(12); BAR();
    cf lam, coef, l16, l64; f32x16 ur = zero16, ui = zero16;
    f32x16 acc[4] = {zero16, zero16, zero16, zero16};
    const unsigned cs0 = wa * 16384u, cs1 = L_R1 + wa * 12288u;
    unsigned a_htw = L_HT + wa * 4096 + r32 * 64 + hi * 32, a_htr = L_HT + wa * 4096 + (8 * (g4 >> 1) + tq) * 64 + (g4 & 1) * 32 + tp * 8;
    unsigned a_hst = WS_H + ((2 * c + th) * 56 + 4 * j + 2 * chh) * 1024 + lo16;
    unsigned a_c[4], a_cn[2], a_x[2], a_ex, e_want; u32x4 dg[2]; cf l256, lw, lo_;
    const int seg = 2 * th + hi;
#pragma unroll
    for (int f = 0; f < 4; ++f) a_c[f] = cs0 + (32 * chh + r32) * 128 + (((2 * f + hi) ^ xsw) << 4);
#pragma unroll
    for (int f = 0; f < 2; ++f) a_cn[f] = cs1 + 8192 + (32 * chh + r32) * 64 + (((2 * f + hi) ^ ((r32 >> 2) & 3)) << 4);
    unsigned a_vh = (lane >> 2) * 896 + (((lane & 3) ^ ((lane >> 4) & 3)) << 4) + 384 + chh * 64, a_vo = ve ^ 64;
    unsigned a_vw = lrow * 896 + (((lane & 7) ^ ((lrow & 2) << 1)) << 4);
    unsigned a_fl = WS_FH + (c * 8 + (lane & 7)) * 128;
    unsigned a_el = (31 - umin(r32, c - 1)) * 512 + 4, a_eo = (32 - c + 2 * th + hi) * 512 + r32 * 16, a_hx = L_EX + 2048 + (32 * chh + r32) * 8;
    {
      LDCH(0); LDCH(CH);
      { const f32x4 c0 = *(LAS f32x4*)(lds + L_EX + (32 * chh + r32) * 16), c1 = *(LAS f32x4*)(lds + L_EX + 1024 + (32 * chh + r32) * 16);
        lam = {c0[0], c0[1]}; coef = {c0[2], c0[3]}; l16 = {c1[0], c1[1]}; l64 = {c1[2], c1[3]}; ud = *(LAS unsigned*)(lds + L_EX + 2048 + (32 * chh + r32) * 4); }
      { const int e = r32 - 8 * hi; const unsigned dv = ud << ((e & 1) * 16);
#pragma unroll
        for (int s2 = 0; s2 < 2; ++s2) {
          dg[s2] = (u32x4){(e >> 1) == 8 * s2 ? dv : 0u, (e >> 1) == 8 * s2 + 1 ? dv : 0u, (e >> 1) == 8 * s2 + 2 ? dv : 0u, (e >> 1) == 8 * s2 + 3 ? dv : 0u};
          a_x[s2] = L_SA + (32 * th + r32) * 128 + (((4 * chh + 2 * s2 + hi) ^ xsw) << 4); PIN(dg[s2]); PIN(a_x[s2]); }
        cf lseg = hi ? l16 : (cf){1.f, 0.f};
        if (th) lseg = cmul(lseg, cmul(l16, l16));
        const cf l128 = cmul(l64, l64), ls128 = cmul(lseg, l128);
        l256 = cmul(l128, l128); lw = th ? ls128 : lseg; lo_ = th ? lseg : ls128; e_want = (c - seg + 3) >> 2; a_ex = L_EX + (32 * chh + r32) * 8;
        PIN(l256.r); PIN(l256.i); PIN(lw.r); PIN(lw.i); PIN(lo_.r); PIN(lo_.i); PIN(e_want); PIN(a_ex); }
      PIN(a_htw); PIN(a_htr); PIN(a_hst); PIN(a_c[0]); PIN(a_c[1]); PIN(a_c[2]); PIN(a_c[3]); PIN(a_cn[0]); PIN(a_cn[1]); PIN(a_vh); PIN(a_vw); PIN(a_fl); PIN(a_el); PIN(a_eo); PIN(a_hx); PIN(a_vo);
      const int trow = 32 * th + 16 * ((r32 >> 2) & 1) + (r32 & 3) + 4 * (r32 >> 3);
      const unsigned xa_off = trow * 128, xa_sw = (trow >> 1) & 7, b_off = 8192 + (32 * chh + r32) * 128;
      bf16x8 gax[4], gar[4], gai[4], gbx[4], gbr[4], gbi[4];
      unsigned gx[3][4], gw[3][4];
#define GB(s_) ((s_) < 49152 ? 0u : (s_) < 107520 ? 49152u : 107520u)
#define GT(s_) ((s_) < 49152 ? 0 : (s_) < 107520 ? 1 : 2)
#pragma unroll
      for (int t = 0; t < 3; ++t)
#pragma unroll
        for (int ks = 0; ks < 4; ++ks) { gx[t][ks] = GB(t * 60000) + xa_off + (((2 * ks + hi) ^ xa_sw) << 4); gw[t][ks] = GB(t * 60000) + b_off + (((2 * ks + hi) ^ xsw) << 4); PIN(gx[t][ks]); PIN(gw[t][ks]); }
#define GRD(B, s_) _Pragma("unroll") for (int ks = 0; ks < 4; ++ks) { B##x[ks] = ldsf(lds + gx[GT(s_)][ks] + ((s_) - GB(s_))); \
        B##r[ks] = ldsf(lds + gw[GT(s_)][ks] + ((s_) - GB(s_))); B##i[ks] = ldsf(lds + gw[GT(s_)][ks] + ((s_) - GB(s_) + 8192)); } SBAR()
#define GMM(B) _Pragma("unroll") for (int ks = 0; ks < 4; ++ks) { ur = MFMA(B##x[ks], B##r[ks], ur); ui = MFMA(B##x[ks], B##i[ks], ui); } PIN(ur); PIN(ui); SBAR()
#define GAP() do { SBAR(); asm volatile("s_nop %0" :: "i"(7)); SBAR(); } while (0)
#define GMS(B) _Pragma("unroll") for (int ks = 0; ks < 4; ++ks) { ur = MFMA(B##x[ks], B##r[ks], ur); GAP(); ui = MFMA(B##x[ks], B##i[ks], ui); GAP(); } PIN(ur); PIN(ui); SBAR()
      __builtin_amdgcn_s_setprio(1);
      STAMP(12);
      VMW(12); lds_barrierA(cAB, 4); GRD(ga, L_SA); GRD(gb, L_SA + CH); GMM(ga);
      lds_wait_<1>(cQD, 4);
      LDCH(2 * CH); GMM(gb); LDCH(3 * CH);
      VMW(12); lds_barrierA(cAB, 8);
      STAMP(13);
      GRD(ga, 0); GRD(gb, CH); LDCH(L_SA); GMM(ga);
      VMW(6); lds_barrierA(cAB, 12);
      STAMP(14);
      GRD(ga, 2 * CH); GMS(gb); GRD(gb, 3 * CH); GMS(ga);
      VMW(0); lds_barrierA(cAB, 16);
      STAMP(15);
      GRD(ga, L_SA); GMS(gb); GMS(ga);
      { const u32x2 t2 = *(LAS u32x2*)(lds + L_TAG);
        tag = ((u64)(unsigned)__builtin_amdgcn_readfirstlane(t2[1]) << 32) | (unsigned)__builtin_amdgcn_readfirstlane(t2[0]); }
    }
    acc[0] = MFMA(ldsf(lds + a_x[0]), BF(dg[0]), acc[0]); acc[0] = MFMA(ldsf(lds + a_x[1]), BF(dg[1]), acc[0]);
    PIN(acc[0]);
    STAMP(1);
    cf e = {0.f, 0.f};
#pragma unroll
    for (int r = 0; r < 16; ++r) {
      const float a = coef.r * ur[r] - coef.i * ui[r], b2 = -(coef.r * ui[r] + coef.i * ur[r]);
      ur[r] = a; ui[r] = b2; e = r ? cfma(lam, e, (cf){a, b2}) : (cf){a, b2};
    }
    *(LAS f32x2*)(lds + a_ex + th * 1024 + hi * 512) = (f32x2){e.r, e.i};
    __builtin_amdgcn_s_setprio(0);
    lds_barrierA(cAB, 20);
    cf g0;
    {
      const f32x2 e0 = *(LAS f32x2*)(lds + a_ex), e1 = *(LAS f32x2*)(lds + a_ex + 512), e2 = *(LAS f32x2*)(lds + a_ex + 1024), e3 = *(LAS f32x2*)(lds + a_ex + 1536);
      __builtin_amdgcn_s_setprio(1);
      const cf E2 = cfma(l16, (cf){e0[0], e0[1]}, (cf){e1[0], e1[1]}), E3 = cfma(l16, E2, (cf){e2[0], e2[1]}), E = cfma(l16, E3, (cf){e3[0], e3[1]});
      if (th) g0 = hi ? E3 : E2; else g0 = hi ? (cf){e0[0], e0[1]} : (cf){0.f, 0.f};
      PIN(g0.r); PIN(g0.i);
      if (th == 0 && hi == 0 && c < 31)
        BST(((u32x4){f2u(E.r), (unsigned)tag, f2u(E.i), (unsigned)tag}), rs_ws, r32 * 16, WS_E + ((2 * j + chh) * 32 + 31 - c) * 512);
    }
    __builtin_amdgcn_s_setprio(0);
    const CAS Params* kp = KARGS();
    {
      const u32x4 rc = mkrs((const char*)(th ? kp->cim : kp->cre) + (size_t)j * 57344, 57344);
      int n2 = (6 * cl + 2 * ((b & 7) % 3)) % 24;
#pragma unroll
      for (int n = 0; n < 24; ++n) {
        DMA(rc, (n & 1) ? a_vo : ve, (n2 & 7) * 7168 + (n2 >> 3) * 128 + chh * 512, (n2 < 16 ? cs0 : cs1 - 16384) + n2 * 1024, 0);
        n2 = n2 == 23 ? 0 : n2 + 1; }
#pragma unroll
      for (int n = 0; n < 4; ++n) DMA(rc, a_vh, n * 14336, cs1, 8192 + n * 1024);
    }
    STAMP(2);
    cf G = g0;
    if (c > 0) {
      const __amdgpu_buffer_rsrc_t re = __builtin_amdgcn_make_buffer_rsrc(ws + WS_E + (2 * j + chh) * 16384, 0, 16384, 0x00020000);
      u32x4 ev[8];
#pragma unroll 1
      for (unsigned s = 0; s < SPIN_CAP; ++s) {
        const unsigned t1 = __builtin_amdgcn_raw_buffer_load_b32(re, a_el, 0, SC1);
        if (wall(t1 == (unsigned)tag)) break;
        __builtin_amdgcn_s_sleep(2);
      }
      MEMBAR();
#pragma unroll 1
      for (unsigned s = 0; s < SPIN_CAP; ++s) {
        unsigned got = 0;
#pragma unroll
        for (int m = 0; m < 8; ++m) ev[m] = BLD(re, a_eo, m * 2048);
        VMW(0);
        __builtin_amdgcn_s_setprio(1);
#pragma unroll
        for (int m = 0; m < 8; ++m) got += ((ev[m][1] ^ (unsigned)tag) | (ev[m][3] ^ (unsigned)tag)) == 0;
        if (wall(got == e_want)) break;
        __builtin_amdgcn_s_setprio(0);
        __builtin_amdgcn_s_sleep(4);
      }
      STAMP(3);
      cf H = {u2f(ev[7][0]), u2f(ev[7][2])};
#pragma unroll
      for (int m = 6; m >= 0; --m) H = cfma(l256, H, (cf){u2f(ev[m][0]), u2f(ev[m][2])});
      H = cfma(l64, (cf){swap_hi(H.r), swap_hi(H.i)}, (cf){swap_lo(H.r), swap_lo(H.i)});
      *(LAS f32x2*)(lds + a_hx + th * 512) = (f32x2){H.r, H.i};
      G = cfma(lw, H, g0);
      __builtin_amdgcn_s_setprio(0);
      lds_barrierA(cPE + 4 * chh, 2);
      const f32x2 ho = *(LAS f32x2*)(lds + a_hx + (th ^ 1) * 512);
      __builtin_amdgcn_s_setprio(1);
      G = cfma(lo_, (cf){ho[0], ho[1]}, G);
    }
    STAMP(4);
    __builtin_amdgcn_s_setprio(1);
    {
#pragma unroll
      for (int r = 0; r < 16; ++r) { G = cfma(lam, G, (cf){ur[r], ui[r]}); ur[r] = G.r; ui[r] = G.i; }
      *(LAS bf16x8*)(lds + a_htw) = pack8(ur, 0); *(LAS bf16x8*)(lds + a_htw + 16) = pack8(ur, 1);
      *(LAS bf16x8*)(lds + a_htw + 2048) = pack8(ui, 0); *(LAS bf16x8*)(lds + a_htw + 2064) = pack8(ui, 1);
      unsigned a_hs2 = a_hst + 28 * 1024; PIN(a_hs2);
      __builtin_amdgcn_s_setprio(0);
      asm volatile("s_waitcnt lgkmcnt(0)" ::: "memory");
#pragma unroll
      for (int k = 0; k < 4; ++k) BST(U4(ldstr2(lds + a_htr + (k >> 1) * 2048 + (k & 1) * 1024, 256)), rs_ws, (k >> 1) ? a_hs2 : a_hst, (k & 1) * 1024);
      MARK();
      publish4(ws, WS_FH + (c * 8 + j) * 128, tag, lane, cPB);
    }
    STAMP(5);
    bf16x8 cfr[14][2];
    const int cflip = chh ? -1 : 1;
#define LDC(f) do { if ((f) < 12) { const unsigned a_ = a_c[(f) & 3] + ((f) < 8 ? ((f) >> 2) * 8192 : cs1 - cs0); cfr[f][0] = ldsf(lds + a_); cfr[f][1] = ldsf(lds + a_ + cflip * 4096); } \
                    else { cfr[f][0] = ldsf(lds + a_cn[(f) & 1]); cfr[f][1] = ldsf(lds + a_cn[(f) & 1] + cflip * 2048); } } while (0)
#pragma unroll
    for (int f = 0; f < 8; ++f) LDC(f);
    lds_signal(cCR);
    STAMP(10);
    lds_wait_<1>(cCR, 4);
    {
      const u32x4 rw = mkrs((const char*)kp->wo + j * 128 + wa * 7168, 401408 - j * 128 - wa * 7168);
      int m2 = (4 * cl + (b & 7)) % 14;
#pragma unroll
      for (int m = 0; m < 14; ++m) { DMA(rw, a_vw, m2 * 28672, wa * 1024 + m2 * 4096, L_WO); m2 = m2 == 13 ? 0 : m2 + 1; }
      if (wa == 0 && lane < 32) *(LAS u32x4*)(lds + L_WO + 57344 + lo16) = (u32x4){0, 0, 0, 0};
    }
    poll(ws, a_fl, tag);
    VMW(0);
    lds_signal(cWO);
    STAMP(6);
    {
      const unsigned hb = WS_H + (2 * c * 56 + 14 * wa) * 1024, h0 = hb + th * 57344, h1 = hb + (th ^ 1) * 57344;
      const unsigned fa = chh * 2048, fb = chh ? 0u - 12288u : 0u;
      u32x4 hf[14][2];
#define LDH(f) do { hf[f][0] = BLD(rs_ws, lo16, h0 + (f) * 1024 + ((f) < 12 ? fa : fb)); hf[f][1] = BLD(rs_ws, lo16, h1 + (f) * 1024 + ((f) < 12 ? fa : fb)); } while (0)
#define MMH(f) do { acc[0] = MFMA(BF(hf[f][0]), cfr[f][0], acc[0]); acc[1] = MFMA(BF(hf[f][0]), cfr[f][1], acc[1]); \
                    acc[2] = MFMA(BF(hf[f][1]), cfr[f][0], acc[2]); acc[3] = MFMA(BF(hf[f][1]), cfr[f][1], acc[3]); \
                    PIN(acc[0]); PIN(acc[1]); PIN(acc[2]); PIN(acc[3]); } while (0)
#pragma unroll
      for (int f = 0; f < 14; ++f) LDH(f);
      SBAR();
      lds_wait_<4>(cGO + 4 * wa, 1);
      STAMP(9);
      __builtin_amdgcn_s_setprio(1);
#pragma unroll
      for (int f = 0; f < 8; ++f) { MMH(f); SBAR(); if (f >= 1 && f < 7) LDC(f + 7); SBAR(); }
      lds_signal(cP4);
#pragma unroll
      for (int f = 8; f < 14; ++f) MMH(f);
    }
    lds_wait(cP4, 4);
    STAMP(11);
    rs_write(lds + L_G2, wa, lane, acc[1], acc[2], acc[3]);
    lds_barrierA(cAB, 24);
    f32x16 y = rs_read(lds + L_G2, wa, lane, acc[0]);
    const __amdgpu_buffer_rsrc_t rs_out = __builtin_amdgcn_make_buffer_rsrc(kp->out, 0, 2048 * 896 * 4, 0x00020000);
    MEMBAR();
#pragma unroll
    for (int r = 0; r < 16; ++r) *(LAS float*)(lds + L_G2 + wa * 12288 + hi * 512 + r32 * 4 + ((r & 3) + 8 * (r >> 2)) * 128) = y[r];
    MEMBAR();
    const unsigned oo = ((64 * c + 32 * th + (lane >> 3)) * 896 + 64 * j + 32 * chh + (lane & 7) * 4) * 4;
#pragma unroll
    for (int k = 0; k < 4; ++k) __builtin_amdgcn_raw_buffer_store_b128(*(LAS u32x4*)(lds + L_G2 + wa * 12288 + k * 1024 + lo16), rs_out, oo + k * 28672, 0, 17);
    STAMP(7);
    STAMP_OUT();
  }
}

extern "C" void kernel_launch(void* const* d_in, const int* in_sizes, int n_in, void* d_out, int out_size, void* d_ws, size_t ws_size,
                              hipStream_t stream) {
  Params p;
  p.xa = (const u16*)d_in[0]; p.lre = (const u16*)d_in[1]; p.lim = (const u16*)d_in[2]; p.bre = (const u16*)d_in[3]; p.bim = (const u16*)d_in[4];
  p.cre = (const u16*)d_in[5]; p.cim = (const u16*)d_in[6]; p.ldt = (const u16*)d_in[7]; p.dsk = (const u16*)d_in[8];
  p.xb = (const u16*)d_in[9]; p.wq = (const u16*)d_in[10]; p.wk = (const u16*)d_in[11]; p.wv = (const u16*)d_in[12]; p.wo = (const u16*)d_in[13];
  p.out = (float*)d_out; p.ws = (char*)d_ws;
  fused_kernel<<<256, 512, 0, stream>>>(p);
}
```

```cpp
#include <hip/hip_runtime.h>
#include <stdint.h>

#define DI __device__ __forceinline__
#define GAS __attribute__((address_space(1)))
#define LAS __attribute__((address_space(3)))
#define CAS __attribute__((address_space(4)))
typedef short bf16x8 __attribute__((ext_vector_type(8)));
typedef short s16x4 __attribute__((ext_vector_type(4)));
typedef float f32x16 __attribute__((ext_vector_type(16)));
typedef float f32x4 __attribute__((ext_vector_type(4)));
typedef float f32x2 __attribute__((ext_vector_type(2)));
typedef unsigned u32x4 __attribute__((ext_vector_type(4)));
typedef unsigned u32x2 __attribute__((ext_vector_type(2)));
typedef __bf16 bf16x2_t __attribute__((ext_vector_type(2)));
typedef unsigned long long u64;
typedef unsigned short u16;
typedef LAS char* lptr;
#define RLX_AGENT __ATOMIC_RELAXED, __HIP_MEMORY_SCOPE_AGENT
#define MFMA(a, b, c) __builtin_amdgcn_mfma_f32_32x32x16_bf16((a), (b), (c), 0, 0, 0)
#define SBAR() __builtin_amdgcn_sched_barrier(0)
#define PIN(x) asm volatile("" : "+v"(x))
#define MEMBAR() asm volatile("" ::: "memory")
#define SC1 16
#define KVW 2

constexpr unsigned WS_EPOCH = 0, WS_FKV = 4096, WS_FH = 8192, WS_E = 0x10000, WS_FP = 0x48000,
                   WS_KV = 0x50000, WS_O = 0x100000, WS_H = 0x300000, WS_END = 0x680000, WS_DBG = 0x900000;
constexpr unsigned SK = 15360, K_W = 8192, K_WQ = 107520;
constexpr unsigned CH = 24576, L_SA = 107520, L_QS = 99328;
constexpr unsigned L_R1 = 107520, L_HT = 65536, L_WO = 0, L_OC = 57856, L_G2 = 107520, L_MISC = 159744, L_EX = L_MISC + 128, L_TOTAL = 163840;
constexpr unsigned cBB = L_MISC, cAB = L_MISC + 4, cCR = L_MISC + 8, cWO = L_MISC + 12, cOC = L_MISC + 16, cPB = L_MISC + 20, cPR = L_MISC + 24, cP4 = L_MISC + 32, cPQ = L_MISC + 52, cGO = L_MISC + 36, cPE = L_MISC + 56, cQD = L_MISC + 72, L_TAG = L_MISC + 64;
constexpr unsigned SPIN_CAP = 1u << 18;

struct Params {
  const u16 *xb, *wq, *wk, *wv; char* ws; const u16 *lre, *lim, *ldt, *dsk, *xa, *bre, *bim, *cre, *cim, *wo;
  float* out;
};

DI float u2f(unsigned v) { return __builtin_bit_cast(float, v); }
DI unsigned f2u(float v) { return __builtin_bit_cast(unsigned, v); }
DI unsigned umin(unsigned a, unsigned b) { return a < b ? a : b; }
DI unsigned umax(unsigned a, unsigned b) { return a > b ? a : b; }
DI bool wall(bool x) { return __builtin_amdgcn_ballot_w64(!x) == 0; }
DI float bf2f(unsigned v) { return u2f(v << 16); }
DI unsigned pk(float a, float b) { f32x2 v = {a, b}; return __builtin_bit_cast(unsigned, __builtin_convertvector(v, bf16x2_t)); }
DI float rbf(float a) { return u2f(pk(0.f, a)); }
DI bf16x8 pack8(const f32x16& x, int s) {
  u32x4 p = {pk(x[8 * s], x[8 * s + 1]), pk(x[8 * s + 2], x[8 * s + 3]), pk(x[8 * s + 4], x[8 * s + 5]), pk(x[8 * s + 6], x[8 * s + 7])};
  return __builtin_bit_cast(bf16x8, p);
}
DI u32x4 mkrs(const void* base, unsigned bytes) { const u64 a = (u64)base; return (u32x4){(unsigned)a, (unsigned)(a >> 32) & 0xffffu, bytes, 0x20000u}; }
#define DMA(rs, voff, soff, ldsb, IMM) asm volatile("s_add_u32 m0, %3, %4\n\ts_nop 0\n\tbuffer_load_dwordx4 %0, %1, %2 offen lds" \
                                                    :: "v"(voff), "s"(rs), "s"(soff), "s"(ldsb), "i"(IMM) : "memory", "scc")
DI bf16x8 ldsf(lptr p) { return *(LAS bf16x8*)p; }
DI s16x4 ldstr(lptr p) { return __builtin_amdgcn_ds_read_tr16_b64_v4i16((LAS s16x4*)p); }
DI bf16x8 ldstr2(lptr p, int second) { s16x4 lo = ldstr(p), hi = ldstr(p + second); return __builtin_shufflevector(lo, hi, 0, 1, 2, 3, 4, 5, 6, 7); }
DI void lds_signal(unsigned c) {
  asm volatile("s_waitcnt lgkmcnt(0)\n\ts_mov_b64 exec, 1\n\tds_add_u32 %0, %1\n\ts_mov_b64 exec, -1" :: "v"(c), "v"(1u) : "memory");
}
template <int SLEEP> DI void lds_wait_(unsigned c, unsigned target) {
  unsigned v, s, n;
  asm volatile("s_mov_b32 %2, 0x400000\n1:\n\tds_read_b32 %0, %3\n\ts_waitcnt lgkmcnt(0)\n\tv_readfirstlane_b32 %1, %0\n\ts_cmp_ge_u32 %1, %4\n\ts_cbranch_scc1 2f\n\t"
               "s_sleep %5\n\ts_sub_u32 %2, %2, 1\n\ts_cbranch_scc0 1b\n2:" : "=&v"(v), "=&s"(s), "=&s"(n) : "v"(c), "i"(target), "i"(SLEEP) : "memory", "scc");
}
#define lds_wait(c, t) lds_wait_<0>(c, t)
#define lds_barrier(c, t) do { lds_signal(c); lds_wait(c, t); } while (0)
#define lds_barrierA(c, t) do { lds_signal(c); lds_wait_<1>(c, t); } while (0)
DI void poll(const char* ws, unsigned off, u64 tag) {
#pragma unroll 1
  for (unsigned s = 0; s < SPIN_CAP; ++s) {
    u64 v = __hip_atomic_load((GAS u64*)(ws + off), RLX_AGENT);
    if (wall(v == tag)) break;
    __builtin_amdgcn_s_sleep(2);
  }
  MEMBAR();
}
DI void publish4(char* ws, unsigned off, u64 tag, int lane, unsigned cnt) {
  unsigned old;
  asm volatile("s_waitcnt vmcnt(0)\n\ts_mov_b64 exec, 1\n\tds_add_rtn_u32 %0, %1, %2\n\ts_waitcnt lgkmcnt(0)\n\ts_mov_b64 exec, -1" : "=&v"(old) : "v"(cnt), "v"(1u) : "memory");
  if (__builtin_amdgcn_readfirstlane(old) == 3 && lane == 0) __hip_atomic_store((GAS u64*)(ws + off), tag, RLX_AGENT);
}
DI float swap_lo(float x) {
  auto r = __builtin_amdgcn_permlane32_swap(f2u(x), f2u(x), false, false); return u2f(r[0]); }
DI float swap_hi(float x) {
  auto r = __builtin_amdgcn_permlane32_swap(f2u(x), f2u(x), false, false); return u2f(r[1]); }
struct cf { float r, i; };
DI cf cmul(cf a, cf b) { return {a.r * b.r - a.i * b.i, a.r * b.i + a.i * b.r}; }
DI cf cfma(cf a, cf b, cf c) { return {__builtin_fmaf(a.r, b.r, __builtin_fmaf(-a.i, b.i, c.r)), __builtin_fmaf(a.r, b.i, __builtin_fmaf(a.i, b.r, c.i))}; }

#define STAMP(k) do {} while (0)
#define STAMP_OUT() do {} while (0)

struct KV { bf16x8 k0, k1, v0, v1; };

DI void rs_write(lptr base, int w, int lane, const f32x16& t1, const f32x16& t2, const f32x16& t3) {
#define RSW(T, I) _Pragma("unroll") for (int q = 0; q < 4; ++q) *(LAS f32x4*)(base + ((w ^ I) * 3 + I - 1) * 4096 + q * 1024 + lane * 16) = (f32x4){T[4 * q], T[4 * q + 1], T[4 * q + 2], T[4 * q + 3]}
  RSW(t1, 1); RSW(t2, 2); RSW(t3, 3);
}
DI f32x16 rs_read(lptr base, int w, int lane, f32x16 a) {
  f32x4 v[12];
#pragma unroll
  for (int s = 0; s < 12; ++s) v[s] = *(LAS f32x4*)(base + w * 12288 + s * 1024 + lane * 16);
  SBAR();
#pragma unroll
  for (int s = 0; s < 12; ++s) { const int q = s & 3; a[4 * q] += v[s][0]; a[4 * q + 1] += v[s][1]; a[4 * q + 2] += v[s][2]; a[4 * q + 3] += v[s][3]; }
  return a;
}

#define VMW_(n) asm volatile("s_waitcnt vmcnt(" #n ")" ::: "memory")
#define VMW(n) VMW_(n)
#define BAR() asm volatile("s_waitcnt lgkmcnt(0)\n\ts_barrier" ::: "memory")
#define BLD(R, V, S) __builtin_amdgcn_raw_buffer_load_b128(R, V, S, SC1)
#define BST(D, R, V, S) __builtin_amdgcn_raw_buffer_store_b128(D, R, (V) + (S), 0, SC1)
#define BF(x) __builtin_bit_cast(bf16x8, x)
#define U4(x) __builtin_bit_cast(u32x4, x)
#define EX(x) __builtin_amdgcn_exp2f(x)
#define KARGS() ({ const CAS Params* kp_ = (const CAS Params*)__builtin_amdgcn_kernarg_segment_ptr(); asm volatile("" : "+s"(kp_)); kp_; })
#define NEXTK() do { k += step; k = k >= 7 ? k - 7 : k; } while (0)

#if defined(__HIP_DEVICE_COMPILE__)
#define NO_PK __attribute__((target("no-packed-fp32-ops")))
#else
#define NO_PK
#endif
__global__ void __launch_bounds__(512, 2) NO_PK fused_kernel(Params p) {
  __shared__ __attribute__((aligned(128))) char smem[L_TOTAL];
  const lptr lds = (lptr)smem;
  const int tid = threadIdx.x, lane = tid & 63, wave = __builtin_amdgcn_readfirstlane(tid >> 6);
  const int b = blockIdx.x, loc = b >> 3, cl = loc >> 3, c = (b & 7) * 4 + cl, j = loc & 7;
  const int lrow = lane >> 3;
  const unsigned ve = lrow * 896 + (((lane & 7) ^ (lrow >> 1)) << 4);
  const int step = cl + 1, k0 = j == 7 ? 0 : j;
  const bool isB = wave < 4;
  const int wa = wave & 3;
  char* const ws = p.ws;
  STAMP(0);
  unsigned ua = 0, ub = 0, uc = 0, ud = 0;
  u32x4 rxa, rbr, rbi; int ka = k0;
  const unsigned vo = (ve ^ 64) + 7168, lba = wa * 2048;
#define NEXTKA() do { ka += step; ka = ka >= 7 ? ka - 7 : ka; } while (0)
#define LDCH(slot) do { NEXTKA(); const unsigned k_ = ka * 128; DMA(rxa, ve, k_, lba, (slot)); DMA(rxa, vo, k_, lba, (slot) + 1024); \
      DMA(rbr, ve, k_, lba, (slot) + 8192); DMA(rbr, vo, k_, lba, (slot) + 9216); DMA(rbi, ve, k_, lba, (slot) + 16384); DMA(rbi, vo, k_, lba, (slot) + 17408); } while (0)
  {
    const u32x4 rx = mkrs((const char*)p.xb + (size_t)c * 57344 + wave * 7168, 7168);
    const unsigned vx = ve ^ ((wave & 1) << 6), lb = wave * 1024;
    int k = k0;
    if (j == 7) {
      const u32x4 rw = mkrs(isB ? p.wk : p.wv, 25088);
      const unsigned vw = wa * 1024 + lane * 16, lw = K_W + (isB ? 0 : 3584) + wa * 1024; const bool half = wa < 3 || lane < 32;
#pragma unroll
      for (int i = 0; i < 7; ++i) { NEXTK(); DMA(rx, vx, k * 128, lb, i * SK); if (half) DMA(rw, vw, k * 3584, lw, i * SK); }
    } else {
      const int iw = wave * 64 + lane, wrow = (iw * 9363) >> 16;
      const unsigned vwq = wrow * 896 + (iw - wrow * 7) * 16;
      const u32x4 rw = mkrs((const char*)p.wq + j * 112, 401408 - j * 112);
      if (wave == 0) { const unsigned co = (64 * j + lane) * 2;
        asm volatile("global_load_ushort %0, %4, %5\n\tglobal_load_ushort %1, %4, %6\n\tglobal_load_ushort %2, %4, %7\n\tglobal_load_ushort %3, %4, %8"
                     : "=&v"(ua), "=&v"(ub), "=&v"(uc), "=&v"(ud) : "v"(co), "s"(p.lre), "s"(p.lim), "s"(p.ldt), "s"(p.dsk) : "memory"); }
      if (wave < 7) {
#pragma unroll
        for (int i = 0; i < 7; ++i) { NEXTK(); DMA(rx, vx, k * 128, lb, i * SK); DMA(rw, vwq, k * 57344, lb, i * SK + K_W); }
      } else {
#pragma unroll
        for (int i = 0; i < 7; ++i) { NEXTK(); DMA(rx, vx, k * 128, lb, i * SK); }
      }
      rxa = mkrs((const char*)p.xa + (size_t)c * 57344 + wa * 14336, 14336);
      rbr = mkrs((const char*)p.bre + (size_t)j * 57344 + wa * 14336, 14336);
      rbi = mkrs((const char*)p.bim + (size_t)j * 57344 + wa * 14336, 14336);
    }
  }
  STAMP(8);
  if (tid < 32 && (tid & ~1) != 16) ((LAS unsigned*)(lds + L_MISC))[tid] = 0;
  const int r32 = lane & 31, hi = lane >> 5;
  const int g4 = lane >> 4, tq = (lane & 15) >> 2, tp = lane & 3;
  const unsigned xsw = (r32 >> 1) & 7, lo16 = lane * 16;
  const int th = wa >> 1, chh = wa & 1;
  u64 tag;
  const __amdgpu_buffer_rsrc_t rs_ws = __builtin_amdgcn_make_buffer_rsrc(ws, 0, WS_END, 0x00020000);
  const f32x16 zero16 = {};
  const unsigned o_own = WS_O + (((2 * c + th) * 16 + 2 * j + chh) * 2) * 1024;
#define MARK() do { const u32x4 ones = {~0u, ~0u, ~0u, ~0u};       \
    BST(ones, rs_ws, lo16, o_own); BST(ones, rs_ws, lo16 + 1024, o_own); } while (0)
#define LDEPOCH() asm volatile("s_load_dwordx2 %0, %1, 0x0\n\ts_waitcnt lgkmcnt(0)" : "=s"(tag) : "s"(ws) : "memory"); tag = (tag | 1) + 2

  if (isB) {
    if (wave == 0 && j < 7) {
      asm volatile("s_waitcnt vmcnt(14)" : "+v"(ua), "+v"(ub), "+v"(uc), "+v"(ud) :: "memory");
      const float pa = bf2f(ua), pb = bf2f(ub), dt = rbf(expf(bf2f(uc)));
      const float er = expf(dt * pa); float sn, cs; sincosf(dt * pb, &sn, &cs);
      const float nr = er * cs - 1.f, ni = er * sn, den = pa * pa + pb * pb;
      const cf lam = {er * cs, -ni};
      cf l16 = cmul(lam, lam); l16 = cmul(l16, l16); l16 = cmul(l16, l16); l16 = cmul(l16, l16);
      cf l64 = cmul(l16, l16); l64 = cmul(l64, l64);
      *(LAS f32x4*)(lds + L_EX + lo16) = (f32x4){lam.r, lam.i, 2.f * (nr * pa + ni * pb) / den, 2.f * (ni * pa - nr * pb) / den};
      *(LAS f32x4*)(lds + L_EX + 1024 + lo16) = (f32x4){l16.r, l16.i, l64.r, l64.i};
      *(LAS unsigned*)(lds + L_EX + 2048 + lane * 4) = ud;
    }
    LDEPOCH();
    if (tid == 0) *(LAS u64*)(lds + L_TAG) = tag;
    bf16x8 Q0[4], Q1[4];
    {
      const unsigned xq_off = (32 * th + r32) * 128, wq_off = (8 * (g4 >> 1) + tq) * 112 + (28 * chh + 16 * (g4 & 1) + 4 * tp) * 2;
      bf16x8 fax[4], faw[4], fbx[4], fbw[4];
      f32x16 q = zero16;
#define QRD(B, X, W) _Pragma("unroll") for (int ks = 0; ks < 4; ++ks) { B##x[ks] = ldsf(lds + (X) + xq_off + (((2 * ks + hi) ^ xsw) << 4)); B##w[ks] = ldstr2(lds + (W) + wq_off + ks * 1792, 448); } SBAR()
#define QMM(B) _Pragma("unroll") for (int ks = 0; ks < 4; ++ks) q = MFMA(B##w[ks], B##x[ks], q); PIN(q); SBAR()
      if (j == 7) {
        VMW(6); BAR(); VMW(0); BAR();
        const int i0 = wa * 128 + lane, r0 = (i0 * 9363) >> 16, r1 = ((i0 + 64) * 9363) >> 16;
        const unsigned vq0 = r0 * 896 + (i0 - r0 * 7) * 16, vq1 = r1 * 896 + (i0 + 64 - r1 * 7) * 16, lb = wave * 2048;
        const u32x4 rw = mkrs((const char*)p.wq + 784, 401408 - 784);
        if (KVW == 1) lds_wait(cPQ, 4);
        if (KVW == 2) lds_wait(cCR, 4);
        int k = k0;
#pragma unroll
        for (int i = 0; i < 7; ++i) { NEXTK(); DMA(rw, vq0, k * 57344, lb, K_WQ + i * 7168); if (wave < 3) DMA(rw, vq1, k * 57344, lb, K_WQ + i * 7168 + 1024); }
        VMW(0);
        lds_barrier(cOC, 4);
        QRD(fa, 0, K_WQ); QRD(fb, SK, K_WQ + 7168); QMM(fa); QRD(fa, 2 * SK, K_WQ + 2 * 7168); QMM(fb); QRD(fb, 3 * SK, K_WQ + 3 * 7168); QMM(fa);
        QRD(fa, 4 * SK, K_WQ + 4 * 7168); QMM(fb); QRD(fb, 5 * SK, K_WQ + 5 * 7168); QMM(fa); QRD(fa, 6 * SK, K_WQ + 6 * 7168); QMM(fb); QMM(fa);
        lds_wait(cPQ, 4);
      } else {
        VMW(6); BAR();
        QRD(fa, 0, K_W); QRD(fb, SK, SK + K_W); QMM(fa); QRD(fa, 2 * SK, 2 * SK + K_W); QMM(fb); QRD(fb, 3 * SK, 3 * SK + K_W); QMM(fa);
        VMW(0); BAR();
        QRD(fa, 4 * SK, 4 * SK + K_W); QMM(fb); QRD(fb, 5 * SK, 5 * SK + K_W); QMM(fa); QRD(fa, 6 * SK, 6 * SK + K_W); QMM(fb); QMM(fa);
      }
      STAMP(1);
      lds_barrier(cQD, 4);
      if (tid == 0) __hip_atomic_store((GAS u64*)(ws + WS_FP + (c * 8 + j) * 128), tag, RLX_AGENT);
      const float qs = 0.18898223650461361f * 1.4426950408889634f;
#pragma unroll
      for (int r = 0; r < 16; ++r) q[r] *= qs;
      if (hi) { q[12] = 0.f; q[13] = 0.f; q[14] = 0.f; q[15] = 0.f; }
      Q0[3] = pack8(q, 0); Q1[3] = pack8(q, 1);
      *(LAS bf16x8*)(lds + L_QS + wave * 2048 + lo16) = Q0[3];
      *(LAS bf16x8*)(lds + L_QS + wave * 2048 + 1024 + lo16) = Q1[3];
      lds_barrier(cQD, 8);
#pragma unroll
      for (int i = 0; i < 3; ++i) { Q0[i] = ldsf(lds + L_QS + (wave ^ (3 - i)) * 2048 + lo16); Q1[i] = ldsf(lds + L_QS + (wave ^ (3 - i)) * 2048 + 1024 + lo16); }
      lds_signal(cQD);
    }
    KV A0, A1, A2, A3;
    A3.v1 = (bf16x8){};
    f32x16 O[4] = {zero16, zero16, zero16, zero16};
    f32x16 Sa, Sb = zero16; u32x4 Pa, Pb = {};
    PIN(O[0]); PIN(O[1]); PIN(O[2]); PIN(O[3]); PIN(Sb); PIN(Pb); PIN(A3.v1);
    STAMP(2);
    poll(ws, WS_FKV + (wave * 8 + (lane & 7)) * 128, tag);
    STAMP(3);
    const __amdgpu_buffer_rsrc_t rs_kv = __builtin_amdgcn_make_buffer_rsrc(ws + WS_KV + wave * 65536, 0, 65536, 0x00020000);
#define LDKV(B, T) do { const unsigned so_ = (((T) + loc) & 15) * 4096; \
                        B.k0 = BF(BLD(rs_kv, lo16, so_)); B.k1 = BF(BLD(rs_kv, lo16, so_ + 1024)); \
                        B.v0 = BF(BLD(rs_kv, lo16, so_ + 2048)); B.v1 = BF(BLD(rs_kv, lo16, so_ + 3072)); } while (0)
#define LDK(B, T) do { const unsigned so_ = (((T) + loc) & 15) * 4096; B.k0 = BF(BLD(rs_kv, lo16, so_)); B.k1 = BF(BLD(rs_kv, lo16, so_ + 1024)); } while (0)
#define LDV(B, T) do { const unsigned so_ = (((T) + loc) & 15) * 4096; B.v0 = BF(BLD(rs_kv, lo16, so_ + 2048)); B.v1 = BF(BLD(rs_kv, lo16, so_ + 3072)); } while (0)
    LDKV(A0, 0); LDKV(A1, 1); LDKV(A2, 2); LDK(A3, 3);
    SBAR();
    Sa = MFMA(A0.k0, Q0[0], zero16); Sa = MFMA(A0.k1, Q1[0], Sa);
    STAMP(9);
#define UNIT(S, Sn, KN, QN, VP, OP, VC, OC, X) do { \
      Pb[2] = pk(Sn[12], Sn[13]); Pb[3] = pk(Sn[14], Sn[15]); \
      if ((QN) >= 0) Sn = MFMA(KN.k0, Q0[(QN) & 3], zero16); S[0] = EX(S[0]); S[1] = EX(S[1]); S[2] = EX(S[2]); S[3] = EX(S[3]); SBAR(); \
      if ((QN) >= 0) Sn = MFMA(KN.k1, Q1[(QN) & 3], Sn); Pa[0] = pk(S[0], S[1]); Pa[1] = pk(S[2], S[3]); S[4] = EX(S[4]); S[5] = EX(S[5]); S[6] = EX(S[6]); S[7] = EX(S[7]); SBAR(); \
      X; SBAR(); \
      OP = MFMA(VP.v1, BF(Pb), OP); Pa[2] = pk(S[4], S[5]); Pa[3] = pk(S[6], S[7]); S[8] = EX(S[8]); S[9] = EX(S[9]); S[10] = EX(S[10]); S[11] = EX(S[11]); SBAR(); \
      OC = MFMA(VC.v0, BF(Pa), OC); Pb[0] = pk(S[8], S[9]); Pb[1] = pk(S[10], S[11]); S[12] = EX(S[12]); S[13] = EX(S[13]); S[14] = EX(S[14]); S[15] = EX(S[15]); SBAR(); \
    } while (0)
#define TILE(CUR, NXT, PRV, T, LD) do { \
      UNIT(Sa, Sb, CUR, 1, PRV, O[3], CUR, O[0], ); \
      LDV(PRV, (T) + 3); SBAR(); \
      UNIT(Sb, Sa, CUR, 2, CUR, O[0], CUR, O[1], ); \
      UNIT(Sa, Sb, CUR, 3, CUR, O[1], CUR, O[2], if (LD) LDK(CUR, (T) + 4)); \
      UNIT(Sb, Sa, NXT, 0, CUR, O[2], CUR, O[3], ); } while (0)
#pragma unroll 1
    for (int t = 0; t < 12; t += 4) {
      TILE(A0, A1, A3, t, 1); TILE(A1, A2, A0, t + 1, 1); TILE(A2, A3, A1, t + 2, 1); TILE(A3, A0, A2, t + 3, 1);
    }
    TILE(A0, A1, A3, 12, 0);
    const lptr oc = lds + L_OC + lo16;
#define OUTP(T, I) _Pragma("unroll") for (int q4 = 0; q4 < 4; ++q4) *(LAS f32x4*)(oc + ((wave ^ I) * 3 + I - 1) * 4096 + q4 * 1024) = (f32x4){T[4 * q4], T[4 * q4 + 1], T[4 * q4 + 2], T[4 * q4 + 3]}
    f32x4 pv[12];
    UNIT(Sa, Sb, A1, 1, A0, O[3], A1, O[0], ); UNIT(Sb, Sa, A1, 2, A1, O[0], A1, O[1], ); UNIT(Sa, Sb, A2, 0, A1, O[1], A1, O[2], );
    UNIT(Sb, Sa, A2, 1, A1, O[2], A2, O[0], ); UNIT(Sa, Sb, A2, 2, A2, O[0], A2, O[1], ); UNIT(Sb, Sa, A3, 0, A2, O[1], A2, O[2], );
    UNIT(Sa, Sb, A3, 1, A2, O[2], A3, O[0], ); UNIT(Sb, Sa, A3, 2, A3, O[0], A3, O[1], lds_wait(j < 7 ? cCR : cPB, 4); lds_wait(cQD, 12));
    UNIT(Sa, Sb, A1, 3, A3, O[1], A3, O[2], OUTP(O[0], 3));
    UNIT(Sb, Sa, A2, 3, A3, O[2], A1, O[3], OUTP(O[1], 2));
    UNIT(Sa, Sb, A3, 3, A1, O[3], A2, O[3], OUTP(O[2], 1); lds_signal(cBB));
    UNIT(Sb, Sa, A3, -1, A2, O[3], A3, O[3], lds_wait(cBB, 4); _Pragma("unroll") for (int s2 = 0; s2 < 12; ++s2) pv[s2] = *(LAS f32x4*)(oc + wave * 12288 + s2 * 1024));
    Pb[2] = pk(Sb[12], Sb[13]); Pb[3] = pk(Sb[14], Sb[15]);
    O[3] = MFMA(A3.v1, BF(Pb), O[3]);
    STAMP(4);
    __builtin_amdgcn_s_setprio(2);
    lds_signal(cGO + 4 * wave);
#pragma unroll
    for (int s2 = 0; s2 < 4; ++s2) pv[s2] = pv[s2] + pv[s2 + 4] + pv[s2 + 8];
    STAMP(10);
    unsigned lo16b = lo16; PIN(lo16b);
    const int lane = lo16b >> 4, r32 = lane & 31, hi = lane >> 5, g4 = lane >> 4, tq = (lane & 15) >> 2, tp = lane & 3;
    f32x16 o = O[3];
#pragma unroll
    for (int r = 0; r < 16; ++r) o[r] += pv[r >> 2][r & 3];
    {
      float l = swap_hi(o[12]);
      float am = 0.f;
#pragma unroll
      for (int r = 0; r < 16; r += 2) am = __builtin_fmaxf(__builtin_fmaxf(am, __builtin_fabsf(o[r])), __builtin_fabsf(o[r + 1]));
      const bool ok = (l >= 0x1p-90f) && (l <= 0x1p110f) && (am <= 0x1p126f);
      const bool slow = !wall(ok);
      if (slow) {
        const __amdgpu_buffer_rsrc_t rk = __builtin_amdgcn_make_buffer_rsrc(ws + WS_KV, 0, 262144, 0x00020000);
        poll(ws, WS_FKV + r32 * 128, tag);
        float m = -__builtin_inff();
#pragma unroll 1
        for (int t = 0; t < 64; ++t) {
          f32x16 s = MFMA(BF(BLD(rk, lo16, t * 4096)), Q0[3], zero16); s = MFMA(BF(BLD(rk, lo16 + 1024, t * 4096)), Q1[3], s);
#pragma unroll
          for (int r = 0; r < 16; ++r) m = __builtin_fmaxf(m, s[r]);
        }
        m = __builtin_fmaxf(swap_lo(m), swap_hi(m));
        o = zero16;
#pragma unroll 1
        for (int t = 0; t < 64; ++t) {
          f32x16 s = MFMA(BF(BLD(rk, lo16, t * 4096)), Q0[3], zero16); s = MFMA(BF(BLD(rk, lo16 + 1024, t * 4096)), Q1[3], s);
#pragma unroll
          for (int r = 0; r < 16; ++r) s[r] = EX(s[r] - m);
          o = MFMA(BF(BLD(rk, lo16 + 2048, t * 4096)), pack8(s, 0), o); o = MFMA(BF(BLD(rk, lo16 + 3072, t * 4096)), pack8(s, 1), o);
        }
        l = swap_hi(o[12]);
      }
      const float inv = __builtin_amdgcn_rcpf(l);
#pragma unroll
      for (int r = 0; r < 16; ++r) o[r] *= inv;
      if (hi) { o[12] = 0.f; o[13] = 0.f; o[14] = 0.f; o[15] = 0.f; }
      u32x4 f0 = U4(pack8(o, 0)), f1 = U4(pack8(o, 1));
      if (slow) {
#pragma unroll
        for (int k = 0; k < 4; ++k) { f0[k] = umin(f0[k], 0xfffffffeu); f1[k] = umin(f1[k], 0xfffffffeu); }
      }
      BST(f0, rs_ws, lo16, o_own); BST(f1, rs_ws, lo16 + 1024, o_own);
    }
    lds_signal(cOC);
    __builtin_amdgcn_s_setprio(0);
    STAMP(5);
    if (j == 7) { STAMP_OUT(); return; }
    {
      const CAS Params* kp = KARGS();
      const __amdgpu_buffer_rsrc_t rs_out = __builtin_amdgcn_make_buffer_rsrc(kp->out, 0, 2048 * 896 * 4, 0x00020000);
      lds_wait(cWO, 4);
      u32x4 of[16];
      const unsigned ob = WS_O + (((2 * c + th) * 16 + 8 * chh) * 2) * 1024;
#define LDO(k) of[k] = BLD(rs_ws, lo16 + ((k) & 3) * 1024, ob + ((k) >> 2) * 4096)
      const int rowb = 224 * chh + 4 * (g4 >> 1) + tq;
      const unsigned wb = (L_WO + rowb * 128 + ((32 * (g4 & 1) + 8 * tp) ^ ((tq & 2) << 5))) ^ (chh << 6);
      bf16x8 w0[16], w1[16];
#pragma unroll
      for (int k = 0; k < 16; ++k) w1[k] = ldstr2(lds + (wb ^ 64) + (28 * (k >> 1) + 16 * (k & 1)) * 128, 1024);
      SBAR();
      {
        unsigned have = 0;
#pragma unroll 1
        for (unsigned s = 0; s < SPIN_CAP; ++s) {
          const unsigned w = __builtin_amdgcn_raw_buffer_load_b32(rs_ws, (lane & 7) * 2048 + 2044, ob, SC1);
          const unsigned rdy = (s + 1 < SPIN_CAP ? (unsigned)__builtin_amdgcn_ballot_w64(w != ~0u) : ~0u) & 0xffu & ~have;
#pragma unroll
          for (int h = 0; h < 8; ++h) if (rdy >> h & 1) { LDO(2 * h); LDO(2 * h + 1); }
          have |= rdy;
          if (have == 0xffu) break;
          __builtin_amdgcn_s_sleep(2);
        }
      }
      __builtin_amdgcn_s_setprio(2);
      f32x16 acc0 = zero16, acc1 = zero16;
#define OBAD(h) (!wall(umax(umax(umax(umax(of[2 * (h)][0], of[2 * (h)][1]), umax(of[2 * (h)][2], of[2 * (h)][3])), umax(of[2 * (h) + 1][0], of[2 * (h) + 1][1])), umax(of[2 * (h) + 1][2], of[2 * (h) + 1][3])) != ~0u))
#define OHEAD(h) do { \
        if (OBAD(h)) { \
          _Pragma("unroll 1") for (unsigned s = 0; s < SPIN_CAP; ++s) { LDO(2 * h); LDO(2 * h + 1); if (!OBAD(h)) break; __builtin_amdgcn_s_sleep(2); } \
        } \
        acc1 = MFMA(BF(of[2 * h]), w1[2 * h], acc1); acc1 = MFMA(BF(of[2 * h + 1]), w1[2 * h + 1], acc1); PIN(acc1); \
        w0[2 * h] = ldstr2(lds + wb + 28 * h * 128, 1024); w0[2 * h + 1] = ldstr2(lds + wb + (28 * h + 16) * 128, 1024); } while (0)
      OHEAD(0); STAMP(6); OHEAD(1); OHEAD(2); OHEAD(3); OHEAD(4); OHEAD(5); OHEAD(6); OHEAD(7);
      STAMP(11);
      lds_wait(cOC, 4);
#pragma unroll
      for (int k = 0; k < 4; ++k) acc0 = MFMA(BF(of[k]), w0[k], acc0);
      SBAR();
      const lptr tw = lds + L_OC + hi * 512 + r32 * 4;
#pragma unroll
      for (int r = 0; r < 16; ++r) *(LAS float*)(tw + (wave ^ 1) * 4096 + ((r & 3) + 8 * (r >> 2)) * 128) = acc1[r];
      SBAR();
#pragma unroll
      for (int k = 4; k < 8; ++k) acc0 = MFMA(BF(of[k]), w0[k], acc0);
      PIN(acc0);
      lds_signal(cPR + 4 * th);
#pragma unroll
      for (int k = 8; k < 16; ++k) acc0 = MFMA(BF(of[k]), w0[k], acc0);
      PIN(acc0);
      lds_wait(cPR + 4 * th, 2);
      f32x4 pa[4];
#pragma unroll
      for (int k = 0; k < 4; ++k) pa[k] = *(LAS f32x4*)(lds + L_OC + wave * 4096 + k * 1024 + lo16);
#pragma unroll
      for (int r = 0; r < 16; ++r) *(LAS float*)(tw + 16384 + wave * 4096 + ((r & 3) + 8 * (r >> 2)) * 128) = acc0[r];
      MEMBAR();
      STAMP(12);
      const unsigned oo = ((64 * c + 32 * th + (lane >> 3)) * 896 + 448 + 64 * j + 32 * chh + (lane & 7) * 4) * 4;
#pragma unroll
      for (int k = 0; k < 4; ++k)
        __builtin_amdgcn_raw_buffer_store_b128(U4(pa[k] + *(LAS f32x4*)(lds + L_OC + 16384 + wave * 4096 + k * 1024 + lo16)), rs_out, oo + k * 28672, 0, 17);
    }
    STAMP(7);
    STAMP_OUT();
  } else {
    if (j == 7) {
      VMW(6); BAR();
      { const u32x2 t2 = *(LAS u32x2*)(lds + L_TAG);
        tag = ((u64)(unsigned)__builtin_amdgcn_readfirstlane(t2[1]) << 32) | (unsigned)__builtin_amdgcn_readfirstlane(t2[0]); }
      {
        const unsigned xk_off = (32 * chh + r32) * 128, wk_off = K_W + th * 3584 + (8 * (g4 >> 1) + tq) * 56 + (16 * (g4 & 1) + 4 * tp) * 2;
        bf16x8 fax[4], faw[4], fbx[4], fbw[4];
        f32x16 a = zero16;
#define KRD(B, i) _Pragma("unroll") for (int ks = 0; ks < 4; ++ks) { B##x[ks] = ldsf(lds + (i) * SK + xk_off + (((2 * ks + hi) ^ xsw) << 4)); B##w[ks] = ldstr2(lds + (i) * SK + wk_off + ks * 896, 224); } SBAR()
#define KMM(B) if (th) { _Pragma("unroll") for (int ks = 0; ks < 4; ++ks) a = MFMA(B##x[ks], B##w[ks], a); } else { _Pragma("unroll") for (int ks = 0; ks < 4; ++ks) a = MFMA(B##w[ks], B##x[ks], a); } PIN(a); SBAR()
        KRD(fa, 0); KRD(fb, 1); KMM(fa); KRD(fa, 2); KMM(fb); KRD(fb, 3); KMM(fa);
        VMW(0); BAR();
        KRD(fa, 4); KMM(fb); KRD(fb, 5); KMM(fa); KRD(fa, 6); KMM(fb); KMM(fa);
        STAMP(1);
        if (th) {
#pragma unroll
          for (int r = 0; r < 16; ++r) a[r] = r32 < 28 ? a[r] : (r32 == 28 ? 1.f : 0.f);
        } else if (hi) { a[12] = 0.f; a[13] = 0.f; a[14] = 0.f; a[15] = 0.f; }
        const unsigned ko = WS_KV + (2 * c + chh) * 4096 + th * 2048;
        BST(U4(pack8(a, 0)), rs_ws, lo16, ko); BST(U4(pack8(a, 1)), rs_ws, lo16 + 1024, ko);
        lds_signal(cCR);
        publish4(ws, WS_FKV + c * 128, tag, lane, cPQ);
        STAMP(2);
      }
      lds_wait_<8>(cQD, 8);
      __builtin_amdgcn_s_sleep(64);
      MARK();
      publish4(ws, WS_FH + (c * 8 + 7) * 128, tag, lane, cPB);
      if (c == 0 && wa == 0) {
        __builtin_amdgcn_s_sleep(127);
#pragma unroll 1
        for (int k = 0; k < 4; ++k) poll(ws, WS_FP + (k * 64 + lane) * 128, tag);
        if (lane == 0) __hip_atomic_store((GAS u64*)(ws + WS_EPOCH), tag, RLX_AGENT);
      }
      STAMP_OUT();
      return;
    }
    if (wave < 7) VMW(6); else VMW(3);
    BAR(); LDCH(L_SA); LDCH(L_SA + CH); VMW(12); BAR();
    cf lam, coef, l16, l64; f32x16 ur = zero16, ui = zero16;
    f32x16 acc[4] = {zero16, zero16, zero16, zero16};
    const unsigned cs0 = wa * 16384u, cs1 = L_R1 + wa * 12288u;
    unsigned a_htw = L_HT + wa * 4096 + r32 * 64 + hi * 32, a_htr = L_HT + wa * 4096 + (8 * (g4 >> 1) + tq) * 64 + (g4 & 1) * 32 + tp * 8;
    unsigned a_hst = WS_H + ((2 * c + th) * 56 + 4 * j + 2 * chh) * 1024 + lo16;
    unsigned a_c[4], a_cn[2], a_x[2], a_ex, e_want; u32x4 dg[2]; cf l256, lw, lo_;
    const int seg = 2 * th + hi;
#pragma unroll
    for (int f = 0; f < 4; ++f) a_c[f] = cs0 + (32 * chh + r32) * 128 + (((2 * f + hi) ^ xsw) << 4);
#pragma unroll
    for (int f = 0; f < 2; ++f) a_cn[f] = cs1 + 8192 + (32 * chh + r32) * 64 + (((2 * f + hi) ^ ((r32 >> 2) & 3)) << 4);
    unsigned a_vh = (lane >> 2) * 896 + (((lane & 3) ^ ((lane >> 4) & 3)) << 4) + 384 + chh * 64, a_vo = ve ^ 64;
    unsigned a_vw = lrow * 896 + (((lane & 7) ^ ((lrow & 2) << 1)) << 4);
    unsigned a_fl = WS_FH + (c * 8 + (lane & 7)) * 128;
    unsigned a_el = (31 - umin(r32, c - 1)) * 512 + 4, a_eo = (32 - c + 2 * th + hi) * 512 + r32 * 16, a_hx = L_EX + 2048 + (32 * chh + r32) * 8;
    {
      LDCH(0); LDCH(CH);
      { const f32x4 c0 = *(LAS f32x4*)(lds + L_EX + (32 * chh + r32) * 16), c1 = *(LAS f32x4*)(lds + L_EX + 1024 + (32 * chh + r32) * 16);
        lam = {c0[0], c0[1]}; coef = {c0[2], c0[3]}; l16 = {c1[0], c1[1]}; l64 = {c1[2], c1[3]}; ud = *(LAS unsigned*)(lds + L_EX + 2048 + (32 * chh + r32) * 4); }
      { const int e = r32 - 8 * hi; const unsigned dv = ud << ((e & 1) * 16);
#pragma unroll
        for (int s2 = 0; s2 < 2; ++s2) {
          dg[s2] = (u32x4){(e >> 1) == 8 * s2 ? dv : 0u, (e >> 1) == 8 * s2 + 1 ? dv : 0u, (e >> 1) == 8 * s2 + 2 ? dv : 0u, (e >> 1) == 8 * s2 + 3 ? dv : 0u};
          a_x[s2] = L_SA + (32 * th + r32) * 128 + (((4 * chh + 2 * s2 + hi) ^ xsw) << 4); PIN(dg[s2]); PIN(a_x[s2]); }
        cf lseg = hi ? l16 : (cf){1.f, 0.f};
        if (th) lseg = cmul(lseg, cmul(l16, l16));
        const cf l128 = cmul(l64, l64), ls128 = cmul(lseg, l128);
        l256 = cmul(l128, l128); lw = th ? ls128 : lseg; lo_ = th ? lseg : ls128; e_want = (c - seg + 3) >> 2; a_ex = L_EX + (32 * chh + r32) * 8;
        PIN(l256.r); PIN(l256.i); PIN(lw.r); PIN(lw.i); PIN(lo_.r); PIN(lo_.i); PIN(e_want); PIN(a_ex); }
      PIN(a_htw); PIN(a_htr); PIN(a_hst); PIN(a_c[0]); PIN(a_c[1]); PIN(a_c[2]); PIN(a_c[3]); PIN(a_cn[0]); PIN(a_cn[1]); PIN(a_vh); PIN(a_vw); PIN(a_fl); PIN(a_el); PIN(a_eo); PIN(a_hx); PIN(a_vo);
      const int trow = 32 * th + 16 * ((r32 >> 2) & 1) + (r32 & 3) + 4 * (r32 >> 3);
      const unsigned xa_off = trow * 128, xa_sw = (trow >> 1) & 7, b_off = 8192 + (32 * chh + r32) * 128;
      bf16x8 gax[4], gar[4], gai[4], gbx[4], gbr[4], gbi[4];
      unsigned gx[3][4], gw[3][4];
#define GB(s_) ((s_) < 49152 ? 0u : (s_) < 107520 ? 49152u : 107520u)
#define GT(s_) ((s_) < 49152 ? 0 : (s_) < 107520 ? 1 : 2)
#pragma unroll
      for (int t = 0; t < 3; ++t)
#pragma unroll
        for (int ks = 0; ks < 4; ++ks) { gx[t][ks] = GB(t * 60000) + xa_off + (((2 * ks + hi) ^ xa_sw) << 4); gw[t][ks] = GB(t * 60000) + b_off + (((2 * ks + hi) ^ xsw) << 4); PIN(gx[t][ks]); PIN(gw[t][ks]); }
#define GRD(B, s_) _Pragma("unroll") for (int ks = 0; ks < 4; ++ks) { B##x[ks] = ldsf(lds + gx[GT(s_)][ks] + ((s_) - GB(s_))); \
        B##r[ks] = ldsf(lds + gw[GT(s_)][ks] + ((s_) - GB(s_))); B##i[ks] = ldsf(lds + gw[GT(s_)][ks] + ((s_) - GB(s_) + 8192)); } SBAR()
#define GMM(B) _Pragma("unroll") for (int ks = 0; ks < 4; ++ks) { ur = MFMA(B##x[ks], B##r[ks], ur); ui = MFMA(B##x[ks], B##i[ks], ui); } PIN(ur); PIN(ui); SBAR()
#define GAP() do { SBAR(); asm volatile("s_nop %0" :: "i"(7)); SBAR(); } while (0)
#define GMS(B) _Pragma("unroll") for (int ks = 0; ks < 4; ++ks) { ur = MFMA(B##x[ks], B##r[ks], ur); GAP(); ui = MFMA(B##x[ks], B##i[ks], ui); GAP(); } PIN(ur); PIN(ui); SBAR()
      __builtin_amdgcn_s_setprio(1);
      STAMP(12);
      VMW(12); lds_barrierA(cAB, 4); GRD(ga, L_SA); GRD(gb, L_SA + CH); GMM(ga);
      lds_wait_<1>(cQD, 4);
      LDCH(2 * CH); GMM(gb); LDCH(3 * CH);
      VMW(12); lds_barrierA(cAB, 8);
      STAMP(13);
      GRD(ga, 0); GRD(gb, CH); LDCH(L_SA); GMM(ga);
      VMW(6); lds_barrierA(cAB, 12);
      STAMP(14);
      GRD(ga, 2 * CH); GMS(gb); GRD(gb, 3 * CH); GMS(ga);
      VMW(0); lds_barrierA(cAB, 16);
      STAMP(15);
      GRD(ga, L_SA); GMS(gb); GMS(ga);
      { const u32x2 t2 = *(LAS u32x2*)(lds + L_TAG);
        tag = ((u64)(unsigned)__builtin_amdgcn_readfirstlane(t2[1]) << 32) | (unsigned)__builtin_amdgcn_readfirstlane(t2[0]); }
    }
    acc[0] = MFMA(ldsf(lds + a_x[0]), BF(dg[0]), acc[0]); acc[0] = MFMA(ldsf(lds + a_x[1]), BF(dg[1]), acc[0]);
    PIN(acc[0]);
    STAMP(1);
    cf e = {0.f, 0.f};
#pragma unroll
    for (int r = 0; r < 16; ++r) {
      const float a = coef.r * ur[r] - coef.i * ui[r], b2 = -(coef.r * ui[r] + coef.i * ur[r]);
      ur[r] = a; ui[r] = b2; e = r ? cfma(lam, e, (cf){a, b2}) : (cf){a, b2};
    }
    *(LAS f32x2*)(lds + a_ex + th * 1024 + hi * 512) = (f32x2){e.r, e.i};
    __builtin_amdgcn_s_setprio(0);
    lds_barrierA(cAB, 20);
    cf g0;
    {
      const f32x2 e0 = *(LAS f32x2*)(lds + a_ex), e1 = *(LAS f32x2*)(lds + a_ex + 512), e2 = *(LAS f32x2*)(lds + a_ex + 1024), e3 = *(LAS f32x2*)(lds + a_ex + 1536);
      __builtin_amdgcn_s_setprio(1);
      const cf E2 = cfma(l16, (cf){e0[0], e0[1]}, (cf){e1[0], e1[1]}), E3 = cfma(l16, E2, (cf){e2[0], e2[1]}), E = cfma(l16, E3, (cf){e3[0], e3[1]});
      if (th) g0 = hi ? E3 : E2; else g0 = hi ? (cf){e0[0], e0[1]} : (cf){0.f, 0.f};
      PIN(g0.r); PIN(g0.i);
      if (th == 0 && hi == 0 && c < 31)
        BST(((u32x4){f2u(E.r), (unsigned)tag, f2u(E.i), (unsigned)tag}), rs_ws, r32 * 16, WS_E + ((2 * j + chh) * 32 + 31 - c) * 512);
    }
    __builtin_amdgcn_s_setprio(0);
    const CAS Params* kp = KARGS();
    {
      const u32x4 rc = mkrs((const char*)(th ? kp->cim : kp->cre) + (size_t)j * 57344, 57344);
      int n2 = (6 * cl + 2 * ((b & 7) % 3)) % 24;
#pragma unroll
      for (int n = 0; n < 24; ++n) {
        DMA(rc, (n & 1) ? a_vo : ve, (n2 & 7) * 7168 + (n2 >> 3) * 128 + chh * 512, (n2 < 16 ? cs0 : cs1 - 16384) + n2 * 1024, 0);
        n2 = n2 == 23 ? 0 : n2 + 1; }
#pragma unroll
      for (int n = 0; n < 4; ++n) DMA(rc, a_vh, n * 14336, cs1, 8192 + n * 1024);
    }
    STAMP(2);
    cf G = g0;
    if (c > 0) {
      const __amdgpu_buffer_rsrc_t re = __builtin_amdgcn_make_buffer_rsrc(ws + WS_E + (2 * j + chh) * 16384, 0, 16384, 0x00020000);
      u32x4 ev[8];
#pragma unroll 1
      for (unsigned s = 0; s < SPIN_CAP; ++s) {
        const unsigned t1 = __builtin_amdgcn_raw_buffer_load_b32(re, a_el, 0, SC1);
        if (wall(t1 == (unsigned)tag)) break;
        __builtin_amdgcn_s_sleep(2);
      }
      MEMBAR();
#pragma unroll 1
      for (unsigned s = 0; s < SPIN_CAP; ++s) {
        unsigned got = 0;
#pragma unroll
        for (int m = 0; m < 8; ++m) ev[m] = BLD(re, a_eo, m * 2048);
        VMW(0);
        __builtin_amdgcn_s_setprio(1);
#pragma unroll
        for (int m = 0; m < 8; ++m) got += ((ev[m][1] ^ (unsigned)tag) | (ev[m][3] ^ (unsigned)tag)) == 0;
        if (wall(got == e_want)) break;
        __builtin_amdgcn_s_setprio(0);
        __builtin_amdgcn_s_sleep(4);
      }
      STAMP(3);
      cf H = {u2f(ev[7][0]), u2f(ev[7][2])};
#pragma unroll
      for (int m = 6; m >= 0; --m) H = cfma(l256, H, (cf){u2f(ev[m][0]), u2f(ev[m][2])});
      H = cfma(l64, (cf){swap_hi(H.r), swap_hi(H.i)}, (cf){swap_lo(H.r), swap_lo(H.i)});
      *(LAS f32x2*)(lds + a_hx + th * 512) = (f32x2){H.r, H.i};
      G = cfma(lw, H, g0);
      __builtin_amdgcn_s_setprio(0);
      lds_barrierA(cPE + 4 * chh, 2);
      const f32x2 ho = *(LAS f32x2*)(lds + a_hx + (th ^ 1) * 512);
      __builtin_amdgcn_s_setprio(1);
      G = cfma(lo_, (cf){ho[0], ho[1]}, G);
    }
    STAMP(4);
    __builtin_amdgcn_s_setprio(1);
    {
#pragma unroll
      for (int r = 0; r < 16; ++r) { G = cfma(lam, G, (cf){ur[r], ui[r]}); ur[r] = G.r; ui[r] = G.i; }
      *(LAS bf16x8*)(lds + a_htw) = pack8(ur, 0); *(LAS bf16x8*)(lds + a_htw + 16) = pack8(ur, 1);
      *(LAS bf16x8*)(lds + a_htw + 2048) = pack8(ui, 0); *(LAS bf16x8*)(lds + a_htw + 2064) = pack8(ui, 1);
      unsigned a_hs2 = a_hst + 28 * 1024; PIN(a_hs2);
      __builtin_amdgcn_s_setprio(0);
      asm volatile("s_waitcnt lgkmcnt(0)" ::: "memory");
#pragma unroll
      for (int k = 0; k < 4; ++k) BST(U4(ldstr2(lds + a_htr + (k >> 1) * 2048 + (k & 1) * 1024, 256)), rs_ws, (k >> 1) ? a_hs2 : a_hst, (k & 1) * 1024);
      MARK();
      publish4(ws, WS_FH + (c * 8 + j) * 128, tag, lane, cPB);
    }
    STAMP(5);
    bf16x8 cfr[14][2];
    const int cflip = chh ? -1 : 1;
#define LDC(f) do { if ((f) < 12) { const unsigned a_ = a_c[(f) & 3] + ((f) < 8 ? ((f) >> 2) * 8192 : cs1 - cs0); cfr[f][0] = ldsf(lds + a_); cfr[f][1] = ldsf(lds + a_ + cflip * 4096); } \
                    else { cfr[f][0] = ldsf(lds + a_cn[(f) & 1]); cfr[f][1] = ldsf(lds + a_cn[(f) & 1] + cflip * 2048); } } while (0)
#pragma unroll
    for (int f = 0; f < 8; ++f) LDC(f);
    lds_signal(cCR);
    STAMP(10);
    lds_wait_<1>(cCR, 4);
    {
      const u32x4 rw = mkrs((const char*)kp->wo + j * 128 + wa * 7168, 401408 - j * 128 - wa * 7168);
      int m2 = (4 * cl + (b & 7)) % 14;
#pragma unroll
      for (int m = 0; m < 14; ++m) { DMA(rw, a_vw, m2 * 28672, wa * 1024 + m2 * 4096, L_WO); m2 = m2 == 13 ? 0 : m2 + 1; }
      if (wa == 0 && lane < 32) *(LAS u32x4*)(lds + L_WO + 57344 + lo16) = (u32x4){0, 0, 0, 0};
    }
    poll(ws, a_fl, tag);
    VMW(0);
    lds_signal(cWO);
    STAMP(6);
    {
      const unsigned hb = WS_H + (2 * c * 56 + 14 * wa) * 1024, h0 = hb + th * 57344, h1 = hb + (th ^ 1) * 57344;
      const unsigned fa = chh * 2048, fb = chh ? 0u - 12288u : 0u;
      u32x4 hf[14][2];
#define LDH(f) do { hf[f][0] = BLD(rs_ws, lo16, h0 + (f) * 1024 + ((f) < 12 ? fa : fb)); hf[f][1] = BLD(rs_ws, lo16, h1 + (f) * 1024 + ((f) < 12 ? fa : fb)); } while (0)
#define MMH(f) do { acc[0] = MFMA(BF(hf[f][0]), cfr[f][0], acc[0]); acc[1] = MFMA(BF(hf[f][0]), cfr[f][1], acc[1]); \
                    acc[2] = MFMA(BF(hf[f][1]), cfr[f][0], acc[2]); acc[3] = MFMA(BF(hf[f][1]), cfr[f][1], acc[3]); \
                    PIN(acc[0]); PIN(acc[1]); PIN(acc[2]); PIN(acc[3]); } while (0)
#pragma unroll
      for (int f = 0; f < 14; ++f) LDH(f);
      SBAR();
      lds_wait_<4>(cGO + 4 * wa, 1);
      STAMP(9);
      __builtin_amdgcn_s_setprio(1);
#pragma unroll
      for (int f = 0; f < 8; ++f) { MMH(f); SBAR(); if (f >= 1 && f < 7) LDC(f + 7); SBAR(); }
      lds_signal(cP4);
#pragma unroll
      for (int f = 8; f < 14; ++f) MMH(f);
    }
    lds_wait(cP4, 4);
    STAMP(11);
    rs_write(lds + L_G2, wa, lane, acc[1], acc[2], acc[3]);
    lds_barrierA(cAB, 24);
    f32x16 y = rs_read(lds + L_G2, wa, lane, acc[0]);
    const __amdgpu_buffer_rsrc_t rs_out = __builtin_amdgcn_make_buffer_rsrc(kp->out, 0, 2048 * 896 * 4, 0x00020000);
    MEMBAR();
#pragma unroll
    for (int r = 0; r < 16; ++r) *(LAS float*)(lds + L_G2 + wa * 12288 + hi * 512 + r32 * 4 + ((r & 3) + 8 * (r >> 2)) * 128) = y[r];
    MEMBAR();
    const unsigned oo = ((64 * c + 32 * th + (lane >> 3)) * 896 + 64 * j + 32 * chh + (lane & 7) * 4) * 4;
#pragma unroll
    for (int k = 0; k < 4; ++k) __builtin_amdgcn_raw_buffer_store_b128(*(LAS u32x4*)(lds + L_G2 + wa * 12288 + k * 1024 + lo16), rs_out, oo + k * 28672, 0, 17);
    STAMP(7);
    STAMP_OUT();
  }
}

extern "C" void kernel_launch(void* const* d_in, const int* in_sizes, int n_in, void* d_out, int out_size, void* d_ws, size_t ws_size,
                              hipStream_t stream) {
  Params p;
  p.xa = (const u16*)d_in[0]; p.lre = (const u16*)d_in[1]; p.lim = (const u16*)d_in[2]; p.bre = (const u16*)d_in[3]; p.bim = (const u16*)d_in[4];
  p.cre = (const u16*)d_in[5]; p.cim = (const u16*)d_in[6]; p.ldt = (const u16*)d_in[7]; p.dsk = (const u16*)d_in[8];
  p.xb = (const u16*)d_in[9]; p.wq = (const u16*)d_in[10]; p.wk = (const u16*)d_in[11]; p.wv = (const u16*)d_in[12]; p.wo = (const u16*)d_in[13];
  p.out = (float*)d_out; p.ws = (char*)d_ws;
  fused_kernel<<<256, 512, 0, stream>>>(p);
}
```

```cpp
#include <hip/hip_runtime.h>
#include <stdint.h>

#define DI __device__ __forceinline__
#define GAS __attribute__((address_space(1)))
#define LAS __attribute__((address_space(3)))
#define CAS __attribute__((address_space(4)))
typedef short bf16x8 __attribute__((ext_vector_type(8)));
typedef short s16x4 __attribute__((ext_vector_type(4)));
typedef float f32x16 __attribute__((ext_vector_type(16)));
typedef float f32x4 __attribute__((ext_vector_type(4)));
typedef float f32x2 __attribute__((ext_vector_type(2)));
typedef unsigned u32x4 __attribute__((ext_vector_type(4)));
typedef unsigned u32x2 __attribute__((ext_vector_type(2)));
typedef __bf16 bf16x2_t __attribute__((ext_vector_type(2)));
typedef unsigned long long u64;
typedef unsigned short u16;
typedef LAS char* lptr;
#define RLX_AGENT __ATOMIC_RELAXED, __HIP_MEMORY_SCOPE_AGENT
#define MFMA(a, b, c) __builtin_amdgcn_mfma_f32_32x32x16_bf16((a), (b), (c), 0, 0, 0)
#define SBAR() __builtin_amdgcn_sched_barrier(0)
#define PIN(x) asm volatile("" : "+v"(x))
#define MEMBAR() asm volatile("" ::: "memory")
#define SC1 16

constexpr unsigned WS_EPOCH = 0, WS_FKV = 4096, WS_FH = 8192, WS_E = 0x10000, WS_FP = 0x48000,
                   WS_KV = 0x50000, WS_O = 0x100000, WS_H = 0x300000, WS_END = 0x680000;
constexpr unsigned SK = 15360, K_W = 8192, K_WQ = 107520;
constexpr unsigned CH = 24576, L_SA = 107520, L_QS = 99328;
constexpr unsigned L_R1 = 107520, L_HT = 65536, L_WO = 0, L_OC = 57856, L_G2 = 107520, L_MISC = 159744, L_EX = L_MISC + 128, L_TOTAL = 163840;
constexpr unsigned cBB = L_MISC, cAB = L_MISC + 4, cCR = L_MISC + 8, cWO = L_MISC + 12, cOC = L_MISC + 16, cPB = L_MISC + 20, cPR = L_MISC + 24, cP4 = L_MISC + 32, cPQ = L_MISC + 52, cGO = L_MISC + 36, cPE = L_MISC + 56, cQD = L_MISC + 72, L_TAG = L_MISC + 64;
constexpr unsigned SPIN_CAP = 1u << 18;

struct Params {
  const u16 *xb, *wq, *wk, *wv; char* ws; const u16 *lre, *lim, *ldt, *dsk, *xa, *bre, *bim, *cre, *cim, *wo;
  float* out;
};

DI float u2f(unsigned v) { return __builtin_bit_cast(float, v); }
DI unsigned f2u(float v) { return __builtin_bit_cast(unsigned, v); }
DI unsigned umin(unsigned a, unsigned b) { return a < b ? a : b; }
DI unsigned umax(unsigned a, unsigned b) { return a > b ? a : b; }
DI bool wall(bool x) { return __builtin_amdgcn_ballot_w64(!x) == 0; }
DI float bf2f(unsigned v) { return u2f(v << 16); }
DI unsigned pk(float a, float b) { f32x2 v = {a, b}; return __builtin_bit_cast(unsigned, __builtin_convertvector(v, bf16x2_t)); }
DI float rbf(float a) { return u2f(pk(0.f, a)); }
DI bf16x8 pack8(const f32x16& x, int s) {
  u32x4 p = {pk(x[8 * s], x[8 * s + 1]), pk(x[8 * s + 2], x[8 * s + 3]), pk(x[8 * s + 4], x[8 * s + 5]), pk(x[8 * s + 6], x[8 * s + 7])};
  return __builtin_bit_cast(bf16x8, p);
}
DI u32x4 mkrs(const void* base, unsigned bytes) { const u64 a = (u64)base; return (u32x4){(unsigned)a, (unsigned)(a >> 32) & 0xffffu, bytes, 0x20000u}; }
#define DMA(rs, voff, soff, ldsb, IMM) asm volatile("s_add_u32 m0, %3, %4\n\ts_nop 0\n\tbuffer_load_dwordx4 %0, %1, %2 offen lds" \
                                                    :: "v"(voff), "s"(rs), "s"(soff), "s"(ldsb), "i"(IMM) : "memory", "scc")
DI bf16x8 ldsf(lptr p) { return *(LAS bf16x8*)p; }
DI s16x4 ldstr(lptr p) { return __builtin_amdgcn_ds_read_tr16_b64_v4i16((LAS s16x4*)p); }
DI bf16x8 ldstr2(lptr p, int second) { s16x4 lo = ldstr(p), hi = ldstr(p + second); return __builtin_shufflevector(lo, hi, 0, 1, 2, 3, 4, 5, 6, 7); }
DI void lds_signal(unsigned c) {
  asm volatile("s_waitcnt lgkmcnt(0)\n\ts_mov_b64 exec, 1\n\tds_add_u32 %0, %1\n\ts_mov_b64 exec, -1" :: "v"(c), "v"(1u) : "memory");
}
template <int SLEEP> DI void lds_wait_(unsigned c, unsigned target) {
  unsigned v, s, n;
  asm volatile("s_mov_b32 %2, 0x400000\n1:\n\tds_read_b32 %0, %3\n\ts_waitcnt lgkmcnt(0)\n\tv_readfirstlane_b32 %1, %0\n\ts_cmp_ge_u32 %1, %4\n\ts_cbranch_scc1 2f\n\t"
               "s_sleep %5\n\ts_sub_u32 %2, %2, 1\n\ts_cbranch_scc0 1b\n2:" : "=&v"(v), "=&s"(s), "=&s"(n) : "v"(c), "i"(target), "i"(SLEEP) : "memory", "scc");
}
#define lds_wait(c, t) lds_wait_<0>(c, t)
#define lds_barrier(c, t) do { lds_signal(c); lds_wait(c, t); } while (0)
#define lds_barrierA(c, t) do { lds_signal(c); lds_wait_<1>(c, t); } while (0)
DI void poll(const char* ws, unsigned off, u64 tag) {
#pragma unroll 1
  for (unsigned s = 0; s < SPIN_CAP; ++s) {
    u64 v = __hip_atomic_load((GAS u64*)(ws + off), RLX_AGENT);
    if (wall(v == tag)) break;
    __builtin_amdgcn_s_sleep(2);
  }
  MEMBAR();
}
DI void publish4(char* ws, unsigned off, u64 tag, int lane, unsigned cnt) {
  unsigned old;
  asm volatile("s_waitcnt vmcnt(0)\n\ts_mov_b64 exec, 1\n\tds_add_rtn_u32 %0, %1, %2\n\ts_waitcnt lgkmcnt(0)\n\ts_mov_b64 exec, -1" : "=&v"(old) : "v"(cnt), "v"(1u) : "memory");
  if (__builtin_amdgcn_readfirstlane(old) == 3 && lane == 0) __hip_atomic_store((GAS u64*)(ws + off), tag, RLX_AGENT);
}
DI float swap_lo(float x) {
  auto r = __builtin_amdgcn_permlane32_swap(f2u(x), f2u(x), false, false); return u2f(r[0]); }
DI float swap_hi(float x) {
  auto r = __builtin_amdgcn_permlane32_swap(f2u(x), f2u(x), false, false); return u2f(r[1]); }
struct cf { float r, i; };
DI cf cmul(cf a, cf b) { return {a.r * b.r - a.i * b.i, a.r * b.i + a.i * b.r}; }
DI cf cfma(cf a, cf b, cf c) { return {__builtin_fmaf(a.r, b.r, __builtin_fmaf(-a.i, b.i, c.r)), __builtin_fmaf(a.r, b.i, __builtin_fmaf(a.i, b.r, c.i))}; }

struct KV { bf16x8 k0, k1, v0, v1; };

DI void rs_write(lptr base, int w, int lane, const f32x16& t1, const f32x16& t2, const f32x16& t3) {
#define RSW(T, I) _Pragma("unroll") for (int q = 0; q < 4; ++q) *(LAS f32x4*)(base + ((w ^ I) * 3 + I - 1) * 4096 + q * 1024 + lane * 16) = (f32x4){T[4 * q], T[4 * q + 1], T[4 * q + 2], T[4 * q + 3]}
  RSW(t1, 1); RSW(t2, 2); RSW(t3, 3);
}
DI f32x16 rs_read(lptr base, int w, int lane, f32x16 a) {
  f32x4 v[12];
#pragma unroll
  for (int s = 0; s < 12; ++s) v[s] = *(LAS f32x4*)(base + w * 12288 + s * 1024 + lane * 16);
  SBAR();
#pragma unroll
  for (int s = 0; s < 12; ++s) { const int q = s & 3; a[4 * q] += v[s][0]; a[4 * q + 1] += v[s][1]; a[4 * q + 2] += v[s][2]; a[4 * q + 3] += v[s][3]; }
  return a;
}

#define VMW_(n) asm volatile("s_waitcnt vmcnt(" #n ")" ::: "memory")
#define VMW(n) VMW_(n)
#define BAR() asm volatile("s_waitcnt lgkmcnt(0)\n\ts_barrier" ::: "memory")
#define BLD(R, V, S) __builtin_amdgcn_raw_buffer_load_b128(R, V, S, SC1)
#define BST(D, R, V, S) __builtin_amdgcn_raw_buffer_store_b128(D, R, (V) + (S), 0, SC1)
#define BF(x) __builtin_bit_cast(bf16x8, x)
#define U4(x) __builtin_bit_cast(u32x4, x)
#define EX(x) __builtin_amdgcn_exp2f(x)
#define KARGS() ({ const CAS Params* kp_ = (const CAS Params*)__builtin_amdgcn_kernarg_segment_ptr(); asm volatile("" : "+s"(kp_)); kp_; })
#define NEXTK() do { k += step; k = k >= 7 ? k - 7 : k; } while (0)

#if defined(__HIP_DEVICE_COMPILE__)
#define NO_PK __attribute__((target("no-packed-fp32-ops")))
#else
#define NO_PK
#endif
__global__ void __launch_bounds__(512, 2) NO_PK fused_kernel(Params p) {
  __shared__ __attribute__((aligned(128))) char smem[L_TOTAL];
  const lptr lds = (lptr)smem;
  const int tid = threadIdx.x, lane = tid & 63, wave = __builtin_amdgcn_readfirstlane(tid >> 6);
  const int b = blockIdx.x, loc = b >> 3, cl = loc >> 3, c = (b & 7) * 4 + cl, j = loc & 7;
  const int lrow = lane >> 3;
  const unsigned ve = lrow * 896 + (((lane & 7) ^ (lrow >> 1)) << 4);
  const int step = cl + 1, k0 = j == 7 ? 0 : j;
  const bool isB = wave < 4;
  const int wa = wave & 3;
  char* const ws = p.ws;
  unsigned ua = 0, ub = 0, uc = 0, ud = 0;
  u32x4 rxa, rbr, rbi; int ka = k0;
  const unsigned vo = (ve ^ 64) + 7168, lba = wa * 2048;
#define NEXTKA() do { ka += step; ka = ka >= 7 ? ka - 7 : ka; } while (0)
#define LDCH(slot) do { NEXTKA(); const unsigned k_ = ka * 128; DMA(rxa, ve, k_, lba, (slot)); DMA(rxa, vo, k_, lba, (slot) + 1024); \
      DMA(rbr, ve, k_, lba, (slot) + 8192); DMA(rbr, vo, k_, lba, (slot) + 9216); DMA(rbi, ve, k_, lba, (slot) + 16384); DMA(rbi, vo, k_, lba, (slot) + 17408); } while (0)
  {
    const u32x4 rx = mkrs((const char*)p.xb + (size_t)c * 57344 + wave * 7168, 7168);
    const unsigned vx = ve ^ ((wave & 1) << 6), lb = wave * 1024;
    int k = k0;
    if (j == 7) {
      const u32x4 rw = mkrs(isB ? p.wk : p.wv, 25088);
      const unsigned vw = wa * 1024 + lane * 16, lw = K_W + (isB ? 0 : 3584) + wa * 1024; const bool half = wa < 3 || lane < 32;
#pragma unroll
      for (int i = 0; i < 7; ++i) { NEXTK(); DMA(rx, vx, k * 128, lb, i * SK); if (half) DMA(rw, vw, k * 3584, lw, i * SK); }
    } else {
      const int iw = wave * 64 + lane, wrow = (iw * 9363) >> 16;
      const unsigned vwq = wrow * 896 + (iw - wrow * 7) * 16;
      const u32x4 rw = mkrs((const char*)p.wq + j * 112, 401408 - j * 112);
      if (wave == 0) { const unsigned co = (64 * j + lane) * 2;
        asm volatile("global_load_ushort %0, %4, %5\n\tglobal_load_ushort %1, %4, %6\n\tglobal_load_ushort %2, %4, %7\n\tglobal_load_ushort %3, %4, %8"
                     : "=&v"(ua), "=&v"(ub), "=&v"(uc), "=&v"(ud) : "v"(co), "s"(p.lre), "s"(p.lim), "s"(p.ldt), "s"(p.dsk) : "memory"); }
      if (wave < 7) {
#pragma unroll
        for (int i = 0; i < 7; ++i) { NEXTK(); DMA(rx, vx, k * 128, lb, i * SK); DMA(rw, vwq, k * 57344, lb, i * SK + K_W); }
      } else {
#pragma unroll
        for (int i = 0; i < 7; ++i) { NEXTK(); DMA(rx, vx, k * 128, lb, i * SK); }
      }
      rxa = mkrs((const char*)p.xa + (size_t)c * 57344 + wa * 14336, 14336);
      rbr = mkrs((const char*)p.bre + (size_t)j * 57344 + wa * 14336, 14336);
      rbi = mkrs((const char*)p.bim + (size_t)j * 57344 + wa * 14336, 14336);
    }
  }
  if (tid < 32 && (tid & ~1) != 16) ((LAS unsigned*)(lds + L_MISC))[tid] = 0;
  const int r32 = lane & 31, hi = lane >> 5;
  const int g4 = lane >> 4, tq = (lane & 15) >> 2, tp = lane & 3;
  const unsigned xsw = (r32 >> 1) & 7, lo16 = lane * 16;
  const int th = wa >> 1, chh = wa & 1;
  u64 tag;
  const __amdgpu_buffer_rsrc_t rs_ws = __builtin_amdgcn_make_buffer_rsrc(ws, 0, WS_END, 0x00020000);
  const f32x16 zero16 = {};
  const unsigned o_own = WS_O + (((2 * c + th) * 16 + 2 * j + chh) * 2) * 1024;
#define MARK() do { const u32x4 ones = {~0u, ~0u, ~0u, ~0u};       \
    BST(ones, rs_ws, lo16, o_own); BST(ones, rs_ws, lo16 + 1024, o_own); } while (0)
#define LDEPOCH() asm volatile("s_load_dwordx2 %0, %1, 0x0\n\ts_waitcnt lgkmcnt(0)" : "=s"(tag) : "s"(ws) : "memory"); tag = (tag | 1) + 2

  if (isB) {
    if (wave == 0 && j < 7) {
      asm volatile("s_waitcnt vmcnt(14)" : "+v"(ua), "+v"(ub), "+v"(uc), "+v"(ud) :: "memory");
      const float pa = bf2f(ua), pb = bf2f(ub), dt = rbf(expf(bf2f(uc)));
      const float er = expf(dt * pa); float sn, cs; sincosf(dt * pb, &sn, &cs);
      const float nr = er * cs - 1.f, ni = er * sn, den = pa * pa + pb * pb;
      const cf lam = {er * cs, -ni};
      cf l16 = cmul(lam, lam); l16 = cmul(l16, l16); l16 = cmul(l16, l16); l16 = cmul(l16, l16);
      cf l64 = cmul(l16, l16); l64 = cmul(l64, l64);
      *(LAS f32x4*)(lds + L_EX + lo16) = (f32x4){lam.r, lam.i, 2.f * (nr * pa + ni * pb) / den, 2.f * (ni * pa - nr * pb) / den};
      *(LAS f32x4*)(lds + L_EX + 1024 + lo16) = (f32x4){l16.r, l16.i, l64.r, l64.i};
      *(LAS unsigned*)(lds + L_EX + 2048 + lane * 4) = ud;
    }
    LDEPOCH();
    if (tid == 0) *(LAS u64*)(lds + L_TAG) = tag;
    bf16x8 Q0[4], Q1[4];
    {
      const unsigned xq_off = (32 * th + r32) * 128, wq_off = (8 * (g4 >> 1) + tq) * 112 + (28 * chh + 16 * (g4 & 1) + 4 * tp) * 2;
      bf16x8 fax[4], faw[4], fbx[4], fbw[4];
      f32x16 q = zero16;
#define QRD(B, X, W) _Pragma("unroll") for (int ks = 0; ks < 4; ++ks) { B##x[ks] = ldsf(lds + (X) + xq_off + (((2 * ks + hi) ^ xsw) << 4)); B##w[ks] = ldstr2(lds + (W) + wq_off + ks * 1792, 448); } SBAR()
#define QMM(B) _Pragma("unroll") for (int ks = 0; ks < 4; ++ks) q = MFMA(B##w[ks], B##x[ks], q); PIN(q); SBAR()
      if (j == 7) {
        VMW(6); BAR(); VMW(0); BAR();
        const int i0 = wa * 128 + lane, r0 = (i0 * 9363) >> 16, r1 = ((i0 + 64) * 9363) >> 16;
        const unsigned vq0 = r0 * 896 + (i0 - r0 * 7) * 16, vq1 = r1 * 896 + (i0 + 64 - r1 * 7) * 16, lb = wave * 2048;
        const u32x4 rw = mkrs((const char*)p.wq + 784, 401408 - 784);
        lds_wait(cCR, 4);
        int k = k0;
#pragma unroll
        for (int i = 0; i < 7; ++i) { NEXTK(); DMA(rw, vq0, k * 57344, lb, K_WQ + i * 7168); if (wave < 3) DMA(rw, vq1, k * 57344, lb, K_WQ + i * 7168 + 1024); }
        VMW(0);
        lds_barrier(cOC, 4);
        QRD(fa, 0, K_WQ); QRD(fb, SK, K_WQ + 7168); QMM(fa); QRD(fa, 2 * SK, K_WQ + 2 * 7168); QMM(fb); QRD(fb, 3 * SK, K_WQ + 3 * 7168); QMM(fa);
        QRD(fa, 4 * SK, K_WQ + 4 * 7168); QMM(fb); QRD(fb, 5 * SK, K_WQ + 5 * 7168); QMM(fa); QRD(fa, 6 * SK, K_WQ + 6 * 7168); QMM(fb); QMM(fa);
        lds_wait(cPQ, 4);
      } else {
        VMW(6); BAR();
        QRD(fa, 0, K_W); QRD(fb, SK, SK + K_W); QMM(fa); QRD(fa, 2 * SK, 2 * SK + K_W); QMM(fb); QRD(fb, 3 * SK, 3 * SK + K_W); QMM(fa);
        VMW(0); BAR();
        QRD(fa, 4 * SK, 4 * SK + K_W); QMM(fb); QRD(fb, 5 * SK, 5 * SK + K_W); QMM(fa); QRD(fa, 6 * SK, 6 * SK + K_W); QMM(fb); QMM(fa);
      }
      lds_barrier(cQD, 4);
      if (tid == 0) __hip_atomic_store((GAS u64*)(ws + WS_FP + (c * 8 + j) * 128), tag, RLX_AGENT);
      const float qs = 0.18898223650461361f * 1.4426950408889634f;
#pragma unroll
      for (int r = 0; r < 16; ++r) q[r] *= qs;
      if (hi) { q[12] = 0.f; q[13] = 0.f; q[14] = 0.f; q[15] = 0.f; }
      Q0[3] = pack8(q, 0); Q1[3] = pack8(q, 1);
      *(LAS bf16x8*)(lds + L_QS + wave * 2048 + lo16) = Q0[3];
      *(LAS bf16x8*)(lds + L_QS + wave * 2048 + 1024 + lo16) = Q1[3];
      lds_barrier(cQD, 8);
#pragma unroll
      for (int i = 0; i < 3; ++i) { Q0[i] = ldsf(lds + L_QS + (wave ^ (3 - i)) * 2048 + lo16); Q1[i] = ldsf(lds + L_QS + (wave ^ (3 - i)) * 2048 + 1024 + lo16); }
      lds_signal(cQD);
    }
    KV A0, A1, A2, A3;
    A3.v1 = (bf16x8){};
    f32x16 O[4] = {zero16, zero16, zero16, zero16};
    f32x16 Sa, Sb = zero16; u32x4 Pa, Pb = {};
    PIN(O[0]); PIN(O[1]); PIN(O[2]); PIN(O[3]); PIN(Sb); PIN(Pb); PIN(A3.v1);
    poll(ws, WS_FKV + (wave * 8 + (lane & 7)) * 128, tag);
    const __amdgpu_buffer_rsrc_t rs_kv = __builtin_amdgcn_make_buffer_rsrc(ws + WS_KV + wave * 65536, 0, 65536, 0x00020000);
#define LDKV(B, T) do { const unsigned so_ = (((T) + loc) & 15) * 4096; \
                        B.k0 = BF(BLD(rs_kv, lo16, so_)); B.k1 = BF(BLD(rs_kv, lo16, so_ + 1024)); \
                        B.v0 = BF(BLD(rs_kv, lo16, so_ + 2048)); B.v1 = BF(BLD(rs_kv, lo16, so_ + 3072)); } while (0)
#define LDK(B, T) do { const unsigned so_ = (((T) + loc) & 15) * 4096; B.k0 = BF(BLD(rs_kv, lo16, so_)); B.k1 = BF(BLD(rs_kv, lo16, so_ + 1024)); } while (0)
#define LDV(B, T) do { const unsigned so_ = (((T) + loc) & 15) * 4096; B.v0 = BF(BLD(rs_kv, lo16, so_ + 2048)); B.v1 = BF(BLD(rs_kv, lo16, so_ + 3072)); } while (0)
    LDKV(A0, 0); LDKV(A1, 1); LDKV(A2, 2); LDK(A3, 3);
    SBAR();
    Sa = MFMA(A0.k0, Q0[0], zero16); Sa = MFMA(A0.k1, Q1[0], Sa);
#define UNIT(S, Sn, KN, QN, VP, OP, VC, OC, X) do { \
      Pb[2] = pk(Sn[12], Sn[13]); Pb[3] = pk(Sn[14], Sn[15]); \
      if ((QN) >= 0) Sn = MFMA(KN.k0, Q0[(QN) & 3], zero16); S[0] = EX(S[0]); S[1] = EX(S[1]); S[2] = EX(S[2]); S[3] = EX(S[3]); SBAR(); \
      if ((QN) >= 0) Sn = MFMA(KN.k1, Q1[(QN) & 3], Sn); Pa[0] = pk(S[0], S[1]); Pa[1] = pk(S[2], S[3]); S[4] = EX(S[4]); S[5] = EX(S[5]); S[6] = EX(S[6]); S[7] = EX(S[7]); SBAR(); \
      X; SBAR(); \
      OP = MFMA(VP.v1, BF(Pb), OP); Pa[2] = pk(S[4], S[5]); Pa[3] = pk(S[6], S[7]); S[8] = EX(S[8]); S[9] = EX(S[9]); S[10] = EX(S[10]); S[11] = EX(S[11]); SBAR(); \
      OC = MFMA(VC.v0, BF(Pa), OC); Pb[0] = pk(S[8], S[9]); Pb[1] = pk(S[10], S[11]); S[12] = EX(S[12]); S[13] = EX(S[13]); S[14] = EX(S[14]); S[15] = EX(S[15]); SBAR(); \
    } while (0)
#define TILE(CUR, NXT, PRV, T, LD) do { \
      UNIT(Sa, Sb, CUR, 1, PRV, O[3], CUR, O[0], ); \
      LDV(PRV, (T) + 3); SBAR(); \
      UNIT(Sb, Sa, CUR, 2, CUR, O[0], CUR, O[1], ); \
      UNIT(Sa, Sb, CUR, 3, CUR, O[1], CUR, O[2], if (LD) LDK(CUR, (T) + 4)); \
      UNIT(Sb, Sa, NXT, 0, CUR, O[2], CUR, O[3], ); } while (0)
#pragma unroll 1
    for (int t = 0; t < 12; t += 4) {
      TILE(A0, A1, A3, t, 1); TILE(A1, A2, A0, t + 1, 1); TILE(A2, A3, A1, t + 2, 1); TILE(A3, A0, A2, t + 3, 1);
    }
    TILE(A0, A1, A3, 12, 0);
    const lptr oc = lds + L_OC + lo16;
#define OUTP(T, I) _Pragma("unroll") for (int q4 = 0; q4 < 4; ++q4) *(LAS f32x4*)(oc + ((wave ^ I) * 3 + I - 1) * 4096 + q4 * 1024) = (f32x4){T[4 * q4], T[4 * q4 + 1], T[4 * q4 + 2], T[4 * q4 + 3]}
    f32x4 pv[12];
    UNIT(Sa, Sb, A1, 1, A0, O[3], A1, O[0], ); UNIT(Sb, Sa, A1, 2, A1, O[0], A1, O[1], ); UNIT(Sa, Sb, A2, 0, A1, O[1], A1, O[2], );
    UNIT(Sb, Sa, A2, 1, A1, O[2], A2, O[0], ); UNIT(Sa, Sb, A2, 2, A2, O[0], A2, O[1], ); UNIT(Sb, Sa, A3, 0, A2, O[1], A2, O[2], );
    UNIT(Sa, Sb, A3, 1, A2, O[2], A3, O[0], ); UNIT(Sb, Sa, A3, 2, A3, O[0], A3, O[1], lds_wait(j < 7 ? cCR : cPB, 4); lds_wait(cQD, 12));
    UNIT(Sa, Sb, A1, 3, A3, O[1], A3, O[2], OUTP(O[0], 3));
    UNIT(Sb, Sa, A2, 3, A3, O[2], A1, O[3], OUTP(O[1], 2));
    UNIT(Sa, Sb, A3, 3, A1, O[3], A2, O[3], OUTP(O[2], 1); lds_signal(cBB));
    UNIT(Sb, Sa, A3, -1, A2, O[3], A3, O[3], lds_wait(cBB, 4); _Pragma("unroll") for (int s2 = 0; s2 < 12; ++s2) pv[s2] = *(LAS f32x4*)(oc + wave * 12288 + s2 * 1024));
    Pb[2] = pk(Sb[12], Sb[13]); Pb[3] = pk(Sb[14], Sb[15]);
    O[3] = MFMA(A3.v1, BF(Pb), O[3]);
    __builtin_amdgcn_s_setprio(2);
    lds_signal(cGO + 4 * wave);
#pragma unroll
    for (int s2 = 0; s2 < 4; ++s2) pv[s2] = pv[s2] + pv[s2 + 4] + pv[s2 + 8];
    unsigned lo16b = lo16; PIN(lo16b);
    const int lane = lo16b >> 4, r32 = lane & 31, hi = lane >> 5, g4 = lane >> 4, tq = (lane & 15) >> 2, tp = lane & 3;
    f32x16 o = O[3];
#pragma unroll
    for (int r = 0; r < 16; ++r) o[r] += pv[r >> 2][r & 3];
    {
      float l = swap_hi(o[12]);
      float z0 = 0.f, z1 = 0.f;
#pragma unroll
      for (int r = 0; r < 16; r += 2) { z0 = __builtin_fmaf(o[r], 0.f, z0); z1 = __builtin_fmaf(o[r + 1], 0.f, z1); }
      const bool ok = (l >= 0x1p-90f) && (l <= 0x1p110f) && (z0 + z1 == 0.f);
      const bool slow = !wall(ok);
      if (slow) {
        const __amdgpu_buffer_rsrc_t rk = __builtin_amdgcn_make_buffer_rsrc(ws + WS_KV, 0, 262144, 0x00020000);
        poll(ws, WS_FKV + r32 * 128, tag);
        float m = -__builtin_inff();
#pragma unroll 1
        for (int t = 0; t < 64; ++t) {
          f32x16 s = MFMA(BF(BLD(rk, lo16, t * 4096)), Q0[3], zero16); s = MFMA(BF(BLD(rk, lo16 + 1024, t * 4096)), Q1[3], s);
#pragma unroll
          for (int r = 0; r < 16; ++r) m = __builtin_fmaxf(m, s[r]);
        }
        m = __builtin_fmaxf(swap_lo(m), swap_hi(m));
        o = zero16;
#pragma unroll 1
        for (int t = 0; t < 64; ++t) {
          f32x16 s = MFMA(BF(BLD(rk, lo16, t * 4096)), Q0[3], zero16); s = MFMA(BF(BLD(rk, lo16 + 1024, t * 4096)), Q1[3], s);
#pragma unroll
          for (int r = 0; r < 16; ++r) s[r] = EX(s[r] - m);
          o = MFMA(BF(BLD(rk, lo16 + 2048, t * 4096)), pack8(s, 0), o); o = MFMA(BF(BLD(rk, lo16 + 3072, t * 4096)), pack8(s, 1), o);
        }
        l = swap_hi(o[12]);
      }
      const float inv = __builtin_amdgcn_rcpf(l);
#pragma unroll
      for (int r = 0; r < 16; ++r) o[r] *= inv;
      if (hi) { o[12] = 0.f; o[13] = 0.f; o[14] = 0.f; o[15] = 0.f; }
      u32x4 f0 = U4(pack8(o, 0)), f1 = U4(pack8(o, 1));
      if (slow) {
#pragma unroll
        for (int k = 0; k < 4; ++k) { f0[k] = umin(f0[k], 0xfffffffeu); f1[k] = umin(f1[k], 0xfffffffeu); }
      }
      BST(f0, rs_ws, lo16, o_own); BST(f1, rs_ws, lo16 + 1024, o_own);
    }
    lds_signal(cOC);
    __builtin_amdgcn_s_setprio(0);
    if (j == 7) return;
    {
      const CAS Params* kp = KARGS();
      const __amdgpu_buffer_rsrc_t rs_out = __builtin_amdgcn_make_buffer_rsrc(kp->out, 0, 2048 * 896 * 4, 0x00020000);
      lds_wait(cWO, 4);
      u32x4 of[16];
      const unsigned ob = WS_O + (((2 * c + th) * 16 + 8 * chh) * 2) * 1024;
#define LDO(k) of[k] = BLD(rs_ws, lo16 + ((k) & 3) * 1024, ob + ((k) >> 2) * 4096)
      const int rowb = 224 * chh + 4 * (g4 >> 1) + tq;
      const unsigned wb = (L_WO + rowb * 128 + ((32 * (g4 & 1) + 8 * tp) ^ ((tq & 2) << 5))) ^ (chh << 6);
      bf16x8 w0[16], w1[16];
#pragma unroll
      for (int k = 0; k < 16; ++k) w1[k] = ldstr2(lds + (wb ^ 64) + (28 * (k >> 1) + 16 * (k & 1)) * 128, 1024);
      SBAR();
      {
        unsigned have = 0;
#pragma unroll 1
        for (unsigned s = 0; s < SPIN_CAP; ++s) {
          const unsigned w = __builtin_amdgcn_raw_buffer_load_b32(rs_ws, (lane & 7) * 2048 + 2044, ob, SC1);
          const unsigned rdy = (s + 1 < SPIN_CAP ? (unsigned)__builtin_amdgcn_ballot_w64(w != ~0u) : ~0u) & 0xffu & ~have;
#pragma unroll
          for (int h = 0; h < 8; ++h) if (rdy >> h & 1) { LDO(2 * h); LDO(2 * h + 1); }
          have |= rdy;
          if (have == 0xffu) break;
          __builtin_amdgcn_s_sleep(2);
        }
      }
      __builtin_amdgcn_s_setprio(2);
      f32x16 acc0 = zero16, acc1 = zero16;
#define OBAD(h) (!wall(umax(umax(umax(umax(of[2 * (h)][0], of[2 * (h)][1]), umax(of[2 * (h)][2], of[2 * (h)][3])), umax(of[2 * (h) + 1][0], of[2 * (h) + 1][1])), umax(of[2 * (h) + 1][2], of[2 * (h) + 1][3])) != ~0u))
#define OHEAD(h) do { \
        if (OBAD(h)) { \
          _Pragma("unroll 1") for (unsigned s = 0; s < SPIN_CAP; ++s) { LDO(2 * h); LDO(2 * h + 1); if (!OBAD(h)) break; __builtin_amdgcn_s_sleep(2); } \
        } \
        acc1 = MFMA(BF(of[2 * h]), w1[2 * h], acc1); acc1 = MFMA(BF(of[2 * h + 1]), w1[2 * h + 1], acc1); PIN(acc1); \
        w0[2 * h] = ldstr2(lds + wb + 28 * h * 128, 1024); w0[2 * h + 1] = ldstr2(lds + wb + (28 * h + 16) * 128, 1024); } while (0)
      OHEAD(0); OHEAD(1); OHEAD(2); OHEAD(3); OHEAD(4); OHEAD(5); OHEAD(6); OHEAD(7);
      lds_wait(cOC, 4);
#pragma unroll
      for (int k = 0; k < 4; ++k) acc0 = MFMA(BF(of[k]), w0[k], acc0);
      SBAR();
      const lptr tw = lds + L_OC + hi * 512 + r32 * 4;
#pragma unroll
      for (int r = 0; r < 16; ++r) *(LAS float*)(tw + (wave ^ 1) * 4096 + ((r & 3) + 8 * (r >> 2)) * 128) = acc1[r];
      SBAR();
#pragma unroll
      for (int k = 4; k < 8; ++k) acc0 = MFMA(BF(of[k]), w0[k], acc0);
      PIN(acc0);
      lds_signal(cPR + 4 * th);
#pragma unroll
      for (int k = 8; k < 16; ++k) acc0 = MFMA(BF(of[k]), w0[k], acc0);
      PIN(acc0);
      lds_wait(cPR + 4 * th, 2);
      f32x4 pa[4];
#pragma unroll
      for (int k = 0; k < 4; ++k) pa[k] = *(LAS f32x4*)(lds + L_OC + wave * 4096 + k * 1024 + lo16);
#pragma unroll
      for (int r = 0; r < 16; ++r) *(LAS float*)(tw + 16384 + wave * 4096 + ((r & 3) + 8 * (r >> 2)) * 128) = acc0[r];
      MEMBAR();
      const unsigned oo = ((64 * c + 32 * th + (lane >> 3)) * 896 + 448 + 64 * j + 32 * chh + (lane & 7) * 4) * 4;
#pragma unroll
      for (int k = 0; k < 4; ++k)
        __builtin_amdgcn_raw_buffer_store_b128(U4(pa[k] + *(LAS f32x4*)(lds + L_OC + 16384 + wave * 4096 + k * 1024 + lo16)), rs_out, oo + k * 28672, 0, 17);
    }
  } else {
    if (j == 7) {
      VMW(6); BAR();
      { const u32x2 t2 = *(LAS u32x2*)(lds + L_TAG);
        tag = ((u64)(unsigned)__builtin_amdgcn_readfirstlane(t2[1]) << 32) | (unsigned)__builtin_amdgcn_readfirstlane(t2[0]); }
      {
        const unsigned xk_off = (32 * chh + r32) * 128, wk_off = K_W + th * 3584 + (8 * (g4 >> 1) + tq) * 56 + (16 * (g4 & 1) + 4 * tp) * 2;
        bf16x8 fax[4], faw[4], fbx[4], fbw[4];
        f32x16 a = zero16;
#define KRD(B, i) _Pragma("unroll") for (int ks = 0; ks < 4; ++ks) { B##x[ks] = ldsf(lds + (i) * SK + xk_off + (((2 * ks + hi) ^ xsw) << 4)); B##w[ks] = ldstr2(lds + (i) * SK + wk_off + ks * 896, 224); } SBAR()
#define KMM(B) if (th) { _Pragma("unroll") for (int ks = 0; ks < 4; ++ks) a = MFMA(B##x[ks], B##w[ks], a); } else { _Pragma("unroll") for (int ks = 0; ks < 4; ++ks) a = MFMA(B##w[ks], B##x[ks], a); } PIN(a); SBAR()
        KRD(fa, 0); KRD(fb, 1); KMM(fa); KRD(fa, 2); KMM(fb); KRD(fb, 3); KMM(fa);
        VMW(0); BAR();
        KRD(fa, 4); KMM(fb); KRD(fb, 5); KMM(fa); KRD(fa, 6); KMM(fb); KMM(fa);
        if (th) {
#pragma unroll
          for (int r = 0; r < 16; ++r) a[r] = r32 < 28 ? a[r] : (r32 == 28 ? 1.f : 0.f);
        } else if (hi) { a[12] = 0.f; a[13] = 0.f; a[14] = 0.f; a[15] = 0.f; }
        const unsigned ko = WS_KV + (2 * c + chh) * 4096 + th * 2048;
        BST(U4(pack8(a, 0)), rs_ws, lo16, ko); BST(U4(pack8(a, 1)), rs_ws, lo16 + 1024, ko);
        lds_signal(cCR);
        publish4(ws, WS_FKV + c * 128, tag, lane, cPQ);
      }
      lds_wait_<8>(cQD, 8);
      __builtin_amdgcn_s_sleep(64);
      MARK();
      publish4(ws, WS_FH + (c * 8 + 7) * 128, tag, lane, cPB);
      if (c == 0 && wa == 0) {
        __builtin_amdgcn_s_sleep(127);
#pragma unroll 1
        for (int k = 0; k < 4; ++k) poll(ws, WS_FP + (k * 64 + lane) * 128, tag);
        if (lane == 0) __hip_atomic_store((GAS u64*)(ws + WS_EPOCH), tag, RLX_AGENT);
      }
      return;
    }
    if (wave < 7) VMW(6); else VMW(3);
    BAR(); LDCH(L_SA); LDCH(L_SA + CH); VMW(12); BAR();
    cf lam, coef, l16, l64; f32x16 ur = zero16, ui = zero16;
    f32x16 acc[4] = {zero16, zero16, zero16, zero16};
    const unsigned cs0 = wa * 16384u, cs1 = L_R1 + wa * 12288u;
    unsigned a_htw = L_HT + wa * 4096 + r32 * 64 + hi * 32, a_htr = L_HT + wa * 4096 + (8 * (g4 >> 1) + tq) * 64 + (g4 & 1) * 32 + tp * 8;
    unsigned a_hst = WS_H + ((2 * c + th) * 56 + 4 * j + 2 * chh) * 1024 + lo16;
    unsigned a_c[4], a_cn[2], a_x[2], a_ex, e_want; u32x4 dg[2]; cf l256, lw, lo_;
    const int seg = 2 * th + hi;
#pragma unroll
    for (int f = 0; f < 4; ++f) a_c[f] = cs0 + (32 * chh + r32) * 128 + (((2 * f + hi) ^ xsw) << 4);
#pragma unroll
    for (int f = 0; f < 2; ++f) a_cn[f] = cs1 + 8192 + (32 * chh + r32) * 64 + (((2 * f + hi) ^ ((r32 >> 2) & 3)) << 4);
    unsigned a_vh = (lane >> 2) * 896 + (((lane & 3) ^ ((lane >> 4) & 3)) << 4) + 384 + chh * 64, a_vo = ve ^ 64;
    unsigned a_vw = lrow * 896 + (((lane & 7) ^ ((lrow & 2) << 1)) << 4);
    unsigned a_fl = WS_FH + (c * 8 + (lane & 7)) * 128;
    unsigned a_el = (31 - umin(r32, c - 1)) * 512 + 4, a_eo = (32 - c + 2 * th + hi) * 512 + r32 * 16, a_hx = L_EX + 2048 + (32 * chh + r32) * 8;
    {
      LDCH(0); LDCH(CH);
      { const f32x4 c0 = *(LAS f32x4*)(lds + L_EX + (32 * chh + r32) * 16), c1 = *(LAS f32x4*)(lds + L_EX + 1024 + (32 * chh + r32) * 16);
        lam = {c0[0], c0[1]}; coef = {c0[2], c0[3]}; l16 = {c1[0], c1[1]}; l64 = {c1[2], c1[3]}; ud = *(LAS unsigned*)(lds + L_EX + 2048 + (32 * chh + r32) * 4); }
      { const int e = r32 - 8 * hi; const unsigned dv = ud << ((e & 1) * 16);
#pragma unroll
        for (int s2 = 0; s2 < 2; ++s2) {
          dg[s2] = (u32x4){(e >> 1) == 8 * s2 ? dv : 0u, (e >> 1) == 8 * s2 + 1 ? dv : 0u, (e >> 1) == 8 * s2 + 2 ? dv : 0u, (e >> 1) == 8 * s2 + 3 ? dv : 0u};
          a_x[s2] = L_SA + (32 * th + r32) * 128 + (((4 * chh + 2 * s2 + hi) ^ xsw) << 4); PIN(dg[s2]); PIN(a_x[s2]); }
        cf lseg = hi ? l16 : (cf){1.f, 0.f};
        if (th) lseg = cmul(lseg, cmul(l16, l16));
        const cf l128 = cmul(l64, l64), ls128 = cmul(lseg, l128);
        l256 = cmul(l128, l128); lw = th ? ls128 : lseg; lo_ = th ? lseg : ls128; e_want = (c - seg + 3) >> 2; a_ex = L_EX + (32 * chh + r32) * 8;
        PIN(l256.r); PIN(l256.i); PIN(lw.r); PIN(lw.i); PIN(lo_.r); PIN(lo_.i); PIN(e_want); PIN(a_ex); }
      PIN(a_htw); PIN(a_htr); PIN(a_hst); PIN(a_c[0]); PIN(a_c[1]); PIN(a_c[2]); PIN(a_c[3]); PIN(a_cn[0]); PIN(a_cn[1]); PIN(a_vh); PIN(a_vw); PIN(a_fl); PIN(a_el); PIN(a_eo); PIN(a_hx); PIN(a_vo);
      const int trow = 32 * th + 16 * ((r32 >> 2) & 1) + (r32 & 3) + 4 * (r32 >> 3);
      const unsigned xa_off = trow * 128, xa_sw = (trow >> 1) & 7, b_off = 8192 + (32 * chh + r32) * 128;
      bf16x8 gax[4], gar[4], gai[4], gbx[4], gbr[4], gbi[4];
      unsigned gx[3][4], gw[3][4];
#define GB(s_) ((s_) < 49152 ? 0u : (s_) < 107520 ? 49152u : 107520u)
#define GT(s_) ((s_) < 49152 ? 0 : (s_) < 107520 ? 1 : 2)
#pragma unroll
      for (int t = 0; t < 3; ++t)
#pragma unroll
        for (int ks = 0; ks < 4; ++ks) { gx[t][ks] = GB(t * 60000) + xa_off + (((2 * ks + hi) ^ xa_sw) << 4); gw[t][ks] = GB(t * 60000) + b_off + (((2 * ks + hi) ^ xsw) << 4); PIN(gx[t][ks]); PIN(gw[t][ks]); }
#define GRD(B, s_) _Pragma("unroll") for (int ks = 0; ks < 4; ++ks) { B##x[ks] = ldsf(lds + gx[GT(s_)][ks] + ((s_) - GB(s_))); \
        B##r[ks] = ldsf(lds + gw[GT(s_)][ks] + ((s_) - GB(s_))); B##i[ks] = ldsf(lds + gw[GT(s_)][ks] + ((s_) - GB(s_) + 8192)); } SBAR()
#define GMM(B) _Pragma("unroll") for (int ks = 0; ks < 4; ++ks) { ur = MFMA(B##x[ks], B##r[ks], ur); ui = MFMA(B##x[ks], B##i[ks], ui); } PIN(ur); PIN(ui); SBAR()
#define GAP() do { SBAR(); asm volatile("s_nop %0" :: "i"(7)); SBAR(); } while (0)
#define GMS(B) _Pragma("unroll") for (int ks = 0; ks < 4; ++ks) { ur = MFMA(B##x[ks], B##r[ks], ur); GAP(); ui = MFMA(B##x[ks], B##i[ks], ui); GAP(); } PIN(ur); PIN(ui); SBAR()
      __builtin_amdgcn_s_setprio(1);
      VMW(12); lds_barrierA(cAB, 4); GRD(ga, L_SA); GRD(gb, L_SA + CH); GMM(ga);
      lds_wait_<1>(cQD, 4);
      LDCH(2 * CH); GMM(gb); LDCH(3 * CH);
      VMW(12); lds_barrierA(cAB, 8);
      GRD(ga, 0); GRD(gb, CH); LDCH(L_SA); GMM(ga);
      VMW(6); lds_barrierA(cAB, 12);
      GRD(ga, 2 * CH); GMS(gb); GRD(gb, 3 * CH); GMS(ga);
      VMW(0); lds_barrierA(cAB, 16);
      GRD(ga, L_SA); GMS(gb); GMS(ga);
      { const u32x2 t2 = *(LAS u32x2*)(lds + L_TAG);
        tag = ((u64)(unsigned)__builtin_amdgcn_readfirstlane(t2[1]) << 32) | (unsigned)__builtin_amdgcn_readfirstlane(t2[0]); }
    }
    acc[0] = MFMA(ldsf(lds + a_x[0]), BF(dg[0]), acc[0]); acc[0] = MFMA(ldsf(lds + a_x[1]), BF(dg[1]), acc[0]);
    PIN(acc[0]);
    cf e = {0.f, 0.f};
#pragma unroll
    for (int r = 0; r < 16; ++r) {
      const float a = coef.r * ur[r] - coef.i * ui[r], b2 = -(coef.r * ui[r] + coef.i * ur[r]);
      ur[r] = a; ui[r] = b2; e = r ? cfma(lam, e, (cf){a, b2}) : (cf){a, b2};
    }
    *(LAS f32x2*)(lds + a_ex + th * 1024 + hi * 512) = (f32x2){e.r, e.i};
    __builtin_amdgcn_s_setprio(0);
    lds_barrierA(cAB, 20);
    cf g0;
    {
      const f32x2 e0 = *(LAS f32x2*)(lds + a_ex), e1 = *(LAS f32x2*)(lds + a_ex + 512), e2 = *(LAS f32x2*)(lds + a_ex + 1024), e3 = *(LAS f32x2*)(lds + a_ex + 1536);
      __builtin_amdgcn_s_setprio(1);
      const cf E2 = cfma(l16, (cf){e0[0], e0[1]}, (cf){e1[0], e1[1]}), E3 = cfma(l16, E2, (cf){e2[0], e2[1]}), E = cfma(l16, E3, (cf){e3[0], e3[1]});
      if (th) g0 = hi ? E3 : E2; else g0 = hi ? (cf){e0[0], e0[1]} : (cf){0.f, 0.f};
      PIN(g0.r); PIN(g0.i);
      if (th == 0 && hi == 0 && c < 31)
        BST(((u32x4){f2u(E.r), (unsigned)tag, f2u(E.i), (unsigned)tag}), rs_ws, r32 * 16, WS_E + ((2 * j + chh) * 32 + 31 - c) * 512);
    }
    __builtin_amdgcn_s_setprio(0);
    const CAS Params* kp = KARGS();
    {
      const u32x4 rc = mkrs((const char*)(th ? kp->cim : kp->cre) + (size_t)j * 57344, 57344);
      int n2 = (6 * cl + 2 * ((b & 7) % 3)) % 24;
#pragma unroll
      for (int n = 0; n < 24; ++n) {
        DMA(rc, (n & 1) ? a_vo : ve, (n2 & 7) * 7168 + (n2 >> 3) * 128 + chh * 512, (n2 < 16 ? cs0 : cs1 - 16384) + n2 * 1024, 0);
        n2 = n2 == 23 ? 0 : n2 + 1; }
#pragma unroll
      for (int n = 0; n < 4; ++n) DMA(rc, a_vh, n * 14336, cs1, 8192 + n * 1024);
    }
    cf G = g0;
    if (c > 0) {
      const __amdgpu_buffer_rsrc_t re = __builtin_amdgcn_make_buffer_rsrc(ws + WS_E + (2 * j + chh) * 16384, 0, 16384, 0x00020000);
      u32x4 ev[8];
#pragma unroll 1
      for (unsigned s = 0; s < SPIN_CAP; ++s) {
        const unsigned t1 = __builtin_amdgcn_raw_buffer_load_b32(re, a_el, 0, SC1);
        if (wall(t1 == (unsigned)tag)) break;
        __builtin_amdgcn_s_sleep(2);
      }
      MEMBAR();
#pragma unroll 1
      for (unsigned s = 0; s < SPIN_CAP; ++s) {
        unsigned got = 0;
#pragma unroll
        for (int m = 0; m < 8; ++m) ev[m] = BLD(re, a_eo, m * 2048);
        VMW(0);
        __builtin_amdgcn_s_setprio(1);
#pragma unroll
        for (int m = 0; m < 8; ++m) got += ((ev[m][1] ^ (unsigned)tag) | (ev[m][3] ^ (unsigned)tag)) == 0;
        if (wall(got == e_want)) break;
        __builtin_amdgcn_s_setprio(0);
        __builtin_amdgcn_s_sleep(4);
      }
      cf H = {u2f(ev[7][0]), u2f(ev[7][2])};
#pragma unroll
      for (int m = 6; m >= 0; --m) H = cfma(l256, H, (cf){u2f(ev[m][0]), u2f(ev[m][2])});
      H = cfma(l64, (cf){swap_hi(H.r), swap_hi(H.i)}, (cf){swap_lo(H.r), swap_lo(H.i)});
      *(LAS f32x2*)(lds + a_hx + th * 512) = (f32x2){H.r, H.i};
      G = cfma(lw, H, g0);
      __builtin_amdgcn_s_setprio(0);
      lds_barrierA(cPE + 4 * chh, 2);
      const f32x2 ho = *(LAS f32x2*)(lds + a_hx + (th ^ 1) * 512);
      __builtin_amdgcn_s_setprio(1);
      G = cfma(lo_, (cf){ho[0], ho[1]}, G);
    }
    __builtin_amdgcn_s_setprio(1);
    {
#pragma unroll
      for (int r = 0; r < 16; ++r) { G = cfma(lam, G, (cf){ur[r], ui[r]}); ur[r] = G.r; ui[r] = G.i; }
      *(LAS bf16x8*)(lds + a_htw) = pack8(ur, 0); *(LAS bf16x8*)(lds + a_htw + 16) = pack8(ur, 1);
      *(LAS bf16x8*)(lds + a_htw + 2048) = pack8(ui, 0); *(LAS bf16x8*)(lds + a_htw + 2064) = pack8(ui, 1);
      unsigned a_hs2 = a_hst + 28 * 1024; PIN(a_hs2);
      __builtin_amdgcn_s_setprio(0);
      asm volatile("s_waitcnt lgkmcnt(0)" ::: "memory");
#pragma unroll
      for (int k = 0; k < 4; ++k) BST(U4(ldstr2(lds + a_htr + (k >> 1) * 2048 + (k & 1) * 1024, 256)), rs_ws, (k >> 1) ? a_hs2 : a_hst, (k & 1) * 1024);
      MARK();
      publish4(ws, WS_FH + (c * 8 + j) * 128, tag, lane, cPB);
    }
    bf16x8 cfr[14][2];
    const int cflip = chh ? -1 : 1;
#define LDC(f) do { if ((f) < 12) { const unsigned a_ = a_c[(f) & 3] + ((f) < 8 ? ((f) >> 2) * 8192 : cs1 - cs0); cfr[f][0] = ldsf(lds + a_); cfr[f][1] = ldsf(lds + a_ + cflip * 4096); } \
                    else { cfr[f][0] = ldsf(lds + a_cn[(f) & 1]); cfr[f][1] = ldsf(lds + a_cn[(f) & 1] + cflip * 2048); } } while (0)
#pragma unroll
    for (int f = 0; f < 8; ++f) LDC(f);
    lds_signal(cCR);
    lds_wait_<1>(cCR, 4);
    {
      const u32x4 rw = mkrs((const char*)kp->wo + j * 128 + wa * 7168, 401408 - j * 128 - wa * 7168);
      int m2 = (4 * cl + (b & 7)) % 14;
#pragma unroll
      for (int m = 0; m < 14; ++m) { DMA(rw, a_vw, m2 * 28672, wa * 1024 + m2 * 4096, L_WO); m2 = m2 == 13 ? 0 : m2 + 1; }
      if (wa == 0 && lane < 32) *(LAS u32x4*)(lds + L_WO + 57344 + lo16) = (u32x4){0, 0, 0, 0};
    }
    poll(ws, a_fl, tag);
    VMW(0);
    lds_signal(cWO);
    {
      const unsigned hb = WS_H + (2 * c * 56 + 14 * wa) * 1024, h0 = hb + th * 57344, h1 = hb + (th ^ 1) * 57344;
      const unsigned fa = chh * 2048, fb = chh ? 0u - 12288u : 0u;
      u32x4 hf[14][2];
#define LDH(f) do { hf[f][0] = BLD(rs_ws, lo16, h0 + (f) * 1024 + ((f) < 12 ? fa : fb)); hf[f][1] = BLD(rs_ws, lo16, h1 + (f) * 1024 + ((f) < 12 ? fa : fb)); } while (0)
#define MMH(f) do { acc[0] = MFMA(BF(hf[f][0]), cfr[f][0], acc[0]); acc[1] = MFMA(BF(hf[f][0]), cfr[f][1], acc[1]); \
                    acc[2] = MFMA(BF(hf[f][1]), cfr[f][0], acc[2]); acc[3] = MFMA(BF(hf[f][1]), cfr[f][1], acc[3]); \
                    PIN(acc[0]); PIN(acc[1]); PIN(acc[2]); PIN(acc[3]); } while (0)
#pragma unroll
      for (int f = 0; f < 14; ++f) LDH(f);
      SBAR();
      lds_wait_<4>(cGO + 4 * wa, 1);
      __builtin_amdgcn_s_setprio(1);
#pragma unroll
      for (int f = 0; f < 8; ++f) { MMH(f); SBAR(); if (f >= 1 && f < 7) LDC(f + 7); SBAR(); }
      lds_signal(cP4);
#pragma unroll
      for (int f = 8; f < 14; ++f) MMH(f);
    }
    lds_wait(cP4, 4);
    rs_write(lds + L_G2, wa, lane, acc[1], acc[2], acc[3]);
    lds_barrierA(cAB, 24);
    f32x16 y = rs_read(lds + L_G2, wa, lane, acc[0]);
    const __amdgpu_buffer_rsrc_t rs_out = __builtin_amdgcn_make_buffer_rsrc(kp->out, 0, 2048 * 896 * 4, 0x00020000);
    MEMBAR();
#pragma unroll
    for (int r = 0; r < 16; ++r) *(LAS float*)(lds + L_G2 + wa * 12288 + hi * 512 + r32 * 4 + ((r & 3) + 8 * (r >> 2)) * 128) = y[r];
    MEMBAR();
    const unsigned oo = ((64 * c + 32 * th + (lane >> 3)) * 896 + 64 * j + 32 * chh + (lane & 7) * 4) * 4;
#pragma unroll
    for (int k = 0; k < 4; ++k) __builtin_amdgcn_raw_buffer_store_b128(*(LAS u32x4*)(lds + L_G2 + wa * 12288 + k * 1024 + lo16), rs_out, oo + k * 28672, 0, 17);
  }
}

extern "C" void kernel_launch(void* const* d_in, const int* in_sizes, int n_in, void* d_out, int out_size, void* d_ws, size_t ws_size,
                              hipStream_t stream) {
  Params p;
  p.xa = (const u16*)d_in[0]; p.lre = (const u16*)d_in[1]; p.lim = (const u16*)d_in[2]; p.bre = (const u16*)d_in[3]; p.bim = (const u16*)d_in[4];
  p.cre = (const u16*)d_in[5]; p.cim = (const u16*)d_in[6]; p.ldt = (const u16*)d_in[7]; p.dsk = (const u16*)d_in[8];
  p.xb = (const u16*)d_in[9]; p.wq = (const u16*)d_in[10]; p.wk = (const u16*)d_in[11]; p.wv = (const u16*)d_in[12]; p.wo = (const u16*)d_in[13];
  p.out = (float*)d_out; p.ws = (char*)d_ws;
  fused_kernel<<<256, 512, 0, stream>>>(p);
}
```
